```python
import torch

def _softmax(x, axis=-1):
    return torch.softmax(x, dim=axis)
def _gelu(x):
    return torch.nn.functional.gelu(x, approximate="tanh")
def _silu(x):
    return torch.nn.functional.silu(x)
def _sigmoid(x):
    return torch.sigmoid(x)
def _layernorm(x, axis=-1, eps=1e-5):
    m = x.mean(dim=axis, keepdim=True)
    v = x.var(dim=axis, keepdim=True, unbiased=False)
    return (x - m) / torch.sqrt(v + eps)
def _scatter_add(tgt, idx, src, axis=0):
    return tgt.clone().index_add_(axis, idx, src)
def _segment_sum(src, seg, n):
    out = torch.zeros((n,) + src.shape[1:], dtype=src.dtype)
    return out.index_add_(0, seg, src)
def _pad(x, pw):
    flat = tuple(v for pair in reversed(pw) for v in pair)
    return torch.nn.functional.pad(x, flat)

kernel_name = 'gated_mix__ssm_conv_s5__ssm_conv_h3__prompt'

def setup_inputs():

    _w = (lambda a: (lambda t: t.to(torch.bfloat16) if t.is_floating_point() else t)(torch.as_tensor(a)))
    def _A_setup():
        B, S, D = 1, 2048, 1024
        P = D
        Lambda_re = -0.5 * (lambda _x: torch.exp(torch.as_tensor(_x)))((lambda *s: torch.randn(*s, dtype=torch.bfloat16) * 0.2)(P))
        Lambda_im = (lambda *s: torch.randn(*s, dtype=torch.bfloat16) * 0.4)(P) * 1.0
        B_re = (lambda *s: torch.randn(*s, dtype=torch.bfloat16) * 0.2)(P, D) * (D ** -0.5)
        B_im = (lambda *s: torch.randn(*s, dtype=torch.bfloat16) * 0.2)(P, D) * (D ** -0.5)
        C_re = (lambda *s: torch.randn(*s, dtype=torch.bfloat16) * 0.2)(D, P) * (P ** -0.5)
        C_im = (lambda *s: torch.randn(*s, dtype=torch.bfloat16) * 0.2)(D, P) * (P ** -0.5)
        log_dt = (lambda *s: torch.randn(*s, dtype=torch.bfloat16) * 0.2)(P) * 0.3 - 2.0
        Dskip = (lambda *s: torch.randn(*s, dtype=torch.bfloat16) * 0.05)(D)
        return {
            "x": _w((lambda *s: torch.randn(*s, dtype=torch.bfloat16) * 0.05)(B, S, D)),
            "Lambda_re": _w(Lambda_re),
            "Lambda_im": _w(Lambda_im),
            "B_re": _w(B_re),
            "B_im": _w(B_im),
            "C_re": _w(C_re),
            "C_im": _w(C_im),
            "log_dt": _w(log_dt),
            "Dskip": _w(Dskip),
        }
    _A_in = _A_setup()

    def _B_setup():
        B, S, D = 1, 2048, 1024
        ks = 4
        return {
            "q": _w((lambda *s: torch.randn(*s, dtype=torch.bfloat16) * 0.05)(B, S, D)),
            "k": _w((lambda *s: torch.randn(*s, dtype=torch.bfloat16) * 0.4)(B, S, D)),
            "v": _w((lambda *s: torch.randn(*s, dtype=torch.bfloat16) * 0.2)(B, S, D)),
            "shift_k": _w((lambda *s: torch.randn(*s, dtype=torch.bfloat16) * 0.4)(D, ks) * 0.5),
            "ssm_k": _w((lambda *s: torch.randn(*s, dtype=torch.bfloat16) * 0.4)(D, S) * (1.0 / S ** 0.5)),
            "ssm_D": _w((lambda *s: torch.randn(*s, dtype=torch.bfloat16) * 0.2)(D)),
        }
    _B_in = _B_setup()

    return {
        "A_x": _A_in["x"],
        "A_Lambda_re": _A_in["Lambda_re"],
        "A_Lambda_im": _A_in["Lambda_im"],
        "A_B_re": _A_in["B_re"],
        "A_B_im": _A_in["B_im"],
        "A_C_re": _A_in["C_re"],
        "A_C_im": _A_in["C_im"],
        "A_log_dt": _A_in["log_dt"],
        "A_Dskip": _A_in["Dskip"],
        "B_q": _B_in["q"],
        "B_k": _B_in["k"],
        "B_v": _B_in["v"],
        "B_shift_k": _B_in["shift_k"],
        "B_ssm_k": _B_in["ssm_k"],
        "B_ssm_D": _B_in["ssm_D"],
        "gate_w": _w((lambda *s: torch.randn(*s, dtype=torch.bfloat16) * 0.05)(1024) * 16.0),
    }

def reference(A_x, A_Lambda_re, A_Lambda_im, A_B_re, A_B_im, A_C_re, A_C_im, A_log_dt, A_Dskip, B_q, B_k, B_v, B_shift_k, B_ssm_k, B_ssm_D, gate_w):
    def _A_ref(x, Lambda_re, Lambda_im, B_re, B_im, C_re, C_im, log_dt, Dskip):
        Bsz, L, H = x.shape
        Lam = Lambda_re + 1j * Lambda_im
        Bmat = B_re + 1j * B_im
        Cmat = C_re + 1j * C_im
        dt = torch.exp(log_dt)
        Lam_bar = torch.exp(dt * Lam)
        B_bar = ((Lam_bar - 1.0) / Lam)[:, None] * Bmat
        Bu = torch.einsum("bld,pd->blp", x + 0j, B_bar)
        h = torch.zeros_like(Bu[:, 0, :])
        hs = []
        for t in range(L):
            h = Lam_bar[None, :] * h + Bu[:, t, :]
            hs.append(h)
        hseq = torch.stack(hs, 1)
        y = 2.0 * torch.einsum("blp,dp->bld", hseq, Cmat).real + x * Dskip[None, None, :]
        return y

    def _B_ref(q, k, v, shift_k, ssm_k, ssm_D):
        B, S, D = q.shape
        ks = shift_k.shape[-1]
        k_pad = _pad(k, ((0, 0), (ks - 1, 0), (0, 0)))
        k_shift = torch.zeros_like(k)
        for t in range(ks):
            k_shift = k_shift + k_pad[:, t:t + S, :] * shift_k[:, ks - 1 - t][None, None, :]
        kv = k_shift * v
        L = 2 * S
        kv_pad = _pad(kv, ((0, 0), (0, S), (0, 0)))
        kf_pad = _pad(ssm_k, ((0, 0), (0, S)))
        kv_f = (lambda x, axes=(-2,-1): torch.fft.rfft2(x.float(), dim=axes))(kv_pad[:, None, :, :], axes=(1, 2))
        kf_f = (lambda x, axes=(-2,-1): torch.fft.rfft2(x.float(), dim=axes))(kf_pad[:, None, :], axes=(1, 2))
        kf_f = (lambda x, *d: x.permute(*d))(kf_f, 1, 2, 0)
        y_f = kv_f * kf_f[None, :, :, :]
        y = (lambda x, s, axes=(-2,-1): torch.fft.irfft2(x, s=s, dim=axes).to(torch.bfloat16))(y_f, (1, L), axes=(1, 2))[:, 0, :S, :]
        y = y + kv * ssm_D[None, None, :]
        return q * y

    x = A_x
    g = _sigmoid((x * gate_w).sum(-1, keepdims=True))
    a = _A_ref(x, A_Lambda_re, A_Lambda_im, A_B_re, A_B_im, A_C_re, A_C_im, A_log_dt, A_Dskip)
    b = _B_ref(x, B_k, B_v, B_shift_k, B_ssm_k, B_ssm_D)
    return g * a + (1 - g) * b
```

```cpp
#include <hip/hip_runtime.h>
#include <hip/hip_bf16.h>
#include <stdint.h>

typedef unsigned short u16;
typedef unsigned long long u64;
typedef __attribute__((ext_vector_type(4))) float f32x4;
typedef __attribute__((address_space(3))) f32x4 lds_f32x4;
struct cf { float r, i; };
__device__ __forceinline__ cf cmul(cf a, cf b) { return {a.r * b.r - a.i * b.i, a.r * b.i + a.i * b.r}; }
__device__ __forceinline__ float bf2f(u16 v) { return __uint_as_float(((unsigned)v) << 16); }
__device__ __forceinline__ float rbf(float f) { __bf16 b = (__bf16)f; return bf2f(__builtin_bit_cast(u16, b)); }

__global__ __launch_bounds__(512, 2) void mk_kernel(const u16* x, const u16* Bre, const u16* Bim, const u16* kin, const u16* vin, const u16* gatew, const u16* ssmk, char* ws,
                                                    const u16* Cre, const u16* Cim, float* out, const u16* dskip, const u16* shiftk, const u16* ssmD,
                                                    const u16* lre_, const u16* lim_, const u16* logdt_) {
    const int lane = threadIdx.x & 63, wid = __builtin_amdgcn_readfirstlane(threadIdx.x >> 6);
    const int b = blockIdx.x;
    {
        u64 pc; unsigned len;
        asm volatile(".subsection 1\n.Lmk_end:\n.subsection 0\n\ts_getpc_b64 %0\n.Lmk_code:\n\ts_mov_b32 %1, .Lmk_end-.Lmk_code-4" : "=s"(pc), "=s"(len));
        unsigned off = (unsigned)(((b >> 3) * 8 + wid) * 2 + (lane >> 5)) * 128u;
        off = off < len ? off : len;
        asm volatile("s_mov_b32 m0, 134656\n\ts_nop 0\n\tglobal_load_lds_dword %0, %1" :: "v"(off), "s"(pc) : "memory");
    }
    asm volatile(
"s_cmp_lt_u32 %2, 4\n"
"s_cbranch_scc1 .LC\n"
"s_load_dwordx16 s[16:31], %0, 0\n"
"s_load_dwordx16 s[32:47], %0, 64\n"
"s_and_b32 s48, %1, 1\n"
"s_bfe_u32 s49, %1, 0x20001\n"
"s_bfe_u32 s13, %1, 0x20003\n"
"s_lshr_b32 s50, %1, 5\n"
"s_lshl2_add_u32 s51, s49, s13\n"
"s_lshl3_add_u32 s52, s48, s50\n"
"s_lshl1_add_u32 s82, s48, s49\n"
"s_lshl_b32 s82, s82, 9\n"
"s_and_b32 s59, s50, 1\n"
"s_lshr_b32 s53, %1, 3\n"
"s_and_b32 s54, %1, 7\n"
"s_and_b32 s15, %2, 3\n"
"s_lshl_b32 s55, s15, 11\n"
"s_lshl_b32 s56, s15, 12\n"
"s_mul_i32 s57, s15, 0x2080\n"
"s_add_u32 s57, s57, 0x18000\n"
"s_lshl_b32 s58, s54, 5\n"
"s_add_u32 s58, s58, s53\n"
"s_lshl4_add_u32 s80, s51, s15\n"
"s_lshl_b32 s80, s80, 13\n"
"v_mbcnt_lo_u32_b32 v152, -1, 0\n"
"v_mbcnt_hi_u32_b32 v152, -1, v152\n"
"v_lshrrev_b32 v0, 4, v152\n"
"v_xor_b32 v0, v0, v152\n"
"v_and_b32 v0, 7, v0\n"
"v_lshlrev_b32 v0, 4, v0\n"
"v_lshrrev_b32 v98, 3, v152\n"
"v_lshl_or_b32 v98, v98, 11, v0\n"
"v_xor_b32 v99, 0x4040, v98\n"
"v_subrev_u32 v99, 0x400, v99\n"
"v_and_b32 v136, 0x1fff, v98\n"
"v_lshrrev_b32 v0, 5, v152\n"
"v_lshl_or_b32 v136, v0, 15, v136\n"
"v_xor_b32 v137, 0x10040, v136\n"
"v_subrev_u32 v137, 0x400, v137\n"
"v_lshlrev_b32 v194, 4, v152\n"
"v_and_b32 v0, 0xf0, v194\n"
"v_lshrrev_b32 v4, 4, v152\n"
"v_lshl_or_b32 v2, v4, 11, v0\n"
"s_lshl_b32 s13, s15, 6\n"
"v_xor_b32 v2, s13, v2\n"
"v_xor_b32 v1, 0x4020, v2\n"
"v_xor_b32 v0, 0xe0, v0\n"
"s_lshl2_add_u32 s81, s52, s15\n"
"s_lshl_b32 s81, s81, 15\n"
"v_and_b32 v3, 3, v152\n"
"v_lshlrev_b32 v3, 7, v3\n"
"v_bfe_u32 v4, v152, 2, 2\n"
"v_lshl_or_b32 v3, v4, 11, v3\n"
"s_waitcnt lgkmcnt(0)\n"
"s_sub_u32 s20, s20, 0x800\n"
"s_subb_u32 s21, s21, 0\n"
"s_add_u32 s13, s82, 0\n"
"s_and_b32 s13, s13, 0x7ff\n"
"s_add_u32 s14, s13, s81\n"
"s_add_u32 s13, s13, s80\n"
"s_add_u32 s60, s16, s13\n"
"s_addc_u32 s61, s17, 0\n"
"s_add_u32 s62, s18, s14\n"
"s_addc_u32 s63, s19, 0\n"
"s_add_u32 s64, s20, s14\n"
"s_addc_u32 s65, s21, 0\n"
"s_lshl2_add_u32 s13, s53, s15\n"
"s_lshl_b32 s13, s13, 15\n"
"s_lshl_b32 s14, s54, 8\n"
"s_add_u32 s13, s13, s14\n"
"s_add_u32 s66, s22, s13\n"
"s_addc_u32 s67, s23, 0\n"
"s_add_u32 s68, s24, s13\n"
"s_addc_u32 s69, s25, 0\n"
"s_add_u32 s13, s82, 0x200\n"
"s_and_b32 s13, s13, 0x7ff\n"
"s_lshl3_add_u32 s14, s51, s50\n"
"s_lshl2_add_u32 s14, s14, s15\n"
"s_lshl_b32 s14, s14, 12\n"
"s_add_u32 s13, s13, s14\n"
"s_add_u32 s70, s16, s13\n"
"s_addc_u32 s71, s17, 0\n"
"s_mov_b64 exec, 0xff\n"
"s_mov_b32 m0, 0x20e00\n"
"s_nop 0\n"
"global_load_lds_dword v3, s[70:71]\n"
"s_mov_b64 exec, -1\n"
"s_add_u32 m0, s56, 0x2000\n"
"s_nop 0\n"
"global_load_lds_dwordx4 v98, s[62:63]\n"
"s_add_u32 m0, s56, 0x2000\n"
"s_nop 0\n"
"global_load_lds_dwordx4 v99, s[62:63] offset:1024\n"
"s_add_u32 m0, s56, 0x18000\n"
"s_nop 0\n"
"global_load_lds_dwordx4 v2, s[66:67] nt\n"
"s_add_u32 m0, s56, 0x2000\n"
"s_nop 0\n"
"global_load_lds_dwordx4 v98, s[64:65] offset:2048\n"
"s_add_u32 m0, s56, 0x2000\n"
"s_nop 0\n"
"global_load_lds_dwordx4 v99, s[64:65] offset:3072\n"
"s_add_u32 m0, s56, 0x1c000\n"
"s_nop 0\n"
"global_load_lds_dwordx4 v2, s[68:69] nt\n"
"s_add_u32 m0, s55, 0\n"
"s_nop 0\n"
"global_load_lds_dwordx4 v136, s[60:61]\n"
"s_add_u32 m0, s55, 0\n"
"s_nop 0\n"
"global_load_lds_dwordx4 v137, s[60:61] offset:1024\n"
"s_add_u32 m0, s56, 0x18800\n"
"s_nop 0\n"
"global_load_lds_dwordx4 v1, s[66:67] nt\n"
"s_add_u32 s30, s30, 0x1fffff\n"
"s_addc_u32 s31, s31, 0\n"
"s_and_b32 s30, s30, 0xffe00000\n"
"s_load_dwordx2 s[10:11], s[30:31], 0\n"
"s_lshl_b32 s13, s15, 9\n"
"s_add_u32 s70, s26, s13\n"
"s_addc_u32 s71, s27, 0\n"
"s_mov_b32 exec_hi, 0\n"
"s_add_u32 m0, s13, 0x20600\n"
"s_nop 0\n"
"global_load_lds_dwordx4 v194, s[70:71]\n"
"s_mov_b64 exec, -1\n"
"s_add_u32 m0, s56, 0x7f80\n"
"s_nop 0\n"
"global_load_lds_dwordx4 v98, s[62:63] offset:128\n"
"s_add_u32 m0, s56, 0x7f80\n"
"s_nop 0\n"
"global_load_lds_dwordx4 v99, s[62:63] offset:1152\n"
"s_add_u32 m0, s56, 0x1c800\n"
"s_nop 0\n"
"global_load_lds_dwordx4 v1, s[68:69] nt\n"
"s_add_u32 m0, s56, 0x7f80\n"
"s_nop 0\n"
"global_load_lds_dwordx4 v98, s[64:65] offset:2176\n"
"s_add_u32 m0, s56, 0x7f80\n"
"s_nop 0\n"
"global_load_lds_dwordx4 v99, s[64:65] offset:3200\n"
"s_add_u32 s66, s66, 0x2000\n"
"s_addc_u32 s67, s67, 0\n"
"s_add_u32 s68, s68, 0x2000\n"
"s_addc_u32 s69, s69, 0\n"
"s_add_u32 m0, s56, 0x18400\n"
"s_nop 0\n"
"global_load_lds_dwordx4 v2, s[66:67] nt\n"
"s_add_u32 m0, s55, 0x5f80\n"
"s_nop 0\n"
"global_load_lds_dwordx4 v136, s[60:61] offset:128\n"
"s_add_u32 m0, s55, 0x5f80\n"
"s_nop 0\n"
"global_load_lds_dwordx4 v137, s[60:61] offset:1152\n"
"s_add_u32 m0, s56, 0x1c400\n"
"s_nop 0\n"
"global_load_lds_dwordx4 v2, s[68:69] nt\n"
"s_waitcnt lgkmcnt(0)\n"
"s_add_u32 s10, s10, 1\n"
"s_addc_u32 s11, s11, 0\n"
"s_add_u32 m0, s56, 0xdf00\n"
"s_nop 0\n"
"global_load_lds_dwordx4 v98, s[62:63] offset:256\n"
"s_add_u32 m0, s56, 0xdf00\n"
"s_nop 0\n"
"global_load_lds_dwordx4 v99, s[62:63] offset:1280\n"
"s_add_u32 m0, s56, 0x18c00\n"
"s_nop 0\n"
"global_load_lds_dwordx4 v1, s[66:67] nt\n"
"s_add_u32 m0, s56, 0xdf00\n"
"s_nop 0\n"
"global_load_lds_dwordx4 v98, s[64:65] offset:2304\n"
"s_add_u32 m0, s56, 0xdf00\n"
"s_nop 0\n"
"global_load_lds_dwordx4 v99, s[64:65] offset:3328\n"
"s_add_u32 m0, s56, 0x1cc00\n"
"s_nop 0\n"
"global_load_lds_dwordx4 v1, s[68:69] nt\n"
"s_add_u32 m0, s55, 0xbf00\n"
"s_nop 0\n"
"global_load_lds_dwordx4 v136, s[60:61] offset:256\n"
"s_add_u32 m0, s55, 0xbf00\n"
"s_nop 0\n"
"global_load_lds_dwordx4 v137, s[60:61] offset:1280\n"
"s_lshl_b32 s13, s53, 6\n"
"s_cselect_b32 s14, 3, 0\n"
"s_sub_u32 s13, s13, s14\n"
"s_add_u32 s13, s13, s15\n"
"s_lshl_b32 s13, s13, 11\n"
"s_lshl_b32 s14, s54, 8\n"
"s_add_u32 s13, s13, s14\n"
"s_add_u32 s70, s22, s13\n"
"s_addc_u32 s71, s23, 0\n"
"s_lshl_b32 s13, s15, 8\n"
"s_mov_b64 exec, 0xffff\n"
"s_add_u32 m0, s13, 0x20200\n"
"s_nop 0\n"
"global_load_lds_dwordx4 v0, s[70:71]\n"
"s_mov_b64 exec, -1\n"
"s_add_u32 m0, s56, 0x13e80\n"
"s_nop 0\n"
"global_load_lds_dwordx4 v98, s[62:63] offset:384\n"
"global_load_lds_dwordx4 v99, s[62:63] offset:1408\n"
"global_load_lds_dwordx4 v98, s[64:65] offset:2432\n"
"global_load_lds_dwordx4 v99, s[64:65] offset:3456\n"
"s_add_u32 m0, s55, 0x11e80\n"
"s_nop 0\n"
"global_load_lds_dwordx4 v136, s[60:61] offset:384\n"
"global_load_lds_dwordx4 v137, s[60:61] offset:1408\n"
"v_bfe_u32 v0, v152, 1, 3\n"
"v_lshrrev_b32 v224, 4, v152\n"
"v_xor_b32 v0, v0, v224\n"
"v_and_b32 v224, 15, v152\n"
"v_lshlrev_b32 v224, 7, v224\n"
"v_lshl_or_b32 v224, v0, 4, v224\n"
"s_add_u32 s13, s56, 0x2000\n"
"v_add_u32 v104, s13, v224\n"
"v_xor_b32 v112, 64, v104\n"
"v_add_u32 v120, 0x10000, v104\n"
"v_add_u32 v128, 0x10000, v112\n"
"v_xor_b32 v219, 64, v224\n"
"v_add_u32 v217, 0x10000, v224\n"
"v_add_u32 v216, 0x10000, v219\n"
"s_mul_i32 s97, s15, 0x11e0\n"
"s_add_u32 s97, s97, 0x20200\n"
"v_add_u32 v195, s57, v194\n"
"v_add_u32 v4, s97, v194\n"
"v_add_u32 v229, s56, v194\n"
"v_mov_b32 v0, 0\n"
"v_mov_b32 v1, 0\n"
"v_mov_b32 v2, 0\n"
"v_mov_b32 v3, 0\n"
"s_mov_b64 exec, 0x3fffffff\n"
"ds_write_b128 v4, v[0:3] offset:4096\n"
"s_mov_b64 exec, -1\n"
"s_waitcnt vmcnt(26) lgkmcnt(0)\n"
"s_barrier\n"
"v_bfe_u32 v0, v152, 1, 1\n"
"v_mul_u32_u24 v198, 0x140, v0\n"
"v_lshrrev_b32 v0, 5, v152\n"
"v_lshl_or_b32 v198, v0, 9, v198\n"
"v_bfe_u32 v0, v152, 4, 1\n"
"v_lshl_or_b32 v198, v0, 7, v198\n"
"v_bfe_u32 v0, v152, 2, 2\n"
"v_lshl_or_b32 v198, v0, 4, v198\n"
"v_and_b32 v0, 1, v152\n"
"v_lshl_or_b32 v198, v0, 3, v198\n"
"v_xor_b32 v199, 64, v198\n"
"v_add_u32 v196, 0x20000, v198\n"
"v_add_u32 v197, 0x20000, v199\n"
"v_add_u32 v200, 0x10000, v198\n"
"v_add_u32 v201, 0x10000, v199\n"
"v_and_b32 v205, 3, v152\n"
"v_bfe_u32 v0, v152, 2, 1\n"
"v_lshl_or_b32 v205, v0, 4, v205\n"
"v_lshlrev_b32 v205, 7, v205\n"
"v_lshrrev_b32 v0, 4, v152\n"
"v_add_u32 v202, 0x8000, v194\n"
"s_lshl_b32 s13, s59, 4\n"
"v_xor_b32 v203, s13, v194\n"
"v_lshlrev_b32 v1, 4, v0\n"
"v_xor_b32 v203, v203, v1\n"
"v_and_b32 v203, 0xf0, v203\n"
"v_lshl_or_b32 v203, v0, 15, v203\n"
"v_and_b32 v204, 31, v152\n"
"v_lshlrev_b32 v204, 7, v204\n"
"s_waitcnt vmcnt(16)\n"
"s_barrier\n"
"ds_read_b128 v[0:3], v104 offset:24576\n"
"ds_read_b128 v[4:7], v104 offset:26624\n"
"ds_read_b128 v[8:11], v112 offset:24576\n"
"ds_read_b128 v[12:15], v112 offset:26624\n"
"s_add_u32 s13, s82, 0x200\n"
"s_and_b32 s13, s13, 0x7ff\n"
"s_add_u32 s14, s13, s81\n"
"s_add_u32 s13, s13, s80\n"
"s_add_u32 s60, s16, s13\n"
"s_addc_u32 s61, s17, 0\n"
"s_add_u32 s62, s18, s14\n"
"s_addc_u32 s63, s19, 0\n"
"s_add_u32 s64, s20, s14\n"
"s_addc_u32 s65, s21, 0\n"
"s_add_u32 m0, s56, 0x2000\n"
"s_nop 0\n"
"global_load_lds_dwordx4 v98, s[62:63]\n"
"global_load_lds_dwordx4 v99, s[62:63] offset:1024\n"
"global_load_lds_dwordx4 v98, s[64:65] offset:2048\n"
"global_load_lds_dwordx4 v99, s[64:65] offset:3072\n"
"s_add_u32 m0, s55, 0\n"
"s_nop 0\n"
"global_load_lds_dwordx4 v136, s[60:61]\n"
"global_load_lds_dwordx4 v137, s[60:61] offset:1024\n"
"s_lshl4_add_u32 s83, s51, s52\n"
"s_lshl2_add_u32 s83, s83, s15\n"
"s_lshl_b32 s13, s83, 11\n"
"s_add_u32 s66, s16, s13\n"
"s_addc_u32 s67, s17, 0\n"
"s_add_u32 s68, s66, 0x200000\n"
"s_addc_u32 s69, s67, 0\n"
"global_load_dwordx4 v[16:19], v194, s[66:67]\n"
"global_load_dwordx4 v[20:23], v194, s[66:67] offset:1024\n"
"global_load_dwordx4 v[24:27], v194, s[68:69]\n"
"global_load_dwordx4 v[28:31], v194, s[68:69] offset:1024\n"
"s_waitcnt vmcnt(17) lgkmcnt(0)\n"
"s_barrier\n"
"s_add_u32 m0, s56, 0x7f80\n"
"s_nop 0\n"
"global_load_lds_dwordx4 v98, s[62:63] offset:128\n"
"global_load_lds_dwordx4 v99, s[62:63] offset:1152\n"
"global_load_lds_dwordx4 v98, s[64:65] offset:2176\n"
"global_load_lds_dwordx4 v99, s[64:65] offset:3200\n"
"s_add_u32 m0, s55, 0x5f80\n"
"s_nop 0\n"
"global_load_lds_dwordx4 v136, s[60:61] offset:128\n"
"global_load_lds_dwordx4 v137, s[60:61] offset:1152\n"
"v_mov_b32 v192, s10\n"
"v_mov_b32 v193, s11\n"
"v_lshrrev_b32 v32, 4, v152\n"
"v_and_b32 v208, 0xf0, v194\n"
"v_lshrrev_b32 v206, 1, v208\n"
"s_mul_i32 s13, s15, 0xb00\n"
"s_add_u32 s13, s13, 0x24980\n"
"v_add_u32 v215, s13, v194\n"
"s_add_u32 s13, s13, 0x800\n"
"v_add_u32 v218, s13, v208\n"
"v_lshl_or_b32 v208, v32, 16, v208\n"
"v_xor_b32 v207, 64, v208\n"
"s_mov_b32 s96, 0xff\n"
"s_and_b32 s13, s10, 0xff\n"
"v_mov_b32 v213, s13\n"
"s_sub_u32 s13, 16, s51\n"
"v_add_u32 v33, s13, v32\n"
"v_lshl_or_b32 v214, v33, 7, v206\n"
"v_cmp_gt_u32 vcc, 16, v33\n"
"s_nop 1\n"
"v_cndmask_b32 v220, 0, v213, vcc\n"
"v_cmp_gt_u32 vcc, 12, v33\n"
"s_nop 1\n"
"v_cndmask_b32 v221, 0, v213, vcc\n"
"v_cmp_gt_u32 vcc, 8, v33\n"
"s_nop 1\n"
"v_cndmask_b32 v222, 0, v213, vcc\n"
"v_cmp_gt_u32 vcc, 4, v33\n"
"s_nop 1\n"
"v_cndmask_b32 v223, 0, v213, vcc\n"
"s_waitcnt vmcnt(16)\n"
"s_barrier\n"
"ds_read_b128 v[32:35], v120 offset:8192\n"
"ds_read_b128 v[36:39], v120 offset:10240\n"
"ds_read_b128 v[40:43], v128 offset:8192\n"
"ds_read_b128 v[44:47], v128 offset:10240\n"
"s_add_u32 m0, s56, 0xdf00\n"
"s_nop 0\n"
"global_load_lds_dwordx4 v98, s[62:63] offset:256\n"
"global_load_lds_dwordx4 v99, s[62:63] offset:1280\n"
"global_load_lds_dwordx4 v98, s[64:65] offset:2304\n"
"global_load_lds_dwordx4 v99, s[64:65] offset:3328\n"
"s_add_u32 m0, s55, 0xbf00\n"
"s_nop 0\n"
"global_load_lds_dwordx4 v136, s[60:61] offset:256\n"
"global_load_lds_dwordx4 v137, s[60:61] offset:1280\n"
"s_lshl2_add_u32 s13, s52, s15\n"
"s_lshl_b32 s13, s13, 12\n"
"s_add_u32 s13, s13, 0x100000\n"
"s_add_u32 s76, s30, s13\n"
"s_addc_u32 s77, s31, 0\n"
"s_sub_u32 s13, 15, s51\n"
"s_lshl_b32 s13, s13, 7\n"
"s_add_u32 s84, s76, s13\n"
"s_addc_u32 s85, s77, 0\n"
"s_and_b32 s77, s77, 0xffff\n"
"s_mov_b32 s78, 0x1000\n"
"s_mov_b32 s79, 0x20000\n"
"s_lshl1_add_u32 s13, s51, s48\n"
"s_lshl2_add_u32 s14, s50, s15\n"
"s_and_b32 s87, s14, 24\n"
"s_add_u32 s87, s87, s14\n"
"s_lshl_b32 s88, s13, 19\n"
"s_lshl_b32 s87, s87, 10\n"
"s_add_u32 s88, s88, s87\n"
"s_add_u32 s88, s88, 0x440000\n"
"s_lshr_b32 s86, s88, 8\n"
"s_and_b32 s86, s86, 0x2000\n"
"s_add_u32 s86, s86, s88\n"
"s_add_u32 s86, s30, s86\n"
"s_addc_u32 s87, s31, 0\n"
"s_lshl_b32 s13, s13, 13\n"
"s_lshl_b32 s14, s14, 7\n"
"s_add_u32 s13, s13, s14\n"
"s_add_u32 s13, s13, 0x21000\n"
"s_add_u32 s88, s30, s13\n"
"s_addc_u32 s89, s31, 0\n"
"s_waitcnt vmcnt(16) lgkmcnt(0)\n"
"s_barrier\n"
"s_add_u32 m0, s56, 0x13e80\n"
"s_nop 0\n"
"global_load_lds_dwordx4 v98, s[62:63] offset:384\n"
"global_load_lds_dwordx4 v99, s[62:63] offset:1408\n"
"global_load_lds_dwordx4 v98, s[64:65] offset:2432\n"
"global_load_lds_dwordx4 v99, s[64:65] offset:3456\n"
"s_add_u32 m0, s55, 0x11e80\n"
"s_nop 0\n"
"global_load_lds_dwordx4 v136, s[60:61] offset:384\n"
"global_load_lds_dwordx4 v137, s[60:61] offset:1408\n"
"v_bfe_u32 v48, v152, 1, 3\n"
"v_bfe_u32 v209, v152, 4, 1\n"
"v_xor_b32 v48, v48, v209\n"
"v_lshrrev_b32 v209, 1, v152\n"
"v_and_b32 v209, 16, v209\n"
"v_and_or_b32 v209, v152, 15, v209\n"
"v_lshlrev_b32 v209, 7, v209\n"
"v_lshl_or_b32 v209, v48, 4, v209\n"
"v_add_u32 v209, s56, v209\n"
"v_add_u32 v209, 0xc000, v209\n"
"v_xor_b32 v210, 32, v209\n"
"v_xor_b32 v211, 64, v209\n"
"v_xor_b32 v212, 0x60, v209\n"
"s_waitcnt vmcnt(12)\n"
"s_barrier\n"
"ds_read_b128 v[48:51], v104 offset:24576\n"
"ds_read_b128 v[52:55], v104 offset:26624\n"
"ds_read_b128 v[56:59], v112 offset:24576\n"
"ds_read_b128 v[60:63], v112 offset:26624\n"
"s_add_u32 s13, s82, 0x400\n"
"s_and_b32 s13, s13, 0x7ff\n"
"s_add_u32 s14, s13, s81\n"
"s_add_u32 s13, s13, s80\n"
"s_add_u32 s60, s16, s13\n"
"s_addc_u32 s61, s17, 0\n"
"s_add_u32 s62, s18, s14\n"
"s_addc_u32 s63, s19, 0\n"
"s_add_u32 s64, s20, s14\n"
"s_addc_u32 s65, s21, 0\n"
"s_add_u32 m0, s56, 0x2000\n"
"s_nop 0\n"
"global_load_lds_dwordx4 v98, s[62:63]\n"
"global_load_lds_dwordx4 v99, s[62:63] offset:1024\n"
"global_load_lds_dwordx4 v98, s[64:65] offset:2048\n"
"global_load_lds_dwordx4 v99, s[64:65] offset:3072\n"
"s_add_u32 m0, s55, 0\n"
"s_nop 0\n"
"global_load_lds_dwordx4 v136, s[60:61]\n"
"global_load_lds_dwordx4 v137, s[60:61] offset:1024\n"
"v_add_u32 v72, 0x20600, v194\n"
"ds_read_b128 v[64:67], v72\n"
"ds_read_b128 v[68:71], v72 offset:1024\n"
"v_mov_b32 v96, 0\n"
"v_mov_b32 v97, 0\n"
"s_waitcnt lgkmcnt(1)\n"
"v_lshlrev_b32 v72, 16, v64\n"
"v_and_b32 v73, 0xffff0000, v64\n"
"v_lshlrev_b32 v74, 16, v16\n"
"v_and_b32 v75, 0xffff0000, v16\n"
"v_mul_f32 v74, v74, v72\n"
"v_mul_f32 v75, v75, v73\n"
"v_cvt_pk_bf16_f32 v74, 0, v74\n"
"v_cvt_pk_bf16_f32 v75, 0, v75\n"
"v_add_f32 v96, v96, v74\n"
"v_add_f32 v96, v96, v75\n"
"v_lshlrev_b32 v74, 16, v24\n"
"v_and_b32 v75, 0xffff0000, v24\n"
"v_mul_f32 v74, v74, v72\n"
"v_mul_f32 v75, v75, v73\n"
"v_cvt_pk_bf16_f32 v74, 0, v74\n"
"v_cvt_pk_bf16_f32 v75, 0, v75\n"
"v_add_f32 v97, v97, v74\n"
"v_add_f32 v97, v97, v75\n"
"s_waitcnt vmcnt(12) lgkmcnt(0)\n"
"s_barrier\n"
"s_add_u32 m0, s56, 0x7f80\n"
"s_nop 0\n"
"global_load_lds_dwordx4 v98, s[62:63] offset:128\n"
"global_load_lds_dwordx4 v99, s[62:63] offset:1152\n"
"global_load_lds_dwordx4 v98, s[64:65] offset:2176\n"
"global_load_lds_dwordx4 v99, s[64:65] offset:3200\n"
"s_add_u32 m0, s55, 0x5f80\n"
"s_nop 0\n"
"global_load_lds_dwordx4 v136, s[60:61] offset:128\n"
"global_load_lds_dwordx4 v137, s[60:61] offset:1152\n"
"v_lshlrev_b32 v72, 16, v65\n"
"v_and_b32 v73, 0xffff0000, v65\n"
"v_lshlrev_b32 v74, 16, v17\n"
"v_and_b32 v75, 0xffff0000, v17\n"
"v_mul_f32 v74, v74, v72\n"
"v_mul_f32 v75, v75, v73\n"
"v_cvt_pk_bf16_f32 v74, 0, v74\n"
"v_cvt_pk_bf16_f32 v75, 0, v75\n"
"v_add_f32 v96, v96, v74\n"
"v_add_f32 v96, v96, v75\n"
"v_lshlrev_b32 v74, 16, v25\n"
"v_and_b32 v75, 0xffff0000, v25\n"
"v_mul_f32 v74, v74, v72\n"
"v_mul_f32 v75, v75, v73\n"
"v_cvt_pk_bf16_f32 v74, 0, v74\n"
"v_cvt_pk_bf16_f32 v75, 0, v75\n"
"v_add_f32 v97, v97, v74\n"
"v_add_f32 v97, v97, v75\n"
"s_waitcnt vmcnt(12)\n"
"s_barrier\n"
"ds_read_b128 v[72:75], v120 offset:8192\n"
"ds_read_b128 v[76:79], v120 offset:10240\n"
"ds_read_b128 v[80:83], v128 offset:8192\n"
"ds_read_b128 v[84:87], v128 offset:10240\n"
"s_add_u32 m0, s56, 0xdf00\n"
"s_nop 0\n"
"global_load_lds_dwordx4 v98, s[62:63] offset:256\n"
"global_load_lds_dwordx4 v99, s[62:63] offset:1280\n"
"global_load_lds_dwordx4 v98, s[64:65] offset:2304\n"
"global_load_lds_dwordx4 v99, s[64:65] offset:3328\n"
"s_add_u32 m0, s55, 0xbf00\n"
"s_nop 0\n"
"global_load_lds_dwordx4 v136, s[60:61] offset:256\n"
"global_load_lds_dwordx4 v137, s[60:61] offset:1280\n"
"v_lshlrev_b32 v88, 16, v66\n"
"v_and_b32 v89, 0xffff0000, v66\n"
"v_lshlrev_b32 v90, 16, v18\n"
"v_and_b32 v91, 0xffff0000, v18\n"
"v_mul_f32 v90, v90, v88\n"
"v_mul_f32 v91, v91, v89\n"
"v_cvt_pk_bf16_f32 v90, 0, v90\n"
"v_cvt_pk_bf16_f32 v91, 0, v91\n"
"v_add_f32 v96, v96, v90\n"
"v_add_f32 v96, v96, v91\n"
"v_lshlrev_b32 v90, 16, v26\n"
"v_and_b32 v91, 0xffff0000, v26\n"
"v_mul_f32 v90, v90, v88\n"
"v_mul_f32 v91, v91, v89\n"
"v_cvt_pk_bf16_f32 v90, 0, v90\n"
"v_cvt_pk_bf16_f32 v91, 0, v91\n"
"v_add_f32 v97, v97, v90\n"
"v_add_f32 v97, v97, v91\n"
"v_lshlrev_b32 v88, 16, v67\n"
"v_and_b32 v89, 0xffff0000, v67\n"
"v_lshlrev_b32 v64, 16, v19\n"
"v_and_b32 v65, 0xffff0000, v19\n"
"v_mul_f32 v64, v64, v88\n"
"v_mul_f32 v65, v65, v89\n"
"v_cvt_pk_bf16_f32 v64, 0, v64\n"
"v_cvt_pk_bf16_f32 v65, 0, v65\n"
"v_add_f32 v96, v96, v64\n"
"v_add_f32 v96, v96, v65\n"
"v_lshlrev_b32 v16, 16, v27\n"
"v_and_b32 v17, 0xffff0000, v27\n"
"v_mul_f32 v16, v16, v88\n"
"v_mul_f32 v17, v17, v89\n"
"v_cvt_pk_bf16_f32 v16, 0, v16\n"
"v_cvt_pk_bf16_f32 v17, 0, v17\n"
"v_add_f32 v97, v97, v16\n"
"v_add_f32 v97, v97, v17\n"
"s_waitcnt vmcnt(12) lgkmcnt(0)\n"
"s_barrier\n"
"s_add_u32 m0, s56, 0x13e80\n"
"s_nop 0\n"
"global_load_lds_dwordx4 v98, s[62:63] offset:384\n"
"global_load_lds_dwordx4 v99, s[62:63] offset:1408\n"
"global_load_lds_dwordx4 v98, s[64:65] offset:2432\n"
"global_load_lds_dwordx4 v99, s[64:65] offset:3456\n"
"s_add_u32 m0, s55, 0x11e80\n"
"s_nop 0\n"
"global_load_lds_dwordx4 v136, s[60:61] offset:384\n"
"global_load_lds_dwordx4 v137, s[60:61] offset:1408\n"
"v_lshlrev_b32 v16, 16, v68\n"
"v_and_b32 v17, 0xffff0000, v68\n"
"v_lshlrev_b32 v18, 16, v20\n"
"v_and_b32 v19, 0xffff0000, v20\n"
"v_mul_f32 v18, v18, v16\n"
"v_mul_f32 v19, v19, v17\n"
"v_cvt_pk_bf16_f32 v18, 0, v18\n"
"v_cvt_pk_bf16_f32 v19, 0, v19\n"
"v_add_f32 v96, v96, v18\n"
"v_add_f32 v96, v96, v19\n"
"v_lshlrev_b32 v18, 16, v28\n"
"v_and_b32 v19, 0xffff0000, v28\n"
"v_mul_f32 v18, v18, v16\n"
"v_mul_f32 v19, v19, v17\n"
"v_cvt_pk_bf16_f32 v18, 0, v18\n"
"v_cvt_pk_bf16_f32 v19, 0, v19\n"
"v_add_f32 v97, v97, v18\n"
"v_add_f32 v97, v97, v19\n"
"s_waitcnt vmcnt(12)\n"
"s_barrier\n"
"ds_read_b128 v[16:19], v104 offset:24576\n"
"ds_read_b128 v[24:27], v104 offset:26624\n"
"ds_read_b128 v[64:67], v112 offset:24576\n"
"ds_read_b128 v[88:91], v112 offset:26624\n"
"s_add_u32 s13, s82, 0x600\n"
"s_and_b32 s13, s13, 0x7ff\n"
"s_add_u32 s14, s13, s81\n"
"s_add_u32 s13, s13, s80\n"
"s_add_u32 s60, s16, s13\n"
"s_addc_u32 s61, s17, 0\n"
"s_add_u32 s62, s18, s14\n"
"s_addc_u32 s63, s19, 0\n"
"s_add_u32 s64, s20, s14\n"
"s_addc_u32 s65, s21, 0\n"
"s_add_u32 m0, s56, 0x2000\n"
"s_nop 0\n"
"global_load_lds_dwordx4 v98, s[62:63]\n"
"global_load_lds_dwordx4 v99, s[62:63] offset:1024\n"
"global_load_lds_dwordx4 v98, s[64:65] offset:2048\n"
"global_load_lds_dwordx4 v99, s[64:65] offset:3072\n"
"s_add_u32 m0, s55, 0\n"
"s_nop 0\n"
"global_load_lds_dwordx4 v136, s[60:61]\n"
"global_load_lds_dwordx4 v137, s[60:61] offset:1024\n"
"v_lshlrev_b32 v92, 16, v69\n"
"v_and_b32 v93, 0xffff0000, v69\n"
"v_lshlrev_b32 v94, 16, v21\n"
"v_and_b32 v95, 0xffff0000, v21\n"
"v_mul_f32 v94, v94, v92\n"
"v_mul_f32 v95, v95, v93\n"
"v_cvt_pk_bf16_f32 v94, 0, v94\n"
"v_cvt_pk_bf16_f32 v95, 0, v95\n"
"v_add_f32 v96, v96, v94\n"
"v_add_f32 v96, v96, v95\n"
"v_lshlrev_b32 v94, 16, v29\n"
"v_and_b32 v95, 0xffff0000, v29\n"
"v_mul_f32 v94, v94, v92\n"
"v_mul_f32 v95, v95, v93\n"
"v_cvt_pk_bf16_f32 v94, 0, v94\n"
"v_cvt_pk_bf16_f32 v95, 0, v95\n"
"v_add_f32 v97, v97, v94\n"
"v_add_f32 v97, v97, v95\n"
"v_lshlrev_b32 v92, 16, v70\n"
"v_and_b32 v93, 0xffff0000, v70\n"
"v_lshlrev_b32 v94, 16, v22\n"
"v_and_b32 v95, 0xffff0000, v22\n"
"v_mul_f32 v94, v94, v92\n"
"v_mul_f32 v95, v95, v93\n"
"v_cvt_pk_bf16_f32 v94, 0, v94\n"
"v_cvt_pk_bf16_f32 v95, 0, v95\n"
"v_add_f32 v96, v96, v94\n"
"v_add_f32 v96, v96, v95\n"
"v_lshlrev_b32 v94, 16, v30\n"
"v_and_b32 v95, 0xffff0000, v30\n"
"v_mul_f32 v94, v94, v92\n"
"v_mul_f32 v95, v95, v93\n"
"v_cvt_pk_bf16_f32 v94, 0, v94\n"
"v_cvt_pk_bf16_f32 v95, 0, v95\n"
"v_add_f32 v97, v97, v94\n"
"v_add_f32 v97, v97, v95\n"
"s_waitcnt vmcnt(12) lgkmcnt(0)\n"
"s_barrier\n"
"s_add_u32 m0, s56, 0x7f80\n"
"s_nop 0\n"
"global_load_lds_dwordx4 v98, s[62:63] offset:128\n"
"global_load_lds_dwordx4 v99, s[62:63] offset:1152\n"
"global_load_lds_dwordx4 v98, s[64:65] offset:2176\n"
"global_load_lds_dwordx4 v99, s[64:65] offset:3200\n"
"s_add_u32 m0, s55, 0x5f80\n"
"s_nop 0\n"
"global_load_lds_dwordx4 v136, s[60:61] offset:128\n"
"global_load_lds_dwordx4 v137, s[60:61] offset:1152\n"
"v_lshlrev_b32 v92, 16, v71\n"
"v_and_b32 v93, 0xffff0000, v71\n"
"v_lshlrev_b32 v68, 16, v23\n"
"v_and_b32 v69, 0xffff0000, v23\n"
"v_mul_f32 v68, v68, v92\n"
"v_mul_f32 v69, v69, v93\n"
"v_cvt_pk_bf16_f32 v68, 0, v68\n"
"v_cvt_pk_bf16_f32 v69, 0, v69\n"
"v_add_f32 v96, v96, v68\n"
"v_add_f32 v96, v96, v69\n"
"v_lshlrev_b32 v20, 16, v31\n"
"v_and_b32 v21, 0xffff0000, v31\n"
"v_mul_f32 v20, v20, v92\n"
"v_mul_f32 v21, v21, v93\n"
"v_cvt_pk_bf16_f32 v20, 0, v20\n"
"v_cvt_pk_bf16_f32 v21, 0, v21\n"
"v_add_f32 v97, v97, v20\n"
"v_add_f32 v97, v97, v21\n"
"s_waitcnt vmcnt(12)\n"
"s_barrier\n"
"ds_read_b128 v[20:23], v120 offset:8192\n"
"ds_read_b128 v[28:31], v120 offset:10240\n"
"ds_read_b128 v[68:71], v128 offset:8192\n"
"ds_read_b128 v[92:95], v128 offset:10240\n"
"s_add_u32 m0, s56, 0xdf00\n"
"s_nop 0\n"
"global_load_lds_dwordx4 v98, s[62:63] offset:256\n"
"global_load_lds_dwordx4 v99, s[62:63] offset:1280\n"
"global_load_lds_dwordx4 v98, s[64:65] offset:2304\n"
"global_load_lds_dwordx4 v99, s[64:65] offset:3328\n"
"s_add_u32 m0, s55, 0xbf00\n"
"s_nop 0\n"
"global_load_lds_dwordx4 v136, s[60:61] offset:256\n"
"global_load_lds_dwordx4 v137, s[60:61] offset:1280\n"
"v_add_f32_dpp v96, v96, v96 row_shr:1 row_mask:0xf bank_mask:0xf\n"
"v_add_f32_dpp v97, v97, v97 row_shr:1 row_mask:0xf bank_mask:0xf\n"
"s_nop 0\n"
"v_add_f32_dpp v96, v96, v96 row_shr:2 row_mask:0xf bank_mask:0xf\n"
"v_add_f32_dpp v97, v97, v97 row_shr:2 row_mask:0xf bank_mask:0xf\n"
"s_nop 0\n"
"v_add_f32_dpp v96, v96, v96 row_shr:4 row_mask:0xf bank_mask:0xf\n"
"v_add_f32_dpp v97, v97, v97 row_shr:4 row_mask:0xf bank_mask:0xf\n"
"s_nop 0\n"
"v_add_f32_dpp v96, v96, v96 row_shr:8 row_mask:0xf bank_mask:0xf\n"
"v_add_f32_dpp v97, v97, v97 row_shr:8 row_mask:0xf bank_mask:0xf\n"
"s_nop 0\n"
"v_add_f32_dpp v96, v96, v96 row_bcast:15 row_mask:0xa bank_mask:0xf\n"
"v_add_f32_dpp v97, v97, v97 row_bcast:15 row_mask:0xa bank_mask:0xf\n"
"s_nop 0\n"
"v_add_f32_dpp v96, v96, v96 row_bcast:31 row_mask:0xc bank_mask:0xf\n"
"v_add_f32_dpp v97, v97, v97 row_bcast:31 row_mask:0xc bank_mask:0xf\n"
"v_readlane_b32 s13, v96, 63\n"
"v_readlane_b32 s14, v97, 63\n"
"v_cmp_lt_u32 vcc, 31, v152\n"
"v_mov_b32 v96, s13\n"
"v_mov_b32 v97, s14\n"
"v_cndmask_b32 v96, v96, v97, vcc\n"
"v_cvt_pk_bf16_f32 v96, 0, v96\n"
"v_mul_f32 v96, 0xbfb8aa3b, v96\n"
"v_exp_f32 v96, v96\n"
"s_nop 0\n"
"v_add_f32 v96, 1.0, v96\n"
"v_rcp_f32 v96, v96\n"
"s_nop 0\n"
"v_cvt_pk_bf16_f32 v96, 0, v96\n"
"v_mov_b32 v97, s10\n"
"s_lshl3_add_u32 s13, s83, 0x140000\n"
"v_lshrrev_b32 v100, 5, v152\n"
"v_lshl_add_u32 v100, v100, 13, s13\n"
"global_store_dwordx2 v100, v[96:97], s[30:31] sc1\n"
"s_lshl2_add_u32 s13, s58, s15\n"
"s_lshl_b32 s13, s13, 12\n"
"s_add_u32 s13, s13, 0x2400000\n"
"s_add_u32 s38, s30, s13\n"
"s_addc_u32 s39, s31, 0\n"
"s_and_b32 s13, s54, 6\n"
"s_lshl_b32 s13, s13, 10\n"
"s_add_u32 s13, s13, 0x1000\n"
"s_add_u32 s26, s30, s13\n"
"s_addc_u32 s27, s31, 0\n"
"s_lshl2_add_u32 s13, s58, s15\n"
"s_lshl_b32 s13, s13, 12\n"
"s_add_u32 s28, s28, s13\n"
"s_addc_u32 s29, s29, 0\n"
"s_waitcnt vmcnt(13) lgkmcnt(0)\n"
"s_barrier\n"
"s_add_u32 m0, s56, 0x13e80\n"
"s_nop 0\n"
"global_load_lds_dwordx4 v98, s[62:63] offset:384\n"
"global_load_lds_dwordx4 v99, s[62:63] offset:1408\n"
"global_load_lds_dwordx4 v98, s[64:65] offset:2432\n"
"global_load_lds_dwordx4 v99, s[64:65] offset:3456\n"
"s_add_u32 m0, s55, 0x11e80\n"
"s_nop 0\n"
"global_load_lds_dwordx4 v136, s[60:61] offset:384\n"
"global_load_lds_dwordx4 v137, s[60:61] offset:1408\n"
"v_lshrrev_b32 v96, 4, v152\n"
"v_xor_b32 v96, v96, v152\n"
"v_and_b32 v96, 7, v96\n"
"v_lshlrev_b32 v96, 4, v96\n"
"v_lshrrev_b32 v227, 3, v152\n"
"v_lshl_or_b32 v227, v227, 13, v96\n"
"v_xor_b32 v228, 0xc40, v227\n"
"v_add_u32 v226, 0x800, v227\n"
"v_add_u32 v225, 0x800, v228\n"
"s_lshl1_add_u32 s13, s51, s48\n"
"s_lshl_b32 s13, s13, 13\n"
"s_add_u32 s14, s49, s15\n"
"s_and_b32 s14, s14, 3\n"
"s_lshl_b32 s14, s14, 9\n"
"s_add_u32 s13, s13, s14\n"
"s_add_u32 s13, s13, 0x20000\n"
"s_add_u32 s90, s30, s13\n"
"s_addc_u32 s91, s31, 0\n"
"s_lshl2_add_u32 s14, s50, s15\n"
"s_lshl_b32 s13, s14, 12\n"
"s_add_u32 s13, s13, 0x60000\n"
"s_add_u32 s66, s30, s13\n"
"s_addc_u32 s67, s31, 0\n"
"s_lshl_b32 s14, s14, 18\n"
"s_lshl1_add_u32 s13, s51, s48\n"
"s_lshl_b32 s13, s13, 8\n"
"s_add_u32 s13, s13, s14\n"
"s_add_u32 s13, s13, 0x2c00000\n"
"s_add_u32 s92, s30, s13\n"
"s_addc_u32 s93, s31, 0\n"
"s_lshr_b32 s13, s13, 8\n"
"s_and_b32 s13, s13, 0x2000\n"
"v_or_b32 v203, s13, v203\n"
"s_add_u32 s94, s92, 0x20000\n"
"s_addc_u32 s95, s93, 0\n"
"s_add_u32 s22, s92, 0x4000\n"
"s_addc_u32 s23, s93, 0\n"
"s_add_u32 s24, s94, 0x4000\n"
"s_addc_u32 s25, s95, 0\n"
"s_waitcnt vmcnt(13)\n"
"s_barrier\n"
"ds_read_b128 v[96:99], v104 offset:24576\n"
"ds_read_b128 v[100:103], v104 offset:26624\n"
"ds_read_b128 v[104:107], v112 offset:24576\n"
"ds_read_b128 v[108:111], v112 offset:26624\n"
"s_add_u32 s13, s82, 0\n"
"s_and_b32 s13, s13, 0x7ff\n"
"s_add_u32 s13, s13, s80\n"
"s_add_u32 s13, s13, 0x200000\n"
"s_add_u32 s60, s16, s13\n"
"s_addc_u32 s61, s17, 0\n"
"s_add_u32 m0, s97, 0\n"
"s_nop 0\n"
"global_load_lds_dwordx4 v136, s[60:61]\n"
"global_load_lds_dwordx4 v137, s[60:61] offset:1024\n"
"s_add_u32 m0, s97, 0x700\n"
"s_nop 0\n"
"global_load_lds_dwordx4 v136, s[60:61] offset:256\n"
"global_load_lds_dwordx4 v137, s[60:61] offset:1280\n"
"s_add_u32 s13, s82, 0x200\n"
"s_and_b32 s13, s13, 0x7ff\n"
"s_add_u32 s13, s13, s80\n"
"s_add_u32 s13, s13, 0x200000\n"
"s_add_u32 s60, s16, s13\n"
"s_addc_u32 s61, s17, 0\n"
"s_add_u32 m0, s57, 0x1040\n"
"s_nop 0\n"
"global_load_lds_dwordx4 v136, s[60:61]\n"
"global_load_lds_dwordx4 v137, s[60:61] offset:1024\n"
"s_lshl2_add_u32 s13, s50, s15\n"
"s_lshl_b32 s13, s13, 16\n"
"s_lshl_b32 s14, s48, 10\n"
"s_add_u32 s13, s13, s14\n"
"s_add_u32 s13, s13, 0x200\n"
"s_add_u32 s72, s32, s13\n"
"s_addc_u32 s73, s33, 0\n"
"s_add_u32 s74, s34, s13\n"
"s_addc_u32 s75, s35, 0\n"
"s_sub_u32 s74, s74, 0x800\n"
"s_subb_u32 s75, s75, 0\n"
"s_add_u32 s13, s49, 0\n"
"s_and_b32 s13, s13, 3\n"
"s_lshl_b32 s13, s13, 7\n"
"s_add_u32 s68, s72, s13\n"
"s_addc_u32 s69, s73, 0\n"
"s_add_u32 s70, s74, s13\n"
"s_addc_u32 s71, s75, 0\n"
"s_lshl1_add_u32 s13, s51, s48\n"
"s_lshl_b32 s13, s13, 19\n"
"s_add_u32 s14, s49, s15\n"
"s_and_b32 s42, s14, 2\n"
"s_and_b32 s14, s14, 3\n"
"s_add_u32 s14, s14, s42\n"
"s_lshl_b32 s14, s14, 12\n"
"s_add_u32 s13, s13, s14\n"
"s_add_u32 s13, s13, 0x400000\n"
"s_lshr_b32 s42, s13, 8\n"
"s_and_b32 s42, s42, 0x2000\n"
"s_add_u32 s42, s42, s13\n"
"s_add_u32 s42, s30, s42\n"
"s_addc_u32 s43, s31, 0\n"
"s_lshl1_add_u32 s46, s48, s59\n"
"s_lshl_b32 s46, s46, 16\n"
"s_xor_b32 s44, s46, 0x20000\n"
"s_xor_b32 s45, s46, 0x30000\n"
"s_xor_b32 s47, s46, 0x10000\n"
"s_add_u32 s62, s42, s44\n"
"s_addc_u32 s63, s43, 0\n"
"s_add_u32 s64, s42, s45\n"
"s_addc_u32 s65, s43, 0\n"
"s_add_u32 s18, s42, s46\n"
"s_addc_u32 s19, s43, 0\n"
"s_add_u32 s20, s42, s47\n"
"s_addc_u32 s21, s43, 0\n"
"s_add_u32 s32, s62, 0x40000\n"
"s_addc_u32 s33, s63, 0\n"
"s_add_u32 s34, s64, 0x40000\n"
"s_addc_u32 s35, s65, 0\n"
"s_add_u32 s36, s18, 0x40000\n"
"s_addc_u32 s37, s19, 0\n"
"s_add_u32 s40, s20, 0x40000\n"
"s_addc_u32 s41, s21, 0\n"
"s_waitcnt vmcnt(13) lgkmcnt(0)\n"
"s_barrier\n"
"s_add_u32 m0, s57, 0x1740\n"
"s_nop 0\n"
"global_load_lds_dwordx4 v136, s[60:61] offset:256\n"
"global_load_lds_dwordx4 v137, s[60:61] offset:1280\n"
"s_add_u32 s13, s82, 0x400\n"
"s_and_b32 s13, s13, 0x7ff\n"
"s_add_u32 s13, s13, s80\n"
"s_add_u32 s13, s13, 0x200000\n"
"s_add_u32 s60, s16, s13\n"
"s_addc_u32 s61, s17, 0\n"
"s_add_u32 m0, s55, 0\n"
"s_nop 0\n"
"global_load_lds_dwordx4 v136, s[60:61]\n"
"global_load_lds_dwordx4 v137, s[60:61] offset:1024\n"
"s_add_u32 m0, s55, 0x1f00\n"
"s_nop 0\n"
"global_load_lds_dwordx4 v136, s[60:61] offset:256\n"
"global_load_lds_dwordx4 v137, s[60:61] offset:1280\n"
"s_waitcnt vmcnt(8)\n"
"s_barrier\n"
"s_setprio 1\n"
"ds_read_b128 v[112:115], v120 offset:8192\n"
"ds_read_b128 v[116:119], v120 offset:10240\n"
"ds_read_b128 v[120:123], v128 offset:8192\n"
"ds_read_b128 v[124:127], v128 offset:10240\n"
"s_add_u32 s13, s82, 0x600\n"
"s_and_b32 s13, s13, 0x7ff\n"
"s_add_u32 s13, s13, s80\n"
"s_add_u32 s13, s13, 0x200000\n"
"s_add_u32 s60, s16, s13\n"
"s_addc_u32 s61, s17, 0\n"
"s_add_u32 m0, s55, 0x4000\n"
"s_nop 0\n"
"global_load_lds_dwordx4 v136, s[60:61]\n"
"global_load_lds_dwordx4 v137, s[60:61] offset:1024\n"
"s_add_u32 m0, s55, 0x5f00\n"
"s_nop 0\n"
"global_load_lds_dwordx4 v136, s[60:61] offset:256\n"
"global_load_lds_dwordx4 v137, s[60:61] offset:1280\n"
"s_add_u32 m0, s57, 32\n"
"s_nop 0\n"
"global_load_lds_dwordx4 v194, s[28:29]\n"
"global_load_lds_dwordx4 v194, s[28:29] offset:1024\n"
"global_load_lds_dwordx4 v194, s[28:29] offset:2048\n"
"global_load_lds_dwordx4 v194, s[28:29] offset:3072\n"
"s_waitcnt vmcnt(10) lgkmcnt(0)\n"
"s_barrier\n"
"global_load_dwordx4 v[128:131], v194, s[26:27] sc1\n"
"global_load_dwordx4 v[132:135], v194, s[26:27] offset:1024 sc1\n"
"s_lshl_b32 s81, s11, 16\n"
"s_add_u32 s13, s82, 0\n"
"s_and_b32 s13, s13, 0x7ff\n"
"s_add_u32 s13, s13, s80\n"
"s_add_u32 s13, s13, 0x200080\n"
"s_add_u32 s60, s16, s13\n"
"s_addc_u32 s61, s17, 0\n"
"s_add_u32 m0, s55, 0x8000\n"
"s_nop 0\n"
"global_load_lds_dwordx4 v136, s[60:61]\n"
"global_load_lds_dwordx4 v137, s[60:61] offset:1024\n"
"s_add_u32 m0, s55, 0x9f00\n"
"s_nop 0\n"
"global_load_lds_dwordx4 v136, s[60:61] offset:256\n"
"global_load_lds_dwordx4 v137, s[60:61] offset:1280\n"
"s_add_u32 s13, s82, 0x200\n"
"s_and_b32 s13, s13, 0x7ff\n"
"s_add_u32 s13, s13, s80\n"
"s_add_u32 s13, s13, 0x200080\n"
"s_add_u32 s60, s16, s13\n"
"s_addc_u32 s61, s17, 0\n"
"s_add_u32 m0, s55, 0xc000\n"
"s_nop 0\n"
"global_load_lds_dwordx4 v136, s[60:61]\n"
"global_load_lds_dwordx4 v137, s[60:61] offset:1024\n"
"s_add_u32 m0, s55, 0xdf00\n"
"s_nop 0\n"
"global_load_lds_dwordx4 v136, s[60:61] offset:256\n"
"global_load_lds_dwordx4 v137, s[60:61] offset:1280\n"
"s_add_u32 s13, s82, 0x400\n"
"s_and_b32 s13, s13, 0x7ff\n"
"s_add_u32 s13, s13, s80\n"
"s_add_u32 s13, s13, 0x200080\n"
"s_add_u32 s60, s16, s13\n"
"s_addc_u32 s61, s17, 0\n"
"s_add_u32 m0, s55, 0x10000\n"
"s_nop 0\n"
"global_load_lds_dwordx4 v136, s[60:61]\n"
"global_load_lds_dwordx4 v137, s[60:61] offset:1024\n"
"s_waitcnt vmcnt(16)\n"
"s_barrier\n"
"s_waitcnt vmcnt(11)\n"
"v_xor_b32 v153, s81, v129\n"
"v_bitop3_b32 v153, v153, v131, s81 bitop3:0xf6\n"
"s_waitcnt vmcnt(10)\n"
"v_bitop3_b32 v153, v153, v133, s81 bitop3:0xf6\n"
"v_bitop3_b32 v153, v153, v135, s81 bitop3:0xf6\n"
"v_lshrrev_b32 v138, 16, v153\n"
"v_bitop3_b32 v138, v138, v128, s10 bitop3:0xf6\n"
"v_bitop3_b32 v138, v138, v130, s10 bitop3:0xf6\n"
"v_bitop3_b32 v138, v138, v132, s10 bitop3:0xf6\n"
"v_bitop3_b32 v138, v138, v134, s10 bitop3:0xf6\n"
"v_cmp_ne_u32 vcc, 0, v138\n"
"s_nop 0\n"
"s_cbranch_vccnz .Lr8\n"
".Lb8:\n"
"s_add_u32 m0, s97, 0\n"
"s_nop 0\n"
"global_load_lds_dwordx4 v194, s[38:39] sc1\n"
"global_load_lds_dwordx4 v194, s[38:39] offset:1024 sc1\n"
"global_load_lds_dwordx4 v194, s[38:39] offset:2048 sc1\n"
"global_load_lds_dwordx4 v194, s[38:39] offset:3072 sc1\n"
"s_add_u32 m0, s55, 0x11f00\n"
"s_nop 0\n"
"global_load_lds_dwordx4 v136, s[60:61] offset:256\n"
"global_load_lds_dwordx4 v137, s[60:61] offset:1280\n"
"s_add_u32 s13, s82, 0x600\n"
"s_and_b32 s13, s13, 0x7ff\n"
"s_add_u32 s13, s13, s80\n"
"s_add_u32 s13, s13, 0x200080\n"
"s_add_u32 s60, s16, s13\n"
"s_addc_u32 s61, s17, 0\n"
"s_add_u32 m0, s55, 0x14000\n"
"s_nop 0\n"
"global_load_lds_dwordx4 v136, s[60:61]\n"
"global_load_lds_dwordx4 v137, s[60:61] offset:1024\n"
"s_add_u32 m0, s55, 0x15f00\n"
"s_nop 0\n"
"global_load_lds_dwordx4 v136, s[60:61] offset:256\n"
"global_load_lds_dwordx4 v137, s[60:61] offset:1280\n"
"ds_read_b128 v[136:139], v195 offset:32\n"
"ds_read_b32 v157, v195 offset:48\n"
"ds_read_b128 v[140:143], v195 offset:1056\n"
"ds_read_b32 v156, v195 offset:1072\n"
"ds_read_b128 v[144:147], v195 offset:2080\n"
"ds_read_b32 v155, v195 offset:2096\n"
"ds_read_b128 v[148:151], v195 offset:3104\n"
"ds_read_b32 v154, v195 offset:3120\n"
"s_waitcnt lgkmcnt(7)\n"
"v_alignbit_b32 v136, v137, v136, 16\n"
"v_alignbit_b32 v137, v138, v137, 16\n"
"v_alignbit_b32 v138, v139, v138, 16\n"
"s_waitcnt lgkmcnt(6)\n"
"v_alignbit_b32 v139, v157, v139, 16\n"
"s_waitcnt lgkmcnt(5)\n"
"v_alignbit_b32 v140, v141, v140, 16\n"
"v_alignbit_b32 v141, v142, v141, 16\n"
"v_alignbit_b32 v142, v143, v142, 16\n"
"s_waitcnt lgkmcnt(4)\n"
"v_alignbit_b32 v143, v156, v143, 16\n"
"s_waitcnt lgkmcnt(3)\n"
"v_alignbit_b32 v144, v145, v144, 16\n"
"v_alignbit_b32 v145, v146, v145, 16\n"
"v_alignbit_b32 v146, v147, v146, 16\n"
"s_waitcnt lgkmcnt(2)\n"
"v_alignbit_b32 v147, v155, v147, 16\n"
"s_waitcnt lgkmcnt(1)\n"
"v_alignbit_b32 v148, v149, v148, 16\n"
"v_alignbit_b32 v149, v150, v149, 16\n"
"v_alignbit_b32 v150, v151, v150, 16\n"
"s_waitcnt lgkmcnt(0)\n"
"v_alignbit_b32 v151, v154, v151, 16\n"
"v_bfe_i32 v176, v129, 0, 16\n"
"v_bfe_i32 v153, v131, 0, 16\n"
"v_add_u32 v176, v176, v153\n"
"v_bfe_i32 v153, v133, 0, 16\n"
"v_add_u32 v176, v176, v153\n"
"v_bfe_i32 v153, v135, 0, 16\n"
"v_add_u32 v176, v176, v153\n"
"s_nop 1\n"
"v_add_u32_dpp v176, v176, v176 row_shr:1 row_mask:0xf bank_mask:0xf\n"
"s_nop 1\n"
"v_add_u32_dpp v176, v176, v176 row_shr:2 row_mask:0xf bank_mask:0xf\n"
"s_nop 1\n"
"v_add_u32_dpp v176, v176, v176 row_shr:4 row_mask:0xf bank_mask:0xf\n"
"s_nop 1\n"
"v_add_u32_dpp v176, v176, v176 row_shr:8 row_mask:0xf bank_mask:0xf\n"
"s_nop 1\n"
"v_add_u32_dpp v176, v176, v176 row_bcast:15 row_mask:0xa bank_mask:0xf\n"
"s_nop 1\n"
"v_add_u32_dpp v176, v176, v176 row_bcast:31 row_mask:0xc bank_mask:0xf\n"
"s_nop 0\n"
"v_readlane_b32 s13, v176, 63\n"
"s_nop 0\n"
"s_cmp_gt_i32 s13, 0\n"
"s_cselect_b32 s7, 0x2000, 0\n"
"s_lshl2_add_u32 s13, s15, 0x27580\n"
"v_mov_b32 v177, s13\n"
"v_mov_b32 v176, s7\n"
"s_xor_b32 s86, s86, s7\n"
"s_xor_b32 s62, s62, s7\n"
"s_xor_b32 s64, s64, s7\n"
"s_xor_b32 s18, s18, s7\n"
"s_xor_b32 s20, s20, s7\n"
"s_xor_b32 s32, s32, s7\n"
"s_xor_b32 s34, s34, s7\n"
"s_xor_b32 s36, s36, s7\n"
"s_xor_b32 s40, s40, s7\n"
"s_bfe_u32 s13, %1, 0x20003\n"
"s_lshl_b32 s13, s13, 14\n"
"v_and_b32 v128, 7, v152\n"
"v_bfe_u32 v230, v152, 3, 1\n"
"v_lshlrev_b32 v128, 11, v128\n"
"v_lshl_or_b32 v128, v230, 9, v128\n"
"v_add_u32 v128, s13, v128\n"
"s_mov_b64 exec, 0xffff\n"
"global_load_dword v230, v128, s[68:69] offset:-512\n"
"global_load_dword v231, v128, s[70:71] offset:1536\n"
"s_mov_b64 exec, -1\n"
"s_waitcnt vmcnt(18)\n"
"s_barrier\n"
"ds_read_b128 v[128:131], v224 offset:32768\n"
"ds_read_b128 v[132:135], v224 offset:34816\n"
"ds_read_b128 v[152:155], v224 offset:36864\n"
"ds_read_b128 v[156:159], v224 offset:38912\n"
"ds_read_b128 v[160:163], v219 offset:32768\n"
"ds_read_b128 v[164:167], v219 offset:34816\n"
"ds_read_b128 v[168:171], v219 offset:36864\n"
"ds_read_b128 v[172:175], v219 offset:38912\n"
"ds_write_b32 v177, v176\n"
"ds_write_b128 v195, v[136:139] offset:4192\n"
"ds_write_b128 v195, v[140:143] offset:5216\n"
"ds_write_b128 v195, v[144:147] offset:6240\n"
"ds_write_b128 v195, v[148:151] offset:7264\n"
"s_waitcnt vmcnt(2) lgkmcnt(0)\n"
"s_setprio 0\n"
"s_barrier\n"
"ds_read_b128 v[136:139], v229\n"
"ds_read_b128 v[140:143], v229 offset:1024\n"
"ds_read_b128 v[144:147], v229 offset:2048\n"
"ds_read_b128 v[148:151], v229 offset:3072\n"
"ds_read_b128 v[176:179], v229 offset:16384\n"
"ds_read_b128 v[180:183], v229 offset:17408\n"
"ds_read_b128 v[184:187], v229 offset:18432\n"
"ds_read_b128 v[188:191], v229 offset:19456\n"
"s_waitcnt lgkmcnt(7)\n"
"v_mfma_f32_16x16x32_bf16 v[136:139], v[128:131], v[0:3], v[136:139]\n"
"s_waitcnt lgkmcnt(6)\n"
"v_mfma_f32_16x16x32_bf16 v[140:143], v[128:131], v[4:7], v[140:143]\n"
"ds_read_b128 v[128:131], v224 offset:40960\n"
"s_waitcnt lgkmcnt(6)\n"
"v_mfma_f32_16x16x32_bf16 v[144:147], v[132:135], v[0:3], v[144:147]\n"
"s_waitcnt lgkmcnt(5)\n"
"v_mfma_f32_16x16x32_bf16 v[148:151], v[132:135], v[4:7], v[148:151]\n"
"ds_read_b128 v[132:135], v224 offset:43008\n"
"s_waitcnt lgkmcnt(5)\n"
"v_mfma_f32_16x16x32_bf16 v[176:179], v[152:155], v[0:3], v[176:179]\n"
"s_waitcnt lgkmcnt(4)\n"
"v_mfma_f32_16x16x32_bf16 v[180:183], v[152:155], v[4:7], v[180:183]\n"
"ds_read_b128 v[152:155], v224 offset:45056\n"
"s_waitcnt lgkmcnt(3)\n"
"s_barrier\n"
"v_mfma_f32_16x16x32_bf16 v[184:187], v[156:159], v[0:3], v[184:187]\n"
"v_mfma_f32_16x16x32_bf16 v[188:191], v[156:159], v[4:7], v[188:191]\n"
"ds_read_b128 v[0:3], v224 offset:47104\n"
"v_mfma_f32_16x16x32_bf16 v[136:139], v[160:163], v[8:11], v[136:139]\n"
"v_mfma_f32_16x16x32_bf16 v[140:143], v[160:163], v[12:15], v[140:143]\n"
"ds_read_b128 v[4:7], v219 offset:40960\n"
"v_mfma_f32_16x16x32_bf16 v[144:147], v[164:167], v[8:11], v[144:147]\n"
"v_mfma_f32_16x16x32_bf16 v[148:151], v[164:167], v[12:15], v[148:151]\n"
"ds_read_b128 v[156:159], v219 offset:43008\n"
"v_mfma_f32_16x16x32_bf16 v[176:179], v[168:171], v[8:11], v[176:179]\n"
"v_mfma_f32_16x16x32_bf16 v[180:183], v[168:171], v[12:15], v[180:183]\n"
"ds_read_b128 v[160:163], v219 offset:45056\n"
"v_mfma_f32_16x16x32_bf16 v[184:187], v[172:175], v[8:11], v[184:187]\n"
"v_mfma_f32_16x16x32_bf16 v[188:191], v[172:175], v[12:15], v[188:191]\n"
"ds_read_b128 v[8:11], v219 offset:47104\n"
"s_waitcnt lgkmcnt(6)\n"
"v_mfma_f32_16x16x32_bf16 v[136:139], v[128:131], v[32:35], v[136:139]\n"
"v_mfma_f32_16x16x32_bf16 v[140:143], v[128:131], v[36:39], v[140:143]\n"
"ds_read_b128 v[12:15], v224 offset:49152\n"
"v_mfma_f32_16x16x32_bf16 v[144:147], v[132:135], v[32:35], v[144:147]\n"
"v_mfma_f32_16x16x32_bf16 v[148:151], v[132:135], v[36:39], v[148:151]\n"
"ds_read_b128 v[128:131], v224 offset:51200\n"
"s_waitcnt lgkmcnt(6)\n"
"v_mfma_f32_16x16x32_bf16 v[176:179], v[152:155], v[32:35], v[176:179]\n"
"v_mfma_f32_16x16x32_bf16 v[180:183], v[152:155], v[36:39], v[180:183]\n"
"ds_read_b128 v[132:135], v224 offset:53248\n"
"v_mfma_f32_16x16x32_bf16 v[184:187], v[0:3], v[32:35], v[184:187]\n"
"v_mfma_f32_16x16x32_bf16 v[188:191], v[0:3], v[36:39], v[188:191]\n"
"ds_read_b128 v[0:3], v224 offset:55296\n"
"s_waitcnt lgkmcnt(6)\n"
"v_mfma_f32_16x16x32_bf16 v[136:139], v[4:7], v[40:43], v[136:139]\n"
"v_mfma_f32_16x16x32_bf16 v[140:143], v[4:7], v[44:47], v[140:143]\n"
"ds_read_b128 v[4:7], v219 offset:49152\n"
"s_waitcnt lgkmcnt(5)\n"
"s_barrier\n"
"v_mfma_f32_16x16x32_bf16 v[144:147], v[156:159], v[40:43], v[144:147]\n"
"v_mfma_f32_16x16x32_bf16 v[148:151], v[156:159], v[44:47], v[148:151]\n"
"ds_read_b128 v[32:35], v219 offset:51200\n"
"v_mfma_f32_16x16x32_bf16 v[176:179], v[160:163], v[40:43], v[176:179]\n"
"v_mfma_f32_16x16x32_bf16 v[180:183], v[160:163], v[44:47], v[180:183]\n"
"ds_read_b128 v[36:39], v219 offset:53248\n"
"v_mfma_f32_16x16x32_bf16 v[184:187], v[8:11], v[40:43], v[184:187]\n"
"v_mfma_f32_16x16x32_bf16 v[188:191], v[8:11], v[44:47], v[188:191]\n"
"ds_read_b128 v[8:11], v219 offset:55296\n"
"s_waitcnt lgkmcnt(6)\n"
"v_mfma_f32_16x16x32_bf16 v[136:139], v[12:15], v[48:51], v[136:139]\n"
"v_mfma_f32_16x16x32_bf16 v[140:143], v[12:15], v[52:55], v[140:143]\n"
"ds_read_b128 v[12:15], v224 offset:57344\n"
"v_mfma_f32_16x16x32_bf16 v[144:147], v[128:131], v[48:51], v[144:147]\n"
"v_mfma_f32_16x16x32_bf16 v[148:151], v[128:131], v[52:55], v[148:151]\n"
"ds_read_b128 v[40:43], v224 offset:59392\n"
"s_waitcnt lgkmcnt(6)\n"
"v_mfma_f32_16x16x32_bf16 v[176:179], v[132:135], v[48:51], v[176:179]\n"
"v_mfma_f32_16x16x32_bf16 v[180:183], v[132:135], v[52:55], v[180:183]\n"
"ds_read_b128 v[44:47], v224 offset:61440\n"
"v_mfma_f32_16x16x32_bf16 v[184:187], v[0:3], v[48:51], v[184:187]\n"
"v_mfma_f32_16x16x32_bf16 v[188:191], v[0:3], v[52:55], v[188:191]\n"
"ds_read_b128 v[0:3], v224 offset:63488\n"
"s_waitcnt lgkmcnt(6)\n"
"v_mfma_f32_16x16x32_bf16 v[136:139], v[4:7], v[56:59], v[136:139]\n"
"buffer_load_dwordx2 v[54:55], v214, s[76:79], 0 offen offset:3584 sc1\n"
"buffer_load_dwordx2 v[52:53], v214, s[76:79], 0 offen offset:3072 sc1\n"
"v_mfma_f32_16x16x32_bf16 v[140:143], v[4:7], v[60:63], v[140:143]\n"
"ds_read_b128 v[4:7], v219 offset:57344\n"
"buffer_load_dwordx2 v[50:51], v214, s[76:79], 0 offen offset:2560 sc1\n"
"v_mfma_f32_16x16x32_bf16 v[144:147], v[32:35], v[56:59], v[144:147]\n"
"buffer_load_dwordx2 v[48:49], v214, s[76:79], 0 offen offset:2048 sc1\n"
"v_mfma_f32_16x16x32_bf16 v[148:151], v[32:35], v[60:63], v[148:151]\n"
"ds_read_b128 v[32:35], v219 offset:59392\n"
"s_waitcnt lgkmcnt(6)\n"
"v_mfma_f32_16x16x32_bf16 v[176:179], v[36:39], v[56:59], v[176:179]\n"
"v_mfma_f32_16x16x32_bf16 v[180:183], v[36:39], v[60:63], v[180:183]\n"
"ds_read_b128 v[36:39], v219 offset:61440\n"
"v_mfma_f32_16x16x32_bf16 v[184:187], v[8:11], v[56:59], v[184:187]\n"
"v_mfma_f32_16x16x32_bf16 v[188:191], v[8:11], v[60:63], v[188:191]\n"
"ds_read_b128 v[8:11], v219 offset:63488\n"
"s_waitcnt lgkmcnt(6)\n"
"v_mfma_f32_16x16x32_bf16 v[136:139], v[12:15], v[72:75], v[136:139]\n"
"v_mfma_f32_16x16x32_bf16 v[140:143], v[12:15], v[76:79], v[140:143]\n"
"ds_read_b128 v[12:15], v217\n"
"v_mfma_f32_16x16x32_bf16 v[144:147], v[40:43], v[72:75], v[144:147]\n"
"v_mfma_f32_16x16x32_bf16 v[148:151], v[40:43], v[76:79], v[148:151]\n"
"ds_read_b128 v[40:43], v217 offset:2048\n"
"s_waitcnt lgkmcnt(6)\n"
"v_mfma_f32_16x16x32_bf16 v[176:179], v[44:47], v[72:75], v[176:179]\n"
"v_mfma_f32_16x16x32_bf16 v[180:183], v[44:47], v[76:79], v[180:183]\n"
"ds_read_b128 v[44:47], v217 offset:4096\n"
"v_mfma_f32_16x16x32_bf16 v[184:187], v[0:3], v[72:75], v[184:187]\n"
"v_mfma_f32_16x16x32_bf16 v[188:191], v[0:3], v[76:79], v[188:191]\n"
"ds_read_b128 v[0:3], v217 offset:6144\n"
"s_waitcnt lgkmcnt(6)\n"
"v_mfma_f32_16x16x32_bf16 v[136:139], v[4:7], v[80:83], v[136:139]\n"
"v_mfma_f32_16x16x32_bf16 v[140:143], v[4:7], v[84:87], v[140:143]\n"
"ds_read_b128 v[4:7], v216\n"
"v_mfma_f32_16x16x32_bf16 v[144:147], v[32:35], v[80:83], v[144:147]\n"
"v_mfma_f32_16x16x32_bf16 v[148:151], v[32:35], v[84:87], v[148:151]\n"
"ds_read_b128 v[32:35], v216 offset:2048\n"
"s_waitcnt lgkmcnt(6)\n"
"v_mfma_f32_16x16x32_bf16 v[176:179], v[36:39], v[80:83], v[176:179]\n"
"v_mfma_f32_16x16x32_bf16 v[180:183], v[36:39], v[84:87], v[180:183]\n"
"ds_read_b128 v[36:39], v216 offset:4096\n"
"v_mfma_f32_16x16x32_bf16 v[184:187], v[8:11], v[80:83], v[184:187]\n"
"v_mfma_f32_16x16x32_bf16 v[188:191], v[8:11], v[84:87], v[188:191]\n"
"ds_read_b128 v[8:11], v216 offset:6144\n"
"s_waitcnt lgkmcnt(6)\n"
"v_mfma_f32_16x16x32_bf16 v[136:139], v[12:15], v[16:19], v[136:139]\n"
"v_mfma_f32_16x16x32_bf16 v[140:143], v[12:15], v[24:27], v[140:143]\n"
"ds_read_b128 v[12:15], v217 offset:8192\n"
"v_mfma_f32_16x16x32_bf16 v[144:147], v[40:43], v[16:19], v[144:147]\n"
"v_mfma_f32_16x16x32_bf16 v[148:151], v[40:43], v[24:27], v[148:151]\n"
"ds_read_b128 v[40:43], v217 offset:10240\n"
"s_waitcnt lgkmcnt(6)\n"
"v_mfma_f32_16x16x32_bf16 v[176:179], v[44:47], v[16:19], v[176:179]\n"
"v_mfma_f32_16x16x32_bf16 v[180:183], v[44:47], v[24:27], v[180:183]\n"
"ds_read_b128 v[44:47], v217 offset:12288\n"
"v_mfma_f32_16x16x32_bf16 v[184:187], v[0:3], v[16:19], v[184:187]\n"
"v_mfma_f32_16x16x32_bf16 v[188:191], v[0:3], v[24:27], v[188:191]\n"
"ds_read_b128 v[0:3], v217 offset:14336\n"
"s_waitcnt lgkmcnt(6)\n"
"v_mfma_f32_16x16x32_bf16 v[136:139], v[4:7], v[64:67], v[136:139]\n"
"v_mfma_f32_16x16x32_bf16 v[140:143], v[4:7], v[88:91], v[140:143]\n"
"ds_read_b128 v[4:7], v216 offset:8192\n"
"v_mfma_f32_16x16x32_bf16 v[144:147], v[32:35], v[64:67], v[144:147]\n"
"v_mfma_f32_16x16x32_bf16 v[148:151], v[32:35], v[88:91], v[148:151]\n"
"ds_read_b128 v[16:19], v216 offset:10240\n"
"s_waitcnt lgkmcnt(6)\n"
"v_mfma_f32_16x16x32_bf16 v[176:179], v[36:39], v[64:67], v[176:179]\n"
"v_mfma_f32_16x16x32_bf16 v[180:183], v[36:39], v[88:91], v[180:183]\n"
"ds_read_b128 v[24:27], v216 offset:12288\n"
"v_mfma_f32_16x16x32_bf16 v[184:187], v[8:11], v[64:67], v[184:187]\n"
"v_mfma_f32_16x16x32_bf16 v[188:191], v[8:11], v[88:91], v[188:191]\n"
"ds_read_b128 v[8:11], v216 offset:14336\n"
"s_waitcnt lgkmcnt(6)\n"
"v_mfma_f32_16x16x32_bf16 v[136:139], v[12:15], v[20:23], v[136:139]\n"
"v_mfma_f32_16x16x32_bf16 v[140:143], v[12:15], v[28:31], v[140:143]\n"
"ds_read_b128 v[12:15], v217 offset:16384\n"
"v_mfma_f32_16x16x32_bf16 v[144:147], v[40:43], v[20:23], v[144:147]\n"
"v_mfma_f32_16x16x32_bf16 v[148:151], v[40:43], v[28:31], v[148:151]\n"
"ds_read_b128 v[32:35], v217 offset:18432\n"
"s_waitcnt lgkmcnt(6)\n"
"v_mfma_f32_16x16x32_bf16 v[176:179], v[44:47], v[20:23], v[176:179]\n"
"ds_read_b128 v[36:39], v218 offset:512\n"
"v_mfma_f32_16x16x32_bf16 v[180:183], v[44:47], v[28:31], v[180:183]\n"
"ds_read_b128 v[40:43], v217 offset:20480\n"
"v_mfma_f32_16x16x32_bf16 v[184:187], v[0:3], v[20:23], v[184:187]\n"
"v_mfma_f32_16x16x32_bf16 v[188:191], v[0:3], v[28:31], v[188:191]\n"
"ds_read_b128 v[0:3], v217 offset:22528\n"
"s_waitcnt lgkmcnt(7)\n"
"v_mfma_f32_16x16x32_bf16 v[136:139], v[4:7], v[68:71], v[136:139]\n"
"v_mfma_f32_16x16x32_bf16 v[140:143], v[4:7], v[92:95], v[140:143]\n"
"ds_read_b128 v[4:7], v216 offset:16384\n"
"v_mfma_f32_16x16x32_bf16 v[144:147], v[16:19], v[68:71], v[144:147]\n"
"s_waitcnt vmcnt(3)\n"
"v_xor_b32 v54, v54, v223\n"
"v_xor_b32 v55, v55, v223\n"
"v_mfma_f32_16x16x32_bf16 v[148:151], v[16:19], v[92:95], v[148:151]\n"
"ds_read_b128 v[16:19], v216 offset:18432\n"
"s_waitcnt lgkmcnt(7)\n"
"v_mfma_f32_16x16x32_bf16 v[176:179], v[24:27], v[68:71], v[176:179]\n"
"s_waitcnt vmcnt(2)\n"
"v_xor_b32 v52, v52, v222\n"
"v_xor_b32 v53, v53, v222\n"
"v_mfma_f32_16x16x32_bf16 v[180:183], v[24:27], v[92:95], v[180:183]\n"
"ds_read_b128 v[20:23], v216 offset:20480\n"
"v_mfma_f32_16x16x32_bf16 v[184:187], v[8:11], v[68:71], v[184:187]\n"
"s_waitcnt vmcnt(1)\n"
"v_xor_b32 v50, v50, v221\n"
"v_xor_b32 v51, v51, v221\n"
"v_mfma_f32_16x16x32_bf16 v[188:191], v[8:11], v[92:95], v[188:191]\n"
"ds_read_b128 v[8:11], v216 offset:22528\n"
"s_waitcnt lgkmcnt(7)\n"
"v_mfma_f32_16x16x32_bf16 v[136:139], v[12:15], v[96:99], v[136:139]\n"
"s_waitcnt vmcnt(0)\n"
"v_xor_b32 v48, v48, v220\n"
"v_xor_b32 v49, v49, v220\n"
"v_mfma_f32_16x16x32_bf16 v[140:143], v[12:15], v[100:103], v[140:143]\n"
"ds_read_b128 v[12:15], v217 offset:24576\n"
"v_mfma_f32_16x16x32_bf16 v[144:147], v[32:35], v[96:99], v[144:147]\n"
"v_or3_b32 v44, v54, v55, v52\n"
"v_or3_b32 v44, v44, v53, v50\n"
"v_mfma_f32_16x16x32_bf16 v[148:151], v[32:35], v[100:103], v[148:151]\n"
"ds_read_b128 v[24:27], v217 offset:26624\n"
"s_waitcnt lgkmcnt(6)\n"
"v_mfma_f32_16x16x32_bf16 v[176:179], v[40:43], v[96:99], v[176:179]\n"
"v_or3_b32 v44, v44, v51, v48\n"
"v_bitop3_b32 v44, v44, v49, s96 bitop3:0xa8\n"
"v_xor_b32 v230, v230, v231\n"
"v_mfma_f32_16x16x32_bf16 v[180:183], v[40:43], v[100:103], v[180:183]\n"
"ds_read_b128 v[28:31], v217 offset:28672\n"
"v_mfma_f32_16x16x32_bf16 v[184:187], v[0:3], v[96:99], v[184:187]\n"
"v_cmp_ne_u32 vcc, 0, v44\n"
"s_nop 0\n"
"s_cbranch_vccnz .Lr9\n"
".Lb9:\n"
"v_mfma_f32_16x16x32_bf16 v[188:191], v[0:3], v[100:103], v[188:191]\n"
"ds_read_b128 v[0:3], v217 offset:30720\n"
"s_waitcnt lgkmcnt(6)\n"
"v_mfma_f32_16x16x32_bf16 v[136:139], v[4:7], v[104:107], v[136:139]\n"
"v_fmac_f32 v52, v38, v54\n"
"v_fmac_f32 v53, v38, v55\n"
"v_mfma_f32_16x16x32_bf16 v[140:143], v[4:7], v[108:111], v[140:143]\n"
"ds_read_b128 v[4:7], v216 offset:24576\n"
"v_mfma_f32_16x16x32_bf16 v[144:147], v[16:19], v[104:107], v[144:147]\n"
"v_fma_f32 v52, -v39, v55, v52\n"
"v_fmac_f32 v53, v39, v54\n"
"v_mfma_f32_16x16x32_bf16 v[148:151], v[16:19], v[108:111], v[148:151]\n"
"ds_read_b128 v[16:19], v216 offset:26624\n"
"s_waitcnt lgkmcnt(6)\n"
"v_mfma_f32_16x16x32_bf16 v[176:179], v[20:23], v[104:107], v[176:179]\n"
"v_fmac_f32 v50, v38, v52\n"
"v_fmac_f32 v51, v38, v53\n"
"v_mfma_f32_16x16x32_bf16 v[180:183], v[20:23], v[108:111], v[180:183]\n"
"ds_read_b128 v[20:23], v216 offset:28672\n"
"v_mfma_f32_16x16x32_bf16 v[184:187], v[8:11], v[104:107], v[184:187]\n"
"v_fma_f32 v50, -v39, v53, v50\n"
"v_fmac_f32 v51, v39, v52\n"
"v_mfma_f32_16x16x32_bf16 v[188:191], v[8:11], v[108:111], v[188:191]\n"
"ds_read_b128 v[8:11], v216 offset:30720\n"
"s_waitcnt lgkmcnt(6)\n"
"v_mfma_f32_16x16x32_bf16 v[136:139], v[12:15], v[112:115], v[136:139]\n"
"v_fmac_f32 v48, v38, v50\n"
"v_fmac_f32 v49, v38, v51\n"
"v_mfma_f32_16x16x32_bf16 v[140:143], v[12:15], v[116:119], v[140:143]\n"
"v_fma_f32 v48, -v39, v51, v48\n"
"v_fmac_f32 v49, v39, v50\n"
"v_mfma_f32_16x16x32_bf16 v[144:147], v[24:27], v[112:115], v[144:147]\n"
"v_mfma_f32_16x16x32_bf16 v[148:151], v[24:27], v[116:119], v[148:151]\n"
"s_waitcnt lgkmcnt(4)\n"
"v_mfma_f32_16x16x32_bf16 v[176:179], v[28:31], v[112:115], v[176:179]\n"
"v_mfma_f32_16x16x32_bf16 v[180:183], v[28:31], v[116:119], v[180:183]\n"
"v_mfma_f32_16x16x32_bf16 v[184:187], v[0:3], v[112:115], v[184:187]\n"
"ds_read_b128 v[12:15], v218\n"
"ds_read_b128 v[24:27], v218 offset:256\n"
"v_mfma_f32_16x16x32_bf16 v[188:191], v[0:3], v[116:119], v[188:191]\n"
"ds_read_b128 v[0:3], v215\n"
"s_waitcnt lgkmcnt(5)\n"
"v_mfma_f32_16x16x32_bf16 v[136:139], v[4:7], v[120:123], v[136:139]\n"
"v_mfma_f32_16x16x32_bf16 v[140:143], v[4:7], v[124:127], v[140:143]\n"
"v_mfma_f32_16x16x32_bf16 v[144:147], v[16:19], v[120:123], v[144:147]\n"
"v_mfma_f32_16x16x32_bf16 v[148:151], v[16:19], v[124:127], v[148:151]\n"
"s_waitcnt lgkmcnt(3)\n"
"v_mfma_f32_16x16x32_bf16 v[176:179], v[20:23], v[120:123], v[176:179]\n"
"v_mfma_f32_16x16x32_bf16 v[180:183], v[20:23], v[124:127], v[180:183]\n"
"v_mfma_f32_16x16x32_bf16 v[184:187], v[8:11], v[120:123], v[184:187]\n"
"v_mfma_f32_16x16x32_bf16 v[188:191], v[8:11], v[124:127], v[188:191]\n"
"s_setprio 1\n"
"s_waitcnt lgkmcnt(2)\n"
"v_fma_f32 v4, v12, v136, v137\n"
"v_fma_f32 v6, v12, v140, v141\n"
"v_fma_f32 v4, -v13, v140, v4\n"
"v_fmac_f32 v6, v13, v136\n"
"v_fma_f32 v5, v12, v4, v138\n"
"v_fma_f32 v7, v12, v6, v142\n"
"v_fma_f32 v5, -v13, v6, v5\n"
"v_fmac_f32 v7, v13, v4\n"
"v_fma_f32 v4, v12, v5, v139\n"
"v_fma_f32 v6, v12, v7, v143\n"
"v_fma_f32 v4, -v13, v7, v4\n"
"v_fmac_f32 v6, v13, v5\n"
"v_fma_f32 v5, v12, v4, v144\n"
"v_fma_f32 v7, v12, v6, v148\n"
"v_fma_f32 v5, -v13, v6, v5\n"
"v_fmac_f32 v7, v13, v4\n"
"v_fma_f32 v4, v12, v5, v145\n"
"v_fma_f32 v6, v12, v7, v149\n"
"v_fma_f32 v4, -v13, v7, v4\n"
"v_fmac_f32 v6, v13, v5\n"
"v_fma_f32 v5, v12, v4, v146\n"
"v_fma_f32 v7, v12, v6, v150\n"
"v_fma_f32 v5, -v13, v6, v5\n"
"v_fmac_f32 v7, v13, v4\n"
"v_fma_f32 v4, v12, v5, v147\n"
"v_fma_f32 v6, v12, v7, v151\n"
"v_fma_f32 v4, -v13, v7, v4\n"
"v_fmac_f32 v6, v13, v5\n"
"v_fma_f32 v5, v12, v4, v176\n"
"v_fma_f32 v7, v12, v6, v180\n"
"v_fma_f32 v5, -v13, v6, v5\n"
"v_fmac_f32 v7, v13, v4\n"
"v_fma_f32 v4, v12, v5, v177\n"
"v_fma_f32 v6, v12, v7, v181\n"
"v_fma_f32 v4, -v13, v7, v4\n"
"v_fmac_f32 v6, v13, v5\n"
"v_fma_f32 v5, v12, v4, v178\n"
"v_fma_f32 v7, v12, v6, v182\n"
"v_fma_f32 v5, -v13, v6, v5\n"
"v_fmac_f32 v7, v13, v4\n"
"v_fma_f32 v4, v12, v5, v179\n"
"v_fma_f32 v6, v12, v7, v183\n"
"v_fma_f32 v4, -v13, v7, v4\n"
"v_fmac_f32 v6, v13, v5\n"
"v_fma_f32 v5, v12, v4, v184\n"
"v_fma_f32 v7, v12, v6, v188\n"
"v_fma_f32 v5, -v13, v6, v5\n"
"v_fmac_f32 v7, v13, v4\n"
"v_fma_f32 v4, v12, v5, v185\n"
"v_fma_f32 v6, v12, v7, v189\n"
"v_fma_f32 v4, -v13, v7, v4\n"
"v_fmac_f32 v6, v13, v5\n"
"v_fma_f32 v5, v12, v4, v186\n"
"v_fma_f32 v7, v12, v6, v190\n"
"v_fma_f32 v5, -v13, v6, v5\n"
"v_fmac_f32 v7, v13, v4\n"
"v_fma_f32 v77, v12, v5, v187\n"
"v_fma_f32 v78, v12, v7, v191\n"
"v_fma_f32 v77, -v13, v7, v77\n"
"v_fmac_f32 v78, v13, v5\n"
"v_mov_b32 v6, v77\n"
"v_mov_b32 v7, v78\n"
"v_mul_f32 v76, v14, v136\n"
"v_fma_f32 v76, -v15, v140, v76\n"
"v_permlane16_swap_b32 v77, v6\n"
"v_permlane16_swap_b32 v78, v7\n"
"s_waitcnt lgkmcnt(1)\n"
"v_fmac_f32 v6, v24, v77\n"
"v_fmac_f32 v7, v24, v78\n"
"v_fma_f32 v6, -v25, v78, v6\n"
"v_fmac_f32 v7, v25, v77\n"
"v_mov_b32 v8, v6\n"
"v_mov_b32 v9, v7\n"
"v_mul_f32_e64 v140, -v14, v140\n"
"v_fma_f32 v140, -v15, v136, v140\n"
"v_permlane32_swap_b32 v6, v8\n"
"v_permlane32_swap_b32 v7, v9\n"
"v_fmac_f32 v8, v26, v6\n"
"v_fmac_f32 v9, v26, v7\n"
"v_fma_f32 v8, -v27, v7, v8\n"
"v_fmac_f32 v9, v27, v6\n"
"v_bfi_b32 v4, s96, v213, v8\n"
"v_bfi_b32 v5, s96, v213, v9\n"
"global_store_dwordx2 v206, v[4:5], s[84:85] sc1\n"
"s_waitcnt lgkmcnt(0)\n"
"v_mul_f32 v77, v2, v77\n"
"v_mul_f32 v78, v2, v78\n"
"v_fmac_f32 v77, v0, v6\n"
"v_fmac_f32 v78, v0, v7\n"
"v_fma_f32 v77, -v1, v7, v77\n"
"v_fmac_f32 v78, v1, v6\n"
"v_mul_f32 v79, v14, v137\n"
"v_fma_f32 v79, -v15, v141, v79\n"
"v_mul_f32_e64 v141, -v14, v141\n"
"v_fma_f32 v141, -v15, v137, v141\n"
"v_mul_f32 v93, v14, v138\n"
"v_fma_f32 v93, -v15, v142, v93\n"
"v_mul_f32_e64 v142, -v14, v142\n"
"v_fma_f32 v142, -v15, v138, v142\n"
"v_mul_f32 v95, v14, v139\n"
"v_fma_f32 v95, -v15, v143, v95\n"
"v_mul_f32_e64 v143, -v14, v143\n"
"v_fma_f32 v143, -v15, v139, v143\n"
"v_mul_f32 v92, v14, v144\n"
"v_fma_f32 v92, -v15, v148, v92\n"
"v_mul_f32_e64 v148, -v14, v148\n"
"v_fma_f32 v148, -v15, v144, v148\n"
"v_mul_f32 v94, v14, v145\n"
"v_fma_f32 v94, -v15, v149, v94\n"
"v_mul_f32_e64 v149, -v14, v149\n"
"v_fma_f32 v149, -v15, v145, v149\n"
"v_mul_f32 v110, v14, v146\n"
"v_fma_f32 v110, -v15, v150, v110\n"
"v_mul_f32_e64 v150, -v14, v150\n"
"v_fma_f32 v150, -v15, v146, v150\n"
"v_mul_f32 v111, v14, v147\n"
"v_fma_f32 v111, -v15, v151, v111\n"
"v_mul_f32_e64 v151, -v14, v151\n"
"v_fma_f32 v151, -v15, v147, v151\n"
"v_mul_f32 v120, v14, v176\n"
"v_fma_f32 v120, -v15, v180, v120\n"
"v_mul_f32_e64 v180, -v14, v180\n"
"v_fma_f32 v180, -v15, v176, v180\n"
"v_mul_f32 v121, v14, v177\n"
"v_fma_f32 v121, -v15, v181, v121\n"
"v_mul_f32_e64 v181, -v14, v181\n"
"v_fma_f32 v181, -v15, v177, v181\n"
"v_mul_f32 v124, v14, v178\n"
"v_fma_f32 v124, -v15, v182, v124\n"
"v_mul_f32_e64 v182, -v14, v182\n"
"v_fma_f32 v182, -v15, v178, v182\n"
"v_mul_f32 v125, v14, v179\n"
"v_fma_f32 v125, -v15, v183, v125\n"
"v_mul_f32_e64 v183, -v14, v183\n"
"v_fma_f32 v183, -v15, v179, v183\n"
"v_mul_f32 v144, v14, v184\n"
"v_fma_f32 v144, -v15, v188, v144\n"
"v_mul_f32_e64 v188, -v14, v188\n"
"v_fma_f32 v188, -v15, v184, v188\n"
"v_mul_f32 v146, v14, v185\n"
"v_fma_f32 v146, -v15, v189, v146\n"
"v_mul_f32_e64 v189, -v14, v189\n"
"v_fma_f32 v189, -v15, v185, v189\n"
"v_mul_f32 v145, v14, v186\n"
"v_fma_f32 v145, -v15, v190, v145\n"
"v_mul_f32_e64 v190, -v14, v190\n"
"v_fma_f32 v190, -v15, v186, v190\n"
"v_mul_f32 v147, v14, v187\n"
"v_fma_f32 v147, -v15, v191, v147\n"
"v_mul_f32_e64 v191, -v14, v191\n"
"v_fma_f32 v191, -v15, v187, v191\n"
"s_barrier\n"
"s_add_u32 m0, s56, 0xc000\n"
"s_nop 0\n"
"global_load_lds_dwordx4 v227, s[68:69]\n"
"global_load_lds_dwordx4 v228, s[68:69] offset:1024\n"
"global_load_lds_dwordx4 v227, s[70:71] offset:2048\n"
"global_load_lds_dwordx4 v228, s[70:71] offset:3072\n"
"s_add_u32 m0, s56, 0x10000\n"
"s_nop 0\n"
"global_load_lds_dwordx4 v226, s[68:69]\n"
"global_load_lds_dwordx4 v225, s[68:69] offset:1024\n"
"global_load_lds_dwordx4 v226, s[70:71] offset:2048\n"
"global_load_lds_dwordx4 v225, s[70:71] offset:3072\n"
"s_add_u32 s13, s49, 1\n"
"s_and_b32 s13, s13, 3\n"
"s_lshl_b32 s13, s13, 7\n"
"s_add_u32 s68, s72, s13\n"
"s_addc_u32 s69, s73, 0\n"
"s_add_u32 s70, s74, s13\n"
"s_addc_u32 s71, s75, 0\n"
"s_add_u32 m0, s56, 0x14000\n"
"s_nop 0\n"
"global_load_lds_dwordx4 v227, s[68:69]\n"
"global_load_lds_dwordx4 v228, s[68:69] offset:1024\n"
"global_load_lds_dwordx4 v227, s[70:71] offset:2048\n"
"global_load_lds_dwordx4 v228, s[70:71] offset:3072\n"
"buffer_load_dwordx2 v[66:67], v214, s[76:79], 0 offen offset:1536 sc1\n"
"buffer_load_dwordx2 v[64:65], v214, s[76:79], 0 offen offset:1024 sc1\n"
"buffer_load_dwordx2 v[62:63], v214, s[76:79], 0 offen offset:512 sc1\n"
"buffer_load_dwordx2 v[60:61], v214, s[76:79], 0 offen offset:0 sc1\n"
"ds_read_b128 v[0:3], v215 offset:1024\n"
"s_waitcnt vmcnt(12)\n"
"ds_read_b128 v[4:7], v209\n"
"ds_read_b128 v[8:11], v210\n"
"ds_read_b128 v[16:19], v211\n"
"ds_read_b128 v[20:23], v212\n"
"s_add_u32 m0, s56, 0xc000\n"
"s_waitcnt lgkmcnt(0)\n"
"global_load_lds_dwordx4 v226, s[68:69]\n"
"global_load_lds_dwordx4 v225, s[68:69] offset:1024\n"
"global_load_lds_dwordx4 v226, s[70:71] offset:2048\n"
"global_load_lds_dwordx4 v225, s[70:71] offset:3072\n"
"s_waitcnt vmcnt(12)\n"
"ds_read_b128 v[24:27], v209 offset:16384\n"
"ds_read_b128 v[28:31], v210 offset:16384\n"
"ds_read_b128 v[32:35], v211 offset:16384\n"
"ds_read_b128 v[40:43], v212 offset:16384\n"
"s_add_u32 s13, s49, 2\n"
"s_and_b32 s13, s13, 3\n"
"s_lshl_b32 s13, s13, 7\n"
"s_add_u32 s68, s72, s13\n"
"s_addc_u32 s69, s73, 0\n"
"s_add_u32 s70, s74, s13\n"
"s_addc_u32 s71, s75, 0\n"
"s_add_u32 m0, s56, 0x10000\n"
"s_waitcnt lgkmcnt(0)\n"
"global_load_lds_dwordx4 v227, s[68:69]\n"
"global_load_lds_dwordx4 v228, s[68:69] offset:1024\n"
"global_load_lds_dwordx4 v227, s[70:71] offset:2048\n"
"global_load_lds_dwordx4 v228, s[70:71] offset:3072\n"
"s_waitcnt vmcnt(11)\n"
"v_xor_b32 v66, v66, v213\n"
"v_xor_b32 v67, v67, v213\n"
"s_waitcnt vmcnt(10)\n"
"v_xor_b32 v64, v64, v213\n"
"v_xor_b32 v65, v65, v213\n"
"s_waitcnt vmcnt(9)\n"
"v_xor_b32 v62, v62, v213\n"
"v_xor_b32 v63, v63, v213\n"
"s_waitcnt vmcnt(8)\n"
"v_xor_b32 v60, v60, v213\n"
"v_xor_b32 v61, v61, v213\n"
"v_or3_b32 v44, v66, v67, v64\n"
"v_or3_b32 v44, v44, v65, v62\n"
"v_or3_b32 v44, v44, v63, v60\n"
"v_bitop3_b32 v44, v44, v61, s96 bitop3:0xa8\n"
"v_cmp_ne_u32 vcc, 0, v44\n"
"s_nop 0\n"
"s_cbranch_vccnz .Lr10\n"
".Lb10:\n"
"v_fmac_f32 v66, v38, v48\n"
"v_fmac_f32 v67, v38, v49\n"
"v_fma_f32 v66, -v39, v49, v66\n"
"v_fmac_f32 v67, v39, v48\n"
"ds_read_b128 v[44:47], v209 offset:32768\n"
"ds_read_b128 v[48:51], v210 offset:32768\n"
"ds_read_b128 v[52:55], v211 offset:32768\n"
"ds_read_b128 v[56:59], v212 offset:32768\n"
"v_fmac_f32 v64, v38, v66\n"
"v_fmac_f32 v65, v38, v67\n"
"v_fma_f32 v64, -v39, v67, v64\n"
"v_fmac_f32 v65, v39, v66\n"
"s_add_u32 m0, s56, 0x14000\n"
"s_waitcnt lgkmcnt(0)\n"
"global_load_lds_dwordx4 v226, s[68:69]\n"
"global_load_lds_dwordx4 v225, s[68:69] offset:1024\n"
"v_fmac_f32 v62, v38, v64\n"
"v_fmac_f32 v63, v38, v65\n"
"global_load_lds_dwordx4 v226, s[70:71] offset:2048\n"
"v_fma_f32 v62, -v39, v65, v62\n"
"v_fmac_f32 v63, v39, v64\n"
"global_load_lds_dwordx4 v225, s[70:71] offset:3072\n"
"v_fmac_f32 v60, v38, v62\n"
"v_fmac_f32 v61, v38, v63\n"
"v_fma_f32 v60, -v39, v63, v60\n"
"v_fmac_f32 v61, v39, v62\n"
"v_mul_f32 v80, v2, v60\n"
"v_mul_f32 v81, v2, v61\n"
"v_fma_f32 v80, -v3, v61, v80\n"
"v_fmac_f32 v81, v3, v60\n"
"s_nop 1\n"
"v_permlane16_swap_b32 v80, v81\n"
"v_add_f32 v80, v80, v81\n"
"v_mov_b32 v81, v80\n"
"s_nop 1\n"
"v_permlane32_swap_b32 v80, v81\n"
"s_waitcnt vmcnt(8)\n"
"ds_read_b128 v[60:63], v209\n"
"ds_read_b128 v[64:67], v210\n"
"ds_read_b128 v[68:71], v211\n"
"ds_read_b128 v[72:75], v212\n"
"v_add_f32 v80, v80, v81\n"
"v_mov_b32 v81, v80\n"
"s_nop 1\n"
"v_permlane16_swap_b32 v80, v81\n"
"v_fmac_f32 v77, v0, v80\n"
"s_add_u32 s13, s49, 3\n"
"s_and_b32 s13, s13, 3\n"
"v_fmac_f32 v78, v0, v81\n"
"v_fma_f32 v77, -v1, v81, v77\n"
"s_lshl_b32 s13, s13, 7\n"
"v_fmac_f32 v78, v1, v80\n"
"v_fmac_f32 v76, v36, v77\n"
"s_add_u32 s68, s72, s13\n"
"s_addc_u32 s69, s73, 0\n"
"v_fma_f32 v140, -v36, v78, v140\n"
"v_fma_f32 v76, -v37, v78, v76\n"
"s_add_u32 s70, s74, s13\n"
"s_addc_u32 s71, s75, 0\n"
"v_fma_f32 v140, -v37, v77, v140\n"
"v_fmac_f32 v79, v12, v76\n"
"s_add_u32 m0, s56, 0xc000\n"
"s_waitcnt lgkmcnt(0)\n"
"global_load_lds_dwordx4 v227, s[68:69]\n"
"global_load_lds_dwordx4 v228, s[68:69] offset:1024\n"
"global_load_lds_dwordx4 v227, s[70:71] offset:2048\n"
"global_load_lds_dwordx4 v228, s[70:71] offset:3072\n"
"global_load_dwordx2 v[108:109], v205, s[90:91] sc1\n"
"v_fmac_f32 v141, v12, v140\n"
"v_fmac_f32 v79, v13, v140\n"
"v_fma_f32 v141, -v13, v76, v141\n"
"v_fmac_f32 v93, v12, v79\n"
"v_fmac_f32 v142, v12, v141\n"
"v_fmac_f32 v93, v13, v141\n"
"v_fma_f32 v142, -v13, v79, v142\n"
"v_cvt_pk_bf16_f32 v0, v76, v79\n"
"s_waitcnt vmcnt(9)\n"
"ds_read_b128 v[36:39], v209 offset:16384\n"
"ds_read_b128 v[76:79], v210 offset:16384\n"
"ds_read_b128 v[80:83], v211 offset:16384\n"
"ds_read_b128 v[84:87], v212 offset:16384\n"
"v_cvt_pk_bf16_f32 v88, v140, v141\n"
"v_fmac_f32 v95, v12, v93\n"
"v_fmac_f32 v143, v12, v142\n"
"v_fmac_f32 v95, v13, v142\n"
"s_add_u32 m0, s56, 0x10000\n"
"s_waitcnt lgkmcnt(0)\n"
"global_load_lds_dwordx4 v226, s[68:69]\n"
"global_load_lds_dwordx4 v225, s[68:69] offset:1024\n"
"v_fma_f32 v143, -v13, v93, v143\n"
"v_fmac_f32 v92, v12, v95\n"
"global_load_lds_dwordx4 v226, s[70:71] offset:2048\n"
"v_fmac_f32 v148, v12, v143\n"
"v_fmac_f32 v92, v13, v143\n"
"global_load_lds_dwordx4 v225, s[70:71] offset:3072\n"
"v_fma_f32 v148, -v13, v95, v148\n"
"v_cvt_pk_bf16_f32 v1, v93, v95\n"
"v_cvt_pk_bf16_f32 v89, v142, v143\n"
"v_fmac_f32 v94, v12, v92\n"
"v_fmac_f32 v149, v12, v148\n"
"v_fmac_f32 v94, v13, v148\n"
"v_fma_f32 v149, -v13, v92, v149\n"
"v_fmac_f32 v110, v12, v94\n"
"v_fmac_f32 v150, v12, v149\n"
"v_fmac_f32 v110, v13, v149\n"
"v_fma_f32 v150, -v13, v94, v150\n"
"v_cvt_pk_bf16_f32 v2, v92, v94\n"
"v_cvt_pk_bf16_f32 v90, v148, v149\n"
"v_fmac_f32 v111, v12, v110\n"
"s_waitcnt vmcnt(9)\n"
"ds_read_b128 v[92:95], v209 offset:32768\n"
"ds_read_b128 v[96:99], v210 offset:32768\n"
"ds_read_b128 v[100:103], v211 offset:32768\n"
"ds_read_b128 v[104:107], v212 offset:32768\n"
"s_waitcnt vmcnt(4)\n"
"v_cmp_ne_u64 vcc, s[10:11], v[108:109]\n"
"s_nop 0\n"
"s_cbranch_vccnz .Lr11\n"
".Lb11:\n"
"s_add_u32 s90, s90, 0x1000\n"
"s_addc_u32 s91, s91, 0\n"
"s_waitcnt lgkmcnt(0)\n"
"v_fmac_f32 v151, v12, v150\n"
"v_fmac_f32 v111, v13, v150\n"
"v_fma_f32 v151, -v13, v110, v151\n"
"v_fmac_f32 v120, v12, v111\n"
"v_fmac_f32 v180, v12, v151\n"
"v_fmac_f32 v120, v13, v151\n"
"s_add_u32 m0, s56, 0x14000\n"
"s_nop 0\n"
"global_load_lds_dwordx4 v202, s[62:63] sc1\n"
"v_fma_f32 v180, -v13, v111, v180\n"
"v_cvt_pk_bf16_f32 v3, v110, v111\n"
"global_load_lds_dwordx4 v202, s[62:63] offset:1024 sc1\n"
"v_cvt_pk_bf16_f32 v91, v150, v151\n"
"global_store_dwordx4 v208, v[0:3], s[86:87] sc1\n"
"global_store_dwordx4 v208, v[88:91], s[86:87] offset:512 sc1\n"
"global_load_lds_dwordx4 v202, s[62:63] offset:2048 sc1\n"
"v_fmac_f32 v121, v12, v120\n"
"v_fmac_f32 v181, v12, v180\n"
"v_fmac_f32 v121, v13, v180\n"
"v_fma_f32 v181, -v13, v120, v181\n"
"global_load_lds_dwordx4 v202, s[62:63] offset:3072 sc1\n"
"v_fmac_f32 v124, v12, v121\n"
"v_fmac_f32 v182, v12, v181\n"
"v_fmac_f32 v124, v13, v181\n"
"v_fma_f32 v182, -v13, v121, v182\n"
"ds_read_b128 v[0:3], v209\n"
"ds_read_b128 v[88:91], v210\n"
"ds_read_b128 v[108:111], v211\n"
"ds_read_b128 v[112:115], v212\n"
"s_waitcnt lgkmcnt(0)\n"
"v_cvt_pk_bf16_f32 v116, v120, v121\n"
"v_cvt_pk_bf16_f32 v120, v180, v181\n"
"v_fmac_f32 v125, v12, v124\n"
"v_fmac_f32 v183, v12, v182\n"
"v_fmac_f32 v125, v13, v182\n"
"v_fma_f32 v183, -v13, v124, v183\n"
"s_add_u32 m0, s56, 0xc000\n"
"s_nop 0\n"
"global_load_lds_dwordx4 v194, s[62:63] sc1\n"
"v_fmac_f32 v144, v12, v125\n"
"v_fmac_f32 v188, v12, v183\n"
"global_load_lds_dwordx4 v194, s[62:63] offset:1024 sc1\n"
"v_fmac_f32 v144, v13, v183\n"
"v_fma_f32 v188, -v13, v125, v188\n"
"v_cvt_pk_bf16_f32 v117, v124, v125\n"
"v_cvt_pk_bf16_f32 v121, v182, v183\n"
"global_load_lds_dwordx4 v194, s[62:63] offset:2048 sc1\n"
"v_fmac_f32 v146, v12, v144\n"
"v_fmac_f32 v189, v12, v188\n"
"v_fmac_f32 v146, v13, v188\n"
"v_fma_f32 v189, -v13, v144, v189\n"
"global_load_lds_dwordx4 v194, s[62:63] offset:3072 sc1\n"
"v_fmac_f32 v145, v12, v146\n"
"v_fmac_f32 v190, v12, v189\n"
"v_fmac_f32 v145, v13, v189\n"
"v_fma_f32 v190, -v13, v146, v190\n"
"s_waitcnt vmcnt(10)\n"
"ds_read_b128 v[124:127], v209 offset:16384\n"
"ds_read_b128 v[128:131], v210 offset:16384\n"
"ds_read_b128 v[132:135], v211 offset:16384\n"
"ds_read_b128 v[136:139], v212 offset:16384\n"
"s_waitcnt lgkmcnt(0)\n"
"v_cvt_pk_bf16_f32 v118, v144, v146\n"
"v_cvt_pk_bf16_f32 v122, v188, v189\n"
"v_fmac_f32 v147, v12, v145\n"
"v_fmac_f32 v191, v12, v190\n"
"v_fmac_f32 v147, v13, v190\n"
"v_fma_f32 v191, -v13, v145, v191\n"
"s_add_u32 m0, s56, 0x10000\n"
"s_nop 0\n"
"global_load_lds_dwordx4 v202, s[64:65] sc1\n"
"v_cvt_pk_bf16_f32 v119, v145, v147\n"
"v_cvt_pk_bf16_f32 v123, v190, v191\n"
"global_load_lds_dwordx4 v202, s[64:65] offset:1024 sc1\n"
"global_store_dwordx4 v207, v[116:119], s[86:87] offset:256 sc1\n"
"global_store_dwordx4 v207, v[120:123], s[86:87] offset:768 sc1\n"
"global_load_lds_dwordx4 v202, s[64:65] offset:2048 sc1\n"
"global_load_lds_dwordx4 v202, s[64:65] offset:3072 sc1\n"
"s_waitcnt vmcnt(10)\n"
"s_barrier\n"
"ds_read_b64_tr_b16 v[12:13], v200 offset:16384\n"
"ds_read_b64_tr_b16 v[14:15], v201 offset:16384\n"
"ds_read_b64_tr_b16 v[116:117], v200 offset:17408\n"
"ds_read_b64_tr_b16 v[118:119], v201 offset:17408\n"
"ds_read_b64_tr_b16 v[120:121], v200 offset:18432\n"
"ds_read_b64_tr_b16 v[122:123], v201 offset:18432\n"
"ds_read_b64_tr_b16 v[140:141], v200 offset:19456\n"
"ds_read_b64_tr_b16 v[142:143], v201 offset:19456\n"
"ds_read_b64_tr_b16 v[144:145], v200 offset:20480\n"
"ds_read_b64_tr_b16 v[146:147], v201 offset:20480\n"
"ds_read_b64_tr_b16 v[148:149], v200 offset:21504\n"
"ds_read_b64_tr_b16 v[150:151], v201 offset:21504\n"
"s_setprio 0\n"
"s_waitcnt lgkmcnt(8)\n"
"v_mfma_f32_16x16x32_bf16 v[152:155], v[12:15], v[4:7], 0\n"
"ds_read_b64_tr_b16 v[156:157], v200 offset:22528\n"
"ds_read_b64_tr_b16 v[158:159], v201 offset:22528\n"
"v_mfma_f32_16x16x32_bf16 v[160:163], v[12:15], v[24:27], 0\n"
"v_mfma_f32_16x16x32_bf16 v[152:155], v[116:119], v[8:11], v[152:155]\n"
"ds_read_b64_tr_b16 v[12:13], v200 offset:23552\n"
"ds_read_b64_tr_b16 v[14:15], v201 offset:23552\n"
"v_mfma_f32_16x16x32_bf16 v[160:163], v[116:119], v[28:31], v[160:163]\n"
"s_waitcnt lgkmcnt(8)\n"
"v_mfma_f32_16x16x32_bf16 v[152:155], v[120:123], v[16:19], v[152:155]\n"
"ds_read_b64_tr_b16 v[116:117], v200 offset:24576\n"
"ds_read_b64_tr_b16 v[118:119], v201 offset:24576\n"
"v_mfma_f32_16x16x32_bf16 v[160:163], v[120:123], v[32:35], v[160:163]\n"
"v_mfma_f32_16x16x32_bf16 v[152:155], v[140:143], v[20:23], v[152:155]\n"
"ds_read_b64_tr_b16 v[120:121], v200 offset:25600\n"
"ds_read_b64_tr_b16 v[122:123], v201 offset:25600\n"
"v_mfma_f32_16x16x32_bf16 v[160:163], v[140:143], v[40:43], v[160:163]\n"
"s_waitcnt lgkmcnt(8)\n"
"v_mfma_f32_16x16x32_bf16 v[152:155], v[144:147], v[44:47], v[152:155]\n"
"ds_read_b64_tr_b16 v[140:141], v200 offset:26624\n"
"ds_read_b64_tr_b16 v[142:143], v201 offset:26624\n"
"v_mfma_f32_16x16x32_bf16 v[160:163], v[144:147], v[60:63], v[160:163]\n"
"v_mfma_f32_16x16x32_bf16 v[152:155], v[148:151], v[48:51], v[152:155]\n"
"ds_read_b64_tr_b16 v[144:145], v200 offset:27648\n"
"ds_read_b64_tr_b16 v[146:147], v201 offset:27648\n"
"v_mfma_f32_16x16x32_bf16 v[160:163], v[148:151], v[64:67], v[160:163]\n"
"s_waitcnt vmcnt(2) lgkmcnt(8)\n"
"global_store_dwordx2 v206, v[192:193], s[88:89] sc1\n"
"v_mfma_f32_16x16x32_bf16 v[152:155], v[156:159], v[52:55], v[152:155]\n"
"ds_read_b64_tr_b16 v[148:149], v200 offset:28672\n"
"ds_read_b64_tr_b16 v[150:151], v201 offset:28672\n"
"v_mfma_f32_16x16x32_bf16 v[160:163], v[156:159], v[68:71], v[160:163]\n"
"v_mfma_f32_16x16x32_bf16 v[152:155], v[12:15], v[56:59], v[152:155]\n"
"ds_read_b64_tr_b16 v[156:157], v200 offset:29696\n"
"ds_read_b64_tr_b16 v[158:159], v201 offset:29696\n"
"v_mfma_f32_16x16x32_bf16 v[160:163], v[12:15], v[72:75], v[160:163]\n"
"s_waitcnt lgkmcnt(8)\n"
"v_mfma_f32_16x16x32_bf16 v[152:155], v[116:119], v[36:39], v[152:155]\n"
"ds_read_b64_tr_b16 v[12:13], v200 offset:30720\n"
"ds_read_b64_tr_b16 v[14:15], v201 offset:30720\n"
"v_mfma_f32_16x16x32_bf16 v[160:163], v[116:119], v[92:95], v[160:163]\n"
"v_mfma_f32_16x16x32_bf16 v[152:155], v[120:123], v[76:79], v[152:155]\n"
"ds_read_b64_tr_b16 v[116:117], v200 offset:31744\n"
"ds_read_b64_tr_b16 v[118:119], v201 offset:31744\n"
"v_mfma_f32_16x16x32_bf16 v[160:163], v[120:123], v[96:99], v[160:163]\n"
"s_waitcnt vmcnt(1) lgkmcnt(8)\n"
"s_barrier\n"
"s_add_u32 m0, s56, 0x8000\n"
"s_nop 0\n"
"global_load_lds_dwordx4 v194, s[64:65] sc1\n"
"global_load_lds_dwordx4 v194, s[64:65] offset:1024 sc1\n"
"global_load_lds_dwordx4 v194, s[64:65] offset:2048 sc1\n"
"global_load_lds_dwordx4 v194, s[64:65] offset:3072 sc1\n"
"s_add_u32 m0, s56, 0x4000\n"
"s_nop 0\n"
"global_load_lds_dwordx4 v202, s[18:19] sc1\n"
"global_load_lds_dwordx4 v202, s[18:19] offset:1024 sc1\n"
"global_load_lds_dwordx4 v202, s[18:19] offset:2048 sc1\n"
"global_load_lds_dwordx4 v202, s[18:19] offset:3072 sc1\n"
"v_mfma_f32_16x16x32_bf16 v[152:155], v[140:143], v[80:83], v[152:155]\n"
"ds_read_b64_tr_b16 v[120:121], v200\n"
"ds_read_b64_tr_b16 v[122:123], v201\n"
"v_mfma_f32_16x16x32_bf16 v[160:163], v[140:143], v[100:103], v[160:163]\n"
"v_mfma_f32_16x16x32_bf16 v[152:155], v[144:147], v[84:87], v[152:155]\n"
"ds_read_b64_tr_b16 v[140:141], v200 offset:1024\n"
"ds_read_b64_tr_b16 v[142:143], v201 offset:1024\n"
"v_mfma_f32_16x16x32_bf16 v[160:163], v[144:147], v[104:107], v[160:163]\n"
"s_waitcnt lgkmcnt(8)\n"
"v_mfma_f32_16x16x32_bf16 v[152:155], v[148:151], v[0:3], v[152:155]\n"
"ds_read_b64_tr_b16 v[144:145], v200 offset:2048\n"
"ds_read_b64_tr_b16 v[146:147], v201 offset:2048\n"
"v_mfma_f32_16x16x32_bf16 v[160:163], v[148:151], v[124:127], v[160:163]\n"
"v_mfma_f32_16x16x32_bf16 v[152:155], v[156:159], v[88:91], v[152:155]\n"
"ds_read_b64_tr_b16 v[148:149], v200 offset:3072\n"
"ds_read_b64_tr_b16 v[150:151], v201 offset:3072\n"
"v_mfma_f32_16x16x32_bf16 v[160:163], v[156:159], v[128:131], v[160:163]\n"
"s_waitcnt lgkmcnt(8)\n"
"v_mfma_f32_16x16x32_bf16 v[152:155], v[12:15], v[108:111], v[152:155]\n"
"ds_read_b64_tr_b16 v[156:157], v200 offset:4096\n"
"ds_read_b64_tr_b16 v[158:159], v201 offset:4096\n"
"v_mfma_f32_16x16x32_bf16 v[160:163], v[12:15], v[132:135], v[160:163]\n"
"v_mfma_f32_16x16x32_bf16 v[152:155], v[116:119], v[112:115], v[152:155]\n"
"ds_read_b64_tr_b16 v[12:13], v200 offset:5120\n"
"ds_read_b64_tr_b16 v[14:15], v201 offset:5120\n"
"v_mfma_f32_16x16x32_bf16 v[160:163], v[116:119], v[136:139], v[160:163]\n"
"s_waitcnt lgkmcnt(8)\n"
"v_mfma_f32_16x16x32_bf16 v[116:119], v[120:123], v[4:7], 0\n"
"ds_read_b64_tr_b16 v[164:165], v200 offset:6144\n"
"ds_read_b64_tr_b16 v[166:167], v201 offset:6144\n"
"v_mfma_f32_16x16x32_bf16 v[168:171], v[120:123], v[24:27], 0\n"
"v_mfma_f32_16x16x32_bf16 v[116:119], v[140:143], v[8:11], v[116:119]\n"
"ds_read_b64_tr_b16 v[120:121], v200 offset:7168\n"
"ds_read_b64_tr_b16 v[122:123], v201 offset:7168\n"
"v_mfma_f32_16x16x32_bf16 v[168:171], v[140:143], v[28:31], v[168:171]\n"
"ds_write_b128 v195, v[152:155] offset:4160\n"
"s_waitcnt lgkmcnt(9)\n"
"v_mfma_f32_16x16x32_bf16 v[116:119], v[144:147], v[16:19], v[116:119]\n"
"ds_read_b64_tr_b16 v[140:141], v200 offset:8192\n"
"ds_read_b64_tr_b16 v[142:143], v201 offset:8192\n"
"v_mfma_f32_16x16x32_bf16 v[168:171], v[144:147], v[32:35], v[168:171]\n"
"ds_write_b128 v195, v[160:163] offset:5184\n"
"v_mfma_f32_16x16x32_bf16 v[116:119], v[148:151], v[20:23], v[116:119]\n"
"ds_read_b64_tr_b16 v[144:145], v200 offset:9216\n"
"ds_read_b64_tr_b16 v[146:147], v201 offset:9216\n"
"v_mfma_f32_16x16x32_bf16 v[168:171], v[148:151], v[40:43], v[168:171]\n"
"s_waitcnt lgkmcnt(10)\n"
"v_mfma_f32_16x16x32_bf16 v[116:119], v[156:159], v[44:47], v[116:119]\n"
"ds_read_b64_tr_b16 v[148:149], v200 offset:10240\n"
"ds_read_b64_tr_b16 v[150:151], v201 offset:10240\n"
"v_mfma_f32_16x16x32_bf16 v[168:171], v[156:159], v[60:63], v[168:171]\n"
"s_waitcnt vmcnt(0) lgkmcnt(4)\n"
"s_barrier\n"
"s_add_u32 m0, s56, 0\n"
"s_nop 0\n"
"global_load_lds_dwordx4 v194, s[18:19] sc1\n"
"global_load_lds_dwordx4 v194, s[18:19] offset:1024 sc1\n"
"global_load_lds_dwordx4 v194, s[18:19] offset:2048 sc1\n"
"global_load_lds_dwordx4 v194, s[18:19] offset:3072 sc1\n"
"s_add_u32 m0, s56, 0x20200\n"
"s_nop 0\n"
"global_load_lds_dwordx4 v202, s[20:21] sc1\n"
"global_load_lds_dwordx4 v202, s[20:21] offset:1024 sc1\n"
"global_load_lds_dwordx4 v202, s[20:21] offset:2048 sc1\n"
"global_load_lds_dwordx4 v202, s[20:21] offset:3072 sc1\n"
"v_mfma_f32_16x16x32_bf16 v[116:119], v[12:15], v[48:51], v[116:119]\n"
"ds_read_b64_tr_b16 v[152:153], v200 offset:11264\n"
"ds_read_b64_tr_b16 v[154:155], v201 offset:11264\n"
"v_mfma_f32_16x16x32_bf16 v[168:171], v[12:15], v[64:67], v[168:171]\n"
"v_mfma_f32_16x16x32_bf16 v[116:119], v[164:167], v[52:55], v[116:119]\n"
"ds_read_b64_tr_b16 v[12:13], v200 offset:12288\n"
"ds_read_b64_tr_b16 v[14:15], v201 offset:12288\n"
"v_mfma_f32_16x16x32_bf16 v[168:171], v[164:167], v[68:71], v[168:171]\n"
"v_mfma_f32_16x16x32_bf16 v[116:119], v[120:123], v[56:59], v[116:119]\n"
"ds_read_b64_tr_b16 v[156:157], v200 offset:13312\n"
"ds_read_b64_tr_b16 v[158:159], v201 offset:13312\n"
"v_mfma_f32_16x16x32_bf16 v[168:171], v[120:123], v[72:75], v[168:171]\n"
"s_waitcnt lgkmcnt(8)\n"
"v_mfma_f32_16x16x32_bf16 v[116:119], v[140:143], v[36:39], v[116:119]\n"
"ds_read_b64_tr_b16 v[120:121], v200 offset:14336\n"
"ds_read_b64_tr_b16 v[122:123], v201 offset:14336\n"
"v_mfma_f32_16x16x32_bf16 v[168:171], v[140:143], v[92:95], v[168:171]\n"
"v_mfma_f32_16x16x32_bf16 v[116:119], v[144:147], v[76:79], v[116:119]\n"
"ds_read_b64_tr_b16 v[140:141], v200 offset:15360\n"
"ds_read_b64_tr_b16 v[142:143], v201 offset:15360\n"
"v_mfma_f32_16x16x32_bf16 v[168:171], v[144:147], v[96:99], v[168:171]\n"
"s_waitcnt lgkmcnt(8)\n"
"v_mfma_f32_16x16x32_bf16 v[116:119], v[148:151], v[80:83], v[116:119]\n"
"ds_read_b64_tr_b16 v[144:145], v198 offset:16384\n"
"ds_read_b64_tr_b16 v[146:147], v199 offset:16384\n"
"v_mfma_f32_16x16x32_bf16 v[168:171], v[148:151], v[100:103], v[168:171]\n"
"v_mfma_f32_16x16x32_bf16 v[116:119], v[152:155], v[84:87], v[116:119]\n"
"ds_read_b64_tr_b16 v[148:149], v198 offset:17408\n"
"ds_read_b64_tr_b16 v[150:151], v199 offset:17408\n"
"v_mfma_f32_16x16x32_bf16 v[168:171], v[152:155], v[104:107], v[168:171]\n"
"global_load_dwordx2 v[172:173], v205, s[90:91] sc1\n"
"s_waitcnt lgkmcnt(8)\n"
"v_mfma_f32_16x16x32_bf16 v[116:119], v[12:15], v[0:3], v[116:119]\n"
"ds_read_b64_tr_b16 v[152:153], v198 offset:18432\n"
"ds_read_b64_tr_b16 v[154:155], v199 offset:18432\n"
"v_mfma_f32_16x16x32_bf16 v[168:171], v[12:15], v[124:127], v[168:171]\n"
"v_mfma_f32_16x16x32_bf16 v[116:119], v[156:159], v[88:91], v[116:119]\n"
"ds_read_b64_tr_b16 v[12:13], v198 offset:19456\n"
"ds_read_b64_tr_b16 v[14:15], v199 offset:19456\n"
"v_mfma_f32_16x16x32_bf16 v[168:171], v[156:159], v[128:131], v[168:171]\n"
"s_waitcnt lgkmcnt(8)\n"
"v_mfma_f32_16x16x32_bf16 v[116:119], v[120:123], v[108:111], v[116:119]\n"
"ds_read_b64_tr_b16 v[156:157], v198 offset:20480\n"
"ds_read_b64_tr_b16 v[158:159], v199 offset:20480\n"
"v_mfma_f32_16x16x32_bf16 v[168:171], v[120:123], v[132:135], v[168:171]\n"
"v_mfma_f32_16x16x32_bf16 v[116:119], v[140:143], v[112:115], v[116:119]\n"
"ds_read_b64_tr_b16 v[120:121], v198 offset:21504\n"
"ds_read_b64_tr_b16 v[122:123], v199 offset:21504\n"
"v_mfma_f32_16x16x32_bf16 v[168:171], v[140:143], v[136:139], v[168:171]\n"
"s_waitcnt lgkmcnt(8)\n"
"v_mfma_f32_16x16x32_bf16 v[140:143], v[144:147], v[4:7], 0\n"
"ds_read_b64_tr_b16 v[160:161], v198 offset:22528\n"
"ds_read_b64_tr_b16 v[162:163], v199 offset:22528\n"
"v_mfma_f32_16x16x32_bf16 v[164:167], v[144:147], v[24:27], 0\n"
"v_mfma_f32_16x16x32_bf16 v[140:143], v[148:151], v[8:11], v[140:143]\n"
"ds_read_b64_tr_b16 v[144:145], v198 offset:23552\n"
"ds_read_b64_tr_b16 v[146:147], v199 offset:23552\n"
"v_mfma_f32_16x16x32_bf16 v[164:167], v[148:151], v[28:31], v[164:167]\n"
"ds_write_b128 v195, v[116:119] offset:6208\n"
"s_waitcnt lgkmcnt(9)\n"
"v_mfma_f32_16x16x32_bf16 v[140:143], v[152:155], v[16:19], v[140:143]\n"
"ds_read_b64_tr_b16 v[116:117], v198 offset:24576\n"
"ds_read_b64_tr_b16 v[118:119], v199 offset:24576\n"
"v_mfma_f32_16x16x32_bf16 v[164:167], v[152:155], v[32:35], v[164:167]\n"
"ds_write_b128 v195, v[168:171] offset:7232\n"
"v_mfma_f32_16x16x32_bf16 v[140:143], v[12:15], v[20:23], v[140:143]\n"
"ds_read_b64_tr_b16 v[148:149], v198 offset:25600\n"
"ds_read_b64_tr_b16 v[150:151], v199 offset:25600\n"
"v_mfma_f32_16x16x32_bf16 v[164:167], v[12:15], v[40:43], v[164:167]\n"
"s_waitcnt lgkmcnt(10)\n"
"v_mfma_f32_16x16x32_bf16 v[140:143], v[156:159], v[44:47], v[140:143]\n"
"ds_read_b64_tr_b16 v[12:13], v198 offset:26624\n"
"ds_read_b64_tr_b16 v[14:15], v199 offset:26624\n"
"v_mfma_f32_16x16x32_bf16 v[164:167], v[156:159], v[60:63], v[164:167]\n"
"s_waitcnt vmcnt(1) lgkmcnt(4)\n"
"s_barrier\n"
"s_add_u32 m0, s56, 0xc000\n"
"s_nop 0\n"
"global_load_lds_dwordx4 v194, s[20:21] sc1\n"
"global_load_lds_dwordx4 v194, s[20:21] offset:1024 sc1\n"
"global_load_lds_dwordx4 v194, s[20:21] offset:2048 sc1\n"
"global_load_lds_dwordx4 v194, s[20:21] offset:3072 sc1\n"
"s_waitcnt vmcnt(4)\n"
"v_cmp_ne_u64 vcc, s[10:11], v[172:173]\n"
"s_nop 0\n"
"s_cbranch_vccnz .Lr12\n"
".Lb12:\n"
"s_add_u32 m0, s56, 0x14000\n"
"s_nop 0\n"
"global_load_lds_dwordx4 v202, s[32:33] sc1\n"
"global_load_lds_dwordx4 v202, s[32:33] offset:1024 sc1\n"
"global_load_lds_dwordx4 v202, s[32:33] offset:2048 sc1\n"
"global_load_lds_dwordx4 v202, s[32:33] offset:3072 sc1\n"
"v_mfma_f32_16x16x32_bf16 v[140:143], v[120:123], v[48:51], v[140:143]\n"
"ds_read_b64_tr_b16 v[152:153], v198 offset:27648\n"
"ds_read_b64_tr_b16 v[154:155], v199 offset:27648\n"
"v_mfma_f32_16x16x32_bf16 v[164:167], v[120:123], v[64:67], v[164:167]\n"
"v_mfma_f32_16x16x32_bf16 v[140:143], v[160:163], v[52:55], v[140:143]\n"
"ds_read_b64_tr_b16 v[120:121], v198 offset:28672\n"
"ds_read_b64_tr_b16 v[122:123], v199 offset:28672\n"
"v_mfma_f32_16x16x32_bf16 v[164:167], v[160:163], v[68:71], v[164:167]\n"
"v_mfma_f32_16x16x32_bf16 v[140:143], v[144:147], v[56:59], v[140:143]\n"
"ds_read_b64_tr_b16 v[156:157], v198 offset:29696\n"
"ds_read_b64_tr_b16 v[158:159], v199 offset:29696\n"
"v_mfma_f32_16x16x32_bf16 v[164:167], v[144:147], v[72:75], v[164:167]\n"
"s_waitcnt lgkmcnt(8)\n"
"v_mfma_f32_16x16x32_bf16 v[140:143], v[116:119], v[36:39], v[140:143]\n"
"ds_read_b64_tr_b16 v[144:145], v198 offset:30720\n"
"ds_read_b64_tr_b16 v[146:147], v199 offset:30720\n"
"v_mfma_f32_16x16x32_bf16 v[164:167], v[116:119], v[92:95], v[164:167]\n"
"v_mfma_f32_16x16x32_bf16 v[140:143], v[148:151], v[76:79], v[140:143]\n"
"ds_read_b64_tr_b16 v[116:117], v198 offset:31744\n"
"ds_read_b64_tr_b16 v[118:119], v199 offset:31744\n"
"v_mfma_f32_16x16x32_bf16 v[164:167], v[148:151], v[96:99], v[164:167]\n"
"s_waitcnt lgkmcnt(8)\n"
"v_mfma_f32_16x16x32_bf16 v[140:143], v[12:15], v[80:83], v[140:143]\n"
"ds_read_b64_tr_b16 v[148:149], v196 offset:512\n"
"ds_read_b64_tr_b16 v[150:151], v197 offset:512\n"
"v_mfma_f32_16x16x32_bf16 v[164:167], v[12:15], v[100:103], v[164:167]\n"
"v_mfma_f32_16x16x32_bf16 v[140:143], v[152:155], v[84:87], v[140:143]\n"
"ds_read_b64_tr_b16 v[12:13], v196 offset:1536\n"
"ds_read_b64_tr_b16 v[14:15], v197 offset:1536\n"
"v_mfma_f32_16x16x32_bf16 v[164:167], v[152:155], v[104:107], v[164:167]\n"
"s_waitcnt lgkmcnt(8)\n"
"v_mfma_f32_16x16x32_bf16 v[140:143], v[120:123], v[0:3], v[140:143]\n"
"ds_read_b64_tr_b16 v[152:153], v196 offset:2560\n"
"ds_read_b64_tr_b16 v[154:155], v197 offset:2560\n"
"v_mfma_f32_16x16x32_bf16 v[164:167], v[120:123], v[124:127], v[164:167]\n"
"v_mfma_f32_16x16x32_bf16 v[140:143], v[156:159], v[88:91], v[140:143]\n"
"ds_read_b64_tr_b16 v[120:121], v196 offset:3584\n"
"ds_read_b64_tr_b16 v[122:123], v197 offset:3584\n"
"v_mfma_f32_16x16x32_bf16 v[164:167], v[156:159], v[128:131], v[164:167]\n"
"s_waitcnt lgkmcnt(8)\n"
"v_mfma_f32_16x16x32_bf16 v[140:143], v[144:147], v[108:111], v[140:143]\n"
"ds_read_b64_tr_b16 v[156:157], v196 offset:4608\n"
"ds_read_b64_tr_b16 v[158:159], v197 offset:4608\n"
"v_mfma_f32_16x16x32_bf16 v[164:167], v[144:147], v[132:135], v[164:167]\n"
"v_mfma_f32_16x16x32_bf16 v[140:143], v[116:119], v[112:115], v[140:143]\n"
"ds_read_b64_tr_b16 v[144:145], v196 offset:5632\n"
"ds_read_b64_tr_b16 v[146:147], v197 offset:5632\n"
"v_mfma_f32_16x16x32_bf16 v[164:167], v[116:119], v[136:139], v[164:167]\n"
"s_waitcnt lgkmcnt(8)\n"
"v_mfma_f32_16x16x32_bf16 v[116:119], v[148:151], v[4:7], 0\n"
"ds_read_b64_tr_b16 v[160:161], v196 offset:6656\n"
"ds_read_b64_tr_b16 v[162:163], v197 offset:6656\n"
"v_mfma_f32_16x16x32_bf16 v[168:171], v[148:151], v[24:27], 0\n"
"v_mfma_f32_16x16x32_bf16 v[116:119], v[12:15], v[8:11], v[116:119]\n"
"ds_read_b64_tr_b16 v[148:149], v196 offset:7680\n"
"ds_read_b64_tr_b16 v[150:151], v197 offset:7680\n"
"v_mfma_f32_16x16x32_bf16 v[168:171], v[12:15], v[28:31], v[168:171]\n"
"ds_write_b128 v195, v[140:143] offset:4160\n"
"s_waitcnt lgkmcnt(9)\n"
"v_mfma_f32_16x16x32_bf16 v[116:119], v[152:155], v[16:19], v[116:119]\n"
"ds_read_b64_tr_b16 v[12:13], v196 offset:8704\n"
"ds_read_b64_tr_b16 v[14:15], v197 offset:8704\n"
"v_mfma_f32_16x16x32_bf16 v[168:171], v[152:155], v[32:35], v[168:171]\n"
"ds_write_b128 v195, v[164:167] offset:5184\n"
"v_mfma_f32_16x16x32_bf16 v[116:119], v[120:123], v[20:23], v[116:119]\n"
"ds_read_b64_tr_b16 v[140:141], v196 offset:9728\n"
"ds_read_b64_tr_b16 v[142:143], v197 offset:9728\n"
"v_mfma_f32_16x16x32_bf16 v[168:171], v[120:123], v[40:43], v[168:171]\n"
"s_waitcnt lgkmcnt(10)\n"
"v_mfma_f32_16x16x32_bf16 v[116:119], v[156:159], v[44:47], v[116:119]\n"
"ds_read_b64_tr_b16 v[120:121], v196 offset:10752\n"
"ds_read_b64_tr_b16 v[122:123], v197 offset:10752\n"
"v_mfma_f32_16x16x32_bf16 v[168:171], v[156:159], v[60:63], v[168:171]\n"
"s_waitcnt vmcnt(0) lgkmcnt(4)\n"
"s_barrier\n"
"s_add_u32 m0, s56, 0x8000\n"
"s_nop 0\n"
"global_load_lds_dwordx4 v194, s[32:33] sc1\n"
"global_load_lds_dwordx4 v194, s[32:33] offset:1024 sc1\n"
"global_load_lds_dwordx4 v194, s[32:33] offset:2048 sc1\n"
"global_load_lds_dwordx4 v194, s[32:33] offset:3072 sc1\n"
"s_add_u32 m0, s56, 0x10000\n"
"s_nop 0\n"
"global_load_lds_dwordx4 v202, s[34:35] sc1\n"
"global_load_lds_dwordx4 v202, s[34:35] offset:1024 sc1\n"
"global_load_lds_dwordx4 v202, s[34:35] offset:2048 sc1\n"
"global_load_lds_dwordx4 v202, s[34:35] offset:3072 sc1\n"
"v_mfma_f32_16x16x32_bf16 v[116:119], v[144:147], v[48:51], v[116:119]\n"
"ds_read_b64_tr_b16 v[152:153], v196 offset:11776\n"
"ds_read_b64_tr_b16 v[154:155], v197 offset:11776\n"
"v_mfma_f32_16x16x32_bf16 v[168:171], v[144:147], v[64:67], v[168:171]\n"
"v_mfma_f32_16x16x32_bf16 v[116:119], v[160:163], v[52:55], v[116:119]\n"
"ds_read_b64_tr_b16 v[144:145], v196 offset:12800\n"
"ds_read_b64_tr_b16 v[146:147], v197 offset:12800\n"
"v_mfma_f32_16x16x32_bf16 v[168:171], v[160:163], v[68:71], v[168:171]\n"
"v_mfma_f32_16x16x32_bf16 v[116:119], v[148:151], v[56:59], v[116:119]\n"
"ds_read_b64_tr_b16 v[156:157], v196 offset:13824\n"
"ds_read_b64_tr_b16 v[158:159], v197 offset:13824\n"
"v_mfma_f32_16x16x32_bf16 v[168:171], v[148:151], v[72:75], v[168:171]\n"
"s_waitcnt lgkmcnt(8)\n"
"v_mfma_f32_16x16x32_bf16 v[116:119], v[12:15], v[36:39], v[116:119]\n"
"ds_read_b64_tr_b16 v[148:149], v196 offset:14848\n"
"ds_read_b64_tr_b16 v[150:151], v197 offset:14848\n"
"v_mfma_f32_16x16x32_bf16 v[168:171], v[12:15], v[92:95], v[168:171]\n"
"v_mfma_f32_16x16x32_bf16 v[116:119], v[140:143], v[76:79], v[116:119]\n"
"ds_read_b64_tr_b16 v[12:13], v196 offset:15872\n"
"ds_read_b64_tr_b16 v[14:15], v197 offset:15872\n"
"v_mfma_f32_16x16x32_bf16 v[168:171], v[140:143], v[96:99], v[168:171]\n"
"s_waitcnt lgkmcnt(8)\n"
"v_mfma_f32_16x16x32_bf16 v[116:119], v[120:123], v[80:83], v[116:119]\n"
"ds_read_b64_tr_b16 v[140:141], v200 offset:16384\n"
"ds_read_b64_tr_b16 v[142:143], v201 offset:16384\n"
"v_mfma_f32_16x16x32_bf16 v[168:171], v[120:123], v[100:103], v[168:171]\n"
"v_mfma_f32_16x16x32_bf16 v[116:119], v[152:155], v[84:87], v[116:119]\n"
"ds_read_b64_tr_b16 v[120:121], v200 offset:17408\n"
"ds_read_b64_tr_b16 v[122:123], v201 offset:17408\n"
"v_mfma_f32_16x16x32_bf16 v[168:171], v[152:155], v[104:107], v[168:171]\n"
"s_waitcnt lgkmcnt(8)\n"
"v_mfma_f32_16x16x32_bf16 v[116:119], v[144:147], v[0:3], v[116:119]\n"
"ds_read_b64_tr_b16 v[152:153], v200 offset:18432\n"
"ds_read_b64_tr_b16 v[154:155], v201 offset:18432\n"
"v_mfma_f32_16x16x32_bf16 v[168:171], v[144:147], v[124:127], v[168:171]\n"
"v_mfma_f32_16x16x32_bf16 v[116:119], v[156:159], v[88:91], v[116:119]\n"
"ds_read_b64_tr_b16 v[144:145], v200 offset:19456\n"
"ds_read_b64_tr_b16 v[146:147], v201 offset:19456\n"
"v_mfma_f32_16x16x32_bf16 v[168:171], v[156:159], v[128:131], v[168:171]\n"
"global_load_dwordx2 v[172:173], v204, s[66:67] sc1\n"
"s_waitcnt lgkmcnt(8)\n"
"v_mfma_f32_16x16x32_bf16 v[116:119], v[148:151], v[108:111], v[116:119]\n"
"ds_read_b64_tr_b16 v[156:157], v200 offset:20480\n"
"ds_read_b64_tr_b16 v[158:159], v201 offset:20480\n"
"v_mfma_f32_16x16x32_bf16 v[168:171], v[148:151], v[132:135], v[168:171]\n"
"v_mfma_f32_16x16x32_bf16 v[116:119], v[12:15], v[112:115], v[116:119]\n"
"ds_read_b64_tr_b16 v[148:149], v200 offset:21504\n"
"ds_read_b64_tr_b16 v[150:151], v201 offset:21504\n"
"v_mfma_f32_16x16x32_bf16 v[168:171], v[12:15], v[136:139], v[168:171]\n"
"s_waitcnt lgkmcnt(8)\n"
"v_mfma_f32_16x16x32_bf16 v[12:15], v[140:143], v[4:7], 0\n"
"ds_read_b64_tr_b16 v[160:161], v200 offset:22528\n"
"ds_read_b64_tr_b16 v[162:163], v201 offset:22528\n"
"v_mfma_f32_16x16x32_bf16 v[164:167], v[140:143], v[24:27], 0\n"
"v_mfma_f32_16x16x32_bf16 v[12:15], v[120:123], v[8:11], v[12:15]\n"
"ds_read_b64_tr_b16 v[140:141], v200 offset:23552\n"
"ds_read_b64_tr_b16 v[142:143], v201 offset:23552\n"
"v_mfma_f32_16x16x32_bf16 v[164:167], v[120:123], v[28:31], v[164:167]\n"
"ds_write_b128 v195, v[116:119] offset:6208\n"
"s_waitcnt lgkmcnt(9)\n"
"v_mfma_f32_16x16x32_bf16 v[12:15], v[152:155], v[16:19], v[12:15]\n"
"ds_read_b64_tr_b16 v[116:117], v200 offset:24576\n"
"ds_read_b64_tr_b16 v[118:119], v201 offset:24576\n"
"v_mfma_f32_16x16x32_bf16 v[164:167], v[152:155], v[32:35], v[164:167]\n"
"ds_write_b128 v195, v[168:171] offset:7232\n"
"v_mfma_f32_16x16x32_bf16 v[12:15], v[144:147], v[20:23], v[12:15]\n"
"ds_read_b64_tr_b16 v[120:121], v200 offset:25600\n"
"ds_read_b64_tr_b16 v[122:123], v201 offset:25600\n"
"v_mfma_f32_16x16x32_bf16 v[164:167], v[144:147], v[40:43], v[164:167]\n"
"s_waitcnt lgkmcnt(10)\n"
"v_mfma_f32_16x16x32_bf16 v[12:15], v[156:159], v[44:47], v[12:15]\n"
"ds_read_b64_tr_b16 v[144:145], v200 offset:26624\n"
"ds_read_b64_tr_b16 v[146:147], v201 offset:26624\n"
"v_mfma_f32_16x16x32_bf16 v[164:167], v[156:159], v[60:63], v[164:167]\n"
"s_waitcnt vmcnt(1) lgkmcnt(4)\n"
"s_barrier\n"
"s_add_u32 m0, s56, 0\n"
"s_nop 0\n"
"global_load_lds_dwordx4 v194, s[34:35] sc1\n"
"global_load_lds_dwordx4 v194, s[34:35] offset:1024 sc1\n"
"global_load_lds_dwordx4 v194, s[34:35] offset:2048 sc1\n"
"global_load_lds_dwordx4 v194, s[34:35] offset:3072 sc1\n"
"s_add_u32 m0, s56, 0x4000\n"
"s_nop 0\n"
"global_load_lds_dwordx4 v202, s[36:37] sc1\n"
"global_load_lds_dwordx4 v202, s[36:37] offset:1024 sc1\n"
"global_load_lds_dwordx4 v202, s[36:37] offset:2048 sc1\n"
"global_load_lds_dwordx4 v202, s[36:37] offset:3072 sc1\n"
"s_waitcnt vmcnt(8)\n"
"v_xor_b32 v152, s11, v173\n"
"v_and_b32 v152, 0xffffdfff, v152\n"
"v_bitop3_b32 v152, v152, v172, s10 bitop3:0xf6\n"
"v_cmp_ne_u32 vcc, 0, v152\n"
"s_nop 0\n"
"s_cbranch_vccnz .Lr13\n"
".Lb13:\n"
"v_and_b32 v152, 0x2000, v173\n"
"v_xor_b32 v203, v203, v152\n"
"v_xor_b32 v152, 64, v203\n"
"s_add_u32 m0, s57, 0\n"
"s_nop 0\n"
"global_load_lds_dwordx4 v203, s[92:93] sc1\n"
"s_add_u32 m0, s57, 0x400\n"
"s_nop 0\n"
"global_load_lds_dwordx4 v152, s[94:95] sc1\n"
"s_add_u32 m0, s57, 0x800\n"
"s_nop 0\n"
"global_load_lds_dwordx4 v203, s[22:23] sc1\n"
"s_add_u32 m0, s57, 0xc00\n"
"s_nop 0\n"
"global_load_lds_dwordx4 v152, s[24:25] sc1\n"
"v_mfma_f32_16x16x32_bf16 v[12:15], v[148:151], v[48:51], v[12:15]\n"
"ds_read_b64_tr_b16 v[152:153], v200 offset:27648\n"
"ds_read_b64_tr_b16 v[154:155], v201 offset:27648\n"
"v_mfma_f32_16x16x32_bf16 v[164:167], v[148:151], v[64:67], v[164:167]\n"
"v_mfma_f32_16x16x32_bf16 v[12:15], v[160:163], v[52:55], v[12:15]\n"
"ds_read_b64_tr_b16 v[148:149], v200 offset:28672\n"
"ds_read_b64_tr_b16 v[150:151], v201 offset:28672\n"
"v_mfma_f32_16x16x32_bf16 v[164:167], v[160:163], v[68:71], v[164:167]\n"
"v_mfma_f32_16x16x32_bf16 v[12:15], v[140:143], v[56:59], v[12:15]\n"
"ds_read_b64_tr_b16 v[156:157], v200 offset:29696\n"
"ds_read_b64_tr_b16 v[158:159], v201 offset:29696\n"
"v_mfma_f32_16x16x32_bf16 v[164:167], v[140:143], v[72:75], v[164:167]\n"
"s_waitcnt lgkmcnt(8)\n"
"v_mfma_f32_16x16x32_bf16 v[12:15], v[116:119], v[36:39], v[12:15]\n"
"ds_read_b64_tr_b16 v[140:141], v200 offset:30720\n"
"ds_read_b64_tr_b16 v[142:143], v201 offset:30720\n"
"v_mfma_f32_16x16x32_bf16 v[164:167], v[116:119], v[92:95], v[164:167]\n"
"v_mfma_f32_16x16x32_bf16 v[12:15], v[120:123], v[76:79], v[12:15]\n"
"ds_read_b64_tr_b16 v[116:117], v200 offset:31744\n"
"ds_read_b64_tr_b16 v[118:119], v201 offset:31744\n"
"v_mfma_f32_16x16x32_bf16 v[164:167], v[120:123], v[96:99], v[164:167]\n"
"s_waitcnt lgkmcnt(8)\n"
"v_mfma_f32_16x16x32_bf16 v[12:15], v[144:147], v[80:83], v[12:15]\n"
"ds_read_b64_tr_b16 v[120:121], v200\n"
"ds_read_b64_tr_b16 v[122:123], v201\n"
"v_mfma_f32_16x16x32_bf16 v[164:167], v[144:147], v[100:103], v[164:167]\n"
"v_mfma_f32_16x16x32_bf16 v[12:15], v[152:155], v[84:87], v[12:15]\n"
"ds_read_b64_tr_b16 v[144:145], v200 offset:1024\n"
"ds_read_b64_tr_b16 v[146:147], v201 offset:1024\n"
"v_mfma_f32_16x16x32_bf16 v[164:167], v[152:155], v[104:107], v[164:167]\n"
"s_waitcnt lgkmcnt(8)\n"
"v_mfma_f32_16x16x32_bf16 v[12:15], v[148:151], v[0:3], v[12:15]\n"
"ds_read_b64_tr_b16 v[152:153], v200 offset:2048\n"
"ds_read_b64_tr_b16 v[154:155], v201 offset:2048\n"
"v_mfma_f32_16x16x32_bf16 v[164:167], v[148:151], v[124:127], v[164:167]\n"
"v_mfma_f32_16x16x32_bf16 v[12:15], v[156:159], v[88:91], v[12:15]\n"
"ds_read_b64_tr_b16 v[148:149], v200 offset:3072\n"
"ds_read_b64_tr_b16 v[150:151], v201 offset:3072\n"
"v_mfma_f32_16x16x32_bf16 v[164:167], v[156:159], v[128:131], v[164:167]\n"
"s_waitcnt lgkmcnt(8)\n"
"v_mfma_f32_16x16x32_bf16 v[12:15], v[140:143], v[108:111], v[12:15]\n"
"ds_read_b64_tr_b16 v[156:157], v200 offset:4096\n"
"ds_read_b64_tr_b16 v[158:159], v201 offset:4096\n"
"v_mfma_f32_16x16x32_bf16 v[164:167], v[140:143], v[132:135], v[164:167]\n"
"v_mfma_f32_16x16x32_bf16 v[12:15], v[116:119], v[112:115], v[12:15]\n"
"ds_read_b64_tr_b16 v[140:141], v200 offset:5120\n"
"ds_read_b64_tr_b16 v[142:143], v201 offset:5120\n"
"v_mfma_f32_16x16x32_bf16 v[164:167], v[116:119], v[136:139], v[164:167]\n"
"s_waitcnt lgkmcnt(8)\n"
"v_mfma_f32_16x16x32_bf16 v[116:119], v[120:123], v[4:7], 0\n"
"ds_read_b64_tr_b16 v[160:161], v200 offset:6144\n"
"ds_read_b64_tr_b16 v[162:163], v201 offset:6144\n"
"v_mfma_f32_16x16x32_bf16 v[168:171], v[120:123], v[24:27], 0\n"
"v_mfma_f32_16x16x32_bf16 v[116:119], v[144:147], v[8:11], v[116:119]\n"
"ds_read_b64_tr_b16 v[120:121], v200 offset:7168\n"
"ds_read_b64_tr_b16 v[122:123], v201 offset:7168\n"
"v_mfma_f32_16x16x32_bf16 v[168:171], v[144:147], v[28:31], v[168:171]\n"
"ds_write_b128 v195, v[12:15] offset:4160\n"
"s_waitcnt lgkmcnt(9)\n"
"v_mfma_f32_16x16x32_bf16 v[116:119], v[152:155], v[16:19], v[116:119]\n"
"ds_read_b64_tr_b16 v[12:13], v200 offset:8192\n"
"ds_read_b64_tr_b16 v[14:15], v201 offset:8192\n"
"v_mfma_f32_16x16x32_bf16 v[168:171], v[152:155], v[32:35], v[168:171]\n"
"ds_write_b128 v195, v[164:167] offset:5184\n"
"v_mfma_f32_16x16x32_bf16 v[116:119], v[148:151], v[20:23], v[116:119]\n"
"ds_read_b64_tr_b16 v[144:145], v200 offset:9216\n"
"ds_read_b64_tr_b16 v[146:147], v201 offset:9216\n"
"v_mfma_f32_16x16x32_bf16 v[168:171], v[148:151], v[40:43], v[168:171]\n"
"s_waitcnt lgkmcnt(10)\n"
"v_mfma_f32_16x16x32_bf16 v[116:119], v[156:159], v[44:47], v[116:119]\n"
"ds_read_b64_tr_b16 v[148:149], v200 offset:10240\n"
"ds_read_b64_tr_b16 v[150:151], v201 offset:10240\n"
"v_mfma_f32_16x16x32_bf16 v[168:171], v[156:159], v[60:63], v[168:171]\n"
"s_waitcnt vmcnt(0) lgkmcnt(4)\n"
"s_barrier\n"
"s_add_u32 m0, s56, 0xc000\n"
"s_nop 0\n"
"global_load_lds_dwordx4 v194, s[36:37] sc1\n"
"global_load_lds_dwordx4 v194, s[36:37] offset:1024 sc1\n"
"global_load_lds_dwordx4 v194, s[36:37] offset:2048 sc1\n"
"global_load_lds_dwordx4 v194, s[36:37] offset:3072 sc1\n"
"s_add_u32 m0, s56, 0x20200\n"
"s_nop 0\n"
"global_load_lds_dwordx4 v202, s[40:41] sc1\n"
"global_load_lds_dwordx4 v202, s[40:41] offset:1024 sc1\n"
"global_load_lds_dwordx4 v202, s[40:41] offset:2048 sc1\n"
"global_load_lds_dwordx4 v202, s[40:41] offset:3072 sc1\n"
"v_mfma_f32_16x16x32_bf16 v[116:119], v[140:143], v[48:51], v[116:119]\n"
"ds_read_b64_tr_b16 v[152:153], v200 offset:11264\n"
"ds_read_b64_tr_b16 v[154:155], v201 offset:11264\n"
"v_mfma_f32_16x16x32_bf16 v[168:171], v[140:143], v[64:67], v[168:171]\n"
"v_mfma_f32_16x16x32_bf16 v[116:119], v[160:163], v[52:55], v[116:119]\n"
"ds_read_b64_tr_b16 v[140:141], v200 offset:12288\n"
"ds_read_b64_tr_b16 v[142:143], v201 offset:12288\n"
"v_mfma_f32_16x16x32_bf16 v[168:171], v[160:163], v[68:71], v[168:171]\n"
"v_mfma_f32_16x16x32_bf16 v[116:119], v[120:123], v[56:59], v[116:119]\n"
"ds_read_b64_tr_b16 v[156:157], v200 offset:13312\n"
"ds_read_b64_tr_b16 v[158:159], v201 offset:13312\n"
"v_mfma_f32_16x16x32_bf16 v[168:171], v[120:123], v[72:75], v[168:171]\n"
"s_waitcnt lgkmcnt(8)\n"
"v_mfma_f32_16x16x32_bf16 v[116:119], v[12:15], v[36:39], v[116:119]\n"
"ds_read_b64_tr_b16 v[120:121], v200 offset:14336\n"
"ds_read_b64_tr_b16 v[122:123], v201 offset:14336\n"
"v_mfma_f32_16x16x32_bf16 v[168:171], v[12:15], v[92:95], v[168:171]\n"
"v_mfma_f32_16x16x32_bf16 v[116:119], v[144:147], v[76:79], v[116:119]\n"
"ds_read_b64_tr_b16 v[12:13], v200 offset:15360\n"
"ds_read_b64_tr_b16 v[14:15], v201 offset:15360\n"
"v_mfma_f32_16x16x32_bf16 v[168:171], v[144:147], v[96:99], v[168:171]\n"
"s_waitcnt lgkmcnt(8)\n"
"v_mfma_f32_16x16x32_bf16 v[116:119], v[148:151], v[80:83], v[116:119]\n"
"ds_read_b64_tr_b16 v[144:145], v198 offset:16384\n"
"ds_read_b64_tr_b16 v[146:147], v199 offset:16384\n"
"v_mfma_f32_16x16x32_bf16 v[168:171], v[148:151], v[100:103], v[168:171]\n"
"v_mfma_f32_16x16x32_bf16 v[116:119], v[152:155], v[84:87], v[116:119]\n"
"ds_read_b64_tr_b16 v[148:149], v198 offset:17408\n"
"ds_read_b64_tr_b16 v[150:151], v199 offset:17408\n"
"v_mfma_f32_16x16x32_bf16 v[168:171], v[152:155], v[104:107], v[168:171]\n"
"s_waitcnt lgkmcnt(8)\n"
"v_mfma_f32_16x16x32_bf16 v[116:119], v[140:143], v[0:3], v[116:119]\n"
"ds_read_b64_tr_b16 v[152:153], v198 offset:18432\n"
"ds_read_b64_tr_b16 v[154:155], v199 offset:18432\n"
"v_mfma_f32_16x16x32_bf16 v[168:171], v[140:143], v[124:127], v[168:171]\n"
"v_mfma_f32_16x16x32_bf16 v[116:119], v[156:159], v[88:91], v[116:119]\n"
"ds_read_b64_tr_b16 v[140:141], v198 offset:19456\n"
"ds_read_b64_tr_b16 v[142:143], v199 offset:19456\n"
"v_mfma_f32_16x16x32_bf16 v[168:171], v[156:159], v[128:131], v[168:171]\n"
"s_waitcnt lgkmcnt(8)\n"
"v_mfma_f32_16x16x32_bf16 v[116:119], v[120:123], v[108:111], v[116:119]\n"
"ds_read_b64_tr_b16 v[156:157], v198 offset:20480\n"
"ds_read_b64_tr_b16 v[158:159], v199 offset:20480\n"
"v_mfma_f32_16x16x32_bf16 v[168:171], v[120:123], v[132:135], v[168:171]\n"
"v_mfma_f32_16x16x32_bf16 v[116:119], v[12:15], v[112:115], v[116:119]\n"
"ds_read_b64_tr_b16 v[120:121], v198 offset:21504\n"
"ds_read_b64_tr_b16 v[122:123], v199 offset:21504\n"
"v_mfma_f32_16x16x32_bf16 v[168:171], v[12:15], v[136:139], v[168:171]\n"
"s_waitcnt lgkmcnt(8)\n"
"v_mfma_f32_16x16x32_bf16 v[12:15], v[144:147], v[4:7], 0\n"
"ds_read_b64_tr_b16 v[160:161], v198 offset:22528\n"
"ds_read_b64_tr_b16 v[162:163], v199 offset:22528\n"
"v_mfma_f32_16x16x32_bf16 v[164:167], v[144:147], v[24:27], 0\n"
"v_mfma_f32_16x16x32_bf16 v[12:15], v[148:151], v[8:11], v[12:15]\n"
"ds_read_b64_tr_b16 v[144:145], v198 offset:23552\n"
"ds_read_b64_tr_b16 v[146:147], v199 offset:23552\n"
"v_mfma_f32_16x16x32_bf16 v[164:167], v[148:151], v[28:31], v[164:167]\n"
"ds_write_b128 v195, v[116:119] offset:6208\n"
"s_waitcnt lgkmcnt(9)\n"
"v_mfma_f32_16x16x32_bf16 v[12:15], v[152:155], v[16:19], v[12:15]\n"
"ds_read_b64_tr_b16 v[116:117], v198 offset:24576\n"
"ds_read_b64_tr_b16 v[118:119], v199 offset:24576\n"
"v_mfma_f32_16x16x32_bf16 v[164:167], v[152:155], v[32:35], v[164:167]\n"
"ds_write_b128 v195, v[168:171] offset:7232\n"
"v_mfma_f32_16x16x32_bf16 v[12:15], v[140:143], v[20:23], v[12:15]\n"
"ds_read_b64_tr_b16 v[148:149], v198 offset:25600\n"
"ds_read_b64_tr_b16 v[150:151], v199 offset:25600\n"
"v_mfma_f32_16x16x32_bf16 v[164:167], v[140:143], v[40:43], v[164:167]\n"
"s_waitcnt lgkmcnt(10)\n"
"v_mfma_f32_16x16x32_bf16 v[12:15], v[156:159], v[44:47], v[12:15]\n"
"ds_read_b64_tr_b16 v[140:141], v198 offset:26624\n"
"ds_read_b64_tr_b16 v[142:143], v199 offset:26624\n"
"v_mfma_f32_16x16x32_bf16 v[164:167], v[156:159], v[60:63], v[164:167]\n"
"s_waitcnt vmcnt(0) lgkmcnt(4)\n"
"s_barrier\n"
"s_add_u32 m0, s56, 0x8000\n"
"s_nop 0\n"
"global_load_lds_dwordx4 v194, s[40:41] sc1\n"
"global_load_lds_dwordx4 v194, s[40:41] offset:1024 sc1\n"
"global_load_lds_dwordx4 v194, s[40:41] offset:2048 sc1\n"
"global_load_lds_dwordx4 v194, s[40:41] offset:3072 sc1\n"
"v_mfma_f32_16x16x32_bf16 v[12:15], v[120:123], v[48:51], v[12:15]\n"
"ds_read_b64_tr_b16 v[152:153], v198 offset:27648\n"
"ds_read_b64_tr_b16 v[154:155], v199 offset:27648\n"
"v_mfma_f32_16x16x32_bf16 v[164:167], v[120:123], v[64:67], v[164:167]\n"
"v_mfma_f32_16x16x32_bf16 v[12:15], v[160:163], v[52:55], v[12:15]\n"
"ds_read_b64_tr_b16 v[120:121], v198 offset:28672\n"
"ds_read_b64_tr_b16 v[122:123], v199 offset:28672\n"
"v_mfma_f32_16x16x32_bf16 v[164:167], v[160:163], v[68:71], v[164:167]\n"
"v_mfma_f32_16x16x32_bf16 v[12:15], v[144:147], v[56:59], v[12:15]\n"
"ds_read_b64_tr_b16 v[156:157], v198 offset:29696\n"
"ds_read_b64_tr_b16 v[158:159], v199 offset:29696\n"
"v_mfma_f32_16x16x32_bf16 v[164:167], v[144:147], v[72:75], v[164:167]\n"
"s_waitcnt lgkmcnt(8)\n"
"v_mfma_f32_16x16x32_bf16 v[12:15], v[116:119], v[36:39], v[12:15]\n"
"ds_read_b64_tr_b16 v[144:145], v198 offset:30720\n"
"ds_read_b64_tr_b16 v[146:147], v199 offset:30720\n"
"v_mfma_f32_16x16x32_bf16 v[164:167], v[116:119], v[92:95], v[164:167]\n"
"v_mfma_f32_16x16x32_bf16 v[12:15], v[148:151], v[76:79], v[12:15]\n"
"ds_read_b64_tr_b16 v[116:117], v198 offset:31744\n"
"ds_read_b64_tr_b16 v[118:119], v199 offset:31744\n"
"v_mfma_f32_16x16x32_bf16 v[164:167], v[148:151], v[96:99], v[164:167]\n"
"s_waitcnt lgkmcnt(8)\n"
"v_mfma_f32_16x16x32_bf16 v[12:15], v[140:143], v[80:83], v[12:15]\n"
"ds_read_b64_tr_b16 v[148:149], v196 offset:512\n"
"ds_read_b64_tr_b16 v[150:151], v197 offset:512\n"
"v_mfma_f32_16x16x32_bf16 v[164:167], v[140:143], v[100:103], v[164:167]\n"
"v_mfma_f32_16x16x32_bf16 v[12:15], v[152:155], v[84:87], v[12:15]\n"
"ds_read_b64_tr_b16 v[140:141], v196 offset:1536\n"
"ds_read_b64_tr_b16 v[142:143], v197 offset:1536\n"
"v_mfma_f32_16x16x32_bf16 v[164:167], v[152:155], v[104:107], v[164:167]\n"
"s_waitcnt lgkmcnt(8)\n"
"v_mfma_f32_16x16x32_bf16 v[12:15], v[120:123], v[0:3], v[12:15]\n"
"ds_read_b64_tr_b16 v[152:153], v196 offset:2560\n"
"ds_read_b64_tr_b16 v[154:155], v197 offset:2560\n"
"v_mfma_f32_16x16x32_bf16 v[164:167], v[120:123], v[124:127], v[164:167]\n"
"v_mfma_f32_16x16x32_bf16 v[12:15], v[156:159], v[88:91], v[12:15]\n"
"ds_read_b64_tr_b16 v[120:121], v196 offset:3584\n"
"ds_read_b64_tr_b16 v[122:123], v197 offset:3584\n"
"v_mfma_f32_16x16x32_bf16 v[164:167], v[156:159], v[128:131], v[164:167]\n"
"s_waitcnt lgkmcnt(8)\n"
"v_mfma_f32_16x16x32_bf16 v[12:15], v[144:147], v[108:111], v[12:15]\n"
"ds_read_b64_tr_b16 v[156:157], v196 offset:4608\n"
"ds_read_b64_tr_b16 v[158:159], v197 offset:4608\n"
"v_mfma_f32_16x16x32_bf16 v[164:167], v[144:147], v[132:135], v[164:167]\n"
"v_mfma_f32_16x16x32_bf16 v[12:15], v[116:119], v[112:115], v[12:15]\n"
"ds_read_b64_tr_b16 v[144:145], v196 offset:5632\n"
"ds_read_b64_tr_b16 v[146:147], v197 offset:5632\n"
"v_mfma_f32_16x16x32_bf16 v[164:167], v[116:119], v[136:139], v[164:167]\n"
"s_waitcnt lgkmcnt(8)\n"
"v_mfma_f32_16x16x32_bf16 v[116:119], v[148:151], v[4:7], 0\n"
"ds_read_b64_tr_b16 v[4:5], v196 offset:6656\n"
"ds_read_b64_tr_b16 v[6:7], v197 offset:6656\n"
"v_mfma_f32_16x16x32_bf16 v[160:163], v[148:151], v[24:27], 0\n"
"v_mfma_f32_16x16x32_bf16 v[116:119], v[140:143], v[8:11], v[116:119]\n"
"ds_read_b64_tr_b16 v[8:9], v196 offset:7680\n"
"ds_read_b64_tr_b16 v[10:11], v197 offset:7680\n"
"v_mfma_f32_16x16x32_bf16 v[160:163], v[140:143], v[28:31], v[160:163]\n"
"ds_write_b128 v195, v[12:15] offset:4160\n"
"s_waitcnt lgkmcnt(9)\n"
"v_mfma_f32_16x16x32_bf16 v[116:119], v[152:155], v[16:19], v[116:119]\n"
"ds_read_b64_tr_b16 v[12:13], v196 offset:8704\n"
"ds_read_b64_tr_b16 v[14:15], v197 offset:8704\n"
"v_mfma_f32_16x16x32_bf16 v[160:163], v[152:155], v[32:35], v[160:163]\n"
"ds_write_b128 v195, v[164:167] offset:5184\n"
"v_mfma_f32_16x16x32_bf16 v[116:119], v[120:123], v[20:23], v[116:119]\n"
"ds_read_b64_tr_b16 v[16:17], v196 offset:9728\n"
"ds_read_b64_tr_b16 v[18:19], v197 offset:9728\n"
"v_mfma_f32_16x16x32_bf16 v[160:163], v[120:123], v[40:43], v[160:163]\n"
"s_waitcnt lgkmcnt(10)\n"
"v_mfma_f32_16x16x32_bf16 v[116:119], v[156:159], v[44:47], v[116:119]\n"
"ds_read_b64_tr_b16 v[20:21], v196 offset:10752\n"
"ds_read_b64_tr_b16 v[22:23], v197 offset:10752\n"
"v_mfma_f32_16x16x32_bf16 v[160:163], v[156:159], v[60:63], v[160:163]\n"
"s_waitcnt vmcnt(0) lgkmcnt(4)\n"
"s_barrier\n"
"v_mfma_f32_16x16x32_bf16 v[116:119], v[144:147], v[48:51], v[116:119]\n"
"ds_read_b64_tr_b16 v[24:25], v196 offset:11776\n"
"ds_read_b64_tr_b16 v[26:27], v197 offset:11776\n"
"v_mfma_f32_16x16x32_bf16 v[160:163], v[144:147], v[64:67], v[160:163]\n"
"v_mfma_f32_16x16x32_bf16 v[116:119], v[4:7], v[52:55], v[116:119]\n"
"ds_read_b64_tr_b16 v[28:29], v196 offset:12800\n"
"ds_read_b64_tr_b16 v[30:31], v197 offset:12800\n"
"v_mfma_f32_16x16x32_bf16 v[160:163], v[4:7], v[68:71], v[160:163]\n"
"v_mfma_f32_16x16x32_bf16 v[116:119], v[8:11], v[56:59], v[116:119]\n"
"ds_read_b64_tr_b16 v[4:5], v196 offset:13824\n"
"ds_read_b64_tr_b16 v[6:7], v197 offset:13824\n"
"v_mfma_f32_16x16x32_bf16 v[160:163], v[8:11], v[72:75], v[160:163]\n"
"s_waitcnt lgkmcnt(8)\n"
"v_mfma_f32_16x16x32_bf16 v[116:119], v[12:15], v[36:39], v[116:119]\n"
"ds_read_b64_tr_b16 v[8:9], v196 offset:14848\n"
"ds_read_b64_tr_b16 v[10:11], v197 offset:14848\n"
"v_mfma_f32_16x16x32_bf16 v[160:163], v[12:15], v[92:95], v[160:163]\n"
"v_mfma_f32_16x16x32_bf16 v[116:119], v[16:19], v[76:79], v[116:119]\n"
"ds_read_b64_tr_b16 v[12:13], v196 offset:15872\n"
"ds_read_b64_tr_b16 v[14:15], v197 offset:15872\n"
"v_mfma_f32_16x16x32_bf16 v[160:163], v[16:19], v[96:99], v[160:163]\n"
"s_waitcnt lgkmcnt(8)\n"
"v_mfma_f32_16x16x32_bf16 v[116:119], v[20:23], v[80:83], v[116:119]\n"
"v_mfma_f32_16x16x32_bf16 v[160:163], v[20:23], v[100:103], v[160:163]\n"
"v_mfma_f32_16x16x32_bf16 v[116:119], v[24:27], v[84:87], v[116:119]\n"
"v_mfma_f32_16x16x32_bf16 v[160:163], v[24:27], v[104:107], v[160:163]\n"
"s_waitcnt lgkmcnt(4)\n"
"v_mfma_f32_16x16x32_bf16 v[116:119], v[28:31], v[0:3], v[116:119]\n"
"v_mfma_f32_16x16x32_bf16 v[160:163], v[28:31], v[124:127], v[160:163]\n"
"v_mfma_f32_16x16x32_bf16 v[116:119], v[4:7], v[88:91], v[116:119]\n"
"v_mfma_f32_16x16x32_bf16 v[160:163], v[4:7], v[128:131], v[160:163]\n"
"s_waitcnt lgkmcnt(0)\n"
"v_mfma_f32_16x16x32_bf16 v[116:119], v[8:11], v[108:111], v[116:119]\n"
"v_mfma_f32_16x16x32_bf16 v[160:163], v[8:11], v[132:135], v[160:163]\n"
"v_mfma_f32_16x16x32_bf16 v[116:119], v[12:15], v[112:115], v[116:119]\n"
"v_mfma_f32_16x16x32_bf16 v[160:163], v[12:15], v[136:139], v[160:163]\n"
"s_nop 6\n"
"ds_write_b128 v195, v[116:119] offset:6208\n"
"ds_write_b128 v195, v[160:163] offset:7232\n"
"s_waitcnt lgkmcnt(0)\n"
"s_barrier\n"
"s_endpgm\n"
".Lr8:\n"
"s_mov_b32 s12, 0x40000\n"
".Ll8:\n"
"s_sleep 8\n"
"s_nop 7\n"
"global_load_dwordx4 v[128:131], v194, s[26:27] sc1\n"
"global_load_dwordx4 v[132:135], v194, s[26:27] offset:1024 sc1\n"
"s_waitcnt vmcnt(0)\n"
"v_xor_b32 v153, s81, v129\n"
"v_bitop3_b32 v153, v153, v131, s81 bitop3:0xf6\n"
"v_bitop3_b32 v153, v153, v133, s81 bitop3:0xf6\n"
"v_bitop3_b32 v153, v153, v135, s81 bitop3:0xf6\n"
"v_lshrrev_b32 v138, 16, v153\n"
"v_bitop3_b32 v138, v138, v128, s10 bitop3:0xf6\n"
"v_bitop3_b32 v138, v138, v130, s10 bitop3:0xf6\n"
"v_bitop3_b32 v138, v138, v132, s10 bitop3:0xf6\n"
"v_bitop3_b32 v138, v138, v134, s10 bitop3:0xf6\n"
"v_cmp_ne_u32 vcc, 0, v138\n"
"s_nop 0\n"
"s_cbranch_vccz .Lp8\n"
"s_sub_u32 s12, s12, 1\n"
"s_cbranch_scc0 .Ll8\n"
".Lp8:\n"
"s_nop 7\n"
"s_nop 7\n"
"s_branch .Lb8\n"
".Lr9:\n"
"s_mov_b32 s12, 0x40000\n"
".Ll9:\n"
"s_sleep 3\n"
"s_nop 7\n"
"buffer_load_dwordx2 v[54:55], v214, s[76:79], 0 offen offset:3584 sc1\n"
"buffer_load_dwordx2 v[52:53], v214, s[76:79], 0 offen offset:3072 sc1\n"
"buffer_load_dwordx2 v[50:51], v214, s[76:79], 0 offen offset:2560 sc1\n"
"buffer_load_dwordx2 v[48:49], v214, s[76:79], 0 offen offset:2048 sc1\n"
"s_waitcnt vmcnt(0)\n"
"v_xor_b32 v54, v54, v223\n"
"v_xor_b32 v55, v55, v223\n"
"v_xor_b32 v52, v52, v222\n"
"v_xor_b32 v53, v53, v222\n"
"v_xor_b32 v50, v50, v221\n"
"v_xor_b32 v51, v51, v221\n"
"v_xor_b32 v48, v48, v220\n"
"v_xor_b32 v49, v49, v220\n"
"v_or3_b32 v44, v54, v55, v52\n"
"v_or3_b32 v44, v44, v53, v50\n"
"v_or3_b32 v44, v44, v51, v48\n"
"v_bitop3_b32 v44, v44, v49, s96 bitop3:0xa8\n"
"v_cmp_ne_u32 vcc, 0, v44\n"
"s_nop 0\n"
"s_cbranch_vccz .Lp9\n"
"s_sub_u32 s12, s12, 1\n"
"s_cbranch_scc0 .Ll9\n"
".Lp9:\n"
"s_nop 7\n"
"s_nop 7\n"
"s_branch .Lb9\n"
".Lr10:\n"
"s_mov_b32 s12, 0x40000\n"
".Ll10:\n"
"s_sleep 3\n"
"s_nop 7\n"
"buffer_load_dwordx2 v[66:67], v214, s[76:79], 0 offen offset:1536 sc1\n"
"buffer_load_dwordx2 v[64:65], v214, s[76:79], 0 offen offset:1024 sc1\n"
"buffer_load_dwordx2 v[62:63], v214, s[76:79], 0 offen offset:512 sc1\n"
"buffer_load_dwordx2 v[60:61], v214, s[76:79], 0 offen offset:0 sc1\n"
"s_waitcnt vmcnt(0)\n"
"v_xor_b32 v66, v66, v213\n"
"v_xor_b32 v67, v67, v213\n"
"v_xor_b32 v64, v64, v213\n"
"v_xor_b32 v65, v65, v213\n"
"v_xor_b32 v62, v62, v213\n"
"v_xor_b32 v63, v63, v213\n"
"v_xor_b32 v60, v60, v213\n"
"v_xor_b32 v61, v61, v213\n"
"v_or3_b32 v44, v66, v67, v64\n"
"v_or3_b32 v44, v44, v65, v62\n"
"v_or3_b32 v44, v44, v63, v60\n"
"v_bitop3_b32 v44, v44, v61, s96 bitop3:0xa8\n"
"v_cmp_ne_u32 vcc, 0, v44\n"
"s_nop 0\n"
"s_cbranch_vccz .Lp10\n"
"s_sub_u32 s12, s12, 1\n"
"s_cbranch_scc0 .Ll10\n"
".Lp10:\n"
"s_nop 7\n"
"s_nop 7\n"
"s_branch .Lb10\n"
".Lr11:\n"
"s_mov_b32 s12, 0x40000\n"
".Ll11:\n"
"s_sleep 2\n"
"s_nop 7\n"
"global_load_dwordx2 v[108:109], v205, s[90:91] sc1\n"
"s_waitcnt vmcnt(0)\n"
"v_cmp_ne_u64 vcc, s[10:11], v[108:109]\n"
"s_nop 0\n"
"s_cbranch_vccz .Lp11\n"
"s_sub_u32 s12, s12, 1\n"
"s_cbranch_scc0 .Ll11\n"
".Lp11:\n"
"s_nop 7\n"
"s_nop 7\n"
"s_branch .Lb11\n"
".Lr12:\n"
"s_mov_b32 s12, 0x40000\n"
".Ll12:\n"
"s_sleep 2\n"
"s_nop 7\n"
"global_load_dwordx2 v[172:173], v205, s[90:91] sc1\n"
"s_waitcnt vmcnt(0)\n"
"v_cmp_ne_u64 vcc, s[10:11], v[172:173]\n"
"s_nop 0\n"
"s_cbranch_vccz .Lp12\n"
"s_sub_u32 s12, s12, 1\n"
"s_cbranch_scc0 .Ll12\n"
".Lp12:\n"
"s_nop 7\n"
"s_nop 7\n"
"s_branch .Lb12\n"
".Lr13:\n"
"s_mov_b32 s12, 0x40000\n"
".Ll13:\n"
"s_sleep 6\n"
"s_nop 7\n"
"global_load_dwordx2 v[172:173], v204, s[66:67] sc1\n"
"s_waitcnt vmcnt(0)\n"
"v_xor_b32 v152, s11, v173\n"
"v_and_b32 v152, 0xffffdfff, v152\n"
"v_bitop3_b32 v152, v152, v172, s10 bitop3:0xf6\n"
"v_cmp_ne_u32 vcc, 0, v152\n"
"s_nop 0\n"
"s_cbranch_vccz .Lp13\n"
"s_sub_u32 s12, s12, 1\n"
"s_cbranch_scc0 .Ll13\n"
".Lp13:\n"
"s_nop 7\n"
"s_nop 7\n"
"s_branch .Lb13\n"
".LC:\n"
        :: "{s[0:1]}"(__builtin_amdgcn_kernarg_segment_ptr()), "{s2}"(b), "{s3}"(wid)
        : "memory", "scc");
    unsigned dlat;
    {
        typedef const __attribute__((address_space(1))) u16* gp;
        const __attribute__((address_space(4))) gp* ka = (const __attribute__((address_space(4))) gp*)__builtin_amdgcn_kernarg_segment_ptr();
        asm volatile("" : "+s"(ka));
        const gp lre = ka[14], lim = ka[15], logdt = ka[16];
        const int q = lane >> 4, p = ((b & 1) * 8 + (b >> 5)) * 64 + wid * 16 + (lane & 15);
        unsigned ldt, lr, li;
        {
            const u64 wa = (((u64)ka[7] + 0x1fffffull) & ~0x1fffffull) + (54525952 + (unsigned)((b >> 4) * 16384 + ((b & 15) * 4 + wid) * 128 + (wid & 1) * 8192));
            const u64 wb = wa ^ 8192ull;
            u64 t0, t1, t2; unsigned junk;
            asm volatile("global_load_ushort %0, %7, %8\n\tglobal_load_ushort %1, %7, %9\n\tglobal_load_ushort %2, %7, %10\n\t"
                         "s_memtime %3\n\ts_waitcnt lgkmcnt(0)\n\ts_load_dword %6, %11, 0x0\n\ts_waitcnt lgkmcnt(0)\n\ts_memtime %4\n\ts_waitcnt lgkmcnt(0)\n\t"
                         "s_load_dword %6, %12, 0x0\n\ts_waitcnt lgkmcnt(0)\n\ts_memtime %5\n\ts_waitcnt vmcnt(0) lgkmcnt(0)"
                         : "=&v"(ldt), "=&v"(lr), "=&v"(li), "=&s"(t0), "=&s"(t1), "=&s"(t2), "=&s"(junk)
                         : "v"(p * 2), "s"(logdt), "s"(lre), "s"(lim), "s"(wa), "s"(wb) : "memory");
            const int d = 2 * (int)(unsigned)t1 - (int)(unsigned)t0 - (int)(unsigned)t2;
            dlat = (unsigned)min(max((wid & 1) ? -d : d, -2000), 2000) & 0xffffu;
        }
        const float dt = rbf(expf(bf2f(ldt)));
        const float re = bf2f(lr), im = bf2f(li);
        const float ea = expf(dt * re);
        float sn, cs;
        sincosf(dt * im, &sn, &cs);
        const cf L1 = {ea * cs, ea * sn}, one = {1.f, 0.f};
        const float nr = L1.r - 1.0f, ni = L1.i, den = re * re + im * im;
        const cf cc = {(nr * re + ni * im) / den, (ni * re - nr * im) / den}, g = cmul(cc, L1);
        cf w = L1;
        for (int i = 0; i < 4; ++i) w = cmul(w, w);
        const cf w16 = w, w32 = cmul(w, w), w64 = cmul(w32, w32), w128 = cmul(w64, w64), w256 = cmul(w128, w128);
        const cf wq = cmul((q & 1) ? w16 : one, (q & 2) ? w32 : one), qq = cmul((q & 1) ? w64 : one, (q & 2) ? w128 : one);
        const cf al = q == 3 ? w16 : cf{q == 2 ? 1.f : 0.f, 0.f};
        lds_f32x4* tp = (lds_f32x4*)(size_t)(unsigned)(149888 + wid * 2816 + lane * 16);
        tp[0] = f32x4{al.r, al.i, (float)(q & 1), 0.f};
        tp[64] = f32x4{wq.r, wq.i, qq.r, qq.i};
        if (q < 3) tp[128] = q == 0 ? f32x4{L1.r, L1.i, cc.r, cc.i} : q == 1 ? f32x4{w16.r, w16.i, w32.r, w32.i} : f32x4{g.r, g.i, w256.r, w256.i};
    }
    asm volatile(
"s_load_dwordx16 s[16:31], %0, 0\n"
"s_load_dwordx16 s[32:47], %0, 64\n"
"s_and_b32 s48, %1, 1\n"
"s_bfe_u32 s49, %1, 0x20001\n"
"s_bfe_u32 s13, %1, 0x20003\n"
"s_lshr_b32 s50, %1, 5\n"
"s_lshl2_add_u32 s51, s49, s13\n"
"s_lshl3_add_u32 s52, s48, s50\n"
"s_lshl1_add_u32 s82, s48, s49\n"
"s_lshl_b32 s82, s82, 9\n"
"s_and_b32 s59, s50, 1\n"
"s_lshr_b32 s53, %1, 3\n"
"s_and_b32 s54, %1, 7\n"
"s_and_b32 s15, %2, 3\n"
"s_lshl_b32 s55, s15, 11\n"
"s_lshl_b32 s56, s15, 12\n"
"s_mul_i32 s57, s15, 0x2080\n"
"s_add_u32 s57, s57, 0x18000\n"
"s_lshl_b32 s58, s54, 5\n"
"s_add_u32 s58, s58, s53\n"
"s_lshl4_add_u32 s80, s51, s15\n"
"s_lshl_b32 s80, s80, 13\n"
"v_mbcnt_lo_u32_b32 v228, -1, 0\n"
"v_mbcnt_hi_u32_b32 v228, -1, v228\n"
"v_bfe_u32 v0, v228, 1, 3\n"
"v_lshrrev_b32 v218, 4, v228\n"
"v_xor_b32 v0, v0, v218\n"
"v_and_b32 v218, 15, v228\n"
"v_lshlrev_b32 v218, 7, v218\n"
"v_lshl_or_b32 v218, v0, 4, v218\n"
"s_add_u32 s13, s56, 0x2000\n"
"v_add_u32 v182, s13, v218\n"
"v_xor_b32 v181, 64, v182\n"
"v_xor_b32 v219, 64, v218\n"
"v_add_u32 v180, 0x10000, v182\n"
"v_add_u32 v188, 0x10000, v181\n"
"v_add_u32 v235, 0x10000, v218\n"
"v_add_u32 v234, 0x10000, v219\n"
"v_lshrrev_b32 v52, 3, v228\n"
"v_lshlrev_b32 v52, 5, v52\n"
"s_lshl2_add_u32 s14, s54, s15\n"
"s_lshl_b32 s13, s14, 8\n"
"s_waitcnt lgkmcnt(0)\n"
"s_add_u32 s60, s40, s13\n"
"s_addc_u32 s61, s41, 0\n"
"global_load_dwordx4 v[0:3], v52, s[60:61]\n"
"global_load_dwordx4 v[4:7], v52, s[60:61] offset:16\n"
"s_add_u32 s30, s30, 0x1fffff\n"
"s_addc_u32 s31, s31, 0\n"
"s_and_b32 s30, s30, 0xffe00000\n"
"s_load_dwordx2 s[10:11], s[30:31], 0\n"
"s_lshl_b32 s14, s14, 17\n"
"s_lshl_b32 s13, s53, 7\n"
"s_sub_u32 s14, s14, s13\n"
"s_add_u32 s14, s14, 0x2400f80\n"
"s_add_u32 s60, s30, s14\n"
"s_addc_u32 s61, s31, 0\n"
"s_waitcnt lgkmcnt(0)\n"
"s_add_u32 s10, s10, 1\n"
"s_addc_u32 s11, s11, 0\n"
"s_barrier\n"
"ds_read_b128 v[8:11], v182\n"
"ds_read_b128 v[12:15], v182 offset:2048\n"
"ds_read_b128 v[16:19], v218\n"
"ds_read_b128 v[20:23], v218 offset:2048\n"
"ds_read_b128 v[24:27], v218 offset:4096\n"
"ds_read_b128 v[28:31], v218 offset:6144\n"
"s_waitcnt lgkmcnt(3)\n"
"v_mfma_f32_16x16x32_bf16 v[32:35], v[16:19], v[8:11], 0\n"
"ds_read_b128 v[36:39], v181\n"
"v_and_b32 v93, 7, v228\n"
"v_mfma_f32_16x16x32_bf16 v[40:43], v[16:19], v[12:15], 0\n"
"ds_read_b128 v[16:19], v181 offset:2048\n"
"v_sub_u32 v94, 7, v93\n"
"s_waitcnt lgkmcnt(4)\n"
"v_mfma_f32_16x16x32_bf16 v[44:47], v[20:23], v[8:11], 0\n"
"ds_read_b128 v[48:51], v219\n"
"v_lshlrev_b32 v178, 9, v52\n"
"v_mfma_f32_16x16x32_bf16 v[52:55], v[20:23], v[12:15], 0\n"
"ds_read_b128 v[20:23], v219 offset:2048\n"
"v_lshl_or_b32 v178, v93, 4, v178\n"
"s_waitcnt lgkmcnt(5)\n"
"v_mfma_f32_16x16x32_bf16 v[56:59], v[24:27], v[8:11], 0\n"
"ds_read_b128 v[60:63], v219 offset:4096\n"
"v_lshrrev_b32 v84, 4, v228\n"
"v_mfma_f32_16x16x32_bf16 v[64:67], v[24:27], v[12:15], 0\n"
"ds_read_b128 v[24:27], v219 offset:6144\n"
"s_lshl_b32 s13, s15, 2\n"
"v_add_u32 v84, s13, v84\n"
"s_waitcnt lgkmcnt(6)\n"
"v_mfma_f32_16x16x32_bf16 v[68:71], v[28:31], v[8:11], 0\n"
"v_lshlrev_b32 v84, 4, v84\n"
"v_mfma_f32_16x16x32_bf16 v[72:75], v[28:31], v[12:15], 0\n"
"v_and_or_b32 v84, v228, 8, v84\n"
"s_waitcnt lgkmcnt(0)\n"
"s_barrier\n"
"v_mfma_f32_16x16x32_bf16 v[32:35], v[48:51], v[36:39], v[32:35]\n"
"ds_read_b128 v[28:31], v182 offset:24576\n"
"v_lshlrev_b32 v93, 5, v94\n"
"v_mfma_f32_16x16x32_bf16 v[40:43], v[48:51], v[16:19], v[40:43]\n"
"ds_read_b128 v[48:51], v182 offset:26624\n"
"v_xor_b32 v100, v84, v93\n"
"v_mfma_f32_16x16x32_bf16 v[44:47], v[20:23], v[36:39], v[44:47]\n"
"ds_read_b128 v[76:79], v218 offset:24576\n"
"v_lshl_add_u32 v100, v94, 11, v100\n"
"v_mfma_f32_16x16x32_bf16 v[52:55], v[20:23], v[16:19], v[52:55]\n"
"ds_read_b128 v[20:23], v218 offset:26624\n"
"v_add_u32 v100, 0x18000, v100\n"
"v_mfma_f32_16x16x32_bf16 v[56:59], v[60:63], v[36:39], v[56:59]\n"
"ds_read_b128 v[80:83], v218 offset:28672\n"
"v_add_u32 v93, 0xe0, v93\n"
"v_mfma_f32_16x16x32_bf16 v[64:67], v[60:63], v[16:19], v[64:67]\n"
"ds_read_b128 v[60:63], v218 offset:30720\n"
"v_mfma_f32_16x16x32_bf16 v[68:71], v[24:27], v[36:39], v[68:71]\n"
"v_and_b32 v93, 0xe0, v93\n"
"v_mfma_f32_16x16x32_bf16 v[72:75], v[24:27], v[16:19], v[72:75]\n"
"v_xor_b32 v92, v84, v93\n"
"s_waitcnt lgkmcnt(3)\n"
"v_mfma_f32_16x16x32_bf16 v[32:35], v[76:79], v[28:31], v[32:35]\n"
"ds_read_b128 v[24:27], v181 offset:24576\n"
"v_lshlrev_b32 v93, 11, v94\n"
"v_mfma_f32_16x16x32_bf16 v[40:43], v[76:79], v[48:51], v[40:43]\n"
"ds_read_b128 v[76:79], v181 offset:26624\n"
"v_cmp_eq_u32 vcc, 0, v94\n"
"s_waitcnt lgkmcnt(4)\n"
"v_mfma_f32_16x16x32_bf16 v[44:47], v[20:23], v[28:31], v[44:47]\n"
"ds_read_b128 v[84:87], v219 offset:24576\n"
"v_add_u32 v93, 0x17d00, v93\n"
"v_mfma_f32_16x16x32_bf16 v[52:55], v[20:23], v[48:51], v[52:55]\n"
"ds_read_b128 v[20:23], v219 offset:26624\n"
"v_mov_b32 v95, 0x20200\n"
"s_waitcnt lgkmcnt(5)\n"
"v_mfma_f32_16x16x32_bf16 v[56:59], v[80:83], v[28:31], v[56:59]\n"
"ds_read_b128 v[88:91], v219 offset:28672\n"
"v_cndmask_b32 v93, v93, v95, vcc\n"
"v_mfma_f32_16x16x32_bf16 v[64:67], v[80:83], v[48:51], v[64:67]\n"
"ds_read_b128 v[80:83], v219 offset:30720\n"
"v_or_b32 v95, s53, v94\n"
"s_waitcnt lgkmcnt(6)\n"
"v_mfma_f32_16x16x32_bf16 v[68:71], v[60:63], v[28:31], v[68:71]\n"
"v_cmp_ne_u32 vcc, 0, v95\n"
"v_mfma_f32_16x16x32_bf16 v[72:75], v[60:63], v[48:51], v[72:75]\n"
"v_add_u32 v92, v92, v93\n"
"s_waitcnt lgkmcnt(0)\n"
"s_barrier\n"
"v_mfma_f32_16x16x32_bf16 v[32:35], v[84:87], v[24:27], v[32:35]\n"
"ds_read_b128 v[28:31], v182 offset:49152\n"
"v_lshlrev_b32 v226, 4, v228\n"
"v_mfma_f32_16x16x32_bf16 v[40:43], v[84:87], v[76:79], v[40:43]\n"
"ds_read_b128 v[48:51], v182 offset:51200\n"
"v_cndmask_b32_e64 v108, 0, -1, vcc\n"
"v_mfma_f32_16x16x32_bf16 v[44:47], v[20:23], v[24:27], v[44:47]\n"
"ds_read_b128 v[60:63], v218 offset:49152\n"
"v_mfma_f32_16x16x32_bf16 v[52:55], v[20:23], v[76:79], v[52:55]\n"
"ds_read_b128 v[20:23], v218 offset:51200\n"
"v_mov_b32 v224, s10\n"
"v_mfma_f32_16x16x32_bf16 v[56:59], v[88:91], v[24:27], v[56:59]\n"
"ds_read_b128 v[84:87], v218 offset:53248\n"
"v_mov_b32 v225, s11\n"
"v_mfma_f32_16x16x32_bf16 v[64:67], v[88:91], v[76:79], v[64:67]\n"
"ds_read_b128 v[88:91], v218 offset:55296\n"
"s_lshl2_add_u32 s13, s58, s15\n"
"s_lshl3_add_u32 s14, s13, 0x1000\n"
"v_mov_b32 v179, s14\n"
"s_lshl_b32 s13, s13, 1\n"
"v_mov_b32 v223, s13\n"
"v_lshlrev_b32 v183, 11, v223\n"
"global_load_ushort v223, v223, s[42:43]\n"
"global_load_ushort v183, v183, s[28:29]\n"
"v_mfma_f32_16x16x32_bf16 v[68:71], v[80:83], v[24:27], v[68:71]\n"
"v_lshrrev_b32 v93, 4, v228\n"
"v_mfma_f32_16x16x32_bf16 v[72:75], v[80:83], v[76:79], v[72:75]\n"
"v_and_b32 v217, 0xf0, v226\n"
"s_waitcnt lgkmcnt(3)\n"
"v_mfma_f32_16x16x32_bf16 v[32:35], v[60:63], v[28:31], v[32:35]\n"
"ds_read_b128 v[24:27], v181 offset:49152\n"
"v_lshrrev_b32 v227, 1, v217\n"
"v_mfma_f32_16x16x32_bf16 v[40:43], v[60:63], v[48:51], v[40:43]\n"
"ds_read_b128 v[60:63], v181 offset:51200\n"
"s_mul_i32 s13, s15, 0xb00\n"
"s_add_u32 s13, s13, 0x24980\n"
"v_add_u32 v221, s13, v226\n"
"s_add_u32 s13, s13, 0x800\n"
"v_add_u32 v222, s13, v217\n"
"s_waitcnt lgkmcnt(4)\n"
"v_mfma_f32_16x16x32_bf16 v[44:47], v[20:23], v[28:31], v[44:47]\n"
"ds_read_b128 v[76:79], v219 offset:49152\n"
"v_lshl_or_b32 v217, v93, 16, v217\n"
"v_mfma_f32_16x16x32_bf16 v[52:55], v[20:23], v[48:51], v[52:55]\n"
"ds_read_b128 v[20:23], v219 offset:51200\n"
"v_xor_b32 v216, 64, v217\n"
"s_waitcnt lgkmcnt(5)\n"
"v_mfma_f32_16x16x32_bf16 v[56:59], v[84:87], v[28:31], v[56:59]\n"
"ds_read_b128 v[80:83], v219 offset:53248\n"
"s_mov_b32 s96, 0xff\n"
"s_and_b32 s13, s10, 0xff\n"
"v_mov_b32 v236, s13\n"
"v_mfma_f32_16x16x32_bf16 v[64:67], v[84:87], v[48:51], v[64:67]\n"
"ds_read_b128 v[84:87], v219 offset:55296\n"
"s_sub_u32 s13, 16, s51\n"
"v_add_u32 v184, s13, v93\n"
"s_waitcnt lgkmcnt(6)\n"
"v_mfma_f32_16x16x32_bf16 v[68:71], v[88:91], v[28:31], v[68:71]\n"
"v_lshl_or_b32 v220, v184, 7, v227\n"
"v_mfma_f32_16x16x32_bf16 v[72:75], v[88:91], v[48:51], v[72:75]\n"
"s_waitcnt lgkmcnt(0)\n"
"s_barrier\n"
"v_mfma_f32_16x16x32_bf16 v[32:35], v[76:79], v[24:27], v[32:35]\n"
"ds_read_b128 v[88:91], v180 offset:8192\n"
"ds_read_b64 v[170:171], v92\n"
"ds_read_b64 v[140:141], v92 offset:256\n"
"ds_read_b64 v[142:143], v92 offset:512\n"
"v_mfma_f32_16x16x32_bf16 v[40:43], v[76:79], v[60:63], v[40:43]\n"
"ds_read_b128 v[76:79], v180 offset:10240\n"
"ds_read_b64 v[172:173], v100\n"
"ds_read_b64 v[144:145], v100 offset:256\n"
"v_mfma_f32_16x16x32_bf16 v[44:47], v[20:23], v[24:27], v[44:47]\n"
"ds_read_b128 v[92:95], v235 offset:8192\n"
"ds_read_b64 v[174:175], v100 offset:512\n"
"ds_read_b64 v[146:147], v100 offset:768\n"
"ds_read_b64 v[164:165], v100 offset:1024\n"
"v_mfma_f32_16x16x32_bf16 v[52:55], v[20:23], v[60:63], v[52:55]\n"
"ds_read_b128 v[20:23], v235 offset:10240\n"
"ds_read_b64 v[166:167], v100 offset:1280\n"
"ds_read_b64 v[168:169], v100 offset:1536\n"
"v_mfma_f32_16x16x32_bf16 v[56:59], v[80:83], v[24:27], v[56:59]\n"
"ds_read_b128 v[96:99], v235 offset:12288\n"
"ds_read_b64 v[176:177], v100 offset:1792\n"
"ds_read_b64 v[154:155], v100 offset:16384\n"
"v_mfma_f32_16x16x32_bf16 v[64:67], v[80:83], v[60:63], v[64:67]\n"
"ds_read_b128 v[80:83], v235 offset:14336\n"
"ds_read_b64 v[162:163], v100 offset:16640\n"
"ds_read_b64 v[156:157], v100 offset:16896\n"
"ds_read_b64 v[158:159], v100 offset:17152\n"
"v_mfma_f32_16x16x32_bf16 v[68:71], v[84:87], v[24:27], v[68:71]\n"
"ds_read_b64 v[148:149], v100 offset:17408\n"
"ds_read_b64 v[150:151], v100 offset:17664\n"
"v_mfma_f32_16x16x32_bf16 v[72:75], v[84:87], v[60:63], v[72:75]\n"
"ds_read_b64 v[160:161], v100 offset:17920\n"
"ds_read_b64 v[152:153], v100 offset:18176\n"
"s_waitcnt lgkmcnt(15)\n"
"v_mfma_f32_16x16x32_bf16 v[32:35], v[92:95], v[88:91], v[32:35]\n"
"ds_read_b128 v[84:87], v188 offset:8192\n"
"v_mfma_f32_16x16x32_bf16 v[40:43], v[92:95], v[76:79], v[40:43]\n"
"ds_read_b128 v[92:95], v188 offset:10240\n"
"s_waitcnt lgkmcnt(15)\n"
"v_mfma_f32_16x16x32_bf16 v[44:47], v[20:23], v[88:91], v[44:47]\n"
"ds_read_b128 v[100:103], v234 offset:8192\n"
"v_and_b32 v170, v170, v108\n"
"v_and_b32 v171, v171, v108\n"
"v_and_b32 v140, v140, v108\n"
"v_mfma_f32_16x16x32_bf16 v[52:55], v[20:23], v[76:79], v[52:55]\n"
"ds_read_b128 v[20:23], v234 offset:10240\n"
"v_and_b32 v141, v141, v108\n"
"v_and_b32 v142, v142, v108\n"
"s_waitcnt lgkmcnt(14)\n"
"v_mfma_f32_16x16x32_bf16 v[56:59], v[96:99], v[88:91], v[56:59]\n"
"ds_read_b128 v[104:107], v234 offset:12288\n"
"v_and_b32 v143, v143, v108\n"
"s_waitcnt vmcnt(3)\n"
"v_lshlrev_b32 v108, 16, v0\n"
"v_and_b32 v109, 0xffff0000, v0\n"
"v_mfma_f32_16x16x32_bf16 v[64:67], v[96:99], v[76:79], v[64:67]\n"
"ds_read_b128 v[96:99], v234 offset:14336\n"
"v_lshlrev_b32 v118, 16, v1\n"
"v_and_b32 v128, 0xffff0000, v1\n"
"s_waitcnt lgkmcnt(13)\n"
"v_mfma_f32_16x16x32_bf16 v[68:71], v[80:83], v[88:91], v[68:71]\n"
"v_lshlrev_b32 v121, 16, v170\n"
"v_lshlrev_b32 v129, 16, v140\n"
"v_lshlrev_b32 v119, 16, v142\n"
"v_mfma_f32_16x16x32_bf16 v[72:75], v[80:83], v[76:79], v[72:75]\n"
"v_lshlrev_b32 v110, 16, v172\n"
"v_lshlrev_b32 v111, 16, v144\n"
"s_waitcnt lgkmcnt(0)\n"
"s_barrier\n"
"v_mfma_f32_16x16x32_bf16 v[32:35], v[100:103], v[84:87], v[32:35]\n"
"ds_read_b128 v[76:79], v182\n"
"v_lshlrev_b32 v113, 16, v174\n"
"v_lshlrev_b32 v114, 16, v146\n"
"v_lshlrev_b32 v112, 16, v164\n"
"v_mfma_f32_16x16x32_bf16 v[40:43], v[100:103], v[92:95], v[40:43]\n"
"ds_read_b128 v[80:83], v182 offset:2048\n"
"v_lshlrev_b32 v115, 16, v166\n"
"v_lshlrev_b32 v116, 16, v168\n"
"v_mfma_f32_16x16x32_bf16 v[44:47], v[20:23], v[84:87], v[44:47]\n"
"ds_read_b128 v[88:91], v218\n"
"v_lshlrev_b32 v117, 16, v176\n"
"v_mul_f32 v120, v121, v128\n"
"v_mfma_f32_16x16x32_bf16 v[52:55], v[20:23], v[92:95], v[52:55]\n"
"ds_read_b128 v[20:23], v218 offset:2048\n"
"v_mul_f32 v121, v129, v128\n"
"v_mul_f32 v122, v119, v128\n"
"v_mul_f32 v123, v110, v128\n"
"v_mfma_f32_16x16x32_bf16 v[56:59], v[104:107], v[84:87], v[56:59]\n"
"ds_read_b128 v[100:103], v218 offset:4096\n"
"v_mul_f32 v124, v111, v128\n"
"v_mul_f32 v125, v113, v128\n"
"v_mfma_f32_16x16x32_bf16 v[64:67], v[104:107], v[92:95], v[64:67]\n"
"ds_read_b128 v[104:107], v218 offset:6144\n"
"v_mul_f32 v126, v114, v128\n"
"v_mul_f32 v127, v112, v128\n"
"v_fmac_f32 v120, v129, v118\n"
"v_mfma_f32_16x16x32_bf16 v[68:71], v[96:99], v[84:87], v[68:71]\n"
"v_fmac_f32 v121, v119, v118\n"
"v_fmac_f32 v122, v110, v118\n"
"v_mfma_f32_16x16x32_bf16 v[72:75], v[96:99], v[92:95], v[72:75]\n"
"v_fmac_f32 v123, v111, v118\n"
"v_fmac_f32 v124, v113, v118\n"
"v_fmac_f32 v125, v114, v118\n"
"s_waitcnt lgkmcnt(3)\n"
"v_mfma_f32_16x16x32_bf16 v[32:35], v[88:91], v[76:79], v[32:35]\n"
"ds_read_b128 v[84:87], v181\n"
"v_fmac_f32 v126, v112, v118\n"
"v_fmac_f32 v127, v115, v118\n"
"v_mfma_f32_16x16x32_bf16 v[40:43], v[88:91], v[80:83], v[40:43]\n"
"ds_read_b128 v[88:91], v181 offset:2048\n"
"v_fmac_f32 v120, v119, v109\n"
"v_fmac_f32 v121, v110, v109\n"
"v_fmac_f32 v122, v111, v109\n"
"s_waitcnt lgkmcnt(4)\n"
"v_mfma_f32_16x16x32_bf16 v[44:47], v[20:23], v[76:79], v[44:47]\n"
"ds_read_b128 v[92:95], v219\n"
"v_fmac_f32 v123, v113, v109\n"
"v_fmac_f32 v124, v114, v109\n"
"v_mfma_f32_16x16x32_bf16 v[52:55], v[20:23], v[80:83], v[52:55]\n"
"ds_read_b128 v[20:23], v219 offset:2048\n"
"v_fmac_f32 v125, v112, v109\n"
"v_fmac_f32 v126, v115, v109\n"
"s_waitcnt lgkmcnt(5)\n"
"v_mfma_f32_16x16x32_bf16 v[56:59], v[100:103], v[76:79], v[56:59]\n"
"ds_read_b128 v[96:99], v219 offset:4096\n"
"v_fmac_f32 v127, v116, v109\n"
"v_fmac_f32 v120, v110, v108\n"
"v_fmac_f32 v121, v111, v108\n"
"v_mfma_f32_16x16x32_bf16 v[64:67], v[100:103], v[80:83], v[64:67]\n"
"ds_read_b128 v[100:103], v219 offset:6144\n"
"v_fmac_f32 v122, v113, v108\n"
"v_fmac_f32 v123, v114, v108\n"
"s_waitcnt lgkmcnt(6)\n"
"v_mfma_f32_16x16x32_bf16 v[68:71], v[104:107], v[76:79], v[68:71]\n"
"v_fmac_f32 v124, v112, v108\n"
"v_fmac_f32 v125, v115, v108\n"
"v_fmac_f32 v126, v116, v108\n"
"v_mfma_f32_16x16x32_bf16 v[72:75], v[104:107], v[80:83], v[72:75]\n"
"v_fmac_f32 v127, v117, v108\n"
"v_lshlrev_b32 v112, 16, v154\n"
"s_waitcnt lgkmcnt(0)\n"
"s_barrier\n"
"v_mfma_f32_16x16x32_bf16 v[32:35], v[92:95], v[84:87], v[32:35]\n"
"ds_read_b128 v[104:107], v182 offset:24576\n"
"v_lshlrev_b32 v113, 16, v162\n"
"v_lshlrev_b32 v114, 16, v156\n"
"v_lshlrev_b32 v116, 16, v158\n"
"v_mfma_f32_16x16x32_bf16 v[40:43], v[92:95], v[88:91], v[40:43]\n"
"ds_read_b128 v[92:95], v182 offset:26624\n"
"v_lshlrev_b32 v118, 16, v148\n"
"v_lshlrev_b32 v117, 16, v150\n"
"v_mfma_f32_16x16x32_bf16 v[44:47], v[20:23], v[84:87], v[44:47]\n"
"ds_read_b128 v[108:111], v218 offset:24576\n"
"v_lshlrev_b32 v119, 16, v160\n"
"v_lshlrev_b32 v128, 16, v152\n"
"v_mfma_f32_16x16x32_bf16 v[52:55], v[20:23], v[88:91], v[52:55]\n"
"ds_read_b128 v[20:23], v218 offset:26624\n"
"v_mul_f32 v120, v120, v112\n"
"v_mul_f32 v121, v121, v113\n"
"v_mul_f32 v122, v122, v114\n"
"v_mfma_f32_16x16x32_bf16 v[56:59], v[96:99], v[84:87], v[56:59]\n"
"ds_read_b128 v[112:115], v218 offset:28672\n"
"v_mul_f32 v123, v123, v116\n"
"v_mul_f32 v124, v124, v118\n"
"v_mfma_f32_16x16x32_bf16 v[64:67], v[96:99], v[88:91], v[64:67]\n"
"ds_read_b128 v[96:99], v218 offset:30720\n"
"v_mul_f32 v125, v125, v117\n"
"v_mul_f32 v126, v126, v119\n"
"v_mul_f32 v127, v127, v128\n"
"v_mfma_f32_16x16x32_bf16 v[68:71], v[100:103], v[84:87], v[68:71]\n"
"v_cvt_pk_bf16_f32 v119, v121, v120\n"
"v_cvt_pk_bf16_f32 v118, v123, v122\n"
"v_mfma_f32_16x16x32_bf16 v[72:75], v[100:103], v[88:91], v[72:75]\n"
"v_cvt_pk_bf16_f32 v117, v125, v124\n"
"v_cvt_pk_bf16_f32 v116, v127, v126\n"
"global_store_dwordx4 v178, v[116:119], s[60:61] sc1\n"
"s_add_u32 s60, s60, 0x1000\n"
"s_addc_u32 s61, s61, 0\n"
"s_waitcnt lgkmcnt(3)\n"
"v_mfma_f32_16x16x32_bf16 v[32:35], v[108:111], v[104:107], v[32:35]\n"
"ds_read_b128 v[100:103], v181 offset:24576\n"
"v_lshlrev_b32 v120, 16, v2\n"
"v_and_b32 v121, 0xffff0000, v2\n"
"v_mfma_f32_16x16x32_bf16 v[40:43], v[108:111], v[92:95], v[40:43]\n"
"ds_read_b128 v[108:111], v181 offset:26624\n"
"v_lshlrev_b32 v138, 16, v3\n"
"v_and_b32 v185, 0xffff0000, v3\n"
"v_and_b32 v129, 0xffff0000, v170\n"
"s_waitcnt lgkmcnt(4)\n"
"v_mfma_f32_16x16x32_bf16 v[44:47], v[20:23], v[104:107], v[44:47]\n"
"ds_read_b128 v[0:3], v219 offset:24576\n"
"v_and_b32 v186, 0xffff0000, v140\n"
"v_and_b32 v139, 0xffff0000, v142\n"
"v_mfma_f32_16x16x32_bf16 v[52:55], v[20:23], v[92:95], v[52:55]\n"
"ds_read_b128 v[20:23], v219 offset:26624\n"
"v_and_b32 v123, 0xffff0000, v172\n"
"v_and_b32 v124, 0xffff0000, v144\n"
"s_waitcnt lgkmcnt(5)\n"
"v_mfma_f32_16x16x32_bf16 v[56:59], v[112:115], v[104:107], v[56:59]\n"
"ds_read_b128 v[116:119], v219 offset:28672\n"
"v_and_b32 v125, 0xffff0000, v174\n"
"v_and_b32 v126, 0xffff0000, v146\n"
"v_and_b32 v127, 0xffff0000, v164\n"
"v_mfma_f32_16x16x32_bf16 v[64:67], v[112:115], v[92:95], v[64:67]\n"
"ds_read_b128 v[112:115], v219 offset:30720\n"
"v_and_b32 v136, 0xffff0000, v166\n"
"v_and_b32 v137, 0xffff0000, v168\n"
"s_waitcnt lgkmcnt(6)\n"
"v_mfma_f32_16x16x32_bf16 v[68:71], v[96:99], v[104:107], v[68:71]\n"
"v_and_b32 v122, 0xffff0000, v176\n"
"v_mul_f32 v128, v129, v185\n"
"v_mul_f32 v129, v186, v185\n"
"v_mfma_f32_16x16x32_bf16 v[72:75], v[96:99], v[92:95], v[72:75]\n"
"v_mul_f32 v130, v139, v185\n"
"v_mul_f32 v131, v123, v185\n"
"s_waitcnt lgkmcnt(0)\n"
"s_barrier\n"
"v_mfma_f32_16x16x32_bf16 v[32:35], v[0:3], v[100:103], v[32:35]\n"
"ds_read_b128 v[92:95], v182 offset:49152\n"
"v_mul_f32 v132, v124, v185\n"
"v_mul_f32 v133, v125, v185\n"
"v_mul_f32 v134, v126, v185\n"
"v_mfma_f32_16x16x32_bf16 v[40:43], v[0:3], v[108:111], v[40:43]\n"
"ds_read_b128 v[0:3], v182 offset:51200\n"
"v_mul_f32 v135, v127, v185\n"
"v_fmac_f32 v128, v186, v138\n"
"v_mfma_f32_16x16x32_bf16 v[44:47], v[20:23], v[100:103], v[44:47]\n"
"ds_read_b128 v[96:99], v218 offset:49152\n"
"v_fmac_f32 v129, v139, v138\n"
"v_fmac_f32 v130, v123, v138\n"
"v_mfma_f32_16x16x32_bf16 v[52:55], v[20:23], v[108:111], v[52:55]\n"
"ds_read_b128 v[20:23], v218 offset:51200\n"
"v_fmac_f32 v131, v124, v138\n"
"v_fmac_f32 v132, v125, v138\n"
"v_fmac_f32 v133, v126, v138\n"
"v_mfma_f32_16x16x32_bf16 v[56:59], v[116:119], v[100:103], v[56:59]\n"
"ds_read_b128 v[104:107], v218 offset:53248\n"
"v_fmac_f32 v134, v127, v138\n"
"v_fmac_f32 v135, v136, v138\n"
"v_mfma_f32_16x16x32_bf16 v[64:67], v[116:119], v[108:111], v[64:67]\n"
"ds_read_b128 v[116:119], v218 offset:55296\n"
"v_fmac_f32 v128, v139, v121\n"
"v_fmac_f32 v129, v123, v121\n"
"v_fmac_f32 v130, v124, v121\n"
"v_mfma_f32_16x16x32_bf16 v[68:71], v[112:115], v[100:103], v[68:71]\n"
"v_fmac_f32 v131, v125, v121\n"
"v_fmac_f32 v132, v126, v121\n"
"v_mfma_f32_16x16x32_bf16 v[72:75], v[112:115], v[108:111], v[72:75]\n"
"v_fmac_f32 v133, v127, v121\n"
"v_fmac_f32 v134, v136, v121\n"
"v_fmac_f32 v135, v137, v121\n"
"s_waitcnt lgkmcnt(3)\n"
"v_mfma_f32_16x16x32_bf16 v[32:35], v[96:99], v[92:95], v[32:35]\n"
"ds_read_b128 v[100:103], v181 offset:49152\n"
"v_fmac_f32 v128, v123, v120\n"
"v_fmac_f32 v129, v124, v120\n"
"v_mfma_f32_16x16x32_bf16 v[40:43], v[96:99], v[0:3], v[40:43]\n"
"ds_read_b128 v[96:99], v181 offset:51200\n"
"v_fmac_f32 v130, v125, v120\n"
"v_fmac_f32 v131, v126, v120\n"
"v_fmac_f32 v132, v127, v120\n"
"s_waitcnt lgkmcnt(4)\n"
"v_mfma_f32_16x16x32_bf16 v[44:47], v[20:23], v[92:95], v[44:47]\n"
"ds_read_b128 v[108:111], v219 offset:49152\n"
"v_fmac_f32 v133, v136, v120\n"
"v_fmac_f32 v134, v137, v120\n"
"v_mfma_f32_16x16x32_bf16 v[52:55], v[20:23], v[0:3], v[52:55]\n"
"ds_read_b128 v[20:23], v219 offset:51200\n"
"v_fmac_f32 v135, v122, v120\n"
"v_and_b32 v125, 0xffff0000, v154\n"
"s_waitcnt lgkmcnt(5)\n"
"v_mfma_f32_16x16x32_bf16 v[56:59], v[104:107], v[92:95], v[56:59]\n"
"ds_read_b128 v[112:115], v219 offset:53248\n"
"v_and_b32 v126, 0xffff0000, v162\n"
"v_and_b32 v127, 0xffff0000, v156\n"
"v_and_b32 v120, 0xffff0000, v158\n"
"v_mfma_f32_16x16x32_bf16 v[64:67], v[104:107], v[0:3], v[64:67]\n"
"ds_read_b128 v[104:107], v219 offset:55296\n"
"v_and_b32 v123, 0xffff0000, v148\n"
"v_and_b32 v124, 0xffff0000, v150\n"
"s_waitcnt lgkmcnt(6)\n"
"v_mfma_f32_16x16x32_bf16 v[68:71], v[116:119], v[92:95], v[68:71]\n"
"v_and_b32 v121, 0xffff0000, v160\n"
"v_and_b32 v122, 0xffff0000, v152\n"
"v_mul_f32 v128, v128, v125\n"
"v_mfma_f32_16x16x32_bf16 v[72:75], v[116:119], v[0:3], v[72:75]\n"
"v_mul_f32 v129, v129, v126\n"
"v_mul_f32 v130, v130, v127\n"
"s_waitcnt lgkmcnt(0)\n"
"s_barrier\n"
"v_mfma_f32_16x16x32_bf16 v[32:35], v[108:111], v[100:103], v[32:35]\n"
"ds_read_b128 v[116:119], v180 offset:8192\n"
"v_mul_f32 v131, v131, v120\n"
"v_mul_f32 v132, v132, v123\n"
"v_mul_f32 v133, v133, v124\n"
"v_mfma_f32_16x16x32_bf16 v[40:43], v[108:111], v[96:99], v[40:43]\n"
"ds_read_b128 v[108:111], v180 offset:10240\n"
"v_mul_f32 v134, v134, v121\n"
"v_mul_f32 v135, v135, v122\n"
"v_mfma_f32_16x16x32_bf16 v[44:47], v[20:23], v[100:103], v[44:47]\n"
"ds_read_b128 v[120:123], v235 offset:8192\n"
"v_cvt_pk_bf16_f32 v127, v129, v128\n"
"v_cvt_pk_bf16_f32 v126, v131, v130\n"
"v_mfma_f32_16x16x32_bf16 v[52:55], v[20:23], v[96:99], v[52:55]\n"
"ds_read_b128 v[20:23], v235 offset:10240\n"
"v_cvt_pk_bf16_f32 v125, v133, v132\n"
"v_cvt_pk_bf16_f32 v124, v135, v134\n"
"global_store_dwordx4 v178, v[124:127], s[60:61] sc1\n"
"s_add_u32 s60, s60, 0x1000\n"
"s_addc_u32 s61, s61, 0\n"
"v_mfma_f32_16x16x32_bf16 v[56:59], v[112:115], v[100:103], v[56:59]\n"
"ds_read_b128 v[124:127], v235 offset:12288\n"
"s_waitcnt vmcnt(4)\n"
"v_lshlrev_b32 v136, 16, v4\n"
"v_and_b32 v137, 0xffff0000, v4\n"
"v_mfma_f32_16x16x32_bf16 v[64:67], v[112:115], v[96:99], v[64:67]\n"
"ds_read_b128 v[112:115], v235 offset:14336\n"
"v_lshlrev_b32 v200, 16, v5\n"
"v_and_b32 v202, 0xffff0000, v5\n"
"v_lshlrev_b32 v132, 16, v171\n"
"v_mfma_f32_16x16x32_bf16 v[68:71], v[104:107], v[100:103], v[68:71]\n"
"v_lshlrev_b32 v203, 16, v141\n"
"v_lshlrev_b32 v201, 16, v143\n"
"v_mfma_f32_16x16x32_bf16 v[72:75], v[104:107], v[96:99], v[72:75]\n"
"v_lshlrev_b32 v139, 16, v173\n"
"v_lshlrev_b32 v194, 16, v145\n"
"v_lshlrev_b32 v138, 16, v175\n"
"s_waitcnt lgkmcnt(3)\n"
"v_mfma_f32_16x16x32_bf16 v[32:35], v[120:123], v[116:119], v[32:35]\n"
"ds_read_b128 v[104:107], v188 offset:8192\n"
"v_lshlrev_b32 v195, 16, v147\n"
"v_lshlrev_b32 v196, 16, v165\n"
"v_mfma_f32_16x16x32_bf16 v[40:43], v[120:123], v[108:111], v[40:43]\n"
"ds_read_b128 v[120:123], v188 offset:10240\n"
"v_lshlrev_b32 v198, 16, v167\n"
"v_lshlrev_b32 v199, 16, v169\n"
"v_lshlrev_b32 v197, 16, v177\n"
"s_waitcnt lgkmcnt(4)\n"
"v_mfma_f32_16x16x32_bf16 v[44:47], v[20:23], v[116:119], v[44:47]\n"
"ds_read_b128 v[128:131], v234 offset:8192\n"
"v_mul_f32 v185, v132, v202\n"
"v_mul_f32 v186, v203, v202\n"
"v_mfma_f32_16x16x32_bf16 v[52:55], v[20:23], v[108:111], v[52:55]\n"
"ds_read_b128 v[20:23], v234 offset:10240\n"
"v_mul_f32 v187, v201, v202\n"
"v_mul_f32 v189, v139, v202\n"
"s_waitcnt lgkmcnt(5)\n"
"v_mfma_f32_16x16x32_bf16 v[56:59], v[124:127], v[116:119], v[56:59]\n"
"ds_read_b128 v[132:135], v234 offset:12288\n"
"v_mul_f32 v190, v194, v202\n"
"v_mul_f32 v192, v138, v202\n"
"v_mul_f32 v191, v195, v202\n"
"v_mfma_f32_16x16x32_bf16 v[64:67], v[124:127], v[108:111], v[64:67]\n"
"ds_read_b128 v[124:127], v234 offset:14336\n"
"v_mul_f32 v193, v196, v202\n"
"v_fmac_f32 v185, v203, v200\n"
"s_waitcnt lgkmcnt(6)\n"
"v_mfma_f32_16x16x32_bf16 v[68:71], v[112:115], v[116:119], v[68:71]\n"
"v_fmac_f32 v186, v201, v200\n"
"v_fmac_f32 v187, v139, v200\n"
"v_fmac_f32 v189, v194, v200\n"
"v_mfma_f32_16x16x32_bf16 v[72:75], v[112:115], v[108:111], v[72:75]\n"
"v_fmac_f32 v190, v138, v200\n"
"v_fmac_f32 v192, v195, v200\n"
"s_waitcnt lgkmcnt(0)\n"
"s_barrier\n"
"v_mfma_f32_16x16x32_bf16 v[32:35], v[128:131], v[104:107], v[32:35]\n"
"ds_read_b128 v[108:111], v182\n"
"v_fmac_f32 v191, v196, v200\n"
"v_fmac_f32 v193, v198, v200\n"
"v_fmac_f32 v185, v201, v137\n"
"v_mfma_f32_16x16x32_bf16 v[40:43], v[128:131], v[120:123], v[40:43]\n"
"ds_read_b128 v[112:115], v182 offset:2048\n"
"v_fmac_f32 v186, v139, v137\n"
"v_fmac_f32 v187, v194, v137\n"
"v_mfma_f32_16x16x32_bf16 v[44:47], v[20:23], v[104:107], v[44:47]\n"
"ds_read_b128 v[116:119], v218\n"
"v_fmac_f32 v189, v138, v137\n"
"v_fmac_f32 v190, v195, v137\n"
"v_fmac_f32 v192, v196, v137\n"
"v_mfma_f32_16x16x32_bf16 v[52:55], v[20:23], v[120:123], v[52:55]\n"
"ds_read_b128 v[20:23], v218 offset:2048\n"
"v_fmac_f32 v191, v198, v137\n"
"v_fmac_f32 v193, v199, v137\n"
"v_mfma_f32_16x16x32_bf16 v[56:59], v[132:135], v[104:107], v[56:59]\n"
"ds_read_b128 v[128:131], v218 offset:4096\n"
"v_fmac_f32 v185, v139, v136\n"
"v_fmac_f32 v186, v194, v136\n"
"v_mfma_f32_16x16x32_bf16 v[64:67], v[132:135], v[120:123], v[64:67]\n"
"ds_read_b128 v[132:135], v218 offset:6144\n"
"v_fmac_f32 v187, v138, v136\n"
"v_fmac_f32 v189, v195, v136\n"
"v_fmac_f32 v190, v196, v136\n"
"v_mfma_f32_16x16x32_bf16 v[68:71], v[124:127], v[104:107], v[68:71]\n"
"v_fmac_f32 v192, v198, v136\n"
"v_fmac_f32 v191, v199, v136\n"
"v_mfma_f32_16x16x32_bf16 v[72:75], v[124:127], v[120:123], v[72:75]\n"
"v_fmac_f32 v193, v197, v136\n"
"v_lshlrev_b32 v125, 16, v155\n"
"v_lshlrev_b32 v124, 16, v163\n"
"s_waitcnt lgkmcnt(3)\n"
"v_mfma_f32_16x16x32_bf16 v[32:35], v[116:119], v[108:111], v[32:35]\n"
"ds_read_b128 v[104:107], v181\n"
"v_lshlrev_b32 v126, 16, v157\n"
"v_lshlrev_b32 v136, 16, v159\n"
"v_mfma_f32_16x16x32_bf16 v[40:43], v[116:119], v[112:115], v[40:43]\n"
"ds_read_b128 v[116:119], v181 offset:2048\n"
"v_lshlrev_b32 v138, 16, v149\n"
"v_lshlrev_b32 v139, 16, v151\n"
"v_lshlrev_b32 v137, 16, v161\n"
"s_waitcnt lgkmcnt(4)\n"
"v_mfma_f32_16x16x32_bf16 v[44:47], v[20:23], v[108:111], v[44:47]\n"
"ds_read_b128 v[120:123], v219\n"
"v_lshlrev_b32 v194, 16, v153\n"
"v_mul_f32 v185, v185, v125\n"
"v_mfma_f32_16x16x32_bf16 v[52:55], v[20:23], v[112:115], v[52:55]\n"
"ds_read_b128 v[20:23], v219 offset:2048\n"
"v_mul_f32 v186, v186, v124\n"
"v_mul_f32 v187, v187, v126\n"
"s_waitcnt lgkmcnt(5)\n"
"v_mfma_f32_16x16x32_bf16 v[56:59], v[128:131], v[108:111], v[56:59]\n"
"ds_read_b128 v[124:127], v219 offset:4096\n"
"v_mul_f32 v189, v189, v136\n"
"v_mul_f32 v190, v190, v138\n"
"v_mul_f32 v192, v192, v139\n"
"v_mfma_f32_16x16x32_bf16 v[64:67], v[128:131], v[112:115], v[64:67]\n"
"ds_read_b128 v[128:131], v219 offset:6144\n"
"v_mul_f32 v191, v191, v137\n"
"v_mul_f32 v193, v193, v194\n"
"s_waitcnt lgkmcnt(6)\n"
"v_mfma_f32_16x16x32_bf16 v[68:71], v[132:135], v[108:111], v[68:71]\n"
"v_cvt_pk_bf16_f32 v139, v186, v185\n"
"v_cvt_pk_bf16_f32 v138, v189, v187\n"
"v_cvt_pk_bf16_f32 v137, v192, v190\n"
"v_mfma_f32_16x16x32_bf16 v[72:75], v[132:135], v[112:115], v[72:75]\n"
"v_cvt_pk_bf16_f32 v136, v193, v191\n"
"global_store_dwordx4 v178, v[136:139], s[60:61] sc1\n"
"s_add_u32 s60, s60, 0x1000\n"
"s_addc_u32 s61, s61, 0\n"
"s_waitcnt lgkmcnt(0)\n"
"s_barrier\n"
"v_mfma_f32_16x16x32_bf16 v[32:35], v[120:123], v[104:107], v[32:35]\n"
"ds_read_b128 v[132:135], v182 offset:24576\n"
"v_lshlrev_b32 v185, 16, v6\n"
"v_and_b32 v186, 0xffff0000, v6\n"
"v_lshlrev_b32 v192, 16, v7\n"
"v_mfma_f32_16x16x32_bf16 v[40:43], v[120:123], v[116:119], v[40:43]\n"
"ds_read_b128 v[120:123], v182 offset:26624\n"
"v_and_b32 v194, 0xffff0000, v7\n"
"v_and_b32 v196, 0xffff0000, v171\n"
"v_mfma_f32_16x16x32_bf16 v[44:47], v[20:23], v[104:107], v[44:47]\n"
"ds_read_b128 v[4:7], v218 offset:24576\n"
"v_and_b32 v195, 0xffff0000, v141\n"
"v_and_b32 v193, 0xffff0000, v143\n"
"v_and_b32 v187, 0xffff0000, v173\n"
"v_mfma_f32_16x16x32_bf16 v[52:55], v[20:23], v[116:119], v[52:55]\n"
"ds_read_b128 v[20:23], v218 offset:26624\n"
"v_and_b32 v172, 0xffff0000, v145\n"
"v_and_b32 v173, 0xffff0000, v175\n"
"v_mfma_f32_16x16x32_bf16 v[56:59], v[124:127], v[104:107], v[56:59]\n"
"ds_read_b128 v[136:139], v218 offset:28672\n"
"v_and_b32 v174, 0xffff0000, v147\n"
"v_and_b32 v175, 0xffff0000, v165\n"
"v_mfma_f32_16x16x32_bf16 v[64:67], v[124:127], v[116:119], v[64:67]\n"
"ds_read_b128 v[124:127], v218 offset:30720\n"
"v_and_b32 v189, 0xffff0000, v167\n"
"v_and_b32 v190, 0xffff0000, v169\n"
"v_and_b32 v191, 0xffff0000, v177\n"
"v_mfma_f32_16x16x32_bf16 v[68:71], v[128:131], v[104:107], v[68:71]\n"
"v_mul_f32 v164, v196, v194\n"
"v_mul_f32 v166, v195, v194\n"
"v_mfma_f32_16x16x32_bf16 v[72:75], v[128:131], v[116:119], v[72:75]\n"
"v_mul_f32 v165, v193, v194\n"
"v_mul_f32 v167, v187, v194\n"
"v_mul_f32 v168, v172, v194\n"
"s_waitcnt lgkmcnt(3)\n"
"v_mfma_f32_16x16x32_bf16 v[32:35], v[4:7], v[132:135], v[32:35]\n"
"ds_read_b128 v[128:131], v181 offset:24576\n"
"v_mul_f32 v170, v173, v194\n"
"v_mul_f32 v169, v174, v194\n"
"v_mfma_f32_16x16x32_bf16 v[40:43], v[4:7], v[120:123], v[40:43]\n"
"ds_read_b128 v[4:7], v181 offset:26624\n"
"v_mul_f32 v171, v175, v194\n"
"v_fmac_f32 v164, v195, v192\n"
"v_fmac_f32 v166, v193, v192\n"
"s_waitcnt lgkmcnt(4)\n"
"v_mfma_f32_16x16x32_bf16 v[44:47], v[20:23], v[132:135], v[44:47]\n"
"ds_read_b128 v[140:143], v219 offset:24576\n"
"v_fmac_f32 v165, v187, v192\n"
"v_fmac_f32 v167, v172, v192\n"
"v_mfma_f32_16x16x32_bf16 v[52:55], v[20:23], v[120:123], v[52:55]\n"
"ds_read_b128 v[20:23], v219 offset:26624\n"
"v_fmac_f32 v168, v173, v192\n"
"v_fmac_f32 v170, v174, v192\n"
"s_waitcnt lgkmcnt(5)\n"
"v_mfma_f32_16x16x32_bf16 v[56:59], v[136:139], v[132:135], v[56:59]\n"
"ds_read_b128 v[144:147], v219 offset:28672\n"
"v_fmac_f32 v169, v175, v192\n"
"v_fmac_f32 v171, v189, v192\n"
"v_fmac_f32 v164, v193, v186\n"
"v_mfma_f32_16x16x32_bf16 v[64:67], v[136:139], v[120:123], v[64:67]\n"
"ds_read_b128 v[136:139], v219 offset:30720\n"
"v_fmac_f32 v166, v187, v186\n"
"v_fmac_f32 v165, v172, v186\n"
"s_waitcnt lgkmcnt(6)\n"
"v_mfma_f32_16x16x32_bf16 v[68:71], v[124:127], v[132:135], v[68:71]\n"
"v_fmac_f32 v167, v173, v186\n"
"v_fmac_f32 v168, v174, v186\n"
"v_fmac_f32 v170, v175, v186\n"
"v_mfma_f32_16x16x32_bf16 v[72:75], v[124:127], v[120:123], v[72:75]\n"
"v_fmac_f32 v169, v189, v186\n"
"v_fmac_f32 v171, v190, v186\n"
"s_waitcnt lgkmcnt(0)\n"
"s_barrier\n"
"v_mfma_f32_16x16x32_bf16 v[32:35], v[140:143], v[128:131], v[32:35]\n"
"ds_read_b128 v[120:123], v182 offset:49152\n"
"v_fmac_f32 v164, v187, v185\n"
"v_fmac_f32 v166, v172, v185\n"
"v_fmac_f32 v165, v173, v185\n"
"v_mfma_f32_16x16x32_bf16 v[40:43], v[140:143], v[4:7], v[40:43]\n"
"ds_read_b128 v[124:127], v182 offset:51200\n"
"v_fmac_f32 v167, v174, v185\n"
"v_fmac_f32 v168, v175, v185\n"
"v_mfma_f32_16x16x32_bf16 v[44:47], v[20:23], v[128:131], v[44:47]\n"
"ds_read_b128 v[132:135], v218 offset:49152\n"
"v_fmac_f32 v170, v189, v185\n"
"v_fmac_f32 v169, v190, v185\n"
"v_fmac_f32 v171, v191, v185\n"
"v_mfma_f32_16x16x32_bf16 v[52:55], v[20:23], v[4:7], v[52:55]\n"
"ds_read_b128 v[20:23], v218 offset:51200\n"
"v_and_b32 v172, 0xffff0000, v155\n"
"v_and_b32 v154, 0xffff0000, v163\n"
"v_mfma_f32_16x16x32_bf16 v[56:59], v[144:147], v[128:131], v[56:59]\n"
"ds_read_b128 v[140:143], v218 offset:53248\n"
"v_and_b32 v155, 0xffff0000, v157\n"
"v_and_b32 v156, 0xffff0000, v159\n"
"v_mfma_f32_16x16x32_bf16 v[64:67], v[144:147], v[4:7], v[64:67]\n"
"ds_read_b128 v[144:147], v218 offset:55296\n"
"v_and_b32 v157, 0xffff0000, v149\n"
"v_and_b32 v158, 0xffff0000, v151\n"
"v_and_b32 v148, 0xffff0000, v161\n"
"v_mfma_f32_16x16x32_bf16 v[68:71], v[136:139], v[128:131], v[68:71]\n"
"v_and_b32 v149, 0xffff0000, v153\n"
"v_mul_f32 v164, v164, v172\n"
"v_mfma_f32_16x16x32_bf16 v[72:75], v[136:139], v[4:7], v[72:75]\n"
"v_mul_f32 v166, v166, v154\n"
"v_mul_f32 v165, v165, v155\n"
"v_mul_f32 v167, v167, v156\n"
"s_waitcnt lgkmcnt(3)\n"
"v_mfma_f32_16x16x32_bf16 v[32:35], v[132:135], v[120:123], v[32:35]\n"
"ds_read_b128 v[4:7], v181 offset:49152\n"
"v_mul_f32 v168, v168, v157\n"
"v_mul_f32 v170, v170, v158\n"
"v_mfma_f32_16x16x32_bf16 v[40:43], v[132:135], v[124:127], v[40:43]\n"
"ds_read_b128 v[128:131], v181 offset:51200\n"
"v_mul_f32 v169, v169, v148\n"
"v_mul_f32 v171, v171, v149\n"
"v_cvt_pk_bf16_f32 v135, v166, v164\n"
"s_waitcnt lgkmcnt(4)\n"
"v_mfma_f32_16x16x32_bf16 v[44:47], v[20:23], v[120:123], v[44:47]\n"
"ds_read_b128 v[136:139], v219 offset:49152\n"
"v_cvt_pk_bf16_f32 v134, v167, v165\n"
"v_cvt_pk_bf16_f32 v133, v170, v168\n"
"v_mfma_f32_16x16x32_bf16 v[52:55], v[20:23], v[124:127], v[52:55]\n"
"ds_read_b128 v[20:23], v219 offset:51200\n"
"v_cvt_pk_bf16_f32 v132, v171, v169\n"
"global_store_dwordx4 v178, v[132:135], s[60:61] sc1\n"
"s_add_u32 s60, s60, 0x1000\n"
"s_addc_u32 s61, s61, 0\n"
"s_waitcnt lgkmcnt(5)\n"
"v_mfma_f32_16x16x32_bf16 v[56:59], v[140:143], v[120:123], v[56:59]\n"
"ds_read_b128 v[132:135], v219 offset:53248\n"
"v_cmp_gt_u32 vcc, 16, v184\n"
"s_nop 1\n"
"v_cndmask_b32 v241, 0, v236, vcc\n"
"v_mfma_f32_16x16x32_bf16 v[64:67], v[140:143], v[124:127], v[64:67]\n"
"ds_read_b128 v[140:143], v219 offset:55296\n"
"s_waitcnt lgkmcnt(6)\n"
"v_mfma_f32_16x16x32_bf16 v[68:71], v[144:147], v[120:123], v[68:71]\n"
"v_mfma_f32_16x16x32_bf16 v[72:75], v[144:147], v[124:127], v[72:75]\n"
"s_waitcnt lgkmcnt(0)\n"
"s_barrier\n"
"v_mfma_f32_16x16x32_bf16 v[32:35], v[136:139], v[4:7], v[32:35]\n"
"ds_read_b128 v[144:147], v180 offset:8192\n"
"v_mfma_f32_16x16x32_bf16 v[40:43], v[136:139], v[128:131], v[40:43]\n"
"ds_read_b128 v[136:139], v180 offset:10240\n"
"v_cmp_gt_u32 vcc, 12, v184\n"
"s_nop 1\n"
"v_cndmask_b32 v242, 0, v236, vcc\n"
"v_mfma_f32_16x16x32_bf16 v[44:47], v[20:23], v[4:7], v[44:47]\n"
"ds_read_b128 v[148:151], v235 offset:8192\n"
"v_mfma_f32_16x16x32_bf16 v[52:55], v[20:23], v[128:131], v[52:55]\n"
"ds_read_b128 v[20:23], v235 offset:10240\n"
"v_mfma_f32_16x16x32_bf16 v[56:59], v[132:135], v[4:7], v[56:59]\n"
"ds_read_b128 v[152:155], v235 offset:12288\n"
"v_mfma_f32_16x16x32_bf16 v[64:67], v[132:135], v[128:131], v[64:67]\n"
"ds_read_b128 v[132:135], v235 offset:14336\n"
"v_mfma_f32_16x16x32_bf16 v[68:71], v[140:143], v[4:7], v[68:71]\n"
"v_mfma_f32_16x16x32_bf16 v[72:75], v[140:143], v[128:131], v[72:75]\n"
"v_cmp_gt_u32 vcc, 8, v184\n"
"s_nop 1\n"
"v_cndmask_b32 v243, 0, v236, vcc\n"
"s_waitcnt lgkmcnt(3)\n"
"v_mfma_f32_16x16x32_bf16 v[32:35], v[148:151], v[144:147], v[32:35]\n"
"ds_read_b128 v[140:143], v188 offset:8192\n"
"v_mfma_f32_16x16x32_bf16 v[40:43], v[148:151], v[136:139], v[40:43]\n"
"ds_read_b128 v[148:151], v188 offset:10240\n"
"s_waitcnt lgkmcnt(4)\n"
"v_mfma_f32_16x16x32_bf16 v[44:47], v[20:23], v[144:147], v[44:47]\n"
"ds_read_b128 v[156:159], v234 offset:8192\n"
"v_mfma_f32_16x16x32_bf16 v[52:55], v[20:23], v[136:139], v[52:55]\n"
"ds_read_b128 v[20:23], v234 offset:10240\n"
"s_waitcnt lgkmcnt(5)\n"
"v_mfma_f32_16x16x32_bf16 v[56:59], v[152:155], v[144:147], v[56:59]\n"
"ds_read_b128 v[160:163], v234 offset:12288\n"
"v_cmp_gt_u32 vcc, 4, v184\n"
"s_nop 1\n"
"v_cndmask_b32 v244, 0, v236, vcc\n"
"v_mfma_f32_16x16x32_bf16 v[64:67], v[152:155], v[136:139], v[64:67]\n"
"ds_read_b128 v[152:155], v234 offset:14336\n"
"s_waitcnt lgkmcnt(6)\n"
"v_mfma_f32_16x16x32_bf16 v[68:71], v[132:135], v[144:147], v[68:71]\n"
"v_mfma_f32_16x16x32_bf16 v[72:75], v[132:135], v[136:139], v[72:75]\n"
"s_waitcnt lgkmcnt(0)\n"
"s_barrier\n"
"v_mfma_f32_16x16x32_bf16 v[32:35], v[156:159], v[140:143], v[32:35]\n"
"ds_read_b128 v[132:135], v182\n"
"v_mfma_f32_16x16x32_bf16 v[40:43], v[156:159], v[148:151], v[40:43]\n"
"ds_read_b128 v[136:139], v182 offset:2048\n"
"v_mfma_f32_16x16x32_bf16 v[44:47], v[20:23], v[140:143], v[44:47]\n"
"ds_read_b128 v[144:147], v218\n"
"s_lshl2_add_u32 s13, s52, s15\n"
"s_lshl_b32 s13, s13, 12\n"
"s_add_u32 s13, s13, 0x100000\n"
"s_add_u32 s76, s30, s13\n"
"s_addc_u32 s77, s31, 0\n"
"v_mfma_f32_16x16x32_bf16 v[52:55], v[20:23], v[148:151], v[52:55]\n"
"ds_read_b128 v[20:23], v218 offset:2048\n"
"v_mfma_f32_16x16x32_bf16 v[56:59], v[160:163], v[140:143], v[56:59]\n"
"ds_read_b128 v[156:159], v218 offset:4096\n"
"v_mfma_f32_16x16x32_bf16 v[64:67], v[160:163], v[148:151], v[64:67]\n"
"ds_read_b128 v[160:163], v218 offset:6144\n"
"v_mfma_f32_16x16x32_bf16 v[68:71], v[152:155], v[140:143], v[68:71]\n"
"s_lshl_b32 s12, s11, 16\n"
"s_or_b32 s12, s12, %3\n"
"v_mov_b32 v140, s10\n"
"v_mov_b32 v141, s12\n"
"s_waitcnt vmcnt(0)\n"
"global_store_dwordx2 v179, v[140:141], s[30:31] sc1\n"
"v_mfma_f32_16x16x32_bf16 v[72:75], v[152:155], v[148:151], v[72:75]\n"
"s_sub_u32 s13, 15, s51\n"
"s_lshl_b32 s13, s13, 7\n"
"s_add_u32 s64, s76, s13\n"
"s_addc_u32 s65, s77, 0\n"
"s_waitcnt lgkmcnt(3)\n"
"v_mfma_f32_16x16x32_bf16 v[32:35], v[144:147], v[132:135], v[32:35]\n"
"ds_read_b128 v[140:143], v181\n"
"v_mfma_f32_16x16x32_bf16 v[40:43], v[144:147], v[136:139], v[40:43]\n"
"ds_read_b128 v[144:147], v181 offset:2048\n"
"s_waitcnt lgkmcnt(4)\n"
"v_mfma_f32_16x16x32_bf16 v[44:47], v[20:23], v[132:135], v[44:47]\n"
"ds_read_b128 v[148:151], v219\n"
"v_mfma_f32_16x16x32_bf16 v[52:55], v[20:23], v[136:139], v[52:55]\n"
"ds_read_b128 v[20:23], v219 offset:2048\n"
"s_waitcnt lgkmcnt(5)\n"
"v_mfma_f32_16x16x32_bf16 v[56:59], v[156:159], v[132:135], v[56:59]\n"
"ds_read_b128 v[152:155], v219 offset:4096\n"
"v_mfma_f32_16x16x32_bf16 v[64:67], v[156:159], v[136:139], v[64:67]\n"
"ds_read_b128 v[156:159], v219 offset:6144\n"
"s_and_b32 s77, s77, 0xffff\n"
"s_mov_b32 s78, 0x1000\n"
"s_mov_b32 s79, 0x20000\n"
"s_waitcnt lgkmcnt(6)\n"
"v_mfma_f32_16x16x32_bf16 v[68:71], v[160:163], v[132:135], v[68:71]\n"
"v_mfma_f32_16x16x32_bf16 v[72:75], v[160:163], v[136:139], v[72:75]\n"
"s_waitcnt lgkmcnt(0)\n"
"s_barrier\n"
"v_mfma_f32_16x16x32_bf16 v[32:35], v[148:151], v[140:143], v[32:35]\n"
"ds_read_b128 v[160:163], v182 offset:24576\n"
"v_mfma_f32_16x16x32_bf16 v[40:43], v[148:151], v[144:147], v[40:43]\n"
"ds_read_b128 v[148:151], v182 offset:26624\n"
"v_mfma_f32_16x16x32_bf16 v[44:47], v[20:23], v[140:143], v[44:47]\n"
"ds_read_b128 v[164:167], v218 offset:24576\n"
"s_lshl1_add_u32 s13, s51, s48\n"
"s_lshl2_add_u32 s14, s50, s15\n"
"s_and_b32 s67, s14, 24\n"
"s_add_u32 s67, s67, s14\n"
"s_lshl_b32 s62, s13, 19\n"
"s_lshl_b32 s67, s67, 10\n"
"s_add_u32 s62, s62, s67\n"
"s_add_u32 s62, s62, 0x400000\n"
"s_lshr_b32 s66, s62, 8\n"
"s_and_b32 s66, s66, 0x2000\n"
"s_add_u32 s66, s66, s62\n"
"s_add_u32 s66, s30, s66\n"
"s_addc_u32 s67, s31, 0\n"
"s_lshl_b32 s13, s13, 13\n"
"s_lshl_b32 s14, s14, 7\n"
"s_add_u32 s13, s13, s14\n"
"s_add_u32 s13, s13, 0x20000\n"
"s_add_u32 s62, s30, s13\n"
"s_addc_u32 s63, s31, 0\n"
"v_mfma_f32_16x16x32_bf16 v[52:55], v[20:23], v[144:147], v[52:55]\n"
"ds_read_b128 v[20:23], v218 offset:26624\n"
"v_mfma_f32_16x16x32_bf16 v[56:59], v[152:155], v[140:143], v[56:59]\n"
"ds_read_b128 v[168:171], v218 offset:28672\n"
"v_mfma_f32_16x16x32_bf16 v[64:67], v[152:155], v[144:147], v[64:67]\n"
"ds_read_b128 v[152:155], v218 offset:30720\n"
"v_mfma_f32_16x16x32_bf16 v[68:71], v[156:159], v[140:143], v[68:71]\n"
"v_mfma_f32_16x16x32_bf16 v[72:75], v[156:159], v[144:147], v[72:75]\n"
"s_waitcnt lgkmcnt(3)\n"
"v_mfma_f32_16x16x32_bf16 v[32:35], v[164:167], v[160:163], v[32:35]\n"
"ds_read_b128 v[156:159], v181 offset:24576\n"
"s_lshl2_add_u32 s13, s50, s15\n"
"s_lshl_b32 s13, s13, 16\n"
"s_lshl_b32 s14, s48, 10\n"
"s_add_u32 s13, s13, s14\n"
"s_add_u32 s13, s13, 0\n"
"s_add_u32 s72, s32, s13\n"
"s_addc_u32 s73, s33, 0\n"
"s_add_u32 s74, s34, s13\n"
"s_addc_u32 s75, s35, 0\n"
"s_sub_u32 s74, s74, 0x800\n"
"s_subb_u32 s75, s75, 0\n"
"v_mfma_f32_16x16x32_bf16 v[40:43], v[164:167], v[148:151], v[40:43]\n"
"ds_read_b128 v[164:167], v181 offset:26624\n"
"s_waitcnt lgkmcnt(4)\n"
"v_mfma_f32_16x16x32_bf16 v[44:47], v[20:23], v[160:163], v[44:47]\n"
"ds_read_b128 v[172:175], v219 offset:24576\n"
"v_mfma_f32_16x16x32_bf16 v[52:55], v[20:23], v[148:151], v[52:55]\n"
"ds_read_b128 v[20:23], v219 offset:26624\n"
"s_waitcnt lgkmcnt(5)\n"
"v_mfma_f32_16x16x32_bf16 v[56:59], v[168:171], v[160:163], v[56:59]\n"
"ds_read_b128 v[176:179], v219 offset:28672\n"
"v_mfma_f32_16x16x32_bf16 v[64:67], v[168:171], v[148:151], v[64:67]\n"
"ds_read_b128 v[168:171], v219 offset:30720\n"
"s_add_u32 s13, s49, 0\n"
"s_and_b32 s13, s13, 3\n"
"s_lshl_b32 s13, s13, 7\n"
"s_add_u32 s68, s72, s13\n"
"s_addc_u32 s69, s73, 0\n"
"s_add_u32 s70, s74, s13\n"
"s_addc_u32 s71, s75, 0\n"
"s_waitcnt lgkmcnt(6)\n"
"v_mfma_f32_16x16x32_bf16 v[68:71], v[152:155], v[160:163], v[68:71]\n"
"v_mfma_f32_16x16x32_bf16 v[72:75], v[152:155], v[148:151], v[72:75]\n"
"s_waitcnt lgkmcnt(0)\n"
"s_barrier\n"
"v_mfma_f32_16x16x32_bf16 v[32:35], v[172:175], v[156:159], v[32:35]\n"
"ds_read_b128 v[148:151], v182 offset:49152\n"
"v_mfma_f32_16x16x32_bf16 v[40:43], v[172:175], v[164:167], v[40:43]\n"
"ds_read_b128 v[152:155], v182 offset:51200\n"
"v_mfma_f32_16x16x32_bf16 v[44:47], v[20:23], v[156:159], v[44:47]\n"
"ds_read_b128 v[160:163], v218 offset:49152\n"
"v_mfma_f32_16x16x32_bf16 v[52:55], v[20:23], v[164:167], v[52:55]\n"
"ds_read_b128 v[20:23], v218 offset:51200\n"
"v_lshlrev_b32 v223, 16, v223\n"
"v_lshlrev_b32 v183, 16, v183\n"
"v_add_f32 v223, v223, v183\n"
"v_cvt_pk_bf16_f32 v223, v223, 0\n"
"v_mfma_f32_16x16x32_bf16 v[56:59], v[176:179], v[156:159], v[56:59]\n"
"ds_read_b128 v[172:175], v218 offset:53248\n"
"v_mfma_f32_16x16x32_bf16 v[64:67], v[176:179], v[164:167], v[64:67]\n"
"ds_read_b128 v[176:179], v218 offset:55296\n"
"v_mfma_f32_16x16x32_bf16 v[68:71], v[168:171], v[156:159], v[68:71]\n"
"v_mfma_f32_16x16x32_bf16 v[72:75], v[168:171], v[164:167], v[72:75]\n"
"s_waitcnt lgkmcnt(3)\n"
"v_mfma_f32_16x16x32_bf16 v[32:35], v[160:163], v[148:151], v[32:35]\n"
"ds_read_b128 v[156:159], v181 offset:49152\n"
"s_mul_i32 s97, s15, 0x11e0\n"
"s_add_u32 s97, s97, 0x20200\n"
"v_mfma_f32_16x16x32_bf16 v[40:43], v[160:163], v[152:155], v[40:43]\n"
"ds_read_b128 v[160:163], v181 offset:51200\n"
"s_waitcnt lgkmcnt(4)\n"
"v_mfma_f32_16x16x32_bf16 v[44:47], v[20:23], v[148:151], v[44:47]\n"
"ds_read_b128 v[164:167], v219 offset:49152\n"
"v_mfma_f32_16x16x32_bf16 v[52:55], v[20:23], v[152:155], v[52:55]\n"
"ds_read_b128 v[20:23], v219 offset:51200\n"
"s_waitcnt lgkmcnt(5)\n"
"v_mfma_f32_16x16x32_bf16 v[56:59], v[172:175], v[148:151], v[56:59]\n"
"ds_read_b128 v[168:171], v219 offset:53248\n"
"v_mfma_f32_16x16x32_bf16 v[64:67], v[172:175], v[152:155], v[64:67]\n"
"ds_read_b128 v[172:175], v219 offset:55296\n"
"s_waitcnt lgkmcnt(6)\n"
"v_mfma_f32_16x16x32_bf16 v[68:71], v[176:179], v[148:151], v[68:71]\n"
"v_bfe_u32 v184, v228, 1, 3\n"
"v_bfe_u32 v237, v228, 4, 1\n"
"v_mfma_f32_16x16x32_bf16 v[72:75], v[176:179], v[152:155], v[72:75]\n"
"v_xor_b32 v184, v184, v237\n"
"v_lshrrev_b32 v237, 1, v228\n"
"s_waitcnt lgkmcnt(0)\n"
"s_barrier\n"
"v_mfma_f32_16x16x32_bf16 v[32:35], v[164:167], v[156:159], v[32:35]\n"
"ds_read_b128 v[176:179], v180 offset:8192\n"
"v_and_b32 v237, 16, v237\n"
"v_and_or_b32 v237, v228, 15, v237\n"
"v_mfma_f32_16x16x32_bf16 v[40:43], v[164:167], v[160:163], v[40:43]\n"
"ds_read_b128 v[164:167], v180 offset:10240\n"
"v_lshlrev_b32 v237, 7, v237\n"
"v_mfma_f32_16x16x32_bf16 v[44:47], v[20:23], v[156:159], v[44:47]\n"
"ds_read_b128 v[180:183], v235 offset:8192\n"
"v_lshl_or_b32 v237, v184, 4, v237\n"
"v_add_u32 v237, s56, v237\n"
"v_mfma_f32_16x16x32_bf16 v[52:55], v[20:23], v[160:163], v[52:55]\n"
"ds_read_b128 v[20:23], v235 offset:10240\n"
"v_xor_b32 v238, 32, v237\n"
"v_xor_b32 v239, 64, v237\n"
"v_mfma_f32_16x16x32_bf16 v[56:59], v[168:171], v[156:159], v[56:59]\n"
"ds_read_b128 v[184:187], v235 offset:12288\n"
"v_xor_b32 v240, 0x60, v237\n"
"v_mfma_f32_16x16x32_bf16 v[64:67], v[168:171], v[160:163], v[64:67]\n"
"ds_read_b128 v[168:171], v235 offset:14336\n"
"v_and_b32 v192, 15, v228\n"
"v_lshrrev_b32 v189, 4, v228\n"
"v_mfma_f32_16x16x32_bf16 v[68:71], v[172:175], v[156:159], v[68:71]\n"
"v_lshl_add_u32 v229, v189, 3, v192\n"
"v_lshlrev_b32 v230, 4, v189\n"
"v_mfma_f32_16x16x32_bf16 v[72:75], v[172:175], v[160:163], v[72:75]\n"
"v_lshlrev_b32 v189, 5, v192\n"
"v_sub_u32 v230, v230, v189\n"
"s_waitcnt lgkmcnt(3)\n"
"v_mfma_f32_16x16x32_bf16 v[32:35], v[180:183], v[176:179], v[32:35]\n"
"ds_read_b128 v[172:175], v188 offset:8192\n"
"v_add_u32 v230, s97, v230\n"
"v_mfma_f32_16x16x32_bf16 v[40:43], v[180:183], v[164:167], v[40:43]\n"
"ds_read_b128 v[180:183], v188 offset:10240\n"
"v_add_u32 v230, 0x1e0, v230\n"
"v_and_b32 v192, 1, v192\n"
"s_waitcnt lgkmcnt(4)\n"
"v_mfma_f32_16x16x32_bf16 v[44:47], v[20:23], v[176:179], v[44:47]\n"
"ds_read_b128 v[188:191], v234 offset:8192\n"
"v_mul_u32_u24 v192, 0x103e, v192\n"
"v_lshlrev_b32 v229, 1, v229\n"
"v_mfma_f32_16x16x32_bf16 v[52:55], v[20:23], v[164:167], v[52:55]\n"
"ds_read_b128 v[20:23], v234 offset:10240\n"
"v_sub_u32 v229, v229, v192\n"
"s_waitcnt lgkmcnt(5)\n"
"v_mfma_f32_16x16x32_bf16 v[56:59], v[184:187], v[176:179], v[56:59]\n"
"ds_read_b128 v[192:195], v234 offset:12288\n"
"v_add_u32 v246, 0x20000, v218\n"
"v_add_u32 v245, 0x20000, v219\n"
"v_add_u32 v229, s57, v229\n"
"v_add_u32 v229, 0x1040, v229\n"
"v_mfma_f32_16x16x32_bf16 v[64:67], v[184:187], v[164:167], v[64:67]\n"
"ds_read_b128 v[184:187], v234 offset:14336\n"
"v_add_u32 v231, 0x400, v229\n"
"v_add_u32 v232, 0x800, v229\n"
"s_waitcnt lgkmcnt(6)\n"
"v_mfma_f32_16x16x32_bf16 v[68:71], v[168:171], v[176:179], v[68:71]\n"
"v_add_u32 v233, 0xc00, v229\n"
"v_mfma_f32_16x16x32_bf16 v[72:75], v[168:171], v[164:167], v[72:75]\n"
"s_waitcnt lgkmcnt(3)\n"
"v_mfma_f32_16x16x32_bf16 v[32:35], v[188:191], v[172:175], v[32:35]\n"
"ds_read_b128 v[164:167], v246 offset:512\n"
"v_mfma_f32_16x16x32_bf16 v[40:43], v[188:191], v[180:183], v[40:43]\n"
"ds_read_b128 v[168:171], v246 offset:5088\n"
"s_waitcnt lgkmcnt(4)\n"
"v_mfma_f32_16x16x32_bf16 v[44:47], v[20:23], v[172:175], v[44:47]\n"
"ds_read_b128 v[176:179], v246 offset:9664\n"
"v_mfma_f32_16x16x32_bf16 v[52:55], v[20:23], v[180:183], v[52:55]\n"
"ds_read_b128 v[20:23], v246 offset:14240\n"
"s_waitcnt lgkmcnt(5)\n"
"v_mfma_f32_16x16x32_bf16 v[56:59], v[192:195], v[172:175], v[56:59]\n"
"ds_read_b128 v[188:191], v222\n"
"v_mfma_f32_16x16x32_bf16 v[64:67], v[192:195], v[180:183], v[64:67]\n"
"ds_read_b128 v[192:195], v222 offset:256\n"
"s_waitcnt lgkmcnt(6)\n"
"v_mfma_f32_16x16x32_bf16 v[68:71], v[184:187], v[172:175], v[68:71]\n"
"ds_read_b128 v[172:175], v221\n"
"v_mfma_f32_16x16x32_bf16 v[72:75], v[184:187], v[180:183], v[72:75]\n"
"s_waitcnt lgkmcnt(6)\n"
"v_mfma_f32_16x16x32_bf16 v[180:183], v[164:167], v[8:11], 0\n"
"v_mfma_f32_16x16x32_bf16 v[184:187], v[164:167], v[12:15], 0\n"
"ds_read_b128 v[164:167], v245 offset:512\n"
"s_waitcnt lgkmcnt(6)\n"
"v_mfma_f32_16x16x32_bf16 v[196:199], v[168:171], v[8:11], 0\n"
"v_mfma_f32_16x16x32_bf16 v[200:203], v[168:171], v[12:15], 0\n"
"ds_read_b128 v[168:171], v245 offset:5088\n"
"s_waitcnt lgkmcnt(6)\n"
"v_mfma_f32_16x16x32_bf16 v[204:207], v[176:179], v[8:11], 0\n"
"s_waitcnt lgkmcnt(4)\n"
"v_fma_f32 v247, v188, v32, v33\n"
"v_fma_f32 v249, v188, v40, v41\n"
"v_mfma_f32_16x16x32_bf16 v[208:211], v[176:179], v[12:15], 0\n"
"ds_read_b128 v[176:179], v245 offset:9664\n"
"v_mfma_f32_16x16x32_bf16 v[212:215], v[20:23], v[8:11], 0\n"
"v_fma_f32 v247, -v189, v40, v247\n"
"v_fmac_f32 v249, v189, v32\n"
"v_mfma_f32_16x16x32_bf16 v[8:11], v[20:23], v[12:15], 0\n"
"ds_read_b128 v[12:15], v245 offset:14240\n"
"s_waitcnt lgkmcnt(3)\n"
"v_mfma_f32_16x16x32_bf16 v[180:183], v[164:167], v[36:39], v[180:183]\n"
"v_fma_f32 v248, v188, v247, v34\n"
"v_fma_f32 v250, v188, v249, v42\n"
"v_mfma_f32_16x16x32_bf16 v[184:187], v[164:167], v[16:19], v[184:187]\n"
"ds_read_b128 v[20:23], v246 offset:2560\n"
"s_waitcnt lgkmcnt(3)\n"
"v_mfma_f32_16x16x32_bf16 v[196:199], v[168:171], v[36:39], v[196:199]\n"
"v_fma_f32 v248, -v189, v249, v248\n"
"v_fmac_f32 v250, v189, v247\n"
"v_mfma_f32_16x16x32_bf16 v[200:203], v[168:171], v[16:19], v[200:203]\n"
"ds_read_b128 v[164:167], v246 offset:7136\n"
"s_waitcnt lgkmcnt(3)\n"
"v_mfma_f32_16x16x32_bf16 v[204:207], v[176:179], v[36:39], v[204:207]\n"
"v_fma_f32 v247, v188, v248, v35\n"
"v_fma_f32 v249, v188, v250, v43\n"
"v_mfma_f32_16x16x32_bf16 v[208:211], v[176:179], v[16:19], v[208:211]\n"
"ds_read_b128 v[168:171], v246 offset:11712\n"
"s_waitcnt lgkmcnt(3)\n"
"v_mfma_f32_16x16x32_bf16 v[212:215], v[12:15], v[36:39], v[212:215]\n"
"v_fma_f32 v247, -v189, v250, v247\n"
"v_fmac_f32 v249, v189, v248\n"
"v_mfma_f32_16x16x32_bf16 v[8:11], v[12:15], v[16:19], v[8:11]\n"
"ds_read_b128 v[12:15], v246 offset:16288\n"
"s_waitcnt lgkmcnt(3)\n"
"v_mfma_f32_16x16x32_bf16 v[180:183], v[20:23], v[28:31], v[180:183]\n"
"ds_read_b128 v[16:19], v245 offset:2560\n"
"v_fma_f32 v176, v188, v247, v44\n"
"v_fma_f32 v178, v188, v249, v52\n"
"v_mfma_f32_16x16x32_bf16 v[184:187], v[20:23], v[48:51], v[184:187]\n"
"ds_read_b128 v[20:23], v245 offset:7136\n"
"s_waitcnt lgkmcnt(4)\n"
"v_mfma_f32_16x16x32_bf16 v[196:199], v[164:167], v[28:31], v[196:199]\n"
"v_fma_f32 v176, -v189, v249, v176\n"
"v_fmac_f32 v178, v189, v247\n"
"v_mfma_f32_16x16x32_bf16 v[200:203], v[164:167], v[48:51], v[200:203]\n"
"ds_read_b128 v[36:39], v245 offset:11712\n"
"s_waitcnt lgkmcnt(4)\n"
"s_barrier\n"
"v_mfma_f32_16x16x32_bf16 v[204:207], v[168:171], v[28:31], v[204:207]\n"
"ds_read_b128 v[164:167], v245 offset:16288\n"
"v_fma_f32 v177, v188, v176, v45\n"
"v_fma_f32 v179, v188, v178, v53\n"
"v_mfma_f32_16x16x32_bf16 v[208:211], v[168:171], v[48:51], v[208:211]\n"
"ds_read_b128 v[168:171], v235 offset:36928\n"
"s_waitcnt lgkmcnt(5)\n"
"v_mfma_f32_16x16x32_bf16 v[212:215], v[12:15], v[28:31], v[212:215]\n"
"v_fma_f32 v177, -v189, v178, v177\n"
"v_fmac_f32 v179, v189, v176\n"
"v_mfma_f32_16x16x32_bf16 v[8:11], v[12:15], v[48:51], v[8:11]\n"
"ds_read_b128 v[12:15], v235 offset:45248\n"
"s_waitcnt lgkmcnt(5)\n"
"v_mfma_f32_16x16x32_bf16 v[180:183], v[16:19], v[24:27], v[180:183]\n"
"ds_read_b128 v[28:31], v235 offset:53568\n"
"v_fma_f32 v176, v188, v177, v46\n"
"v_fma_f32 v178, v188, v179, v54\n"
"v_mfma_f32_16x16x32_bf16 v[184:187], v[16:19], v[60:63], v[184:187]\n"
"ds_read_b128 v[16:19], v235 offset:61888\n"
"s_waitcnt lgkmcnt(6)\n"
"v_mfma_f32_16x16x32_bf16 v[196:199], v[20:23], v[24:27], v[196:199]\n"
"v_fma_f32 v176, -v189, v179, v176\n"
"v_fmac_f32 v178, v189, v177\n"
"v_mfma_f32_16x16x32_bf16 v[200:203], v[20:23], v[60:63], v[200:203]\n"
"ds_read_b128 v[20:23], v234 offset:36928\n"
"s_waitcnt lgkmcnt(5)\n"
"v_mfma_f32_16x16x32_bf16 v[204:207], v[36:39], v[24:27], v[204:207]\n"
"ds_read_b128 v[48:51], v234 offset:45248\n"
"v_fma_f32 v177, v188, v176, v47\n"
"v_fma_f32 v179, v188, v178, v55\n"
"v_mfma_f32_16x16x32_bf16 v[208:211], v[36:39], v[60:63], v[208:211]\n"
"ds_read_b128 v[36:39], v234 offset:53568\n"
"v_fma_f32 v177, -v189, v178, v177\n"
"v_fmac_f32 v179, v189, v176\n"
"v_mfma_f32_16x16x32_bf16 v[212:215], v[164:167], v[24:27], v[212:215]\n"
"v_fma_f32 v176, v188, v177, v56\n"
"v_fma_f32 v178, v188, v179, v64\n"
"v_mfma_f32_16x16x32_bf16 v[8:11], v[164:167], v[60:63], v[8:11]\n"
"ds_read_b128 v[24:27], v234 offset:61888\n"
"s_waitcnt lgkmcnt(6)\n"
"v_mfma_f32_16x16x32_bf16 v[180:183], v[168:171], v[76:79], v[180:183]\n"
"v_fma_f32 v176, -v189, v179, v176\n"
"v_fmac_f32 v178, v189, v177\n"
"v_mfma_f32_16x16x32_bf16 v[184:187], v[168:171], v[80:83], v[184:187]\n"
"ds_read_b128 v[60:63], v235 offset:38976\n"
"v_fma_f32 v164, v188, v176, v57\n"
"v_fma_f32 v166, v188, v178, v65\n"
"v_mfma_f32_16x16x32_bf16 v[196:199], v[12:15], v[76:79], v[196:199]\n"
"v_fma_f32 v164, -v189, v178, v164\n"
"v_fmac_f32 v166, v189, v176\n"
"v_mfma_f32_16x16x32_bf16 v[200:203], v[12:15], v[80:83], v[200:203]\n"
"ds_read_b128 v[12:15], v235 offset:47296\n"
"s_waitcnt lgkmcnt(6)\n"
"v_mfma_f32_16x16x32_bf16 v[204:207], v[28:31], v[76:79], v[204:207]\n"
"v_fma_f32 v165, v188, v164, v58\n"
"v_fma_f32 v167, v188, v166, v66\n"
"v_mfma_f32_16x16x32_bf16 v[208:211], v[28:31], v[80:83], v[208:211]\n"
"ds_read_b128 v[28:31], v235 offset:55616\n"
"v_fma_f32 v165, -v189, v166, v165\n"
"v_fmac_f32 v167, v189, v164\n"
"v_mfma_f32_16x16x32_bf16 v[212:215], v[16:19], v[76:79], v[212:215]\n"
"v_fma_f32 v76, v188, v165, v59\n"
"v_fma_f32 v78, v188, v167, v67\n"
"v_mfma_f32_16x16x32_bf16 v[8:11], v[16:19], v[80:83], v[8:11]\n"
"ds_read_b128 v[16:19], v235 offset:63936\n"
"s_waitcnt lgkmcnt(6)\n"
"v_mfma_f32_16x16x32_bf16 v[180:183], v[20:23], v[84:87], v[180:183]\n"
"v_fma_f32 v76, -v189, v167, v76\n"
"v_fmac_f32 v78, v189, v165\n"
"v_mfma_f32_16x16x32_bf16 v[184:187], v[20:23], v[88:91], v[184:187]\n"
"ds_read_b128 v[20:23], v234 offset:38976\n"
"v_fma_f32 v77, v188, v76, v68\n"
"v_fma_f32 v79, v188, v78, v72\n"
"v_mfma_f32_16x16x32_bf16 v[196:199], v[48:51], v[84:87], v[196:199]\n"
"v_fma_f32 v77, -v189, v78, v77\n"
"v_fmac_f32 v79, v189, v76\n"
"v_mfma_f32_16x16x32_bf16 v[200:203], v[48:51], v[88:91], v[200:203]\n"
"ds_read_b128 v[48:51], v234 offset:47296\n"
"s_waitcnt lgkmcnt(6)\n"
"v_mfma_f32_16x16x32_bf16 v[204:207], v[36:39], v[84:87], v[204:207]\n"
"v_fma_f32 v76, v188, v77, v69\n"
"v_fma_f32 v80, v188, v79, v73\n"
"v_mfma_f32_16x16x32_bf16 v[208:211], v[36:39], v[88:91], v[208:211]\n"
"ds_read_b128 v[36:39], v234 offset:55616\n"
"v_fma_f32 v76, -v189, v79, v76\n"
"v_fmac_f32 v80, v189, v77\n"
"v_mfma_f32_16x16x32_bf16 v[212:215], v[24:27], v[84:87], v[212:215]\n"
"v_fma_f32 v78, v188, v76, v70\n"
"v_fma_f32 v79, v188, v80, v74\n"
"v_mfma_f32_16x16x32_bf16 v[8:11], v[24:27], v[88:91], v[8:11]\n"
"ds_read_b128 v[24:27], v234 offset:63936\n"
"s_waitcnt lgkmcnt(6)\n"
"v_mfma_f32_16x16x32_bf16 v[180:183], v[60:63], v[92:95], v[180:183]\n"
"v_fma_f32 v78, -v189, v80, v78\n"
"v_fmac_f32 v79, v189, v76\n"
"v_mfma_f32_16x16x32_bf16 v[184:187], v[60:63], v[0:3], v[184:187]\n"
"ds_read_b128 v[60:63], v218\n"
"v_fma_f32 v76, v188, v78, v71\n"
"v_fma_f32 v77, v188, v79, v75\n"
"v_mfma_f32_16x16x32_bf16 v[196:199], v[12:15], v[92:95], v[196:199]\n"
"v_fma_f32 v76, -v189, v79, v76\n"
"v_fmac_f32 v77, v189, v78\n"
"v_mfma_f32_16x16x32_bf16 v[200:203], v[12:15], v[0:3], v[200:203]\n"
"ds_read_b128 v[12:15], v218 offset:2048\n"
"s_waitcnt lgkmcnt(6)\n"
"v_mfma_f32_16x16x32_bf16 v[204:207], v[28:31], v[92:95], v[204:207]\n"
"v_mov_b32 v79, v76\n"
"v_mov_b32 v80, v77\n"
"v_mfma_f32_16x16x32_bf16 v[208:211], v[28:31], v[0:3], v[208:211]\n"
"ds_read_b128 v[28:31], v218 offset:4096\n"
"s_waitcnt lgkmcnt(3)\n"
"s_barrier\n"
"v_mfma_f32_16x16x32_bf16 v[212:215], v[16:19], v[92:95], v[212:215]\n"
"v_mul_f32 v78, v190, v32\n"
"v_fma_f32 v78, -v191, v40, v78\n"
"v_mfma_f32_16x16x32_bf16 v[8:11], v[16:19], v[0:3], v[8:11]\n"
"ds_read_b128 v[0:3], v218 offset:6144\n"
"v_mfma_f32_16x16x32_bf16 v[180:183], v[20:23], v[100:103], v[180:183]\n"
"v_permlane16_swap_b32 v76, v79\n"
"v_permlane16_swap_b32 v77, v80\n"
"v_mfma_f32_16x16x32_bf16 v[184:187], v[20:23], v[96:99], v[184:187]\n"
"ds_read_b128 v[16:19], v219\n"
"v_fmac_f32 v79, v192, v76\n"
"v_fmac_f32 v80, v192, v77\n"
"v_mfma_f32_16x16x32_bf16 v[196:199], v[48:51], v[100:103], v[196:199]\n"
"v_fma_f32 v79, -v193, v77, v79\n"
"v_fmac_f32 v80, v193, v76\n"
"v_mfma_f32_16x16x32_bf16 v[200:203], v[48:51], v[96:99], v[200:203]\n"
"ds_read_b128 v[20:23], v219 offset:2048\n"
"v_mfma_f32_16x16x32_bf16 v[204:207], v[36:39], v[100:103], v[204:207]\n"
"v_mov_b32 v81, v79\n"
"v_mov_b32 v82, v80\n"
"v_mfma_f32_16x16x32_bf16 v[208:211], v[36:39], v[96:99], v[208:211]\n"
"ds_read_b128 v[36:39], v219 offset:4096\n"
"v_mul_f32_e64 v40, -v190, v40\n"
"v_fma_f32 v40, -v191, v32, v40\n"
"v_mfma_f32_16x16x32_bf16 v[212:215], v[24:27], v[100:103], v[212:215]\n"
"v_permlane32_swap_b32 v79, v81\n"
"v_permlane32_swap_b32 v80, v82\n"
"v_mfma_f32_16x16x32_bf16 v[8:11], v[24:27], v[96:99], v[8:11]\n"
"ds_read_b128 v[24:27], v219 offset:6144\n"
"s_waitcnt lgkmcnt(6)\n"
"v_mfma_f32_16x16x32_bf16 v[180:183], v[60:63], v[108:111], v[180:183]\n"
"v_fmac_f32 v81, v194, v79\n"
"v_fmac_f32 v82, v194, v80\n"
"v_mfma_f32_16x16x32_bf16 v[184:187], v[60:63], v[112:115], v[184:187]\n"
"ds_read_b128 v[48:51], v218 offset:8192\n"
"v_fma_f32 v81, -v195, v80, v81\n"
"v_fmac_f32 v82, v195, v79\n"
"v_mfma_f32_16x16x32_bf16 v[196:199], v[12:15], v[108:111], v[196:199]\n"
"v_bfi_b32 v60, s96, v236, v81\n"
"v_bfi_b32 v61, s96, v236, v82\n"
"v_mfma_f32_16x16x32_bf16 v[200:203], v[12:15], v[112:115], v[200:203]\n"
"ds_read_b128 v[12:15], v218 offset:10240\n"
"s_waitcnt lgkmcnt(6)\n"
"v_mfma_f32_16x16x32_bf16 v[204:207], v[28:31], v[108:111], v[204:207]\n"
"s_setprio 3\n"
"global_store_dwordx2 v227, v[60:61], s[64:65] offset:2048 sc1\n"
"s_setprio 0\n"
"v_mfma_f32_16x16x32_bf16 v[208:211], v[28:31], v[112:115], v[208:211]\n"
"ds_read_b128 v[28:31], v218 offset:12288\n"
"v_mfma_f32_16x16x32_bf16 v[212:215], v[0:3], v[108:111], v[212:215]\n"
"v_mul_f32 v76, v174, v76\n"
"v_mul_f32 v77, v174, v77\n"
"v_mfma_f32_16x16x32_bf16 v[8:11], v[0:3], v[112:115], v[8:11]\n"
"ds_read_b128 v[0:3], v218 offset:14336\n"
"s_waitcnt lgkmcnt(6)\n"
"v_mfma_f32_16x16x32_bf16 v[180:183], v[16:19], v[104:107], v[180:183]\n"
"v_fmac_f32 v76, v172, v79\n"
"v_fmac_f32 v77, v172, v80\n"
"v_mfma_f32_16x16x32_bf16 v[184:187], v[16:19], v[116:119], v[184:187]\n"
"ds_read_b128 v[16:19], v219 offset:8192\n"
"v_mfma_f32_16x16x32_bf16 v[196:199], v[20:23], v[104:107], v[196:199]\n"
"v_fma_f32 v76, -v173, v80, v76\n"
"v_fmac_f32 v77, v173, v79\n"
"v_mfma_f32_16x16x32_bf16 v[200:203], v[20:23], v[116:119], v[200:203]\n"
"ds_read_b128 v[20:23], v219 offset:10240\n"
"s_waitcnt lgkmcnt(6)\n"
"v_mfma_f32_16x16x32_bf16 v[204:207], v[36:39], v[104:107], v[204:207]\n"
"v_mul_f32 v60, v190, v33\n"
"v_fma_f32 v60, -v191, v41, v60\n"
"v_mfma_f32_16x16x32_bf16 v[208:211], v[36:39], v[116:119], v[208:211]\n"
"ds_read_b128 v[36:39], v219 offset:12288\n"
"v_mfma_f32_16x16x32_bf16 v[212:215], v[24:27], v[104:107], v[212:215]\n"
"v_mul_f32_e64 v41, -v190, v41\n"
"v_fma_f32 v41, -v191, v33, v41\n"
"v_mfma_f32_16x16x32_bf16 v[8:11], v[24:27], v[116:119], v[8:11]\n"
"ds_read_b128 v[24:27], v219 offset:14336\n"
"s_waitcnt lgkmcnt(6)\n"
"v_mfma_f32_16x16x32_bf16 v[180:183], v[48:51], v[120:123], v[180:183]\n"
"v_mul_f32 v79, v190, v34\n"
"v_fma_f32 v79, -v191, v42, v79\n"
"v_mfma_f32_16x16x32_bf16 v[184:187], v[48:51], v[124:127], v[184:187]\n"
"ds_read_b128 v[48:51], v218 offset:16384\n"
"v_mfma_f32_16x16x32_bf16 v[196:199], v[12:15], v[120:123], v[196:199]\n"
"v_mul_f32_e64 v42, -v190, v42\n"
"v_fma_f32 v42, -v191, v34, v42\n"
"v_mfma_f32_16x16x32_bf16 v[200:203], v[12:15], v[124:127], v[200:203]\n"
"ds_read_b128 v[12:15], v218 offset:18432\n"
"s_waitcnt lgkmcnt(6)\n"
"v_mfma_f32_16x16x32_bf16 v[204:207], v[28:31], v[120:123], v[204:207]\n"
"v_mul_f32 v80, v190, v35\n"
"v_fma_f32 v80, -v191, v43, v80\n"
"v_mfma_f32_16x16x32_bf16 v[208:211], v[28:31], v[124:127], v[208:211]\n"
"ds_read_b128 v[28:31], v218 offset:20480\n"
"v_mfma_f32_16x16x32_bf16 v[212:215], v[0:3], v[120:123], v[212:215]\n"
"v_mul_f32_e64 v43, -v190, v43\n"
"v_fma_f32 v43, -v191, v35, v43\n"
"v_mfma_f32_16x16x32_bf16 v[8:11], v[0:3], v[124:127], v[8:11]\n"
"ds_read_b128 v[0:3], v218 offset:22528\n"
"s_waitcnt lgkmcnt(6)\n"
"v_mfma_f32_16x16x32_bf16 v[180:183], v[16:19], v[4:7], v[180:183]\n"
"v_mul_f32 v81, v190, v44\n"
"v_fma_f32 v81, -v191, v52, v81\n"
"v_mfma_f32_16x16x32_bf16 v[184:187], v[16:19], v[128:131], v[184:187]\n"
"ds_read_b128 v[16:19], v219 offset:16384\n"
"v_mfma_f32_16x16x32_bf16 v[196:199], v[20:23], v[4:7], v[196:199]\n"
"v_mul_f32_e64 v52, -v190, v52\n"
"v_fma_f32 v52, -v191, v44, v52\n"
"v_mfma_f32_16x16x32_bf16 v[200:203], v[20:23], v[128:131], v[200:203]\n"
"ds_read_b128 v[20:23], v219 offset:18432\n"
"s_waitcnt lgkmcnt(6)\n"
"v_mfma_f32_16x16x32_bf16 v[204:207], v[36:39], v[4:7], v[204:207]\n"
"v_mul_f32 v82, v190, v45\n"
"v_fma_f32 v82, -v191, v53, v82\n"
"v_mfma_f32_16x16x32_bf16 v[208:211], v[36:39], v[128:131], v[208:211]\n"
"ds_read_b128 v[32:35], v219 offset:20480\n"
"v_mfma_f32_16x16x32_bf16 v[212:215], v[24:27], v[4:7], v[212:215]\n"
"v_mul_f32_e64 v53, -v190, v53\n"
"v_fma_f32 v53, -v191, v45, v53\n"
"v_mfma_f32_16x16x32_bf16 v[8:11], v[24:27], v[128:131], v[8:11]\n"
"ds_read_b128 v[4:7], v219 offset:22528\n"
"s_waitcnt lgkmcnt(6)\n"
"v_mfma_f32_16x16x32_bf16 v[180:183], v[48:51], v[132:135], v[180:183]\n"
"v_mul_f32 v84, v190, v46\n"
"v_fma_f32 v84, -v191, v54, v84\n"
"v_mfma_f32_16x16x32_bf16 v[184:187], v[48:51], v[136:139], v[184:187]\n"
"ds_read_b128 v[24:27], v218 offset:24576\n"
"v_mfma_f32_16x16x32_bf16 v[196:199], v[12:15], v[132:135], v[196:199]\n"
"v_mul_f32_e64 v54, -v190, v54\n"
"v_fma_f32 v54, -v191, v46, v54\n"
"v_mfma_f32_16x16x32_bf16 v[200:203], v[12:15], v[136:139], v[200:203]\n"
"ds_read_b128 v[12:15], v219 offset:24576\n"
"s_waitcnt lgkmcnt(6)\n"
"v_mfma_f32_16x16x32_bf16 v[204:207], v[28:31], v[132:135], v[204:207]\n"
"v_add_u32 v36, s56, v226\n"
"v_lshrrev_b32 v37, 4, v228\n"
"v_mfma_f32_16x16x32_bf16 v[208:211], v[28:31], v[136:139], v[208:211]\n"
"ds_read_b128 v[28:31], v218 offset:26624\n"
"v_mfma_f32_16x16x32_bf16 v[212:215], v[0:3], v[132:135], v[212:215]\n"
"v_xor_b32 v37, v37, v228\n"
"v_and_b32 v37, 7, v37\n"
"v_mfma_f32_16x16x32_bf16 v[8:11], v[0:3], v[136:139], v[8:11]\n"
"ds_read_b128 v[0:3], v219 offset:26624\n"
"s_waitcnt lgkmcnt(6)\n"
"v_mfma_f32_16x16x32_bf16 v[180:183], v[16:19], v[140:143], v[180:183]\n"
"v_lshlrev_b32 v37, 4, v37\n"
"v_lshrrev_b32 v85, 3, v228\n"
"v_mfma_f32_16x16x32_bf16 v[184:187], v[16:19], v[144:147], v[184:187]\n"
"ds_read_b128 v[16:19], v218 offset:28672\n"
"v_mfma_f32_16x16x32_bf16 v[196:199], v[20:23], v[140:143], v[196:199]\n"
"v_lshl_or_b32 v85, v85, 13, v37\n"
"v_xor_b32 v86, 0xc40, v85\n"
"v_mfma_f32_16x16x32_bf16 v[200:203], v[20:23], v[144:147], v[200:203]\n"
"ds_read_b128 v[20:23], v219 offset:28672\n"
"s_waitcnt lgkmcnt(6)\n"
"v_mfma_f32_16x16x32_bf16 v[204:207], v[32:35], v[140:143], v[204:207]\n"
"v_add_u32 v97, 0x800, v85\n"
"v_add_u32 v96, 0x800, v86\n"
"v_mfma_f32_16x16x32_bf16 v[208:211], v[32:35], v[144:147], v[208:211]\n"
"ds_read_b128 v[32:35], v218 offset:30720\n"
"v_mfma_f32_16x16x32_bf16 v[212:215], v[4:7], v[140:143], v[212:215]\n"
"v_mul_f32 v89, v190, v47\n"
"v_fma_f32 v89, -v191, v55, v89\n"
"v_mfma_f32_16x16x32_bf16 v[8:11], v[4:7], v[144:147], v[8:11]\n"
"ds_read_b128 v[4:7], v219 offset:30720\n"
"s_waitcnt lgkmcnt(6)\n"
"v_mfma_f32_16x16x32_bf16 v[180:183], v[24:27], v[148:151], v[180:183]\n"
"v_mul_f32_e64 v55, -v190, v55\n"
"v_fma_f32 v55, -v191, v47, v55\n"
"v_mfma_f32_16x16x32_bf16 v[184:187], v[24:27], v[152:155], v[184:187]\n"
"v_mul_f32 v87, v190, v56\n"
"v_fma_f32 v87, -v191, v64, v87\n"
"v_mfma_f32_16x16x32_bf16 v[180:183], v[12:15], v[156:159], v[180:183]\n"
"v_mul_f32_e64 v64, -v190, v64\n"
"v_fma_f32 v64, -v191, v56, v64\n"
"v_mfma_f32_16x16x32_bf16 v[184:187], v[12:15], v[160:163], v[184:187]\n"
"s_waitcnt lgkmcnt(4)\n"
"v_mfma_f32_16x16x32_bf16 v[196:199], v[28:31], v[148:151], v[196:199]\n"
"v_mul_f32 v88, v190, v57\n"
"v_fma_f32 v88, -v191, v65, v88\n"
"v_mfma_f32_16x16x32_bf16 v[200:203], v[28:31], v[152:155], v[200:203]\n"
"s_waitcnt lgkmcnt(0)\n"
"s_barrier\n"
"v_mfma_f32_16x16x32_bf16 v[196:199], v[0:3], v[156:159], v[196:199]\n"
"v_mul_f32_e64 v65, -v190, v65\n"
"v_fma_f32 v65, -v191, v57, v65\n"
"v_mfma_f32_16x16x32_bf16 v[200:203], v[0:3], v[160:163], v[200:203]\n"
"v_mfma_f32_16x16x32_bf16 v[204:207], v[16:19], v[148:151], v[204:207]\n"
"ds_write_b128 v36, v[180:183]\n"
"v_mfma_f32_16x16x32_bf16 v[208:211], v[16:19], v[152:155], v[208:211]\n"
"v_mul_f32 v98, v190, v58\n"
"v_fma_f32 v98, -v191, v66, v98\n"
"v_mfma_f32_16x16x32_bf16 v[204:207], v[20:23], v[156:159], v[204:207]\n"
"ds_write_b128 v36, v[184:187] offset:1024\n"
"v_mfma_f32_16x16x32_bf16 v[208:211], v[20:23], v[160:163], v[208:211]\n"
"v_mfma_f32_16x16x32_bf16 v[212:215], v[32:35], v[148:151], v[212:215]\n"
"ds_write_b128 v36, v[196:199] offset:2048\n"
"v_mfma_f32_16x16x32_bf16 v[8:11], v[32:35], v[152:155], v[8:11]\n"
"v_mul_f32_e64 v66, -v190, v66\n"
"v_fma_f32 v66, -v191, v58, v66\n"
"v_mfma_f32_16x16x32_bf16 v[212:215], v[4:7], v[156:159], v[212:215]\n"
"ds_write_b128 v36, v[200:203] offset:3072\n"
"v_mfma_f32_16x16x32_bf16 v[8:11], v[4:7], v[160:163], v[8:11]\n"
"v_mul_f32 v99, v190, v59\n"
"v_fma_f32 v99, -v191, v67, v99\n"
"ds_write_b128 v36, v[204:207] offset:16384\n"
"ds_write_b128 v36, v[208:211] offset:17408\n"
"s_nop 1\n"
"ds_write_b128 v36, v[212:215] offset:18432\n"
"s_nop 0\n"
"ds_write_b128 v36, v[8:11] offset:19456\n"
"s_waitcnt lgkmcnt(0)\n"
"s_barrier\n"
"buffer_load_dwordx2 v[28:29], v220, s[76:79], 0 offen offset:3584 sc1\n"
"buffer_load_dwordx2 v[26:27], v220, s[76:79], 0 offen offset:3072 sc1\n"
"buffer_load_dwordx2 v[24:25], v220, s[76:79], 0 offen offset:2560 sc1\n"
"buffer_load_dwordx2 v[44:45], v220, s[76:79], 0 offen offset:2048 sc1\n"
"v_mul_f32_e64 v67, -v190, v67\n"
"v_fma_f32 v67, -v191, v59, v67\n"
"v_mul_f32 v100, v190, v68\n"
"v_fma_f32 v100, -v191, v72, v100\n"
"s_barrier\n"
"v_mul_f32_e64 v72, -v190, v72\n"
"v_fma_f32 v72, -v191, v68, v72\n"
"v_mul_f32 v101, v190, v69\n"
"v_fma_f32 v101, -v191, v73, v101\n"
"s_add_u32 m0, s56, 0\n"
"s_nop 0\n"
"global_load_lds_dwordx4 v85, s[68:69]\n"
"global_load_lds_dwordx4 v86, s[68:69] offset:1024\n"
"v_mul_f32_e64 v73, -v190, v73\n"
"v_fma_f32 v73, -v191, v69, v73\n"
"global_load_lds_dwordx4 v85, s[70:71] offset:2048\n"
"v_mul_f32 v116, v190, v70\n"
"v_fma_f32 v116, -v191, v74, v116\n"
"global_load_lds_dwordx4 v86, s[70:71] offset:3072\n"
"v_mul_f32_e64 v74, -v190, v74\n"
"v_fma_f32 v74, -v191, v70, v74\n"
"v_mul_f32 v117, v190, v71\n"
"v_fma_f32 v117, -v191, v75, v117\n"
"s_add_u32 m0, s56, 0x4000\n"
"s_nop 0\n"
"global_load_lds_dwordx4 v97, s[68:69]\n"
"global_load_lds_dwordx4 v96, s[68:69] offset:1024\n"
"v_mul_f32_e64 v75, -v190, v75\n"
"v_fma_f32 v75, -v191, v71, v75\n"
"global_load_lds_dwordx4 v97, s[70:71] offset:2048\n"
"v_and_b32 v8, 15, v228\n"
"v_lshrrev_b32 v9, 4, v228\n"
"global_load_lds_dwordx4 v96, s[70:71] offset:3072\n"
"v_lshl_add_u32 v8, v9, 3, v8\n"
"v_sub_u32 v8, 15, v8\n"
"v_and_b32 v9, 1, v8\n"
"v_lshlrev_b32 v9, 4, v9\n"
"s_lshl2_add_u32 s12, s15, 0x27580\n"
"v_mov_b32 v11, s12\n"
"ds_read_b32 v11, v11\n"
"v_mov_b32 v10, 0xffff0000\n"
"v_lshlrev_b32 v10, v9, v10\n"
"v_lshlrev_b32 v9, v9, v223\n"
"v_ashrrev_i32 v8, 1, v8\n"
"v_cmp_eq_u32_e64 s[92:93], 0, v8\n"
"v_cmp_gt_i32_e64 s[94:95], 0, v8\n"
"s_barrier\n"
"s_add_u32 s13, s49, 1\n"
"s_and_b32 s13, s13, 3\n"
"v_cndmask_b32_e64 v184, 0, v9, s[92:93]\n"
"v_cndmask_b32_e64 v185, 0, v10, s[92:93]\n"
"s_lshl_b32 s13, s13, 7\n"
"v_cndmask_b32_e64 v185, v185, -1, s[94:95]\n"
"v_cmp_eq_u32_e64 s[92:93], 1, v8\n"
"s_add_u32 s68, s72, s13\n"
"s_addc_u32 s69, s73, 0\n"
"v_cmp_gt_i32_e64 s[94:95], 1, v8\n"
"v_cndmask_b32_e64 v186, 0, v9, s[92:93]\n"
"s_add_u32 s70, s74, s13\n"
"s_addc_u32 s71, s75, 0\n"
"v_cndmask_b32_e64 v187, 0, v10, s[92:93]\n"
"v_cndmask_b32_e64 v187, v187, -1, s[94:95]\n"
"s_add_u32 m0, s56, 0x8000\n"
"s_nop 0\n"
"global_load_lds_dwordx4 v85, s[68:69]\n"
"global_load_lds_dwordx4 v86, s[68:69] offset:1024\n"
"global_load_lds_dwordx4 v85, s[70:71] offset:2048\n"
"global_load_lds_dwordx4 v86, s[70:71] offset:3072\n"
"v_cmp_eq_u32_e64 s[92:93], 2, v8\n"
"v_cmp_gt_i32_e64 s[94:95], 2, v8\n"
"s_nop 0\n"
"v_cndmask_b32_e64 v192, 0, v9, s[92:93]\n"
"v_cndmask_b32_e64 v193, 0, v10, s[92:93]\n"
"ds_read_b128 v[0:3], v222 offset:512\n"
"v_cndmask_b32_e64 v193, v193, -1, s[94:95]\n"
"v_cmp_eq_u32_e64 s[92:93], 3, v8\n"
"ds_read_b128 v[4:7], v221 offset:1024\n"
"s_waitcnt lgkmcnt(2)\n"
"v_readfirstlane_b32 s7, v11\n"
"s_nop 0\n"
"s_xor_b32 s66, s66, s7\n"
"v_cmp_gt_i32_e64 s[94:95], 3, v8\n"
"v_cndmask_b32_e64 v194, 0, v9, s[92:93]\n"
"v_cndmask_b32_e64 v195, 0, v10, s[92:93]\n"
"v_cndmask_b32_e64 v195, v195, -1, s[94:95]\n"
"s_waitcnt vmcnt(15)\n"
"v_xor_b32 v28, v28, v244\n"
"v_xor_b32 v29, v29, v244\n"
"s_waitcnt vmcnt(8)\n"
"ds_read_b128 v[8:11], v237\n"
"ds_read_b128 v[12:15], v238\n"
"ds_read_b128 v[16:19], v239\n"
"ds_read_b128 v[20:23], v240\n"
"v_xor_b32 v26, v26, v243\n"
"v_xor_b32 v27, v27, v243\n"
"v_xor_b32 v24, v24, v242\n"
"v_xor_b32 v25, v25, v242\n"
"s_add_u32 m0, s56, 0\n"
"s_waitcnt lgkmcnt(0)\n"
"global_load_lds_dwordx4 v97, s[68:69]\n"
"global_load_lds_dwordx4 v96, s[68:69] offset:1024\n"
"v_xor_b32 v44, v44, v241\n"
"v_xor_b32 v45, v45, v241\n"
"global_load_lds_dwordx4 v97, s[70:71] offset:2048\n"
"v_or3_b32 v30, v28, v29, v26\n"
"v_or3_b32 v30, v30, v27, v24\n"
"global_load_lds_dwordx4 v96, s[70:71] offset:3072\n"
"v_or3_b32 v30, v30, v25, v44\n"
"v_bitop3_b32 v30, v30, v45, s96 bitop3:0xa8\n"
"v_cmp_ne_u32 vcc, 0, v30\n"
"s_nop 0\n"
"s_cbranch_vccnz .Lr1\n"
".Lb1:\n"
"v_fmac_f32 v26, v2, v28\n"
"v_fmac_f32 v27, v2, v29\n"
"v_fma_f32 v26, -v3, v29, v26\n"
"v_fmac_f32 v27, v3, v28\n"
"v_fmac_f32 v24, v2, v26\n"
"v_fmac_f32 v25, v2, v27\n"
"v_fma_f32 v24, -v3, v27, v24\n"
"v_fmac_f32 v25, v3, v26\n"
"v_fmac_f32 v44, v2, v24\n"
"v_fmac_f32 v45, v2, v25\n"
"v_fma_f32 v44, -v3, v25, v44\n"
"v_fmac_f32 v45, v3, v24\n"
"v_mul_f32 v46, v6, v44\n"
"v_mul_f32 v47, v6, v45\n"
"s_waitcnt vmcnt(8)\n"
"ds_read_b128 v[24:27], v237 offset:16384\n"
"ds_read_b128 v[28:31], v238 offset:16384\n"
"ds_read_b128 v[32:35], v239 offset:16384\n"
"ds_read_b128 v[36:39], v240 offset:16384\n"
"v_fma_f32 v46, -v7, v45, v46\n"
"v_fmac_f32 v47, v7, v44\n"
"s_nop 1\n"
"v_permlane16_swap_b32 v46, v47\n"
"v_add_f32 v46, v46, v47\n"
"s_add_u32 s13, s49, 2\n"
"s_and_b32 s13, s13, 3\n"
"v_mov_b32 v47, v46\n"
"s_nop 1\n"
"v_permlane32_swap_b32 v46, v47\n"
"s_lshl_b32 s13, s13, 7\n"
"v_add_f32 v46, v46, v47\n"
"v_mov_b32 v47, v46\n"
"s_add_u32 s68, s72, s13\n"
"s_addc_u32 s69, s73, 0\n"
"v_permlane16_swap_b32 v46, v47\n"
"v_fmac_f32 v76, v4, v46\n"
"s_add_u32 s70, s74, s13\n"
"s_addc_u32 s71, s75, 0\n"
"v_fmac_f32 v77, v4, v47\n"
"v_fma_f32 v76, -v5, v47, v76\n"
"s_add_u32 m0, s56, 0x4000\n"
"s_waitcnt lgkmcnt(0)\n"
"global_load_lds_dwordx4 v85, s[68:69]\n"
"global_load_lds_dwordx4 v86, s[68:69] offset:1024\n"
"global_load_lds_dwordx4 v85, s[70:71] offset:2048\n"
"global_load_lds_dwordx4 v86, s[70:71] offset:3072\n"
"v_fmac_f32 v77, v5, v46\n"
"v_fmac_f32 v78, v0, v76\n"
"v_fma_f32 v40, -v0, v77, v40\n"
"v_fma_f32 v78, -v1, v77, v78\n"
"v_fma_f32 v40, -v1, v76, v40\n"
"v_fmac_f32 v60, v188, v78\n"
"v_fmac_f32 v41, v188, v40\n"
"v_fmac_f32 v60, v189, v40\n"
"v_fma_f32 v41, -v189, v78, v41\n"
"v_fmac_f32 v79, v188, v60\n"
"v_fmac_f32 v42, v188, v41\n"
"v_fmac_f32 v79, v189, v41\n"
"s_waitcnt vmcnt(8)\n"
"ds_read_b128 v[0:3], v237 offset:32768\n"
"ds_read_b128 v[4:7], v238 offset:32768\n"
"ds_read_b128 v[44:47], v239 offset:32768\n"
"ds_read_b128 v[48:51], v240 offset:32768\n"
"v_fma_f32 v42, -v189, v60, v42\n"
"v_cvt_pk_bf16_f32 v56, v78, v60\n"
"v_cvt_pk_bf16_f32 v60, v40, v41\n"
"v_fmac_f32 v80, v188, v79\n"
"s_add_u32 m0, s56, 0x8000\n"
"s_waitcnt lgkmcnt(0)\n"
"global_load_lds_dwordx4 v97, s[68:69]\n"
"global_load_lds_dwordx4 v96, s[68:69] offset:1024\n"
"v_fmac_f32 v43, v188, v42\n"
"v_fmac_f32 v80, v189, v42\n"
"global_load_lds_dwordx4 v97, s[70:71] offset:2048\n"
"v_fma_f32 v43, -v189, v79, v43\n"
"v_fmac_f32 v81, v188, v80\n"
"global_load_lds_dwordx4 v96, s[70:71] offset:3072\n"
"v_fmac_f32 v52, v188, v43\n"
"v_fmac_f32 v81, v189, v43\n"
"v_fma_f32 v52, -v189, v80, v52\n"
"v_cvt_pk_bf16_f32 v57, v79, v80\n"
"v_cvt_pk_bf16_f32 v61, v42, v43\n"
"v_fmac_f32 v82, v188, v81\n"
"v_fmac_f32 v53, v188, v52\n"
"v_fmac_f32 v82, v189, v52\n"
"v_fma_f32 v53, -v189, v81, v53\n"
"v_fmac_f32 v84, v188, v82\n"
"v_fmac_f32 v54, v188, v53\n"
"v_fmac_f32 v84, v189, v53\n"
"v_fma_f32 v54, -v189, v82, v54\n"
"v_cvt_pk_bf16_f32 v58, v81, v82\n"
"v_cvt_pk_bf16_f32 v62, v52, v53\n"
"v_fmac_f32 v89, v188, v84\n"
"s_waitcnt vmcnt(8)\n"
"ds_read_b128 v[40:43], v237\n"
"ds_read_b128 v[68:71], v238\n"
"ds_read_b128 v[76:79], v239\n"
"ds_read_b128 v[80:83], v240\n"
"v_fmac_f32 v55, v188, v54\n"
"v_fmac_f32 v89, v189, v54\n"
"v_fma_f32 v55, -v189, v84, v55\n"
"v_fmac_f32 v87, v188, v89\n"
"s_add_u32 s13, s49, 3\n"
"s_and_b32 s13, s13, 3\n"
"v_fmac_f32 v64, v188, v55\n"
"v_fmac_f32 v87, v189, v55\n"
"s_lshl_b32 s13, s13, 7\n"
"v_fma_f32 v64, -v189, v89, v64\n"
"v_cvt_pk_bf16_f32 v59, v84, v89\n"
"s_add_u32 s68, s72, s13\n"
"s_addc_u32 s69, s73, 0\n"
"v_cvt_pk_bf16_f32 v63, v54, v55\n"
"global_store_dwordx4 v217, v[56:59], s[66:67] sc1\n"
"s_add_u32 s70, s74, s13\n"
"s_addc_u32 s71, s75, 0\n"
"global_store_dwordx4 v217, v[60:63], s[66:67] offset:512 sc1\n"
"s_add_u32 m0, s56, 0\n"
"s_waitcnt lgkmcnt(0)\n"
"global_load_lds_dwordx4 v85, s[68:69]\n"
"global_load_lds_dwordx4 v86, s[68:69] offset:1024\n"
"global_load_lds_dwordx4 v85, s[70:71] offset:2048\n"
"global_load_lds_dwordx4 v86, s[70:71] offset:3072\n"
"v_fmac_f32 v88, v188, v87\n"
"v_fmac_f32 v65, v188, v64\n"
"v_fmac_f32 v88, v189, v64\n"
"v_fma_f32 v65, -v189, v87, v65\n"
"v_fmac_f32 v98, v188, v88\n"
"v_fmac_f32 v66, v188, v65\n"
"v_fmac_f32 v98, v189, v65\n"
"v_fma_f32 v66, -v189, v88, v66\n"
"v_cvt_pk_bf16_f32 v52, v87, v88\n"
"v_cvt_pk_bf16_f32 v56, v64, v65\n"
"v_fmac_f32 v99, v188, v98\n"
"v_fmac_f32 v67, v188, v66\n"
"s_waitcnt vmcnt(10)\n"
"ds_read_b128 v[60:63], v237 offset:16384\n"
"ds_read_b128 v[84:87], v238 offset:16384\n"
"ds_read_b128 v[88:91], v239 offset:16384\n"
"ds_read_b128 v[92:95], v240 offset:16384\n"
"v_fmac_f32 v99, v189, v66\n"
"v_fma_f32 v67, -v189, v98, v67\n"
"v_fmac_f32 v100, v188, v99\n"
"v_fmac_f32 v72, v188, v67\n"
"s_add_u32 m0, s56, 0x4000\n"
"s_waitcnt lgkmcnt(0)\n"
"global_load_lds_dwordx4 v97, s[68:69]\n"
"global_load_lds_dwordx4 v96, s[68:69] offset:1024\n"
"v_fmac_f32 v100, v189, v67\n"
"v_fma_f32 v72, -v189, v99, v72\n"
"global_load_lds_dwordx4 v97, s[70:71] offset:2048\n"
"v_cvt_pk_bf16_f32 v53, v98, v99\n"
"v_cvt_pk_bf16_f32 v57, v66, v67\n"
"global_load_lds_dwordx4 v96, s[70:71] offset:3072\n"
"v_fmac_f32 v101, v188, v100\n"
"v_fmac_f32 v73, v188, v72\n"
"v_fmac_f32 v101, v189, v72\n"
"v_fma_f32 v73, -v189, v100, v73\n"
"v_fmac_f32 v116, v188, v101\n"
"v_fmac_f32 v74, v188, v73\n"
"ds_read2_b32 v[64:65], v233 offset0:128 offset1:129\n"
"v_fmac_f32 v116, v189, v73\n"
"v_fma_f32 v74, -v189, v101, v74\n"
"ds_read2_b32 v[66:67], v233 offset0:130 offset1:131\n"
"ds_read_b128 v[96:99], v230 offset:3584\n"
"v_cvt_pk_bf16_f32 v54, v100, v101\n"
"v_cvt_pk_bf16_f32 v58, v72, v73\n"
"ds_read2_b32 v[100:101], v233 offset0:0 offset1:1\n"
"ds_read2_b32 v[102:103], v233 offset0:2 offset1:3\n"
"v_fmac_f32 v117, v188, v116\n"
"v_fmac_f32 v75, v188, v74\n"
"ds_read_b128 v[104:107], v230 offset:3072\n"
"ds_read2_b32 v[108:109], v232 offset0:128 offset1:129\n"
"v_fmac_f32 v117, v189, v74\n"
"v_fma_f32 v75, -v189, v116, v75\n"
"ds_read2_b32 v[110:111], v232 offset0:130 offset1:131\n"
"ds_read_b128 v[112:115], v230 offset:2560\n"
"v_cvt_pk_bf16_f32 v55, v116, v117\n"
"v_cvt_pk_bf16_f32 v59, v74, v75\n"
"global_store_dwordx4 v216, v[52:55], s[66:67] offset:256 sc1\n"
"global_store_dwordx4 v216, v[56:59], s[66:67] offset:768 sc1\n"
"s_barrier\n"
"s_waitcnt lgkmcnt(6)\n"
"v_mfma_f32_16x16x32_bf16 v[52:55], v[64:67], v[96:99], 0\n"
"ds_read_b128 v[56:59], v230\n"
"ds_read2_b32 v[64:65], v232 offset0:0 offset1:1\n"
"ds_read2_b32 v[66:67], v232 offset0:2 offset1:3\n"
"s_waitcnt lgkmcnt(7)\n"
"v_mfma_f32_16x16x32_bf16 v[72:75], v[100:103], v[96:99], 0\n"
"ds_read2_b32 v[116:117], v229 offset0:0 offset1:1\n"
"ds_read_b128 v[120:123], v230 offset:2048\n"
"ds_read2_b32 v[124:125], v231 offset0:128 offset1:129\n"
"s_waitcnt lgkmcnt(9)\n"
"v_mfma_f32_16x16x32_bf16 v[52:55], v[100:103], v[104:107], v[52:55]\n"
"ds_read2_b32 v[118:119], v229 offset0:2 offset1:3\n"
"ds_read2_b32 v[126:127], v231 offset0:130 offset1:131\n"
"ds_read_b128 v[100:103], v230 offset:1536\n"
"s_waitcnt lgkmcnt(10)\n"
"v_mfma_f32_16x16x32_bf16 v[128:131], v[108:111], v[96:99], 0\n"
"ds_read2_b32 v[132:133], v233 offset0:144 offset1:145\n"
"ds_read2_b32 v[136:137], v231 offset0:0 offset1:1\n"
"ds_read2_b32 v[138:139], v231 offset0:2 offset1:3\n"
"v_mfma_f32_16x16x32_bf16 v[72:75], v[108:111], v[104:107], v[72:75]\n"
"ds_read2_b32 v[134:135], v233 offset0:146 offset1:147\n"
"ds_read_b128 v[140:143], v230 offset:1024\n"
"ds_read_b128 v[144:147], v230 offset:512\n"
"s_waitcnt lgkmcnt(15)\n"
"v_mfma_f32_16x16x32_bf16 v[52:55], v[108:111], v[112:115], v[52:55]\n"
"ds_read2_b32 v[108:109], v233 offset0:16 offset1:17\n"
"s_waitcnt lgkmcnt(13)\n"
"v_mfma_f32_16x16x32_bf16 v[148:151], v[64:67], v[96:99], 0\n"
"ds_read2_b32 v[110:111], v233 offset0:18 offset1:19\n"
"v_mfma_f32_16x16x32_bf16 v[128:131], v[64:67], v[104:107], v[128:131]\n"
"ds_read2_b32 v[152:153], v232 offset0:144 offset1:145\n"
"v_mfma_f32_16x16x32_bf16 v[72:75], v[64:67], v[112:115], v[72:75]\n"
"ds_read2_b32 v[154:155], v232 offset0:146 offset1:147\n"
"s_waitcnt lgkmcnt(14)\n"
"v_mfma_f32_16x16x32_bf16 v[52:55], v[64:67], v[120:123], v[52:55]\n"
"ds_read2_b32 v[64:65], v229 offset0:128 offset1:129\n"
"s_waitcnt lgkmcnt(12)\n"
"v_mfma_f32_16x16x32_bf16 v[156:159], v[124:127], v[96:99], 0\n"
"ds_read2_b32 v[66:67], v229 offset0:130 offset1:131\n"
"v_mfma_f32_16x16x32_bf16 v[148:151], v[124:127], v[104:107], v[148:151]\n"
"ds_read2_b32 v[160:161], v232 offset0:16 offset1:17\n"
"v_mfma_f32_16x16x32_bf16 v[128:131], v[124:127], v[112:115], v[128:131]\n"
"ds_read2_b32 v[162:163], v232 offset0:18 offset1:19\n"
"v_mfma_f32_16x16x32_bf16 v[72:75], v[124:127], v[120:123], v[72:75]\n"
"s_waitcnt lgkmcnt(14)\n"
"v_mfma_f32_16x16x32_bf16 v[52:55], v[124:127], v[100:103], v[52:55]\n"
"s_waitcnt vmcnt(12)\n"
"ds_read_b128 v[124:127], v237 offset:32768\n"
"s_waitcnt lgkmcnt(12)\n"
"v_mfma_f32_16x16x32_bf16 v[164:167], v[136:139], v[96:99], 0\n"
"ds_read_b128 v[168:171], v238 offset:32768\n"
"ds_read_b128 v[172:175], v239 offset:32768\n"
"v_mfma_f32_16x16x32_bf16 v[156:159], v[136:139], v[104:107], v[156:159]\n"
"ds_read2_b32 v[176:177], v231 offset0:144 offset1:145\n"
"ds_read_b128 v[180:183], v240 offset:32768\n"
"v_mfma_f32_16x16x32_bf16 v[148:151], v[136:139], v[112:115], v[148:151]\n"
"ds_read2_b32 v[178:179], v231 offset0:146 offset1:147\n"
"v_mfma_f32_16x16x32_bf16 v[128:131], v[136:139], v[120:123], v[128:131]\n"
"v_and_or_b32 v116, v116, v185, v184\n"
"v_and_or_b32 v117, v117, v187, v186\n"
"v_mfma_f32_16x16x32_bf16 v[72:75], v[136:139], v[100:103], v[72:75]\n"
"v_and_or_b32 v118, v118, v193, v192\n"
"v_and_or_b32 v119, v119, v195, v194\n"
"s_waitcnt lgkmcnt(15)\n"
"v_mfma_f32_16x16x32_bf16 v[52:55], v[136:139], v[140:143], v[52:55]\n"
"s_waitcnt lgkmcnt(8)\n"
"v_mfma_f32_16x16x32_bf16 v[136:139], v[64:67], v[96:99], 0\n"
"v_mfma_f32_16x16x32_bf16 v[164:167], v[64:67], v[104:107], v[164:167]\n"
"ds_read2_b32 v[184:185], v231 offset0:16 offset1:17\n"
"v_mfma_f32_16x16x32_bf16 v[156:159], v[64:67], v[112:115], v[156:159]\n"
"ds_read2_b32 v[186:187], v231 offset0:18 offset1:19\n"
"v_mfma_f32_16x16x32_bf16 v[148:151], v[64:67], v[120:123], v[148:151]\n"
"v_mfma_f32_16x16x32_bf16 v[128:131], v[64:67], v[100:103], v[128:131]\n"
"v_mfma_f32_16x16x32_bf16 v[72:75], v[64:67], v[140:143], v[72:75]\n"
"v_mfma_f32_16x16x32_bf16 v[52:55], v[64:67], v[144:147], v[52:55]\n"
"v_mfma_f32_16x16x32_bf16 v[64:67], v[116:119], v[96:99], 0\n"
"v_mfma_f32_16x16x32_bf16 v[136:139], v[116:119], v[104:107], v[136:139]\n"
"ds_read_b128 v[96:99], v230 offset:3648\n"
"v_mfma_f32_16x16x32_bf16 v[164:167], v[116:119], v[112:115], v[164:167]\n"
"ds_read_b128 v[104:107], v230 offset:3136\n"
"v_mfma_f32_16x16x32_bf16 v[156:159], v[116:119], v[120:123], v[156:159]\n"
"ds_read_b128 v[112:115], v230 offset:2624\n"
"v_mfma_f32_16x16x32_bf16 v[148:151], v[116:119], v[100:103], v[148:151]\n"
"ds_read_b128 v[100:103], v230 offset:2112\n"
"v_mfma_f32_16x16x32_bf16 v[128:131], v[116:119], v[140:143], v[128:131]\n"
"ds_read_b128 v[120:123], v230 offset:1600\n"
"v_mfma_f32_16x16x32_bf16 v[72:75], v[116:119], v[144:147], v[72:75]\n"
"ds_read_b128 v[140:143], v230 offset:1088\n"
"v_mfma_f32_16x16x32_bf16 v[52:55], v[116:119], v[56:59], v[52:55]\n"
"ds_read_b128 v[56:59], v230 offset:576\n"
"s_waitcnt lgkmcnt(6)\n"
"v_mfma_f32_16x16x32_bf16 v[52:55], v[132:135], v[96:99], v[52:55]\n"
"ds_read_b128 v[116:119], v230 offset:64\n"
"v_mfma_f32_16x16x32_bf16 v[72:75], v[108:111], v[96:99], v[72:75]\n"
"ds_read2_b32 v[132:133], v229 offset0:16 offset1:17\n"
"s_waitcnt lgkmcnt(7)\n"
"v_mfma_f32_16x16x32_bf16 v[52:55], v[108:111], v[104:107], v[52:55]\n"
"ds_read2_b32 v[134:135], v229 offset0:18 offset1:19\n"
"v_mfma_f32_16x16x32_bf16 v[128:131], v[152:155], v[96:99], v[128:131]\n"
"ds_read2_b32 v[108:109], v233 offset0:160 offset1:161\n"
"v_mfma_f32_16x16x32_bf16 v[72:75], v[152:155], v[104:107], v[72:75]\n"
"ds_read2_b32 v[110:111], v233 offset0:162 offset1:163\n"
"s_waitcnt lgkmcnt(9)\n"
"v_mfma_f32_16x16x32_bf16 v[52:55], v[152:155], v[112:115], v[52:55]\n"
"ds_read2_b32 v[144:145], v233 offset0:32 offset1:33\n"
"s_waitcnt vmcnt(6)\n"
"v_mfma_f32_16x16x32_bf16 v[148:151], v[160:163], v[96:99], v[148:151]\n"
"ds_read2_b32 v[146:147], v233 offset0:34 offset1:35\n"
"ds_read_b128 v[152:155], v237\n"
"v_mfma_f32_16x16x32_bf16 v[128:131], v[160:163], v[104:107], v[128:131]\n"
"ds_read2_b32 v[188:189], v232 offset0:160 offset1:161\n"
"ds_read_b128 v[192:195], v238\n"
"v_mfma_f32_16x16x32_bf16 v[72:75], v[160:163], v[112:115], v[72:75]\n"
"ds_read2_b32 v[190:191], v232 offset0:162 offset1:163\n"
"s_waitcnt lgkmcnt(14)\n"
"v_mfma_f32_16x16x32_bf16 v[52:55], v[160:163], v[100:103], v[52:55]\n"
"ds_read2_b32 v[160:161], v229 offset0:144 offset1:145\n"
"ds_read_b128 v[196:199], v239\n"
"v_mfma_f32_16x16x32_bf16 v[156:159], v[176:179], v[96:99], v[156:159]\n"
"ds_read2_b32 v[162:163], v229 offset0:146 offset1:147\n"
"ds_read_b128 v[200:203], v240\n"
"v_mfma_f32_16x16x32_bf16 v[148:151], v[176:179], v[104:107], v[148:151]\n"
"ds_read2_b32 v[204:205], v232 offset0:32 offset1:33\n"
"v_mfma_f32_16x16x32_bf16 v[128:131], v[176:179], v[112:115], v[128:131]\n"
"ds_read2_b32 v[206:207], v232 offset0:34 offset1:35\n"
"v_mfma_f32_16x16x32_bf16 v[72:75], v[176:179], v[100:103], v[72:75]\n"
"s_waitcnt lgkmcnt(15)\n"
"v_mfma_f32_16x16x32_bf16 v[52:55], v[176:179], v[120:123], v[52:55]\n"
"v_mfma_f32_16x16x32_bf16 v[164:167], v[184:187], v[96:99], v[164:167]\n"
"v_mfma_f32_16x16x32_bf16 v[156:159], v[184:187], v[104:107], v[156:159]\n"
"ds_read2_b32 v[176:177], v231 offset0:160 offset1:161\n"
"v_mfma_f32_16x16x32_bf16 v[148:151], v[184:187], v[112:115], v[148:151]\n"
"ds_read2_b32 v[178:179], v231 offset0:162 offset1:163\n"
"v_mfma_f32_16x16x32_bf16 v[128:131], v[184:187], v[100:103], v[128:131]\n"
"v_mfma_f32_16x16x32_bf16 v[72:75], v[184:187], v[120:123], v[72:75]\n"
"v_mfma_f32_16x16x32_bf16 v[52:55], v[184:187], v[140:143], v[52:55]\n"
"s_waitcnt lgkmcnt(5)\n"
"v_mfma_f32_16x16x32_bf16 v[136:139], v[160:163], v[96:99], v[136:139]\n"
"v_mfma_f32_16x16x32_bf16 v[164:167], v[160:163], v[104:107], v[164:167]\n"
"ds_read2_b32 v[184:185], v231 offset0:32 offset1:33\n"
"v_mfma_f32_16x16x32_bf16 v[156:159], v[160:163], v[112:115], v[156:159]\n"
"ds_read2_b32 v[186:187], v231 offset0:34 offset1:35\n"
"v_mfma_f32_16x16x32_bf16 v[148:151], v[160:163], v[100:103], v[148:151]\n"
"s_waitcnt vmcnt(2)\n"
"ds_read_b128 v[208:211], v237 offset:16384\n"
"v_mfma_f32_16x16x32_bf16 v[128:131], v[160:163], v[120:123], v[128:131]\n"
"ds_read_b128 v[212:215], v238 offset:16384\n"
"ds_read_b128 v[216:219], v239 offset:16384\n"
"v_mfma_f32_16x16x32_bf16 v[72:75], v[160:163], v[140:143], v[72:75]\n"
"ds_read_b128 v[220:223], v240 offset:16384\n"
"s_lshl_b32 s94, s58, 9\n"
"v_mfma_f32_16x16x32_bf16 v[52:55], v[160:163], v[56:59], v[52:55]\n"
"s_setprio 3\n"
"s_waitcnt vmcnt(0)\n"
"global_store_dwordx2 v227, v[224:225], s[62:63] sc1\n"
"s_setprio 0\n"
"v_mfma_f32_16x16x32_bf16 v[64:67], v[132:135], v[96:99], v[64:67]\n"
"s_add_u32 s94, s94, s15\n"
"s_lshl_b32 s94, s94, 6\n"
"v_mfma_f32_16x16x32_bf16 v[136:139], v[132:135], v[104:107], v[136:139]\n"
"ds_read_b128 v[96:99], v230 offset:3712\n"
"s_add_u32 s94, s94, 0x2c01000\n"
"v_mfma_f32_16x16x32_bf16 v[164:167], v[132:135], v[112:115], v[164:167]\n"
"ds_read_b128 v[104:107], v230 offset:3200\n"
"s_lshr_b32 s26, s94, 8\n"
"v_mfma_f32_16x16x32_bf16 v[156:159], v[132:135], v[100:103], v[156:159]\n"
"ds_read_b128 v[100:103], v230 offset:2688\n"
"s_and_b32 s26, s26, 0x2000\n"
"v_mfma_f32_16x16x32_bf16 v[148:151], v[132:135], v[120:123], v[148:151]\n"
"ds_read_b128 v[112:115], v230 offset:2176\n"
"s_add_u32 s26, s26, s94\n"
"v_mfma_f32_16x16x32_bf16 v[128:131], v[132:135], v[140:143], v[128:131]\n"
"ds_read_b128 v[120:123], v230 offset:1664\n"
"v_mfma_f32_16x16x32_bf16 v[72:75], v[132:135], v[56:59], v[72:75]\n"
"ds_read_b128 v[56:59], v230 offset:1152\n"
"v_mfma_f32_16x16x32_bf16 v[52:55], v[132:135], v[116:119], v[52:55]\n"
"ds_read_b128 v[116:119], v230 offset:640\n"
"s_waitcnt lgkmcnt(6)\n"
"v_mfma_f32_16x16x32_bf16 v[52:55], v[108:111], v[96:99], v[52:55]\n"
"ds_read_b128 v[108:111], v230 offset:128\n"
"v_mfma_f32_16x16x32_bf16 v[72:75], v[144:147], v[96:99], v[72:75]\n"
"ds_read2_b32 v[132:133], v229 offset0:32 offset1:33\n"
"s_waitcnt lgkmcnt(7)\n"
"v_mfma_f32_16x16x32_bf16 v[52:55], v[144:147], v[104:107], v[52:55]\n"
"ds_read2_b32 v[134:135], v229 offset0:34 offset1:35\n"
"v_mfma_f32_16x16x32_bf16 v[128:131], v[188:191], v[96:99], v[128:131]\n"
"ds_read2_b32 v[140:141], v233 offset0:176 offset1:177\n"
"v_mfma_f32_16x16x32_bf16 v[72:75], v[188:191], v[104:107], v[72:75]\n"
"ds_read2_b32 v[142:143], v233 offset0:178 offset1:179\n"
"s_waitcnt lgkmcnt(9)\n"
"v_mfma_f32_16x16x32_bf16 v[52:55], v[188:191], v[100:103], v[52:55]\n"
"ds_read2_b32 v[144:145], v233 offset0:48 offset1:49\n"
"v_mfma_f32_16x16x32_bf16 v[148:151], v[204:207], v[96:99], v[148:151]\n"
"ds_read2_b32 v[146:147], v233 offset0:50 offset1:51\n"
"v_mfma_f32_16x16x32_bf16 v[128:131], v[204:207], v[104:107], v[128:131]\n"
"ds_read2_b32 v[160:161], v232 offset0:176 offset1:177\n"
"v_mfma_f32_16x16x32_bf16 v[72:75], v[204:207], v[100:103], v[72:75]\n"
"ds_read2_b32 v[162:163], v232 offset0:178 offset1:179\n"
"s_waitcnt lgkmcnt(12)\n"
"v_mfma_f32_16x16x32_bf16 v[52:55], v[204:207], v[112:115], v[52:55]\n"
"ds_read2_b32 v[188:189], v229 offset0:160 offset1:161\n"
"v_mfma_f32_16x16x32_bf16 v[156:159], v[176:179], v[96:99], v[156:159]\n"
"ds_read2_b32 v[190:191], v229 offset0:162 offset1:163\n"
"v_mfma_f32_16x16x32_bf16 v[148:151], v[176:179], v[104:107], v[148:151]\n"
"ds_read2_b32 v[204:205], v232 offset0:48 offset1:49\n"
"v_mfma_f32_16x16x32_bf16 v[128:131], v[176:179], v[100:103], v[128:131]\n"
"ds_read2_b32 v[206:207], v232 offset0:50 offset1:51\n"
"v_mfma_f32_16x16x32_bf16 v[72:75], v[176:179], v[112:115], v[72:75]\n"
"s_waitcnt lgkmcnt(15)\n"
"v_mfma_f32_16x16x32_bf16 v[52:55], v[176:179], v[120:123], v[52:55]\n"
"s_add_u32 s26, s30, s26\n"
"s_addc_u32 s27, s31, 0\n"
"v_mfma_f32_16x16x32_bf16 v[164:167], v[184:187], v[96:99], v[164:167]\n"
"s_lshl2_add_u32 s95, s58, s15\n"
"s_lshl_b32 s95, s95, 7\n"
"v_mfma_f32_16x16x32_bf16 v[156:159], v[184:187], v[104:107], v[156:159]\n"
"ds_read2_b32 v[176:177], v231 offset0:176 offset1:177\n"
"s_add_u32 s95, s95, 0x60000\n"
"v_mfma_f32_16x16x32_bf16 v[148:151], v[184:187], v[100:103], v[148:151]\n"
"ds_read2_b32 v[178:179], v231 offset0:178 offset1:179\n"
"v_mfma_f32_16x16x32_bf16 v[128:131], v[184:187], v[112:115], v[128:131]\n"
"s_add_u32 s40, s30, s95\n"
"s_addc_u32 s41, s31, 0\n"
"v_mfma_f32_16x16x32_bf16 v[72:75], v[184:187], v[120:123], v[72:75]\n"
"s_lshl2_add_u32 s94, %1, s15\n"
"s_waitcnt lgkmcnt(15)\n"
"v_mfma_f32_16x16x32_bf16 v[52:55], v[184:187], v[56:59], v[52:55]\n"
"s_lshl_b32 s94, s94, 14\n"
"s_add_u32 s95, s94, 0x1400000\n"
"s_waitcnt lgkmcnt(4)\n"
"v_mfma_f32_16x16x32_bf16 v[136:139], v[188:191], v[96:99], v[136:139]\n"
"s_lshr_b32 s60, s95, 8\n"
"s_and_b32 s60, s60, 0x2000\n"
"v_mfma_f32_16x16x32_bf16 v[164:167], v[188:191], v[104:107], v[164:167]\n"
"ds_read2_b32 v[184:185], v231 offset0:48 offset1:49\n"
"s_add_u32 s60, s60, s95\n"
"v_mfma_f32_16x16x32_bf16 v[156:159], v[188:191], v[100:103], v[156:159]\n"
"ds_read2_b32 v[186:187], v231 offset0:50 offset1:51\n"
"v_mfma_f32_16x16x32_bf16 v[148:151], v[188:191], v[112:115], v[148:151]\n"
"s_add_u32 s60, s30, s60\n"
"s_addc_u32 s61, s31, 0\n"
"v_mfma_f32_16x16x32_bf16 v[128:131], v[188:191], v[120:123], v[128:131]\n"
"s_xor_b32 s94, s94, 0x10000\n"
"s_add_u32 s94, s94, 0x1400000\n"
"v_mfma_f32_16x16x32_bf16 v[72:75], v[188:191], v[56:59], v[72:75]\n"
"s_lshr_b32 s42, s94, 8\n"
"s_and_b32 s42, s42, 0x2000\n"
"v_mfma_f32_16x16x32_bf16 v[52:55], v[188:191], v[116:119], v[52:55]\n"
"s_add_u32 s42, s42, s94\n"
"v_mfma_f32_16x16x32_bf16 v[64:67], v[132:135], v[96:99], v[64:67]\n"
"s_add_u32 s42, s30, s42\n"
"s_addc_u32 s43, s31, 0\n"
"v_mfma_f32_16x16x32_bf16 v[136:139], v[132:135], v[104:107], v[136:139]\n"
"ds_read_b128 v[96:99], v230 offset:3776\n"
"s_xor_b32 s26, s26, s7\n"
"v_mfma_f32_16x16x32_bf16 v[164:167], v[132:135], v[100:103], v[164:167]\n"
"ds_read_b128 v[100:103], v230 offset:3264\n"
"s_xor_b32 s60, s60, s7\n"
"v_mfma_f32_16x16x32_bf16 v[156:159], v[132:135], v[112:115], v[156:159]\n"
"ds_read_b128 v[104:107], v230 offset:2752\n"
"s_xor_b32 s42, s42, s7\n"
"v_mfma_f32_16x16x32_bf16 v[148:151], v[132:135], v[120:123], v[148:151]\n"
"ds_read_b128 v[112:115], v230 offset:2240\n"
"v_mfma_f32_16x16x32_bf16 v[128:131], v[132:135], v[56:59], v[128:131]\n"
"ds_read_b128 v[56:59], v230 offset:1728\n"
"v_mfma_f32_16x16x32_bf16 v[72:75], v[132:135], v[116:119], v[72:75]\n"
"ds_read_b128 v[116:119], v230 offset:1216\n"
"v_mfma_f32_16x16x32_bf16 v[52:55], v[132:135], v[108:111], v[52:55]\n"
"ds_read_b128 v[108:111], v230 offset:704\n"
"s_waitcnt lgkmcnt(6)\n"
"v_mfma_f32_16x16x32_bf16 v[52:55], v[140:143], v[96:99], v[52:55]\n"
"ds_read_b128 v[120:123], v230 offset:192\n"
"v_mfma_f32_16x16x32_bf16 v[72:75], v[144:147], v[96:99], v[72:75]\n"
"ds_read2_b32 v[132:133], v229 offset0:48 offset1:49\n"
"s_waitcnt lgkmcnt(7)\n"
"v_mfma_f32_16x16x32_bf16 v[52:55], v[144:147], v[100:103], v[52:55]\n"
"ds_read2_b32 v[134:135], v229 offset0:50 offset1:51\n"
"v_mfma_f32_16x16x32_bf16 v[128:131], v[160:163], v[96:99], v[128:131]\n"
"ds_read2_b32 v[140:141], v233 offset0:192 offset1:193\n"
"v_mfma_f32_16x16x32_bf16 v[72:75], v[160:163], v[100:103], v[72:75]\n"
"ds_read2_b32 v[142:143], v233 offset0:194 offset1:195\n"
"s_waitcnt lgkmcnt(9)\n"
"v_mfma_f32_16x16x32_bf16 v[52:55], v[160:163], v[104:107], v[52:55]\n"
"ds_read2_b32 v[144:145], v233 offset0:64 offset1:65\n"
"v_mfma_f32_16x16x32_bf16 v[148:151], v[204:207], v[96:99], v[148:151]\n"
"ds_read2_b32 v[146:147], v233 offset0:66 offset1:67\n"
"v_mfma_f32_16x16x32_bf16 v[128:131], v[204:207], v[100:103], v[128:131]\n"
"ds_read2_b32 v[160:161], v232 offset0:192 offset1:193\n"
"v_mfma_f32_16x16x32_bf16 v[72:75], v[204:207], v[104:107], v[72:75]\n"
"ds_read2_b32 v[162:163], v232 offset0:194 offset1:195\n"
"s_waitcnt lgkmcnt(12)\n"
"v_mfma_f32_16x16x32_bf16 v[52:55], v[204:207], v[112:115], v[52:55]\n"
"ds_read2_b32 v[188:189], v229 offset0:176 offset1:177\n"
"v_mfma_f32_16x16x32_bf16 v[156:159], v[176:179], v[96:99], v[156:159]\n"
"ds_read2_b32 v[190:191], v229 offset0:178 offset1:179\n"
"v_mfma_f32_16x16x32_bf16 v[148:151], v[176:179], v[100:103], v[148:151]\n"
"ds_read2_b32 v[204:205], v232 offset0:64 offset1:65\n"
"v_mfma_f32_16x16x32_bf16 v[128:131], v[176:179], v[104:107], v[128:131]\n"
"ds_read2_b32 v[206:207], v232 offset0:66 offset1:67\n"
"v_mfma_f32_16x16x32_bf16 v[72:75], v[176:179], v[112:115], v[72:75]\n"
"s_waitcnt lgkmcnt(15)\n"
"v_mfma_f32_16x16x32_bf16 v[52:55], v[176:179], v[56:59], v[52:55]\n"
"s_add_u32 s68, s26, 0x4000\n"
"s_addc_u32 s69, s27, 0\n"
"v_mfma_f32_16x16x32_bf16 v[164:167], v[184:187], v[96:99], v[164:167]\n"
"s_lshl1_add_u32 s46, s48, s59\n"
"s_lshl_b32 s46, s46, 16\n"
"v_mfma_f32_16x16x32_bf16 v[156:159], v[184:187], v[100:103], v[156:159]\n"
"ds_read2_b32 v[176:177], v231 offset0:192 offset1:193\n"
"s_xor_b32 s44, s46, 0x20000\n"
"v_mfma_f32_16x16x32_bf16 v[148:151], v[184:187], v[104:107], v[148:151]\n"
"ds_read2_b32 v[178:179], v231 offset0:194 offset1:195\n"
"s_xor_b32 s45, s46, 0x30000\n"
"v_mfma_f32_16x16x32_bf16 v[128:131], v[184:187], v[112:115], v[128:131]\n"
"s_xor_b32 s47, s46, 0x10000\n"
"s_lshl2_add_u32 s95, s50, s15\n"
"v_mfma_f32_16x16x32_bf16 v[72:75], v[184:187], v[56:59], v[72:75]\n"
"s_lshl_b32 s95, s95, 6\n"
"s_waitcnt lgkmcnt(15)\n"
"v_mfma_f32_16x16x32_bf16 v[52:55], v[184:187], v[116:119], v[52:55]\n"
"s_lshl_b32 s94, s51, 17\n"
"s_add_u32 s94, s94, s95\n"
"s_waitcnt lgkmcnt(4)\n"
"v_mfma_f32_16x16x32_bf16 v[136:139], v[188:191], v[96:99], v[136:139]\n"
"s_add_u32 s16, s16, s94\n"
"s_addc_u32 s17, s17, 0\n"
"v_mfma_f32_16x16x32_bf16 v[164:167], v[188:191], v[100:103], v[164:167]\n"
"ds_read2_b32 v[184:185], v231 offset0:64 offset1:65\n"
"s_lshl1_add_u32 s94, s94, 0x1000\n"
"v_mfma_f32_16x16x32_bf16 v[156:159], v[188:191], v[104:107], v[156:159]\n"
"ds_read2_b32 v[186:187], v231 offset0:66 offset1:67\n"
"v_mfma_f32_16x16x32_bf16 v[148:151], v[188:191], v[112:115], v[148:151]\n"
"s_add_u32 s36, s36, s94\n"
"s_addc_u32 s37, s37, 0\n"
"v_mfma_f32_16x16x32_bf16 v[128:131], v[188:191], v[56:59], v[128:131]\n"
"s_add_u32 s38, s38, s95\n"
"s_addc_u32 s39, s39, 0\n"
"v_mfma_f32_16x16x32_bf16 v[72:75], v[188:191], v[116:119], v[72:75]\n"
"s_lshl_b32 s95, s51, 9\n"
"s_add_u32 s95, s95, 0x140000\n"
"v_mfma_f32_16x16x32_bf16 v[52:55], v[188:191], v[108:111], v[52:55]\n"
"s_lshr_b32 s94, s46, 9\n"
"s_add_u32 s94, s94, s95\n"
"v_mfma_f32_16x16x32_bf16 v[64:67], v[132:135], v[96:99], v[64:67]\n"
"s_add_u32 s18, s30, s94\n"
"s_addc_u32 s19, s31, 0\n"
"v_mfma_f32_16x16x32_bf16 v[136:139], v[132:135], v[100:103], v[136:139]\n"
"ds_read_b128 v[96:99], v230 offset:3840\n"
"s_lshr_b32 s94, s46, 1\n"
"v_mfma_f32_16x16x32_bf16 v[164:167], v[132:135], v[104:107], v[164:167]\n"
"ds_read_b128 v[100:103], v230 offset:3328\n"
"s_add_u32 s94, s94, 0\n"
"v_mfma_f32_16x16x32_bf16 v[156:159], v[132:135], v[112:115], v[156:159]\n"
"ds_read_b128 v[104:107], v230 offset:2816\n"
"v_mfma_f32_16x16x32_bf16 v[148:151], v[132:135], v[56:59], v[148:151]\n"
"ds_read_b128 v[56:59], v230 offset:2304\n"
"v_mfma_f32_16x16x32_bf16 v[128:131], v[132:135], v[116:119], v[128:131]\n"
"ds_read_b128 v[112:115], v230 offset:1792\n"
"v_mfma_f32_16x16x32_bf16 v[72:75], v[132:135], v[108:111], v[72:75]\n"
"ds_read_b128 v[108:111], v230 offset:1280\n"
"v_mfma_f32_16x16x32_bf16 v[52:55], v[132:135], v[120:123], v[52:55]\n"
"ds_read_b128 v[116:119], v230 offset:768\n"
"s_waitcnt lgkmcnt(6)\n"
"v_mfma_f32_16x16x32_bf16 v[52:55], v[140:143], v[96:99], v[52:55]\n"
"ds_read_b128 v[120:123], v230 offset:256\n"
"v_mfma_f32_16x16x32_bf16 v[72:75], v[144:147], v[96:99], v[72:75]\n"
"ds_read2_b32 v[132:133], v229 offset0:64 offset1:65\n"
"s_waitcnt lgkmcnt(7)\n"
"v_mfma_f32_16x16x32_bf16 v[52:55], v[144:147], v[100:103], v[52:55]\n"
"ds_read2_b32 v[134:135], v229 offset0:66 offset1:67\n"
"v_mfma_f32_16x16x32_bf16 v[128:131], v[160:163], v[96:99], v[128:131]\n"
"ds_read2_b32 v[140:141], v233 offset0:208 offset1:209\n"
"v_mfma_f32_16x16x32_bf16 v[72:75], v[160:163], v[100:103], v[72:75]\n"
"ds_read2_b32 v[142:143], v233 offset0:210 offset1:211\n"
"s_waitcnt lgkmcnt(9)\n"
"v_mfma_f32_16x16x32_bf16 v[52:55], v[160:163], v[104:107], v[52:55]\n"
"ds_read2_b32 v[144:145], v233 offset0:80 offset1:81\n"
"v_mfma_f32_16x16x32_bf16 v[148:151], v[204:207], v[96:99], v[148:151]\n"
"ds_read2_b32 v[146:147], v233 offset0:82 offset1:83\n"
"v_mfma_f32_16x16x32_bf16 v[128:131], v[204:207], v[100:103], v[128:131]\n"
"ds_read2_b32 v[160:161], v232 offset0:208 offset1:209\n"
"v_mfma_f32_16x16x32_bf16 v[72:75], v[204:207], v[104:107], v[72:75]\n"
"ds_read2_b32 v[162:163], v232 offset0:210 offset1:211\n"
"s_waitcnt lgkmcnt(12)\n"
"v_mfma_f32_16x16x32_bf16 v[52:55], v[204:207], v[56:59], v[52:55]\n"
"ds_read2_b32 v[188:189], v229 offset0:192 offset1:193\n"
"v_mfma_f32_16x16x32_bf16 v[156:159], v[176:179], v[96:99], v[156:159]\n"
"ds_read2_b32 v[190:191], v229 offset0:194 offset1:195\n"
"v_mfma_f32_16x16x32_bf16 v[148:151], v[176:179], v[100:103], v[148:151]\n"
"ds_read2_b32 v[204:205], v232 offset0:80 offset1:81\n"
"v_mfma_f32_16x16x32_bf16 v[128:131], v[176:179], v[104:107], v[128:131]\n"
"ds_read2_b32 v[206:207], v232 offset0:82 offset1:83\n"
"v_mfma_f32_16x16x32_bf16 v[72:75], v[176:179], v[56:59], v[72:75]\n"
"s_waitcnt lgkmcnt(15)\n"
"v_mfma_f32_16x16x32_bf16 v[52:55], v[176:179], v[112:115], v[52:55]\n"
"s_add_u32 s32, s16, s94\n"
"s_addc_u32 s33, s17, 0\n"
"v_mfma_f32_16x16x32_bf16 v[164:167], v[184:187], v[96:99], v[164:167]\n"
"s_add_u32 s94, s46, 0\n"
"v_mfma_f32_16x16x32_bf16 v[156:159], v[184:187], v[100:103], v[156:159]\n"
"ds_read2_b32 v[176:177], v231 offset0:208 offset1:209\n"
"v_mfma_f32_16x16x32_bf16 v[148:151], v[184:187], v[104:107], v[148:151]\n"
"ds_read2_b32 v[178:179], v231 offset0:210 offset1:211\n"
"v_mfma_f32_16x16x32_bf16 v[128:131], v[184:187], v[56:59], v[128:131]\n"
"s_add_u32 s86, s36, s94\n"
"s_addc_u32 s87, s37, 0\n"
"v_mfma_f32_16x16x32_bf16 v[72:75], v[184:187], v[112:115], v[72:75]\n"
"s_lshr_b32 s94, s47, 9\n"
"s_waitcnt lgkmcnt(15)\n"
"v_mfma_f32_16x16x32_bf16 v[52:55], v[184:187], v[108:111], v[52:55]\n"
"s_add_u32 s94, s94, s95\n"
"s_waitcnt lgkmcnt(4)\n"
"v_mfma_f32_16x16x32_bf16 v[136:139], v[188:191], v[96:99], v[136:139]\n"
"s_add_u32 s20, s30, s94\n"
"s_addc_u32 s21, s31, 0\n"
"v_mfma_f32_16x16x32_bf16 v[164:167], v[188:191], v[100:103], v[164:167]\n"
"ds_read2_b32 v[184:185], v231 offset0:80 offset1:81\n"
"s_lshr_b32 s94, s47, 1\n"
"v_mfma_f32_16x16x32_bf16 v[156:159], v[188:191], v[104:107], v[156:159]\n"
"ds_read2_b32 v[186:187], v231 offset0:82 offset1:83\n"
"s_add_u32 s94, s94, 0\n"
"v_mfma_f32_16x16x32_bf16 v[148:151], v[188:191], v[56:59], v[148:151]\n"
"s_add_u32 s34, s16, s94\n"
"s_addc_u32 s35, s17, 0\n"
"v_mfma_f32_16x16x32_bf16 v[128:131], v[188:191], v[112:115], v[128:131]\n"
"s_add_u32 s94, s47, 0\n"
"v_mfma_f32_16x16x32_bf16 v[72:75], v[188:191], v[108:111], v[72:75]\n"
"s_add_u32 s88, s36, s94\n"
"s_addc_u32 s89, s37, 0\n"
"v_mfma_f32_16x16x32_bf16 v[52:55], v[188:191], v[116:119], v[52:55]\n"
"s_lshr_b32 s94, s46, 9\n"
"s_add_u32 s94, s94, s95\n"
"v_mfma_f32_16x16x32_bf16 v[64:67], v[132:135], v[96:99], v[64:67]\n"
"s_add_u32 s94, s94, 0x2000\n"
"v_mfma_f32_16x16x32_bf16 v[136:139], v[132:135], v[100:103], v[136:139]\n"
"ds_read_b128 v[96:99], v230 offset:3904\n"
"v_mfma_f32_16x16x32_bf16 v[164:167], v[132:135], v[104:107], v[164:167]\n"
"ds_read_b128 v[100:103], v230 offset:3392\n"
"v_mfma_f32_16x16x32_bf16 v[156:159], v[132:135], v[56:59], v[156:159]\n"
"ds_read_b128 v[56:59], v230 offset:2880\n"
"v_mfma_f32_16x16x32_bf16 v[148:151], v[132:135], v[112:115], v[148:151]\n"
"ds_read_b128 v[104:107], v230 offset:2368\n"
"v_mfma_f32_16x16x32_bf16 v[128:131], v[132:135], v[108:111], v[128:131]\n"
"ds_read_b128 v[108:111], v230 offset:1856\n"
"v_mfma_f32_16x16x32_bf16 v[72:75], v[132:135], v[116:119], v[72:75]\n"
"ds_read_b128 v[112:115], v230 offset:1344\n"
"v_mfma_f32_16x16x32_bf16 v[52:55], v[132:135], v[120:123], v[52:55]\n"
"ds_read_b128 v[116:119], v230 offset:832\n"
"s_waitcnt lgkmcnt(6)\n"
"v_mfma_f32_16x16x32_bf16 v[52:55], v[140:143], v[96:99], v[52:55]\n"
"ds_read_b128 v[120:123], v230 offset:320\n"
"v_mfma_f32_16x16x32_bf16 v[72:75], v[144:147], v[96:99], v[72:75]\n"
"ds_read2_b32 v[132:133], v229 offset0:80 offset1:81\n"
"s_waitcnt lgkmcnt(7)\n"
"v_mfma_f32_16x16x32_bf16 v[52:55], v[144:147], v[100:103], v[52:55]\n"
"ds_read2_b32 v[134:135], v229 offset0:82 offset1:83\n"
"v_mfma_f32_16x16x32_bf16 v[128:131], v[160:163], v[96:99], v[128:131]\n"
"ds_read2_b32 v[140:141], v233 offset0:224 offset1:225\n"
"v_mfma_f32_16x16x32_bf16 v[72:75], v[160:163], v[100:103], v[72:75]\n"
"ds_read2_b32 v[142:143], v233 offset0:226 offset1:227\n"
"s_waitcnt lgkmcnt(9)\n"
"v_mfma_f32_16x16x32_bf16 v[52:55], v[160:163], v[56:59], v[52:55]\n"
"ds_read2_b32 v[144:145], v233 offset0:96 offset1:97\n"
"v_mfma_f32_16x16x32_bf16 v[148:151], v[204:207], v[96:99], v[148:151]\n"
"ds_read2_b32 v[146:147], v233 offset0:98 offset1:99\n"
"v_mfma_f32_16x16x32_bf16 v[128:131], v[204:207], v[100:103], v[128:131]\n"
"ds_read2_b32 v[160:161], v232 offset0:224 offset1:225\n"
"v_mfma_f32_16x16x32_bf16 v[72:75], v[204:207], v[56:59], v[72:75]\n"
"ds_read2_b32 v[162:163], v232 offset0:226 offset1:227\n"
"s_waitcnt lgkmcnt(12)\n"
"v_mfma_f32_16x16x32_bf16 v[52:55], v[204:207], v[104:107], v[52:55]\n"
"ds_read2_b32 v[188:189], v229 offset0:208 offset1:209\n"
"v_mfma_f32_16x16x32_bf16 v[156:159], v[176:179], v[96:99], v[156:159]\n"
"ds_read2_b32 v[190:191], v229 offset0:210 offset1:211\n"
"v_mfma_f32_16x16x32_bf16 v[148:151], v[176:179], v[100:103], v[148:151]\n"
"ds_read2_b32 v[204:205], v232 offset0:96 offset1:97\n"
"v_mfma_f32_16x16x32_bf16 v[128:131], v[176:179], v[56:59], v[128:131]\n"
"ds_read2_b32 v[206:207], v232 offset0:98 offset1:99\n"
"v_mfma_f32_16x16x32_bf16 v[72:75], v[176:179], v[104:107], v[72:75]\n"
"s_waitcnt lgkmcnt(15)\n"
"v_mfma_f32_16x16x32_bf16 v[52:55], v[176:179], v[108:111], v[52:55]\n"
"s_add_u32 s22, s30, s94\n"
"s_addc_u32 s23, s31, 0\n"
"v_mfma_f32_16x16x32_bf16 v[164:167], v[184:187], v[96:99], v[164:167]\n"
"s_lshr_b32 s94, s46, 1\n"
"s_add_u32 s94, s94, 0x200000\n"
"v_mfma_f32_16x16x32_bf16 v[156:159], v[184:187], v[100:103], v[156:159]\n"
"ds_read2_b32 v[176:177], v231 offset0:224 offset1:225\n"
"v_mfma_f32_16x16x32_bf16 v[148:151], v[184:187], v[56:59], v[148:151]\n"
"ds_read2_b32 v[178:179], v231 offset0:226 offset1:227\n"
"v_mfma_f32_16x16x32_bf16 v[128:131], v[184:187], v[104:107], v[128:131]\n"
"s_add_u32 s28, s16, s94\n"
"s_addc_u32 s29, s17, 0\n"
"v_mfma_f32_16x16x32_bf16 v[72:75], v[184:187], v[108:111], v[72:75]\n"
"s_add_u32 s94, s46, 0x400000\n"
"s_waitcnt lgkmcnt(15)\n"
"v_mfma_f32_16x16x32_bf16 v[52:55], v[184:187], v[112:115], v[52:55]\n"
"s_add_u32 s90, s36, s94\n"
"s_addc_u32 s91, s37, 0\n"
"s_waitcnt lgkmcnt(4)\n"
"v_mfma_f32_16x16x32_bf16 v[136:139], v[188:191], v[96:99], v[136:139]\n"
"s_lshr_b32 s94, s47, 9\n"
"s_add_u32 s94, s94, s95\n"
"v_mfma_f32_16x16x32_bf16 v[164:167], v[188:191], v[100:103], v[164:167]\n"
"ds_read2_b32 v[184:185], v231 offset0:96 offset1:97\n"
"s_add_u32 s94, s94, 0x2000\n"
"v_mfma_f32_16x16x32_bf16 v[156:159], v[188:191], v[56:59], v[156:159]\n"
"ds_read2_b32 v[186:187], v231 offset0:98 offset1:99\n"
"v_mfma_f32_16x16x32_bf16 v[148:151], v[188:191], v[104:107], v[148:151]\n"
"s_add_u32 s24, s30, s94\n"
"s_addc_u32 s25, s31, 0\n"
"v_mfma_f32_16x16x32_bf16 v[128:131], v[188:191], v[108:111], v[128:131]\n"
"s_lshr_b32 s94, s47, 1\n"
"s_add_u32 s94, s94, 0x200000\n"
"v_mfma_f32_16x16x32_bf16 v[72:75], v[188:191], v[112:115], v[72:75]\n"
"s_add_u32 s84, s16, s94\n"
"s_addc_u32 s85, s17, 0\n"
"v_mfma_f32_16x16x32_bf16 v[52:55], v[188:191], v[116:119], v[52:55]\n"
"s_add_u32 s94, s47, 0x400000\n"
"v_mfma_f32_16x16x32_bf16 v[64:67], v[132:135], v[96:99], v[64:67]\n"
"s_add_u32 s92, s36, s94\n"
"s_addc_u32 s93, s37, 0\n"
"v_mfma_f32_16x16x32_bf16 v[136:139], v[132:135], v[100:103], v[136:139]\n"
"ds_read_b128 v[96:99], v230 offset:3968\n"
"v_mfma_f32_16x16x32_bf16 v[164:167], v[132:135], v[56:59], v[164:167]\n"
"ds_read_b128 v[56:59], v230 offset:3456\n"
"v_mfma_f32_16x16x32_bf16 v[156:159], v[132:135], v[104:107], v[156:159]\n"
"ds_read_b128 v[100:103], v230 offset:2944\n"
"v_mfma_f32_16x16x32_bf16 v[148:151], v[132:135], v[108:111], v[148:151]\n"
"ds_read_b128 v[104:107], v230 offset:2432\n"
"v_mfma_f32_16x16x32_bf16 v[128:131], v[132:135], v[112:115], v[128:131]\n"
"ds_read_b128 v[108:111], v230 offset:1920\n"
"v_mfma_f32_16x16x32_bf16 v[72:75], v[132:135], v[116:119], v[72:75]\n"
"ds_read_b128 v[112:115], v230 offset:1408\n"
"v_mfma_f32_16x16x32_bf16 v[52:55], v[132:135], v[120:123], v[52:55]\n"
"ds_read_b128 v[116:119], v230 offset:896\n"
"s_waitcnt lgkmcnt(6)\n"
"v_mfma_f32_16x16x32_bf16 v[52:55], v[140:143], v[96:99], v[52:55]\n"
"ds_read_b128 v[120:123], v230 offset:384\n"
"v_mfma_f32_16x16x32_bf16 v[72:75], v[144:147], v[96:99], v[72:75]\n"
"ds_read2_b32 v[132:133], v229 offset0:96 offset1:97\n"
"s_waitcnt lgkmcnt(7)\n"
"v_mfma_f32_16x16x32_bf16 v[52:55], v[144:147], v[56:59], v[52:55]\n"
"ds_read2_b32 v[134:135], v229 offset0:98 offset1:99\n"
"v_mfma_f32_16x16x32_bf16 v[128:131], v[160:163], v[96:99], v[128:131]\n"
"ds_read2_b32 v[140:141], v233 offset0:240 offset1:241\n"
"v_mfma_f32_16x16x32_bf16 v[72:75], v[160:163], v[56:59], v[72:75]\n"
"ds_read2_b32 v[142:143], v233 offset0:242 offset1:243\n"
"s_waitcnt lgkmcnt(9)\n"
"v_mfma_f32_16x16x32_bf16 v[52:55], v[160:163], v[100:103], v[52:55]\n"
"ds_read2_b32 v[144:145], v233 offset0:112 offset1:113\n"
"v_mfma_f32_16x16x32_bf16 v[148:151], v[204:207], v[96:99], v[148:151]\n"
"ds_read2_b32 v[146:147], v233 offset0:114 offset1:115\n"
"v_mfma_f32_16x16x32_bf16 v[128:131], v[204:207], v[56:59], v[128:131]\n"
"ds_read2_b32 v[160:161], v232 offset0:240 offset1:241\n"
"v_mfma_f32_16x16x32_bf16 v[72:75], v[204:207], v[100:103], v[72:75]\n"
"ds_read2_b32 v[162:163], v232 offset0:242 offset1:243\n"
"s_waitcnt lgkmcnt(12)\n"
"v_mfma_f32_16x16x32_bf16 v[52:55], v[204:207], v[104:107], v[52:55]\n"
"ds_read2_b32 v[188:189], v229 offset0:224 offset1:225\n"
"v_mfma_f32_16x16x32_bf16 v[156:159], v[176:179], v[96:99], v[156:159]\n"
"ds_read2_b32 v[190:191], v229 offset0:226 offset1:227\n"
"v_mfma_f32_16x16x32_bf16 v[148:151], v[176:179], v[56:59], v[148:151]\n"
"ds_read2_b32 v[204:205], v232 offset0:112 offset1:113\n"
"v_mfma_f32_16x16x32_bf16 v[128:131], v[176:179], v[100:103], v[128:131]\n"
"ds_read2_b32 v[206:207], v232 offset0:114 offset1:115\n"
"v_mfma_f32_16x16x32_bf16 v[72:75], v[176:179], v[104:107], v[72:75]\n"
"s_waitcnt lgkmcnt(15)\n"
"v_mfma_f32_16x16x32_bf16 v[52:55], v[176:179], v[108:111], v[52:55]\n"
"v_bfe_u32 v233, v228, 1, 1\n"
"v_mul_u32_u24 v232, 0x140, v233\n"
"v_mfma_f32_16x16x32_bf16 v[164:167], v[184:187], v[96:99], v[164:167]\n"
"v_lshrrev_b32 v233, 5, v228\n"
"v_lshl_or_b32 v232, v233, 9, v232\n"
"v_mfma_f32_16x16x32_bf16 v[156:159], v[184:187], v[56:59], v[156:159]\n"
"ds_read2_b32 v[176:177], v231 offset0:240 offset1:241\n"
"v_mfma_f32_16x16x32_bf16 v[148:151], v[184:187], v[100:103], v[148:151]\n"
"ds_read2_b32 v[178:179], v231 offset0:242 offset1:243\n"
"v_mfma_f32_16x16x32_bf16 v[128:131], v[184:187], v[104:107], v[128:131]\n"
"v_bfe_u32 v233, v228, 4, 1\n"
"v_lshl_or_b32 v232, v233, 7, v232\n"
"v_mfma_f32_16x16x32_bf16 v[72:75], v[184:187], v[108:111], v[72:75]\n"
"s_waitcnt lgkmcnt(15)\n"
"v_mfma_f32_16x16x32_bf16 v[52:55], v[184:187], v[112:115], v[52:55]\n"
"v_bfe_u32 v233, v228, 2, 2\n"
"v_lshl_or_b32 v232, v233, 4, v232\n"
"s_waitcnt lgkmcnt(4)\n"
"v_mfma_f32_16x16x32_bf16 v[136:139], v[188:191], v[96:99], v[136:139]\n"
"v_and_b32 v233, 1, v228\n"
"v_lshl_or_b32 v232, v233, 3, v232\n"
"v_mfma_f32_16x16x32_bf16 v[164:167], v[188:191], v[56:59], v[164:167]\n"
"ds_read2_b32 v[184:185], v231 offset0:112 offset1:113\n"
"v_mfma_f32_16x16x32_bf16 v[156:159], v[188:191], v[100:103], v[156:159]\n"
"ds_read2_b32 v[186:187], v231 offset0:114 offset1:115\n"
"v_mfma_f32_16x16x32_bf16 v[148:151], v[188:191], v[104:107], v[148:151]\n"
"v_xor_b32 v233, 64, v232\n"
"v_add_u32 v231, 0x10000, v232\n"
"v_mfma_f32_16x16x32_bf16 v[128:131], v[188:191], v[108:111], v[128:131]\n"
"v_add_u32 v231, 0x10000, v233\n"
"v_add_u32 v231, s57, v226\n"
"v_mfma_f32_16x16x32_bf16 v[72:75], v[188:191], v[112:115], v[72:75]\n"
"v_lshrrev_b32 v234, 3, v228\n"
"v_and_b32 v235, 7, v228\n"
"v_mfma_f32_16x16x32_bf16 v[52:55], v[188:191], v[116:119], v[52:55]\n"
"v_lshlrev_b32 v188, 13, v234\n"
"v_lshl_or_b32 v188, v235, 4, v188\n"
"v_mfma_f32_16x16x32_bf16 v[64:67], v[132:135], v[96:99], v[64:67]\n"
"v_lshrrev_b32 v191, 5, v228\n"
"v_lshlrev_b32 v190, 5, v191\n"
"v_mfma_f32_16x16x32_bf16 v[136:139], v[132:135], v[56:59], v[136:139]\n"
"ds_read_b128 v[56:59], v230 offset:4032\n"
"v_mfma_f32_16x16x32_bf16 v[164:167], v[132:135], v[100:103], v[164:167]\n"
"ds_read_b128 v[96:99], v230 offset:3520\n"
"v_mfma_f32_16x16x32_bf16 v[156:159], v[132:135], v[104:107], v[156:159]\n"
"ds_read_b128 v[100:103], v230 offset:3008\n"
"v_mfma_f32_16x16x32_bf16 v[148:151], v[132:135], v[108:111], v[148:151]\n"
"ds_read_b128 v[104:107], v230 offset:2496\n"
"v_mfma_f32_16x16x32_bf16 v[128:131], v[132:135], v[112:115], v[128:131]\n"
"ds_read_b128 v[108:111], v230 offset:1984\n"
"v_mfma_f32_16x16x32_bf16 v[72:75], v[132:135], v[116:119], v[72:75]\n"
"ds_read_b128 v[112:115], v230 offset:1472\n"
"v_mfma_f32_16x16x32_bf16 v[52:55], v[132:135], v[120:123], v[52:55]\n"
"ds_read_b128 v[116:119], v230 offset:960\n"
"s_waitcnt lgkmcnt(6)\n"
"v_mfma_f32_16x16x32_bf16 v[52:55], v[140:143], v[56:59], v[52:55]\n"
"ds_read_b128 v[120:123], v230 offset:448\n"
"v_mfma_f32_16x16x32_bf16 v[72:75], v[144:147], v[56:59], v[72:75]\n"
"ds_read2_b32 v[132:133], v229 offset0:112 offset1:113\n"
"s_waitcnt lgkmcnt(7)\n"
"v_mfma_f32_16x16x32_bf16 v[52:55], v[144:147], v[96:99], v[52:55]\n"
"ds_read2_b32 v[134:135], v229 offset0:114 offset1:115\n"
"v_mfma_f32_16x16x32_bf16 v[128:131], v[160:163], v[56:59], v[128:131]\n"
"v_lshlrev_b32 v191, 1, v191\n"
"v_xor_b32 v191, v191, v235\n"
"v_mfma_f32_16x16x32_bf16 v[72:75], v[160:163], v[96:99], v[72:75]\n"
"s_waitcnt lgkmcnt(7)\n"
"v_mfma_f32_16x16x32_bf16 v[52:55], v[160:163], v[100:103], v[52:55]\n"
"v_lshlrev_b32 v189, 8, v235\n"
"v_lshl_or_b32 v189, v191, 4, v189\n"
"v_mfma_f32_16x16x32_bf16 v[148:151], v[204:207], v[56:59], v[148:151]\n"
"v_and_b32 v191, 3, v234\n"
"v_lshl_or_b32 v189, v191, 2, v189\n"
"v_mfma_f32_16x16x32_bf16 v[128:131], v[204:207], v[96:99], v[128:131]\n"
"v_bfe_u32 v191, v228, 1, 4\n"
"v_lshl_or_b32 v190, v191, 8, v190\n"
"v_mfma_f32_16x16x32_bf16 v[72:75], v[204:207], v[100:103], v[72:75]\n"
"s_waitcnt lgkmcnt(6)\n"
"v_mfma_f32_16x16x32_bf16 v[52:55], v[204:207], v[104:107], v[52:55]\n"
"ds_read2_b32 v[140:141], v229 offset0:240 offset1:241\n"
"v_mfma_f32_16x16x32_bf16 v[156:159], v[176:179], v[56:59], v[156:159]\n"
"ds_read2_b32 v[142:143], v229 offset0:242 offset1:243\n"
"v_mfma_f32_16x16x32_bf16 v[148:151], v[176:179], v[96:99], v[148:151]\n"
"v_and_b32 v191, 1, v228\n"
"v_lshl_or_b32 v190, v191, 4, v190\n"
"v_mfma_f32_16x16x32_bf16 v[128:131], v[176:179], v[100:103], v[128:131]\n"
"v_mfma_f32_16x16x32_bf16 v[72:75], v[176:179], v[104:107], v[72:75]\n"
"s_waitcnt lgkmcnt(7)\n"
"v_mfma_f32_16x16x32_bf16 v[52:55], v[176:179], v[108:111], v[52:55]\n"
"v_mfma_f32_16x16x32_bf16 v[164:167], v[184:187], v[56:59], v[164:167]\n"
"v_mfma_f32_16x16x32_bf16 v[156:159], v[184:187], v[96:99], v[156:159]\n"
"v_mfma_f32_16x16x32_bf16 v[148:151], v[184:187], v[100:103], v[148:151]\n"
"v_mfma_f32_16x16x32_bf16 v[128:131], v[184:187], v[104:107], v[128:131]\n"
"v_mfma_f32_16x16x32_bf16 v[72:75], v[184:187], v[108:111], v[72:75]\n"
"s_waitcnt lgkmcnt(6)\n"
"v_mfma_f32_16x16x32_bf16 v[52:55], v[184:187], v[112:115], v[52:55]\n"
"s_waitcnt lgkmcnt(0)\n"
"v_mfma_f32_16x16x32_bf16 v[136:139], v[140:143], v[56:59], v[136:139]\n"
"v_mfma_f32_16x16x32_bf16 v[164:167], v[140:143], v[96:99], v[164:167]\n"
"v_mfma_f32_16x16x32_bf16 v[156:159], v[140:143], v[100:103], v[156:159]\n"
"v_mfma_f32_16x16x32_bf16 v[148:151], v[140:143], v[104:107], v[148:151]\n"
"v_mfma_f32_16x16x32_bf16 v[128:131], v[140:143], v[108:111], v[128:131]\n"
"s_barrier\n"
"v_mfma_f32_16x16x32_bf16 v[72:75], v[140:143], v[112:115], v[72:75]\n"
"v_mfma_f32_16x16x32_bf16 v[52:55], v[140:143], v[116:119], v[52:55]\n"
"v_mfma_f32_16x16x32_bf16 v[64:67], v[132:135], v[56:59], v[64:67]\n"
"v_mfma_f32_16x16x32_bf16 v[136:139], v[132:135], v[96:99], v[136:139]\n"
"v_mfma_f32_16x16x32_bf16 v[164:167], v[132:135], v[100:103], v[164:167]\n"
"v_mfma_f32_16x16x32_bf16 v[156:159], v[132:135], v[104:107], v[156:159]\n"
"v_mfma_f32_16x16x32_bf16 v[148:151], v[132:135], v[108:111], v[148:151]\n"
"v_mfma_f32_16x16x32_bf16 v[128:131], v[132:135], v[112:115], v[128:131]\n"
"v_mfma_f32_16x16x32_bf16 v[72:75], v[132:135], v[116:119], v[72:75]\n"
"v_mfma_f32_16x16x32_bf16 v[52:55], v[132:135], v[120:123], v[52:55]\n"
"s_nop 0\n"
"v_cvt_pk_bf16_f32 v56, v64, v65\n"
"v_cvt_pk_bf16_f32 v58, v136, v137\n"
"v_cvt_pk_bf16_f32 v57, v66, v67\n"
"v_cvt_pk_bf16_f32 v59, v138, v139\n"
"v_permlane16_swap_b32 v56, v58\n"
"s_nop 0\n"
"v_permlane16_swap_b32 v57, v59\n"
"global_store_dwordx4 v190, v[56:59], s[26:27] offset:-4096 sc1\n"
"s_nop 1\n"
"v_cvt_pk_bf16_f32 v56, v164, v165\n"
"v_cvt_pk_bf16_f32 v58, v156, v157\n"
"v_cvt_pk_bf16_f32 v57, v166, v167\n"
"v_cvt_pk_bf16_f32 v59, v158, v159\n"
"v_permlane16_swap_b32 v56, v58\n"
"s_nop 0\n"
"v_permlane16_swap_b32 v57, v59\n"
"global_store_dwordx4 v190, v[56:59], s[26:27] sc1\n"
"s_nop 1\n"
"v_cvt_pk_bf16_f32 v56, v148, v149\n"
"v_cvt_pk_bf16_f32 v58, v128, v129\n"
"v_cvt_pk_bf16_f32 v57, v150, v151\n"
"v_cvt_pk_bf16_f32 v59, v130, v131\n"
"v_permlane16_swap_b32 v56, v58\n"
"s_nop 0\n"
"v_permlane16_swap_b32 v57, v59\n"
"global_store_dwordx4 v190, v[56:59], s[68:69] offset:-4096 sc1\n"
"s_nop 1\n"
"v_cvt_pk_bf16_f32 v56, v72, v73\n"
"v_cvt_pk_bf16_f32 v58, v52, v53\n"
"v_cvt_pk_bf16_f32 v57, v74, v75\n"
"v_cvt_pk_bf16_f32 v59, v54, v55\n"
"v_permlane16_swap_b32 v56, v58\n"
"s_nop 0\n"
"v_permlane16_swap_b32 v57, v59\n"
"global_store_dwordx4 v190, v[56:59], s[68:69] sc1\n"
"s_barrier\n"
"ds_read_b64_tr_b16 v[52:53], v232 offset:49152\n"
"ds_read_b64_tr_b16 v[54:55], v233 offset:49152\n"
"ds_read_b64_tr_b16 v[56:57], v232 offset:50176\n"
"ds_read_b64_tr_b16 v[58:59], v233 offset:50176\n"
"ds_read_b64_tr_b16 v[64:65], v232 offset:51200\n"
"ds_read_b64_tr_b16 v[66:67], v233 offset:51200\n"
"ds_read_b64_tr_b16 v[72:73], v232 offset:52224\n"
"ds_read_b64_tr_b16 v[74:75], v233 offset:52224\n"
"ds_read_b64_tr_b16 v[96:97], v232 offset:53248\n"
"ds_read_b64_tr_b16 v[98:99], v233 offset:53248\n"
"ds_read_b64_tr_b16 v[100:101], v232 offset:54272\n"
"ds_read_b64_tr_b16 v[102:103], v233 offset:54272\n"
"s_waitcnt lgkmcnt(8)\n"
"v_mfma_f32_16x16x32_bf16 v[104:107], v[52:55], v[8:11], 0\n"
"ds_read_b64_tr_b16 v[108:109], v232 offset:55296\n"
"ds_read_b64_tr_b16 v[110:111], v233 offset:55296\n"
"v_mfma_f32_16x16x32_bf16 v[112:115], v[52:55], v[24:27], 0\n"
"v_mfma_f32_16x16x32_bf16 v[104:107], v[56:59], v[12:15], v[104:107]\n"
"ds_read_b64_tr_b16 v[52:53], v232 offset:56320\n"
"ds_read_b64_tr_b16 v[54:55], v233 offset:56320\n"
"v_mfma_f32_16x16x32_bf16 v[112:115], v[56:59], v[28:31], v[112:115]\n"
"s_waitcnt lgkmcnt(8)\n"
"v_mfma_f32_16x16x32_bf16 v[104:107], v[64:67], v[16:19], v[104:107]\n"
"ds_read_b64_tr_b16 v[56:57], v232 offset:57344\n"
"ds_read_b64_tr_b16 v[58:59], v233 offset:57344\n"
"v_mfma_f32_16x16x32_bf16 v[112:115], v[64:67], v[32:35], v[112:115]\n"
"s_lshr_b32 s13, s10, 4\n"
"s_add_u32 s13, s13, s10\n"
"s_and_b32 s13, s13, 15\n"
"v_mov_b32 v165, s13\n"
"s_and_b32 s14, s10, 15\n"
"s_lshl4_add_u32 s13, s13, s14\n"
"v_mov_b32 v164, s13\n"
"s_mov_b32 s81, 0x38000100\n"
"v_mfma_f32_16x16x32_bf16 v[104:107], v[72:75], v[20:23], v[104:107]\n"
"ds_read_b64_tr_b16 v[64:65], v232 offset:58368\n"
"ds_read_b64_tr_b16 v[66:67], v233 offset:58368\n"
"v_mfma_f32_16x16x32_bf16 v[112:115], v[72:75], v[36:39], v[112:115]\n"
"s_waitcnt lgkmcnt(8)\n"
"v_mfma_f32_16x16x32_bf16 v[104:107], v[96:99], v[0:3], v[104:107]\n"
"ds_read_b64_tr_b16 v[72:73], v232 offset:59392\n"
"ds_read_b64_tr_b16 v[74:75], v233 offset:59392\n"
"v_mfma_f32_16x16x32_bf16 v[112:115], v[96:99], v[40:43], v[112:115]\n"
"v_mfma_f32_16x16x32_bf16 v[104:107], v[100:103], v[4:7], v[104:107]\n"
"ds_read_b64_tr_b16 v[96:97], v232 offset:60416\n"
"ds_read_b64_tr_b16 v[98:99], v233 offset:60416\n"
"v_mfma_f32_16x16x32_bf16 v[112:115], v[100:103], v[68:71], v[112:115]\n"
"s_waitcnt lgkmcnt(8)\n"
"v_mfma_f32_16x16x32_bf16 v[104:107], v[108:111], v[44:47], v[104:107]\n"
"ds_read_b64_tr_b16 v[100:101], v232 offset:61440\n"
"ds_read_b64_tr_b16 v[102:103], v233 offset:61440\n"
"v_mfma_f32_16x16x32_bf16 v[112:115], v[108:111], v[76:79], v[112:115]\n"
"v_mfma_f32_16x16x32_bf16 v[104:107], v[52:55], v[48:51], v[104:107]\n"
"ds_read_b64_tr_b16 v[108:109], v232 offset:62464\n"
"ds_read_b64_tr_b16 v[110:111], v233 offset:62464\n"
"v_mfma_f32_16x16x32_bf16 v[112:115], v[52:55], v[80:83], v[112:115]\n"
"s_waitcnt lgkmcnt(8)\n"
"v_mfma_f32_16x16x32_bf16 v[104:107], v[56:59], v[60:63], v[104:107]\n"
"ds_read_b64_tr_b16 v[52:53], v232 offset:63488\n"
"ds_read_b64_tr_b16 v[54:55], v233 offset:63488\n"
"v_mfma_f32_16x16x32_bf16 v[112:115], v[56:59], v[124:127], v[112:115]\n"
"v_mfma_f32_16x16x32_bf16 v[104:107], v[64:67], v[84:87], v[104:107]\n"
"ds_read_b64_tr_b16 v[56:57], v232 offset:64512\n"
"ds_read_b64_tr_b16 v[58:59], v233 offset:64512\n"
"v_lshrrev_b32 v179, 1, v188\n"
"v_and_b32 v116, 56, v179\n"
"global_load_dwordx2 v[144:145], v116, s[38:39]\n"
"v_mfma_f32_16x16x32_bf16 v[112:115], v[64:67], v[168:171], v[112:115]\n"
"s_waitcnt lgkmcnt(8)\n"
"v_mfma_f32_16x16x32_bf16 v[104:107], v[72:75], v[88:91], v[104:107]\n"
"v_lshrrev_b32 v166, 9, v188\n"
"v_and_b32 v166, 0x70, v166\n"
"v_add_u32 v189, s57, v189\n"
"v_mfma_f32_16x16x32_bf16 v[112:115], v[72:75], v[172:175], v[112:115]\n"
"v_mfma_f32_16x16x32_bf16 v[104:107], v[96:99], v[92:95], v[104:107]\n"
"v_mfma_f32_16x16x32_bf16 v[112:115], v[96:99], v[180:183], v[112:115]\n"
"s_waitcnt lgkmcnt(0)\n"
"s_barrier\n"
"v_mfma_f32_16x16x32_bf16 v[104:107], v[100:103], v[152:155], v[104:107]\n"
"ds_read_b64_tr_b16 v[64:65], v232 offset:32768\n"
"ds_read_b64_tr_b16 v[66:67], v233 offset:32768\n"
"v_mfma_f32_16x16x32_bf16 v[112:115], v[100:103], v[208:211], v[112:115]\n"
"ds_read_b64_tr_b16 v[72:73], v232 offset:33792\n"
"ds_read_b64_tr_b16 v[74:75], v233 offset:33792\n"
"v_mfma_f32_16x16x32_bf16 v[104:107], v[108:111], v[192:195], v[104:107]\n"
"ds_read_b64_tr_b16 v[96:97], v232 offset:34816\n"
"ds_read_b64_tr_b16 v[98:99], v233 offset:34816\n"
"v_mfma_f32_16x16x32_bf16 v[112:115], v[108:111], v[212:215], v[112:115]\n"
"ds_read_b64_tr_b16 v[100:101], v232 offset:35840\n"
"ds_read_b64_tr_b16 v[102:103], v233 offset:35840\n"
"v_mfma_f32_16x16x32_bf16 v[104:107], v[52:55], v[196:199], v[104:107]\n"
"ds_read_b64_tr_b16 v[108:109], v232 offset:36864\n"
"ds_read_b64_tr_b16 v[110:111], v233 offset:36864\n"
"ds_read_b128 v[116:119], v231 offset:4160\n"
"ds_read_b128 v[120:123], v231 offset:5184\n"
"v_mfma_f32_16x16x32_bf16 v[112:115], v[52:55], v[216:219], v[112:115]\n"
"v_mfma_f32_16x16x32_bf16 v[104:107], v[56:59], v[200:203], v[104:107]\n"
"ds_read_b64_tr_b16 v[52:53], v232 offset:37888\n"
"ds_read_b64_tr_b16 v[54:55], v233 offset:37888\n"
"v_mfma_f32_16x16x32_bf16 v[112:115], v[56:59], v[220:223], v[112:115]\n"
"s_waitcnt lgkmcnt(10)\n"
"v_mfma_f32_16x16x32_bf16 v[56:59], v[64:67], v[8:11], 0\n"
"ds_read_b64_tr_b16 v[128:129], v232 offset:38912\n"
"ds_read_b64_tr_b16 v[130:131], v233 offset:38912\n"
"v_mfma_f32_16x16x32_bf16 v[132:135], v[64:67], v[24:27], 0\n"
"v_mfma_f32_16x16x32_bf16 v[56:59], v[72:75], v[12:15], v[56:59]\n"
"ds_read_b64_tr_b16 v[64:65], v232 offset:39936\n"
"ds_read_b64_tr_b16 v[66:67], v233 offset:39936\n"
"v_mfma_f32_16x16x32_bf16 v[132:135], v[72:75], v[28:31], v[132:135]\n"
"s_waitcnt lgkmcnt(7)\n"
"v_add_f32 v116, v116, v104\n"
"v_add_f32 v117, v117, v105\n"
"v_add_f32 v118, v118, v106\n"
"v_add_f32 v119, v119, v107\n"
"v_mfma_f32_16x16x32_bf16 v[56:59], v[96:99], v[16:19], v[56:59]\n"
"ds_read_b64_tr_b16 v[72:73], v232 offset:40960\n"
"ds_read_b64_tr_b16 v[74:75], v233 offset:40960\n"
"s_waitcnt lgkmcnt(8)\n"
"v_add_f32 v120, v120, v112\n"
"v_add_f32 v121, v121, v113\n"
"v_add_f32 v122, v122, v114\n"
"v_mfma_f32_16x16x32_bf16 v[132:135], v[96:99], v[32:35], v[132:135]\n"
"v_add_f32 v123, v123, v115\n"
"v_max3_f32 v112, |v116|, |v117|, |v118|\n"
"v_max3_f32 v112, v112, |v119|, |v120|\n"
"v_max3_f32 v112, v112, |v121|, |v122|\n"
"v_max_f32_e64 v112, v112, |v123|\n"
"v_mfma_f32_16x16x32_bf16 v[56:59], v[100:103], v[20:23], v[56:59]\n"
"ds_read_b64_tr_b16 v[96:97], v232 offset:41984\n"
"ds_read_b64_tr_b16 v[98:99], v233 offset:41984\n"
"v_lshrrev_b32 v112, 23, v112\n"
"v_sub_u32 v113, 0x7e, v112\n"
"v_ldexp_f32 v116, v116, v113\n"
"v_mfma_f32_16x16x32_bf16 v[132:135], v[100:103], v[36:39], v[132:135]\n"
"v_ldexp_f32 v117, v117, v113\n"
"v_ldexp_f32 v118, v118, v113\n"
"v_ldexp_f32 v119, v119, v113\n"
"v_ldexp_f32 v120, v120, v113\n"
"s_waitcnt lgkmcnt(8)\n"
"v_mfma_f32_16x16x32_bf16 v[56:59], v[108:111], v[0:3], v[56:59]\n"
"ds_read_b64_tr_b16 v[100:101], v232 offset:43008\n"
"ds_read_b64_tr_b16 v[102:103], v233 offset:43008\n"
"v_ldexp_f32 v121, v121, v113\n"
"v_ldexp_f32 v122, v122, v113\n"
"v_ldexp_f32 v123, v123, v113\n"
"v_mfma_f32_16x16x32_bf16 v[132:135], v[108:111], v[40:43], v[132:135]\n"
"v_lshrrev_b32 v113, 4, v112\n"
"v_cvt_pknorm_i16_f32 v104, v116, v117\n"
"v_cvt_pknorm_i16_f32 v105, v118, v119\n"
"v_cvt_pknorm_i16_f32 v106, v120, v121\n"
"v_cvt_pknorm_i16_f32 v107, v122, v123\n"
"v_mfma_f32_16x16x32_bf16 v[56:59], v[52:55], v[4:7], v[56:59]\n"
"ds_read_b64_tr_b16 v[108:109], v232 offset:44032\n"
"ds_read_b64_tr_b16 v[110:111], v233 offset:44032\n"
"v_bfi_b32 v104, 15, v164, v104\n"
"v_bfi_b32 v105, 15, v112, v105\n"
"v_bfi_b32 v106, 15, v165, v106\n"
"v_mfma_f32_16x16x32_bf16 v[132:135], v[52:55], v[68:71], v[132:135]\n"
"v_bfi_b32 v107, 15, v113, v107\n"
"s_waitcnt lgkmcnt(8)\n"
"v_mfma_f32_16x16x32_bf16 v[56:59], v[128:131], v[44:47], v[56:59]\n"
"ds_read_b64_tr_b16 v[52:53], v232 offset:45056\n"
"ds_read_b64_tr_b16 v[54:55], v233 offset:45056\n"
"v_mfma_f32_16x16x32_bf16 v[132:135], v[128:131], v[76:79], v[132:135]\n"
"v_mfma_f32_16x16x32_bf16 v[56:59], v[64:67], v[48:51], v[56:59]\n"
"ds_read_b64_tr_b16 v[112:113], v232 offset:46080\n"
"ds_read_b64_tr_b16 v[114:115], v233 offset:46080\n"
"v_mfma_f32_16x16x32_bf16 v[132:135], v[64:67], v[80:83], v[132:135]\n"
"s_waitcnt lgkmcnt(8)\n"
"v_mfma_f32_16x16x32_bf16 v[56:59], v[72:75], v[60:63], v[56:59]\n"
"ds_read_b64_tr_b16 v[64:65], v232 offset:47104\n"
"ds_read_b64_tr_b16 v[66:67], v233 offset:47104\n"
"s_and_b32 s13, s11, 0xffffdfff\n"
"s_or_b32 s13, s13, s7\n"
"v_mov_b32 v116, s10\n"
"v_mov_b32 v117, s13\n"
"s_waitcnt vmcnt(1)\n"
"global_store_dwordx2 v227, v[116:117], s[40:41] sc1\n"
"v_mfma_f32_16x16x32_bf16 v[132:135], v[72:75], v[124:127], v[132:135]\n"
"v_mfma_f32_16x16x32_bf16 v[56:59], v[96:99], v[84:87], v[56:59]\n"
"ds_read_b64_tr_b16 v[72:73], v232 offset:48128\n"
"ds_read_b64_tr_b16 v[74:75], v233 offset:48128\n"
"global_store_dwordx4 v226, v[104:107], s[42:43] sc1\n"
"s_nop 1\n"
"global_load_dwordx4 v[104:107], v166, s[18:19] sc1\n"
"global_load_dwordx4 v[116:119], v166, s[20:21] sc1\n"
"global_load_dwordx2 v[146:147], v179, s[32:33]\n"
"global_load_dwordx2 v[156:157], v179, s[32:33] offset:2048\n"
"v_mfma_f32_16x16x32_bf16 v[132:135], v[96:99], v[168:171], v[132:135]\n"
"global_load_dwordx2 v[162:163], v179, s[34:35]\n"
"global_load_dwordx2 v[160:161], v179, s[34:35] offset:2048\n"
"s_waitcnt lgkmcnt(8)\n"
"v_mfma_f32_16x16x32_bf16 v[56:59], v[100:103], v[88:91], v[56:59]\n"
"v_mfma_f32_16x16x32_bf16 v[132:135], v[100:103], v[172:175], v[132:135]\n"
"v_mfma_f32_16x16x32_bf16 v[56:59], v[108:111], v[92:95], v[56:59]\n"
"v_mfma_f32_16x16x32_bf16 v[132:135], v[108:111], v[180:183], v[132:135]\n"
"s_waitcnt lgkmcnt(0)\n"
"s_barrier\n"
"v_mfma_f32_16x16x32_bf16 v[56:59], v[52:55], v[152:155], v[56:59]\n"
"ds_read_b64_tr_b16 v[96:97], v232\n"
"ds_read_b64_tr_b16 v[98:99], v233\n"
"v_mfma_f32_16x16x32_bf16 v[132:135], v[52:55], v[208:211], v[132:135]\n"
"ds_read_b64_tr_b16 v[52:53], v232 offset:1024\n"
"ds_read_b64_tr_b16 v[54:55], v233 offset:1024\n"
"v_mfma_f32_16x16x32_bf16 v[56:59], v[112:115], v[192:195], v[56:59]\n"
"ds_read_b64_tr_b16 v[100:101], v232 offset:2048\n"
"ds_read_b64_tr_b16 v[102:103], v233 offset:2048\n"
"v_mfma_f32_16x16x32_bf16 v[132:135], v[112:115], v[212:215], v[132:135]\n"
"ds_read_b64_tr_b16 v[108:109], v232 offset:3072\n"
"ds_read_b64_tr_b16 v[110:111], v233 offset:3072\n"
"v_mfma_f32_16x16x32_bf16 v[56:59], v[64:67], v[196:199], v[56:59]\n"
"ds_read_b64_tr_b16 v[112:113], v232 offset:4096\n"
"ds_read_b64_tr_b16 v[114:115], v233 offset:4096\n"
"ds_read_b128 v[120:123], v231 offset:6208\n"
"ds_read_b128 v[128:131], v231 offset:7232\n"
"v_mfma_f32_16x16x32_bf16 v[132:135], v[64:67], v[216:219], v[132:135]\n"
"v_mfma_f32_16x16x32_bf16 v[56:59], v[72:75], v[200:203], v[56:59]\n"
"ds_read_b64_tr_b16 v[64:65], v232 offset:5120\n"
"ds_read_b64_tr_b16 v[66:67], v233 offset:5120\n"
"v_mfma_f32_16x16x32_bf16 v[132:135], v[72:75], v[220:223], v[132:135]\n"
"s_waitcnt lgkmcnt(10)\n"
"v_mfma_f32_16x16x32_bf16 v[72:75], v[96:99], v[8:11], 0\n"
"ds_read_b64_tr_b16 v[136:137], v232 offset:6144\n"
"ds_read_b64_tr_b16 v[138:139], v233 offset:6144\n"
"v_mfma_f32_16x16x32_bf16 v[140:143], v[96:99], v[24:27], 0\n"
"v_mfma_f32_16x16x32_bf16 v[72:75], v[52:55], v[12:15], v[72:75]\n"
"ds_read_b64_tr_b16 v[96:97], v232 offset:7168\n"
"ds_read_b64_tr_b16 v[98:99], v233 offset:7168\n"
"v_mfma_f32_16x16x32_bf16 v[140:143], v[52:55], v[28:31], v[140:143]\n"
"s_waitcnt lgkmcnt(7)\n"
"v_add_f32 v120, v120, v56\n"
"v_add_f32 v121, v121, v57\n"
"v_add_f32 v122, v122, v58\n"
"v_add_f32 v123, v123, v59\n"
"v_mfma_f32_16x16x32_bf16 v[72:75], v[100:103], v[16:19], v[72:75]\n"
"ds_read_b64_tr_b16 v[52:53], v232 offset:8192\n"
"ds_read_b64_tr_b16 v[54:55], v233 offset:8192\n"
"s_waitcnt lgkmcnt(8)\n"
"v_add_f32 v128, v128, v132\n"
"v_add_f32 v129, v129, v133\n"
"v_add_f32 v130, v130, v134\n"
"v_mfma_f32_16x16x32_bf16 v[140:143], v[100:103], v[32:35], v[140:143]\n"
"v_add_f32 v131, v131, v135\n"
"v_max3_f32 v132, |v120|, |v121|, |v122|\n"
"v_max3_f32 v132, v132, |v123|, |v128|\n"
"v_max3_f32 v132, v132, |v129|, |v130|\n"
"v_max_f32_e64 v132, v132, |v131|\n"
"v_mfma_f32_16x16x32_bf16 v[72:75], v[108:111], v[20:23], v[72:75]\n"
"ds_read_b64_tr_b16 v[56:57], v232 offset:9216\n"
"ds_read_b64_tr_b16 v[58:59], v233 offset:9216\n"
"v_lshrrev_b32 v132, 23, v132\n"
"v_sub_u32 v133, 0x7e, v132\n"
"v_ldexp_f32 v120, v120, v133\n"
"v_mfma_f32_16x16x32_bf16 v[140:143], v[108:111], v[36:39], v[140:143]\n"
"v_ldexp_f32 v121, v121, v133\n"
"v_ldexp_f32 v122, v122, v133\n"
"v_ldexp_f32 v123, v123, v133\n"
"v_ldexp_f32 v128, v128, v133\n"
"s_waitcnt lgkmcnt(8)\n"
"v_mfma_f32_16x16x32_bf16 v[72:75], v[112:115], v[0:3], v[72:75]\n"
"ds_read_b64_tr_b16 v[100:101], v232 offset:10240\n"
"ds_read_b64_tr_b16 v[102:103], v233 offset:10240\n"
"v_ldexp_f32 v129, v129, v133\n"
"v_ldexp_f32 v130, v130, v133\n"
"v_ldexp_f32 v131, v131, v133\n"
"v_mfma_f32_16x16x32_bf16 v[140:143], v[112:115], v[40:43], v[140:143]\n"
"v_lshrrev_b32 v133, 4, v132\n"
"v_cvt_pknorm_i16_f32 v108, v120, v121\n"
"v_cvt_pknorm_i16_f32 v109, v122, v123\n"
"v_cvt_pknorm_i16_f32 v110, v128, v129\n"
"v_cvt_pknorm_i16_f32 v111, v130, v131\n"
"v_mfma_f32_16x16x32_bf16 v[72:75], v[64:67], v[4:7], v[72:75]\n"
"ds_read_b64_tr_b16 v[112:113], v232 offset:11264\n"
"ds_read_b64_tr_b16 v[114:115], v233 offset:11264\n"
"v_bfi_b32 v108, 15, v164, v108\n"
"v_bfi_b32 v109, 15, v132, v109\n"
"v_bfi_b32 v110, 15, v165, v110\n"
"v_mfma_f32_16x16x32_bf16 v[140:143], v[64:67], v[68:71], v[140:143]\n"
"v_bfi_b32 v111, 15, v133, v111\n"
"s_waitcnt lgkmcnt(8)\n"
"v_mfma_f32_16x16x32_bf16 v[72:75], v[136:139], v[44:47], v[72:75]\n"
"ds_read_b64_tr_b16 v[64:65], v232 offset:12288\n"
"ds_read_b64_tr_b16 v[66:67], v233 offset:12288\n"
"v_mfma_f32_16x16x32_bf16 v[140:143], v[136:139], v[76:79], v[140:143]\n"
"v_mfma_f32_16x16x32_bf16 v[72:75], v[96:99], v[48:51], v[72:75]\n"
"ds_read_b64_tr_b16 v[120:121], v232 offset:13312\n"
"ds_read_b64_tr_b16 v[122:123], v233 offset:13312\n"
"v_mfma_f32_16x16x32_bf16 v[140:143], v[96:99], v[80:83], v[140:143]\n"
"s_waitcnt lgkmcnt(8)\n"
"v_mfma_f32_16x16x32_bf16 v[72:75], v[52:55], v[60:63], v[72:75]\n"
"ds_read_b64_tr_b16 v[96:97], v232 offset:14336\n"
"ds_read_b64_tr_b16 v[98:99], v233 offset:14336\n"
"v_mfma_f32_16x16x32_bf16 v[140:143], v[52:55], v[124:127], v[140:143]\n"
"v_mfma_f32_16x16x32_bf16 v[72:75], v[56:59], v[84:87], v[72:75]\n"
"ds_read_b64_tr_b16 v[52:53], v232 offset:15360\n"
"ds_read_b64_tr_b16 v[54:55], v233 offset:15360\n"
"global_store_dwordx4 v226, v[108:111], s[42:43] offset:1024 sc1\n"
"s_nop 1\n"
"global_load_dwordx4 v[108:111], v226, s[60:61] sc1\n"
"v_mfma_f32_16x16x32_bf16 v[140:143], v[56:59], v[168:171], v[140:143]\n"
"s_waitcnt lgkmcnt(8)\n"
"v_mfma_f32_16x16x32_bf16 v[72:75], v[100:103], v[88:91], v[72:75]\n"
"s_waitcnt vmcnt(10)\n"
"v_lshlrev_b32 v178, 16, v144\n"
"v_and_b32 v167, 0xffff0000, v144\n"
"v_lshlrev_b32 v176, 16, v145\n"
"v_and_b32 v177, 0xffff0000, v145\n"
"v_mfma_f32_16x16x32_bf16 v[140:143], v[100:103], v[172:175], v[140:143]\n"
"v_mfma_f32_16x16x32_bf16 v[72:75], v[112:115], v[92:95], v[72:75]\n"
"v_mfma_f32_16x16x32_bf16 v[140:143], v[112:115], v[180:183], v[140:143]\n"
"s_waitcnt lgkmcnt(0)\n"
"s_barrier\n"
"v_mfma_f32_16x16x32_bf16 v[72:75], v[64:67], v[152:155], v[72:75]\n"
"ds_read_b64_tr_b16 v[56:57], v232 offset:49152\n"
"ds_read_b64_tr_b16 v[58:59], v233 offset:49152\n"
"s_waitcnt vmcnt(7)\n"
"v_xor_b32 v112, s10, v105\n"
"v_bitop3_b32 v112, v112, v107, s10 bitop3:0xf6\n"
"s_waitcnt vmcnt(6)\n"
"v_bitop3_b32 v112, v112, v117, s10 bitop3:0xf6\n"
"v_mfma_f32_16x16x32_bf16 v[140:143], v[64:67], v[208:211], v[140:143]\n"
"ds_read_b64_tr_b16 v[64:65], v232 offset:50176\n"
"ds_read_b64_tr_b16 v[66:67], v233 offset:50176\n"
"v_bitop3_b32 v112, v112, v119, s10 bitop3:0xf6\n"
"v_mfma_f32_16x16x32_bf16 v[72:75], v[120:123], v[192:195], v[72:75]\n"
"ds_read_b64_tr_b16 v[100:101], v232 offset:51200\n"
"ds_read_b64_tr_b16 v[102:103], v233 offset:51200\n"
"v_cmp_ne_u32 vcc, 0, v112\n"
"s_nop 0\n"
"s_cbranch_vccnz .Lr6\n"
".Lb6:\n"
"v_sub_f32 v148, 1.0, v104\n"
"v_mfma_f32_16x16x32_bf16 v[140:143], v[120:123], v[212:215], v[140:143]\n"
"ds_read_b64_tr_b16 v[112:113], v232 offset:52224\n"
"ds_read_b64_tr_b16 v[114:115], v233 offset:52224\n"
"v_add_f32 v206, v104, v104\n"
"v_cvt_pk_bf16_f32 v148, 0, v148\n"
"v_mfma_f32_16x16x32_bf16 v[72:75], v[96:99], v[196:199], v[72:75]\n"
"ds_read_b64_tr_b16 v[120:121], v232 offset:53248\n"
"ds_read_b64_tr_b16 v[122:123], v233 offset:53248\n"
"ds_read_b128 v[128:131], v231 offset:4160\n"
"ds_read_b128 v[132:135], v231 offset:5184\n"
"s_waitcnt vmcnt(5)\n"
"v_lshlrev_b32 v187, 16, v146\n"
"v_mfma_f32_16x16x32_bf16 v[140:143], v[96:99], v[216:219], v[140:143]\n"
"v_and_b32 v184, 0xffff0000, v146\n"
"v_lshlrev_b32 v185, 16, v147\n"
"v_and_b32 v186, 0xffff0000, v147\n"
"v_mul_f32 v96, v104, v178\n"
"v_mul_f32 v97, v104, v167\n"
"v_mfma_f32_16x16x32_bf16 v[72:75], v[52:55], v[200:203], v[72:75]\n"
"ds_read_b64_tr_b16 v[136:137], v232 offset:54272\n"
"ds_read_b64_tr_b16 v[138:139], v233 offset:54272\n"
"v_mul_f32 v98, v104, v176\n"
"v_mul_f32 v99, v104, v177\n"
"v_mul_f32 v96, v96, v187\n"
"v_mfma_f32_16x16x32_bf16 v[140:143], v[52:55], v[220:223], v[140:143]\n"
"v_mul_f32 v97, v97, v184\n"
"v_mul_f32 v98, v98, v185\n"
"v_mul_f32 v99, v99, v186\n"
"v_mul_f32 v187, v187, v148\n"
"s_waitcnt lgkmcnt(10)\n"
"v_mfma_f32_16x16x32_bf16 v[52:55], v[56:59], v[8:11], 0\n"
"ds_read_b64_tr_b16 v[144:145], v232 offset:55296\n"
"ds_read_b64_tr_b16 v[146:147], v233 offset:55296\n"
"v_mul_f32 v184, v184, v148\n"
"v_mul_f32 v185, v185, v148\n"
"v_mul_f32 v186, v186, v148\n"
"v_mfma_f32_16x16x32_bf16 v[148:151], v[56:59], v[24:27], 0\n"
"v_sub_f32 v227, 1.0, v106\n"
"v_add_f32 v207, v106, v106\n"
"v_cvt_pk_bf16_f32 v227, 0, v227\n"
"s_waitcnt vmcnt(4)\n"
"v_lshlrev_b32 v190, 16, v156\n"
"v_and_b32 v191, 0xffff0000, v156\n"
"v_mfma_f32_16x16x32_bf16 v[52:55], v[64:67], v[12:15], v[52:55]\n"
"ds_read_b64_tr_b16 v[56:57], v232 offset:56320\n"
"ds_read_b64_tr_b16 v[58:59], v233 offset:56320\n"
"v_lshlrev_b32 v205, 16, v157\n"
"v_and_b32 v204, 0xffff0000, v157\n"
"v_mul_f32 v156, v106, v178\n"
"v_mfma_f32_16x16x32_bf16 v[148:151], v[64:67], v[28:31], v[148:151]\n"
"s_waitcnt lgkmcnt(7)\n"
"v_add_f32 v128, v128, v72\n"
"v_add_f32 v129, v129, v73\n"
"v_add_f32 v130, v130, v74\n"
"v_add_f32 v131, v131, v75\n"
"v_mfma_f32_16x16x32_bf16 v[52:55], v[100:103], v[16:19], v[52:55]\n"
"ds_read_b64_tr_b16 v[64:65], v232 offset:57344\n"
"ds_read_b64_tr_b16 v[66:67], v233 offset:57344\n"
"s_waitcnt lgkmcnt(8)\n"
"v_add_f32 v132, v132, v140\n"
"v_add_f32 v133, v133, v141\n"
"v_add_f32 v134, v134, v142\n"
"v_mfma_f32_16x16x32_bf16 v[148:151], v[100:103], v[32:35], v[148:151]\n"
"v_add_f32 v135, v135, v143\n"
"v_mul_f32 v157, v106, v167\n"
"v_mul_f32 v158, v106, v176\n"
"v_mul_f32 v159, v106, v177\n"
"v_mul_f32 v156, v156, v190\n"
"v_mfma_f32_16x16x32_bf16 v[52:55], v[112:115], v[20:23], v[52:55]\n"
"ds_read_b64_tr_b16 v[72:73], v232 offset:58368\n"
"ds_read_b64_tr_b16 v[74:75], v233 offset:58368\n"
"v_mul_f32 v157, v157, v191\n"
"v_mul_f32 v158, v158, v205\n"
"v_mul_f32 v159, v159, v204\n"
"v_mfma_f32_16x16x32_bf16 v[148:151], v[112:115], v[36:39], v[148:151]\n"
"v_mul_f32 v190, v190, v227\n"
"v_mul_f32 v191, v191, v227\n"
"v_mul_f32 v205, v205, v227\n"
"v_mul_f32 v204, v204, v227\n"
"s_waitcnt lgkmcnt(8)\n"
"v_mfma_f32_16x16x32_bf16 v[52:55], v[120:123], v[0:3], v[52:55]\n"
"ds_read_b64_tr_b16 v[100:101], v232 offset:59392\n"
"ds_read_b64_tr_b16 v[102:103], v233 offset:59392\n"
"v_mfma_f32_16x16x32_bf16 v[148:151], v[120:123], v[40:43], v[148:151]\n"
"v_mfma_f32_16x16x32_bf16 v[52:55], v[136:139], v[4:7], v[52:55]\n"
"ds_read_b64_tr_b16 v[104:105], v232 offset:60416\n"
"ds_read_b64_tr_b16 v[106:107], v233 offset:60416\n"
"v_mfma_f32_16x16x32_bf16 v[148:151], v[136:139], v[68:71], v[148:151]\n"
"s_waitcnt lgkmcnt(8)\n"
"v_mfma_f32_16x16x32_bf16 v[52:55], v[144:147], v[44:47], v[52:55]\n"
"ds_read_b64_tr_b16 v[112:113], v232 offset:61440\n"
"ds_read_b64_tr_b16 v[114:115], v233 offset:61440\n"
"s_waitcnt vmcnt(0)\n"
"v_lshlrev_b32 v120, 4, v110\n"
"v_bfi_b32 v120, 15, v108, v120\n"
"v_mfma_f32_16x16x32_bf16 v[148:151], v[144:147], v[76:79], v[148:151]\n"
"v_cmp_ne_u32_sdwa vcc, v120, v164 src0_sel:BYTE_0 src1_sel:DWORD\n"
"s_nop 0\n"
"s_cbranch_vccnz .Lr2\n"
".Lb2:\n"
"v_lshlrev_b32 v137, 4, v111\n"
"v_bfi_b32 v137, 15, v109, v137\n"
"v_and_b32 v137, 0xff, v137\n"
"v_mfma_f32_16x16x32_bf16 v[52:55], v[56:59], v[48:51], v[52:55]\n"
"ds_read_b64_tr_b16 v[120:121], v232 offset:62464\n"
"ds_read_b64_tr_b16 v[122:123], v233 offset:62464\n"
"v_subrev_u32 v137, 0x7e, v137\n"
"v_ldexp_f32 v136, s81, v137\n"
"v_cvt_f32_i32_sdwa v139, sext(v108) dst_sel:DWORD dst_unused:UNUSED_PAD src0_sel:WORD_1\n"
"v_mfma_f32_16x16x32_bf16 v[148:151], v[56:59], v[80:83], v[148:151]\n"
"v_cvt_f32_i32_sdwa v140, sext(v109) dst_sel:DWORD dst_unused:UNUSED_PAD src0_sel:WORD_1\n"
"v_cvt_f32_i32_sdwa v138, sext(v110) dst_sel:DWORD dst_unused:UNUSED_PAD src0_sel:WORD_1\n"
"v_cvt_f32_i32_sdwa v137, sext(v111) dst_sel:DWORD dst_unused:UNUSED_PAD src0_sel:WORD_1\n"
"v_and_or_b32 v108, v108, -16, 8\n"
"s_waitcnt lgkmcnt(8)\n"
"v_mfma_f32_16x16x32_bf16 v[52:55], v[64:67], v[60:63], v[52:55]\n"
"ds_read_b64_tr_b16 v[56:57], v232 offset:63488\n"
"ds_read_b64_tr_b16 v[58:59], v233 offset:63488\n"
"v_and_or_b32 v109, v109, -16, 8\n"
"v_and_or_b32 v110, v110, -16, 8\n"
"v_and_or_b32 v111, v111, -16, 8\n"
"v_mfma_f32_16x16x32_bf16 v[148:151], v[64:67], v[124:127], v[148:151]\n"
"v_cvt_f32_i32_sdwa v64, sext(v108) dst_sel:DWORD dst_unused:UNUSED_PAD src0_sel:WORD_0\n"
"v_cvt_f32_i32_sdwa v143, sext(v109) dst_sel:DWORD dst_unused:UNUSED_PAD src0_sel:WORD_0\n"
"v_cvt_f32_i32_sdwa v142, sext(v110) dst_sel:DWORD dst_unused:UNUSED_PAD src0_sel:WORD_0\n"
"v_cvt_f32_i32_sdwa v141, sext(v111) dst_sel:DWORD dst_unused:UNUSED_PAD src0_sel:WORD_0\n"
"v_fmac_f32 v128, v136, v64\n"
"v_mfma_f32_16x16x32_bf16 v[52:55], v[72:75], v[84:87], v[52:55]\n"
"ds_read_b64_tr_b16 v[64:65], v232 offset:64512\n"
"ds_read_b64_tr_b16 v[66:67], v233 offset:64512\n"
"v_fmac_f32 v129, v136, v139\n"
"v_fmac_f32 v130, v136, v143\n"
"v_fmac_f32 v131, v136, v140\n"
"global_load_dwordx4 v[108:111], v226, s[60:61] offset:1024 sc1\n"
"v_mfma_f32_16x16x32_bf16 v[148:151], v[72:75], v[168:171], v[148:151]\n"
"v_fmac_f32 v132, v136, v142\n"
"v_fmac_f32 v133, v136, v138\n"
"v_fmac_f32 v134, v136, v141\n"
"v_fmac_f32 v135, v136, v137\n"
"s_waitcnt lgkmcnt(8)\n"
"v_mfma_f32_16x16x32_bf16 v[52:55], v[100:103], v[88:91], v[52:55]\n"
"v_fmac_f32_dpp v96, v128, v206 quad_perm:[0,1,2,3] row_mask:0xf bank_mask:0x3\n"
"v_fmac_f32_dpp v97, v132, v206 quad_perm:[0,1,2,3] row_mask:0xf bank_mask:0x3\n"
"v_fmac_f32_dpp v98, v128, v206 row_ror:8 row_mask:0xf bank_mask:0x3\n"
"v_fmac_f32_dpp v99, v132, v206 row_ror:8 row_mask:0xf bank_mask:0x3\n"
"v_fmac_f32_dpp v96, v130, v206 row_ror:8 row_mask:0xf bank_mask:0xc\n"
"v_mfma_f32_16x16x32_bf16 v[148:151], v[100:103], v[172:175], v[148:151]\n"
"v_fmac_f32_dpp v97, v134, v206 row_ror:8 row_mask:0xf bank_mask:0xc\n"
"v_fmac_f32_dpp v98, v130, v206 quad_perm:[0,1,2,3] row_mask:0xf bank_mask:0xc\n"
"v_fmac_f32_dpp v99, v134, v206 quad_perm:[0,1,2,3] row_mask:0xf bank_mask:0xc\n"
"v_fmac_f32_dpp v156, v129, v207 quad_perm:[0,1,2,3] row_mask:0xf bank_mask:0x3\n"
"v_fmac_f32_dpp v157, v133, v207 quad_perm:[0,1,2,3] row_mask:0xf bank_mask:0x3\n"
"v_mfma_f32_16x16x32_bf16 v[52:55], v[104:107], v[92:95], v[52:55]\n"
"v_fmac_f32_dpp v158, v129, v207 row_ror:8 row_mask:0xf bank_mask:0x3\n"
"v_fmac_f32_dpp v159, v133, v207 row_ror:8 row_mask:0xf bank_mask:0x3\n"
"v_fmac_f32_dpp v156, v131, v207 row_ror:8 row_mask:0xf bank_mask:0xc\n"
"v_fmac_f32_dpp v157, v135, v207 row_ror:8 row_mask:0xf bank_mask:0xc\n"
"v_fmac_f32_dpp v158, v131, v207 quad_perm:[0,1,2,3] row_mask:0xf bank_mask:0xc\n"
"v_mfma_f32_16x16x32_bf16 v[148:151], v[104:107], v[180:183], v[148:151]\n"
"v_fmac_f32_dpp v159, v135, v207 quad_perm:[0,1,2,3] row_mask:0xf bank_mask:0xc\n"
"v_sub_f32 v140, 1.0, v116\n"
"s_waitcnt lgkmcnt(0)\n"
"s_barrier\n"
"v_mfma_f32_16x16x32_bf16 v[52:55], v[112:115], v[152:155], v[52:55]\n"
"ds_read_b64_tr_b16 v[72:73], v232 offset:32768\n"
"ds_read_b64_tr_b16 v[74:75], v233 offset:32768\n"
"v_add_f32 v236, v116, v116\n"
"v_cvt_pk_bf16_f32 v140, 0, v140\n"
"v_lshlrev_b32 v230, 16, v162\n"
"v_mfma_f32_16x16x32_bf16 v[148:151], v[112:115], v[208:211], v[148:151]\n"
"ds_read_b64_tr_b16 v[100:101], v232 offset:33792\n"
"ds_read_b64_tr_b16 v[102:103], v233 offset:33792\n"
"v_and_b32 v234, 0xffff0000, v162\n"
"v_lshlrev_b32 v235, 16, v163\n"
"v_and_b32 v206, 0xffff0000, v163\n"
"v_mfma_f32_16x16x32_bf16 v[52:55], v[120:123], v[192:195], v[52:55]\n"
"ds_read_b64_tr_b16 v[104:105], v232 offset:34816\n"
"ds_read_b64_tr_b16 v[106:107], v233 offset:34816\n"
"v_mul_f32 v112, v116, v178\n"
"v_mul_f32 v113, v116, v167\n"
"v_mul_f32 v114, v116, v176\n"
"v_mfma_f32_16x16x32_bf16 v[148:151], v[120:123], v[212:215], v[148:151]\n"
"ds_read_b64_tr_b16 v[120:121], v232 offset:35840\n"
"ds_read_b64_tr_b16 v[122:123], v233 offset:35840\n"
"v_mul_f32 v115, v116, v177\n"
"v_mul_f32 v112, v112, v230\n"
"v_mfma_f32_16x16x32_bf16 v[52:55], v[56:59], v[196:199], v[52:55]\n"
"ds_read_b64_tr_b16 v[128:129], v232 offset:36864\n"
"ds_read_b64_tr_b16 v[130:131], v233 offset:36864\n"
"ds_read_b128 v[132:135], v231 offset:6208\n"
"ds_read_b128 v[136:139], v231 offset:7232\n"
"v_mul_f32 v113, v113, v234\n"
"v_mfma_f32_16x16x32_bf16 v[148:151], v[56:59], v[216:219], v[148:151]\n"
"v_mul_f32 v114, v114, v235\n"
"v_mul_f32 v115, v115, v206\n"
"v_mul_f32 v230, v230, v140\n"
"v_mul_f32 v234, v234, v140\n"
"v_mul_f32 v235, v235, v140\n"
"v_mfma_f32_16x16x32_bf16 v[52:55], v[64:67], v[200:203], v[52:55]\n"
"ds_read_b64_tr_b16 v[56:57], v232 offset:37888\n"
"ds_read_b64_tr_b16 v[58:59], v233 offset:37888\n"
"v_mul_f32 v206, v206, v140\n"
"v_sub_f32 v238, 1.0, v118\n"
"v_add_f32 v237, v118, v118\n"
"v_mfma_f32_16x16x32_bf16 v[148:151], v[64:67], v[220:223], v[148:151]\n"
"v_cvt_pk_bf16_f32 v238, 0, v238\n"
"v_lshlrev_b32 v228, 16, v160\n"
"v_and_b32 v207, 0xffff0000, v160\n"
"v_lshlrev_b32 v227, 16, v161\n"
"s_waitcnt lgkmcnt(10)\n"
"v_mfma_f32_16x16x32_bf16 v[64:67], v[72:75], v[8:11], 0\n"
"ds_read_b64_tr_b16 v[140:141], v232 offset:38912\n"
"ds_read_b64_tr_b16 v[142:143], v233 offset:38912\n"
"v_and_b32 v229, 0xffff0000, v161\n"
"v_mul_f32 v144, v118, v178\n"
"v_mul_f32 v145, v118, v167\n"
"v_mfma_f32_16x16x32_bf16 v[160:163], v[72:75], v[24:27], 0\n"
"v_mul_f32 v146, v118, v176\n"
"v_mul_f32 v147, v118, v177\n"
"v_mul_f32 v144, v144, v228\n"
"v_mul_f32 v145, v145, v207\n"
"v_mul_f32 v146, v146, v227\n"
"v_mfma_f32_16x16x32_bf16 v[64:67], v[100:103], v[12:15], v[64:67]\n"
"ds_read_b64_tr_b16 v[72:73], v232 offset:39936\n"
"ds_read_b64_tr_b16 v[74:75], v233 offset:39936\n"
"v_mul_f32 v147, v147, v229\n"
"v_mul_f32 v228, v228, v238\n"
"v_mul_f32 v207, v207, v238\n"
"v_mfma_f32_16x16x32_bf16 v[160:163], v[100:103], v[28:31], v[160:163]\n"
"s_waitcnt lgkmcnt(7)\n"
"v_add_f32 v132, v132, v52\n"
"v_add_f32 v133, v133, v53\n"
"v_add_f32 v134, v134, v54\n"
"v_add_f32 v135, v135, v55\n"
"v_mfma_f32_16x16x32_bf16 v[64:67], v[104:107], v[16:19], v[64:67]\n"
"ds_read_b64_tr_b16 v[52:53], v232 offset:40960\n"
"ds_read_b64_tr_b16 v[54:55], v233 offset:40960\n"
"s_waitcnt lgkmcnt(8)\n"
"v_add_f32 v136, v136, v148\n"
"v_add_f32 v137, v137, v149\n"
"v_add_f32 v138, v138, v150\n"
"v_mfma_f32_16x16x32_bf16 v[160:163], v[104:107], v[32:35], v[160:163]\n"
"v_add_f32 v139, v139, v151\n"
"v_mul_f32 v227, v227, v238\n"
"v_mul_f32 v229, v229, v238\n"
"v_mfma_f32_16x16x32_bf16 v[64:67], v[120:123], v[20:23], v[64:67]\n"
"ds_read_b64_tr_b16 v[100:101], v232 offset:41984\n"
"ds_read_b64_tr_b16 v[102:103], v233 offset:41984\n"
"v_mfma_f32_16x16x32_bf16 v[160:163], v[120:123], v[36:39], v[160:163]\n"
"s_waitcnt lgkmcnt(8)\n"
"v_mfma_f32_16x16x32_bf16 v[64:67], v[128:131], v[0:3], v[64:67]\n"
"ds_read_b64_tr_b16 v[104:105], v232 offset:43008\n"
"ds_read_b64_tr_b16 v[106:107], v233 offset:43008\n"
"v_mfma_f32_16x16x32_bf16 v[160:163], v[128:131], v[40:43], v[160:163]\n"
"v_mfma_f32_16x16x32_bf16 v[64:67], v[56:59], v[4:7], v[64:67]\n"
"ds_read_b64_tr_b16 v[116:117], v232 offset:44032\n"
"ds_read_b64_tr_b16 v[118:119], v233 offset:44032\n"
"v_mfma_f32_16x16x32_bf16 v[160:163], v[56:59], v[68:71], v[160:163]\n"
"s_waitcnt lgkmcnt(8)\n"
"v_mfma_f32_16x16x32_bf16 v[64:67], v[140:143], v[44:47], v[64:67]\n"
"ds_read_b64_tr_b16 v[56:57], v232 offset:45056\n"
"ds_read_b64_tr_b16 v[58:59], v233 offset:45056\n"
"s_waitcnt vmcnt(0)\n"
"v_lshlrev_b32 v120, 4, v110\n"
"v_bfi_b32 v120, 15, v108, v120\n"
"v_mfma_f32_16x16x32_bf16 v[160:163], v[140:143], v[76:79], v[160:163]\n"
"v_cmp_ne_u32_sdwa vcc, v120, v164 src0_sel:BYTE_0 src1_sel:DWORD\n"
"s_nop 0\n"
"s_cbranch_vccnz .Lr3\n"
".Lb3:\n"
"v_lshlrev_b32 v128, 4, v111\n"
"v_bfi_b32 v128, 15, v109, v128\n"
"v_and_b32 v128, 0xff, v128\n"
"v_mfma_f32_16x16x32_bf16 v[64:67], v[72:75], v[48:51], v[64:67]\n"
"ds_read_b64_tr_b16 v[120:121], v232 offset:46080\n"
"ds_read_b64_tr_b16 v[122:123], v233 offset:46080\n"
"v_subrev_u32 v128, 0x7e, v128\n"
"v_ldexp_f32 v130, s81, v128\n"
"v_cvt_f32_i32_sdwa v141, sext(v108) dst_sel:DWORD dst_unused:UNUSED_PAD src0_sel:WORD_1\n"
"v_mfma_f32_16x16x32_bf16 v[160:163], v[72:75], v[80:83], v[160:163]\n"
"v_cvt_f32_i32_sdwa v142, sext(v109) dst_sel:DWORD dst_unused:UNUSED_PAD src0_sel:WORD_1\n"
"v_cvt_f32_i32_sdwa v140, sext(v110) dst_sel:DWORD dst_unused:UNUSED_PAD src0_sel:WORD_1\n"
"v_cvt_f32_i32_sdwa v131, sext(v111) dst_sel:DWORD dst_unused:UNUSED_PAD src0_sel:WORD_1\n"
"v_and_or_b32 v108, v108, -16, 8\n"
"s_waitcnt lgkmcnt(8)\n"
"v_mfma_f32_16x16x32_bf16 v[64:67], v[52:55], v[60:63], v[64:67]\n"
"ds_read_b64_tr_b16 v[72:73], v232 offset:47104\n"
"ds_read_b64_tr_b16 v[74:75], v233 offset:47104\n"
"v_and_or_b32 v109, v109, -16, 8\n"
"v_and_or_b32 v110, v110, -16, 8\n"
"v_and_or_b32 v111, v111, -16, 8\n"
"v_xor_b32 v128, 64, v189\n"
"v_xor_b32 v129, 16, v189\n"
"v_mfma_f32_16x16x32_bf16 v[160:163], v[52:55], v[124:127], v[160:163]\n"
"v_cvt_f32_i32_sdwa v52, sext(v108) dst_sel:DWORD dst_unused:UNUSED_PAD src0_sel:WORD_0\n"
"v_cvt_f32_i32_sdwa v149, sext(v109) dst_sel:DWORD dst_unused:UNUSED_PAD src0_sel:WORD_0\n"
"v_cvt_f32_i32_sdwa v148, sext(v110) dst_sel:DWORD dst_unused:UNUSED_PAD src0_sel:WORD_0\n"
"v_cvt_f32_i32_sdwa v143, sext(v111) dst_sel:DWORD dst_unused:UNUSED_PAD src0_sel:WORD_0\n"
"v_fmac_f32 v132, v130, v52\n"
"v_xor_b32 v108, 0x50, v189\n"
"v_mfma_f32_16x16x32_bf16 v[64:67], v[100:103], v[84:87], v[64:67]\n"
"ds_read_b64_tr_b16 v[52:53], v232 offset:48128\n"
"ds_read_b64_tr_b16 v[54:55], v233 offset:48128\n"
"v_fmac_f32 v133, v130, v141\n"
"v_fmac_f32 v134, v130, v149\n"
"v_fmac_f32 v135, v130, v142\n"
"v_mfma_f32_16x16x32_bf16 v[160:163], v[100:103], v[168:171], v[160:163]\n"
"v_fmac_f32 v136, v130, v148\n"
"v_fmac_f32 v137, v130, v140\n"
"v_fmac_f32 v138, v130, v143\n"
"v_fmac_f32 v139, v130, v131\n"
"s_waitcnt lgkmcnt(8)\n"
"v_mfma_f32_16x16x32_bf16 v[64:67], v[104:107], v[88:91], v[64:67]\n"
"v_fmac_f32_dpp v112, v132, v236 quad_perm:[0,1,2,3] row_mask:0xf bank_mask:0x3\n"
"v_fmac_f32_dpp v113, v136, v236 quad_perm:[0,1,2,3] row_mask:0xf bank_mask:0x3\n"
"v_fmac_f32_dpp v114, v132, v236 row_ror:8 row_mask:0xf bank_mask:0x3\n"
"v_fmac_f32_dpp v115, v136, v236 row_ror:8 row_mask:0xf bank_mask:0x3\n"
"v_fmac_f32_dpp v112, v134, v236 row_ror:8 row_mask:0xf bank_mask:0xc\n"
"v_mfma_f32_16x16x32_bf16 v[160:163], v[104:107], v[172:175], v[160:163]\n"
"v_fmac_f32_dpp v113, v138, v236 row_ror:8 row_mask:0xf bank_mask:0xc\n"
"v_fmac_f32_dpp v114, v134, v236 quad_perm:[0,1,2,3] row_mask:0xf bank_mask:0xc\n"
"v_fmac_f32_dpp v115, v138, v236 quad_perm:[0,1,2,3] row_mask:0xf bank_mask:0xc\n"
"v_fmac_f32_dpp v144, v133, v237 quad_perm:[0,1,2,3] row_mask:0xf bank_mask:0x3\n"
"v_fmac_f32_dpp v145, v137, v237 quad_perm:[0,1,2,3] row_mask:0xf bank_mask:0x3\n"
"v_mfma_f32_16x16x32_bf16 v[64:67], v[116:119], v[92:95], v[64:67]\n"
"v_fmac_f32_dpp v146, v133, v237 row_ror:8 row_mask:0xf bank_mask:0x3\n"
"v_fmac_f32_dpp v147, v137, v237 row_ror:8 row_mask:0xf bank_mask:0x3\n"
"v_fmac_f32_dpp v144, v135, v237 row_ror:8 row_mask:0xf bank_mask:0xc\n"
"v_fmac_f32_dpp v145, v139, v237 row_ror:8 row_mask:0xf bank_mask:0xc\n"
"v_fmac_f32_dpp v146, v135, v237 quad_perm:[0,1,2,3] row_mask:0xf bank_mask:0xc\n"
"v_mfma_f32_16x16x32_bf16 v[160:163], v[116:119], v[180:183], v[160:163]\n"
"v_fmac_f32_dpp v147, v139, v237 quad_perm:[0,1,2,3] row_mask:0xf bank_mask:0xc\n"
"s_waitcnt lgkmcnt(0)\n"
"s_barrier\n"
"v_mfma_f32_16x16x32_bf16 v[64:67], v[56:59], v[152:155], v[64:67]\n"
"ds_read_b64_tr_b16 v[100:101], v232\n"
"ds_read_b64_tr_b16 v[102:103], v233\n"
"ds_read2_b32 v[132:133], v189 offset0:0 offset1:32\n"
"ds_read2_b32 v[134:135], v128 offset0:0 offset1:32\n"
"ds_read2_b32 v[140:141], v129 offset0:0 offset1:32\n"
"v_mfma_f32_16x16x32_bf16 v[160:163], v[56:59], v[208:211], v[160:163]\n"
"ds_read_b64_tr_b16 v[56:57], v232 offset:1024\n"
"ds_read_b64_tr_b16 v[58:59], v233 offset:1024\n"
"ds_read2_b32 v[142:143], v108 offset0:0 offset1:32\n"
"v_mfma_f32_16x16x32_bf16 v[64:67], v[120:123], v[192:195], v[64:67]\n"
"ds_read_b64_tr_b16 v[104:105], v232 offset:2048\n"
"ds_read_b64_tr_b16 v[106:107], v233 offset:2048\n"
"v_mfma_f32_16x16x32_bf16 v[160:163], v[120:123], v[212:215], v[160:163]\n"
"ds_read_b64_tr_b16 v[108:109], v232 offset:3072\n"
"ds_read_b64_tr_b16 v[110:111], v233 offset:3072\n"
"v_mfma_f32_16x16x32_bf16 v[64:67], v[72:75], v[196:199], v[64:67]\n"
"ds_read_b64_tr_b16 v[116:117], v232 offset:4096\n"
"ds_read_b64_tr_b16 v[118:119], v233 offset:4096\n"
"ds_read_b128 v[120:123], v231 offset:4160\n"
"ds_read_b128 v[128:131], v231 offset:5184\n"
"v_mfma_f32_16x16x32_bf16 v[160:163], v[72:75], v[216:219], v[160:163]\n"
"s_waitcnt lgkmcnt(13)\n"
"v_lshlrev_b32 v72, 16, v132\n"
"s_waitcnt lgkmcnt(12)\n"
"v_lshlrev_b32 v136, 16, v134\n"
"v_lshlrev_b32 v137, 16, v133\n"
"v_lshlrev_b32 v138, 16, v135\n"
"v_fmac_f32 v96, v187, v72\n"
"v_mfma_f32_16x16x32_bf16 v[64:67], v[52:55], v[200:203], v[64:67]\n"
"ds_read_b64_tr_b16 v[72:73], v232 offset:5120\n"
"ds_read_b64_tr_b16 v[74:75], v233 offset:5120\n"
"v_fmac_f32 v97, v184, v136\n"
"v_fmac_f32 v98, v185, v137\n"
"v_fmac_f32 v99, v186, v138\n"
"v_mfma_f32_16x16x32_bf16 v[160:163], v[52:55], v[220:223], v[160:163]\n"
"v_and_b32 v136, 0xffff0000, v132\n"
"v_and_b32 v137, 0xffff0000, v134\n"
"v_and_b32 v138, 0xffff0000, v133\n"
"v_and_b32 v148, 0xffff0000, v135\n"
"s_waitcnt lgkmcnt(11)\n"
"v_mfma_f32_16x16x32_bf16 v[52:55], v[100:103], v[8:11], 0\n"
"ds_read_b64_tr_b16 v[132:133], v232 offset:6144\n"
"ds_read_b64_tr_b16 v[134:135], v233 offset:6144\n"
"v_fmac_f32 v156, v190, v136\n"
"v_fmac_f32 v157, v191, v137\n"
"v_fmac_f32 v158, v205, v138\n"
"v_mfma_f32_16x16x32_bf16 v[136:139], v[100:103], v[24:27], 0\n"
"v_fmac_f32 v159, v204, v148\n"
"v_lshlrev_b32 v149, 16, v140\n"
"s_waitcnt lgkmcnt(12)\n"
"v_lshlrev_b32 v150, 16, v142\n"
"v_lshlrev_b32 v151, 16, v141\n"
"v_lshlrev_b32 v148, 16, v143\n"
"v_mfma_f32_16x16x32_bf16 v[52:55], v[56:59], v[12:15], v[52:55]\n"
"ds_read_b64_tr_b16 v[100:101], v232 offset:7168\n"
"ds_read_b64_tr_b16 v[102:103], v233 offset:7168\n"
"v_fmac_f32 v112, v230, v149\n"
"v_fmac_f32 v113, v234, v150\n"
"v_fmac_f32 v114, v235, v151\n"
"v_mfma_f32_16x16x32_bf16 v[136:139], v[56:59], v[28:31], v[136:139]\n"
"s_waitcnt lgkmcnt(7)\n"
"v_add_f32 v120, v120, v64\n"
"v_add_f32 v121, v121, v65\n"
"v_add_f32 v122, v122, v66\n"
"v_add_f32 v123, v123, v67\n"
"v_mfma_f32_16x16x32_bf16 v[52:55], v[104:107], v[16:19], v[52:55]\n"
"ds_read_b64_tr_b16 v[56:57], v232 offset:8192\n"
"ds_read_b64_tr_b16 v[58:59], v233 offset:8192\n"
"s_waitcnt lgkmcnt(8)\n"
"v_add_f32 v128, v128, v160\n"
"v_add_f32 v129, v129, v161\n"
"v_add_f32 v130, v130, v162\n"
"v_mfma_f32_16x16x32_bf16 v[136:139], v[104:107], v[32:35], v[136:139]\n"
"v_add_f32 v131, v131, v163\n"
"v_max3_f32 v149, |v120|, |v121|, |v122|\n"
"v_max3_f32 v149, v149, |v123|, |v128|\n"
"v_max3_f32 v149, v149, |v129|, |v130|\n"
"v_max_f32_e64 v149, v149, |v131|\n"
"v_mfma_f32_16x16x32_bf16 v[52:55], v[108:111], v[20:23], v[52:55]\n"
"ds_read_b64_tr_b16 v[64:65], v232 offset:9216\n"
"ds_read_b64_tr_b16 v[66:67], v233 offset:9216\n"
"v_lshrrev_b32 v149, 23, v149\n"
"v_sub_u32 v150, 0x7e, v149\n"
"v_ldexp_f32 v120, v120, v150\n"
"v_mfma_f32_16x16x32_bf16 v[136:139], v[108:111], v[36:39], v[136:139]\n"
"v_ldexp_f32 v121, v121, v150\n"
"v_ldexp_f32 v122, v122, v150\n"
"v_ldexp_f32 v123, v123, v150\n"
"v_ldexp_f32 v128, v128, v150\n"
"s_waitcnt lgkmcnt(8)\n"
"v_mfma_f32_16x16x32_bf16 v[52:55], v[116:119], v[0:3], v[52:55]\n"
"ds_read_b64_tr_b16 v[104:105], v232 offset:10240\n"
"ds_read_b64_tr_b16 v[106:107], v233 offset:10240\n"
"v_ldexp_f32 v129, v129, v150\n"
"v_ldexp_f32 v130, v130, v150\n"
"v_ldexp_f32 v131, v131, v150\n"
"v_mfma_f32_16x16x32_bf16 v[136:139], v[116:119], v[40:43], v[136:139]\n"
"v_lshrrev_b32 v150, 4, v149\n"
"v_cvt_pknorm_i16_f32 v108, v120, v121\n"
"v_cvt_pknorm_i16_f32 v109, v122, v123\n"
"v_cvt_pknorm_i16_f32 v110, v128, v129\n"
"v_cvt_pknorm_i16_f32 v111, v130, v131\n"
"v_mfma_f32_16x16x32_bf16 v[52:55], v[72:75], v[4:7], v[52:55]\n"
"ds_read_b64_tr_b16 v[116:117], v232 offset:11264\n"
"ds_read_b64_tr_b16 v[118:119], v233 offset:11264\n"
"v_bfi_b32 v108, 15, v164, v108\n"
"v_bfi_b32 v109, 15, v149, v109\n"
"v_bfi_b32 v110, 15, v165, v110\n"
"v_mfma_f32_16x16x32_bf16 v[136:139], v[72:75], v[68:71], v[136:139]\n"
"v_bfi_b32 v111, 15, v150, v111\n"
"v_fmac_f32 v115, v206, v148\n"
"v_and_b32 v121, 0xffff0000, v140\n"
"v_and_b32 v120, 0xffff0000, v142\n"
"s_waitcnt lgkmcnt(8)\n"
"v_mfma_f32_16x16x32_bf16 v[52:55], v[132:135], v[44:47], v[52:55]\n"
"ds_read_b64_tr_b16 v[72:73], v232 offset:12288\n"
"ds_read_b64_tr_b16 v[74:75], v233 offset:12288\n"
"v_and_b32 v122, 0xffff0000, v141\n"
"v_and_b32 v123, 0xffff0000, v143\n"
"v_fmac_f32 v144, v228, v121\n"
"v_mfma_f32_16x16x32_bf16 v[136:139], v[132:135], v[76:79], v[136:139]\n"
"v_fmac_f32 v145, v207, v120\n"
"v_fmac_f32 v146, v227, v122\n"
"v_fmac_f32 v147, v229, v123\n"
"v_mfma_f32_16x16x32_bf16 v[52:55], v[100:103], v[48:51], v[52:55]\n"
"ds_read_b64_tr_b16 v[120:121], v232 offset:13312\n"
"ds_read_b64_tr_b16 v[122:123], v233 offset:13312\n"
"v_mfma_f32_16x16x32_bf16 v[136:139], v[100:103], v[80:83], v[136:139]\n"
"s_waitcnt lgkmcnt(8)\n"
"v_mfma_f32_16x16x32_bf16 v[52:55], v[56:59], v[60:63], v[52:55]\n"
"ds_read_b64_tr_b16 v[100:101], v232 offset:14336\n"
"ds_read_b64_tr_b16 v[102:103], v233 offset:14336\n"
"v_mfma_f32_16x16x32_bf16 v[136:139], v[56:59], v[124:127], v[136:139]\n"
"v_mfma_f32_16x16x32_bf16 v[52:55], v[64:67], v[84:87], v[52:55]\n"
"ds_read_b64_tr_b16 v[56:57], v232 offset:15360\n"
"ds_read_b64_tr_b16 v[58:59], v233 offset:15360\n"
"global_store_dwordx4 v226, v[108:111], s[42:43] offset:2048 sc1\n"
"s_nop 1\n"
"global_load_dwordx4 v[108:111], v166, s[22:23] sc1\n"
"global_load_dwordx4 v[128:131], v166, s[24:25] sc1\n"
"global_load_dwordx2 v[162:163], v179, s[28:29]\n"
"global_load_dwordx2 v[150:151], v179, s[28:29] offset:2048\n"
"v_mfma_f32_16x16x32_bf16 v[136:139], v[64:67], v[168:171], v[136:139]\n"
"global_load_dwordx2 v[148:149], v179, s[84:85]\n"
"global_load_dwordx2 v[160:161], v179, s[84:85] offset:2048\n"
"global_store_dwordx4 v188, v[96:99], s[86:87] offset:-4096 sc0 sc1\n"
"global_store_dwordx4 v188, v[156:159], s[86:87] sc0 sc1\n"
"global_store_dwordx4 v188, v[112:115], s[88:89] offset:-4096 sc0 sc1\n"
"global_store_dwordx4 v188, v[144:147], s[88:89] sc0 sc1\n"
"s_waitcnt lgkmcnt(8)\n"
"v_mfma_f32_16x16x32_bf16 v[52:55], v[104:107], v[88:91], v[52:55]\n"
"v_mfma_f32_16x16x32_bf16 v[136:139], v[104:107], v[172:175], v[136:139]\n"
"v_mfma_f32_16x16x32_bf16 v[52:55], v[116:119], v[92:95], v[52:55]\n"
"v_mfma_f32_16x16x32_bf16 v[136:139], v[116:119], v[180:183], v[136:139]\n"
"s_waitcnt lgkmcnt(0)\n"
"s_barrier\n"
"v_mfma_f32_16x16x32_bf16 v[52:55], v[72:75], v[152:155], v[52:55]\n"
"ds_read_b64_tr_b16 v[64:65], v232 offset:49152\n"
"ds_read_b64_tr_b16 v[66:67], v233 offset:49152\n"
"v_mfma_f32_16x16x32_bf16 v[136:139], v[72:75], v[208:211], v[136:139]\n"
"ds_read_b64_tr_b16 v[72:73], v232 offset:50176\n"
"ds_read_b64_tr_b16 v[74:75], v233 offset:50176\n"
"v_mfma_f32_16x16x32_bf16 v[52:55], v[120:123], v[192:195], v[52:55]\n"
"ds_read_b64_tr_b16 v[96:97], v232 offset:51200\n"
"ds_read_b64_tr_b16 v[98:99], v233 offset:51200\n"
"v_mfma_f32_16x16x32_bf16 v[136:139], v[120:123], v[212:215], v[136:139]\n"
"ds_read_b64_tr_b16 v[104:105], v232 offset:52224\n"
"ds_read_b64_tr_b16 v[106:107], v233 offset:52224\n"
"v_mfma_f32_16x16x32_bf16 v[52:55], v[100:103], v[196:199], v[52:55]\n"
"ds_read_b64_tr_b16 v[112:113], v232 offset:53248\n"
"ds_read_b64_tr_b16 v[114:115], v233 offset:53248\n"
"ds_read_b128 v[116:119], v231 offset:6208\n"
"ds_read_b128 v[120:123], v231 offset:7232\n"
"v_add_u32 v189, 0x800, v189\n"
"v_xor_b32 v145, 64, v189\n"
"v_xor_b32 v146, 16, v189\n"
"v_mfma_f32_16x16x32_bf16 v[136:139], v[100:103], v[216:219], v[136:139]\n"
"v_xor_b32 v144, 0x50, v189\n"
"v_mfma_f32_16x16x32_bf16 v[52:55], v[56:59], v[200:203], v[52:55]\n"
"ds_read_b64_tr_b16 v[100:101], v232 offset:54272\n"
"ds_read_b64_tr_b16 v[102:103], v233 offset:54272\n"
"v_mfma_f32_16x16x32_bf16 v[136:139], v[56:59], v[220:223], v[136:139]\n"
"s_waitcnt lgkmcnt(10)\n"
"v_mfma_f32_16x16x32_bf16 v[56:59], v[64:67], v[8:11], 0\n"
"ds_read_b64_tr_b16 v[132:133], v232 offset:55296\n"
"ds_read_b64_tr_b16 v[134:135], v233 offset:55296\n"
"v_mfma_f32_16x16x32_bf16 v[140:143], v[64:67], v[24:27], 0\n"
"v_mfma_f32_16x16x32_bf16 v[56:59], v[72:75], v[12:15], v[56:59]\n"
"ds_read_b64_tr_b16 v[64:65], v232 offset:56320\n"
"ds_read_b64_tr_b16 v[66:67], v233 offset:56320\n"
"v_mfma_f32_16x16x32_bf16 v[140:143], v[72:75], v[28:31], v[140:143]\n"
"s_waitcnt lgkmcnt(7)\n"
"v_add_f32 v116, v116, v52\n"
"v_add_f32 v117, v117, v53\n"
"v_add_f32 v118, v118, v54\n"
"v_add_f32 v119, v119, v55\n"
"v_mfma_f32_16x16x32_bf16 v[56:59], v[96:99], v[16:19], v[56:59]\n"
"ds_read_b64_tr_b16 v[52:53], v232 offset:57344\n"
"ds_read_b64_tr_b16 v[54:55], v233 offset:57344\n"
"s_waitcnt lgkmcnt(8)\n"
"v_add_f32 v120, v120, v136\n"
"v_add_f32 v121, v121, v137\n"
"v_add_f32 v122, v122, v138\n"
"v_mfma_f32_16x16x32_bf16 v[140:143], v[96:99], v[32:35], v[140:143]\n"
"v_add_f32 v123, v123, v139\n"
"v_max3_f32 v136, |v116|, |v117|, |v118|\n"
"v_max3_f32 v136, v136, |v119|, |v120|\n"
"v_max3_f32 v136, v136, |v121|, |v122|\n"
"v_max_f32_e64 v136, v136, |v123|\n"
"v_mfma_f32_16x16x32_bf16 v[56:59], v[104:107], v[20:23], v[56:59]\n"
"ds_read_b64_tr_b16 v[72:73], v232 offset:58368\n"
"ds_read_b64_tr_b16 v[74:75], v233 offset:58368\n"
"v_lshrrev_b32 v136, 23, v136\n"
"v_sub_u32 v137, 0x7e, v136\n"
"v_ldexp_f32 v116, v116, v137\n"
"v_mfma_f32_16x16x32_bf16 v[140:143], v[104:107], v[36:39], v[140:143]\n"
"v_ldexp_f32 v117, v117, v137\n"
"v_ldexp_f32 v118, v118, v137\n"
"v_ldexp_f32 v119, v119, v137\n"
"v_ldexp_f32 v120, v120, v137\n"
"s_waitcnt lgkmcnt(8)\n"
"v_mfma_f32_16x16x32_bf16 v[56:59], v[112:115], v[0:3], v[56:59]\n"
"ds_read_b64_tr_b16 v[96:97], v232 offset:59392\n"
"ds_read_b64_tr_b16 v[98:99], v233 offset:59392\n"
"v_ldexp_f32 v121, v121, v137\n"
"v_ldexp_f32 v122, v122, v137\n"
"v_ldexp_f32 v123, v123, v137\n"
"v_mfma_f32_16x16x32_bf16 v[140:143], v[112:115], v[40:43], v[140:143]\n"
"v_lshrrev_b32 v137, 4, v136\n"
"v_cvt_pknorm_i16_f32 v104, v116, v117\n"
"v_cvt_pknorm_i16_f32 v105, v118, v119\n"
"v_cvt_pknorm_i16_f32 v106, v120, v121\n"
"v_cvt_pknorm_i16_f32 v107, v122, v123\n"
"v_mfma_f32_16x16x32_bf16 v[56:59], v[100:103], v[4:7], v[56:59]\n"
"ds_read_b64_tr_b16 v[112:113], v232 offset:60416\n"
"ds_read_b64_tr_b16 v[114:115], v233 offset:60416\n"
"v_bfi_b32 v104, 15, v164, v104\n"
"v_bfi_b32 v105, 15, v136, v105\n"
"v_bfi_b32 v106, 15, v165, v106\n"
"v_mfma_f32_16x16x32_bf16 v[140:143], v[100:103], v[68:71], v[140:143]\n"
"v_bfi_b32 v107, 15, v137, v107\n"
"ds_read2_b32 v[136:137], v189 offset0:0 offset1:32\n"
"ds_read2_b32 v[138:139], v145 offset0:0 offset1:32\n"
"ds_read2_b32 v[156:157], v146 offset0:0 offset1:32\n"
"s_waitcnt lgkmcnt(11)\n"
"v_mfma_f32_16x16x32_bf16 v[56:59], v[132:135], v[44:47], v[56:59]\n"
"ds_read_b64_tr_b16 v[100:101], v232 offset:61440\n"
"ds_read_b64_tr_b16 v[102:103], v233 offset:61440\n"
"ds_read2_b32 v[158:159], v144 offset0:0 offset1:32\n"
"s_waitcnt vmcnt(9)\n"
"v_xor_b32 v116, s10, v109\n"
"v_bitop3_b32 v116, v116, v111, s10 bitop3:0xf6\n"
"v_mfma_f32_16x16x32_bf16 v[140:143], v[132:135], v[76:79], v[140:143]\n"
"s_waitcnt vmcnt(8)\n"
"v_bitop3_b32 v116, v116, v129, s10 bitop3:0xf6\n"
"v_bitop3_b32 v116, v116, v131, s10 bitop3:0xf6\n"
"v_cmp_ne_u32 vcc, 0, v116\n"
"s_nop 0\n"
"s_cbranch_vccnz .Lr7\n"
".Lb7:\n"
"v_sub_f32 v144, 1.0, v108\n"
"v_mfma_f32_16x16x32_bf16 v[56:59], v[64:67], v[48:51], v[56:59]\n"
"ds_read_b64_tr_b16 v[116:117], v232 offset:62464\n"
"ds_read_b64_tr_b16 v[118:119], v233 offset:62464\n"
"v_add_f32 v165, v108, v108\n"
"v_cvt_pk_bf16_f32 v144, 0, v144\n"
"s_waitcnt lgkmcnt(7)\n"
"v_lshlrev_b32 v133, 16, v136\n"
"v_mfma_f32_16x16x32_bf16 v[140:143], v[64:67], v[80:83], v[140:143]\n"
"s_waitcnt lgkmcnt(6)\n"
"v_lshlrev_b32 v134, 16, v138\n"
"v_lshlrev_b32 v135, 16, v137\n"
"v_lshlrev_b32 v132, 16, v139\n"
"v_mul_f32 v64, v108, v178\n"
"v_mfma_f32_16x16x32_bf16 v[56:59], v[52:55], v[60:63], v[56:59]\n"
"ds_read_b64_tr_b16 v[120:121], v232 offset:63488\n"
"ds_read_b64_tr_b16 v[122:123], v233 offset:63488\n"
"v_mul_f32 v65, v108, v167\n"
"v_mul_f32 v66, v108, v176\n"
"v_mul_f32 v67, v108, v177\n"
"v_mfma_f32_16x16x32_bf16 v[140:143], v[52:55], v[124:127], v[140:143]\n"
"v_fmac_f32 v64, v144, v133\n"
"v_fmac_f32 v65, v144, v134\n"
"v_fmac_f32 v66, v144, v135\n"
"v_fmac_f32 v67, v144, v132\n"
"s_waitcnt vmcnt(7)\n"
"v_lshlrev_b32 v133, 16, v162\n"
"v_mfma_f32_16x16x32_bf16 v[56:59], v[72:75], v[84:87], v[56:59]\n"
"ds_read_b64_tr_b16 v[52:53], v232 offset:64512\n"
"ds_read_b64_tr_b16 v[54:55], v233 offset:64512\n"
"global_store_dwordx4 v226, v[104:107], s[42:43] offset:3072 sc1\n"
"v_and_b32 v134, 0xffff0000, v162\n"
"v_lshlrev_b32 v135, 16, v163\n"
"v_mfma_f32_16x16x32_bf16 v[140:143], v[72:75], v[168:171], v[140:143]\n"
"v_and_b32 v132, 0xffff0000, v163\n"
"v_mul_f32 v64, v64, v133\n"
"v_mul_f32 v65, v65, v134\n"
"v_mul_f32 v66, v66, v135\n"
"v_mfma_f32_16x16x32_bf16 v[56:59], v[96:99], v[88:91], v[56:59]\n"
"v_mul_f32 v67, v67, v132\n"
"v_sub_f32 v104, 1.0, v110\n"
"v_add_f32 v163, v110, v110\n"
"v_cvt_pk_bf16_f32 v104, 0, v104\n"
"v_and_b32 v134, 0xffff0000, v136\n"
"v_mfma_f32_16x16x32_bf16 v[140:143], v[96:99], v[172:175], v[140:143]\n"
"v_and_b32 v135, 0xffff0000, v138\n"
"v_and_b32 v132, 0xffff0000, v137\n"
"v_and_b32 v133, 0xffff0000, v139\n"
"v_mul_f32 v72, v110, v178\n"
"v_mul_f32 v73, v110, v167\n"
"v_mfma_f32_16x16x32_bf16 v[56:59], v[112:115], v[92:95], v[56:59]\n"
"v_mul_f32 v74, v110, v176\n"
"v_mul_f32 v75, v110, v177\n"
"v_fmac_f32 v72, v104, v134\n"
"v_fmac_f32 v73, v104, v135\n"
"v_fmac_f32 v74, v104, v132\n"
"v_mfma_f32_16x16x32_bf16 v[140:143], v[112:115], v[180:183], v[140:143]\n"
"v_fmac_f32 v75, v104, v133\n"
"s_waitcnt vmcnt(7)\n"
"v_lshlrev_b32 v134, 16, v150\n"
"s_waitcnt lgkmcnt(0)\n"
"s_barrier\n"
"v_mfma_f32_16x16x32_bf16 v[56:59], v[100:103], v[152:155], v[56:59]\n"
"ds_read_b64_tr_b16 v[96:97], v232 offset:32768\n"
"ds_read_b64_tr_b16 v[98:99], v233 offset:32768\n"
"global_load_dwordx4 v[104:107], v226, s[60:61] offset:2048 sc1\n"
"v_and_b32 v135, 0xffff0000, v150\n"
"v_lshlrev_b32 v132, 16, v151\n"
"v_mfma_f32_16x16x32_bf16 v[140:143], v[100:103], v[208:211], v[140:143]\n"
"ds_read_b64_tr_b16 v[100:101], v232 offset:33792\n"
"ds_read_b64_tr_b16 v[102:103], v233 offset:33792\n"
"v_and_b32 v133, 0xffff0000, v151\n"
"v_mul_f32 v72, v72, v134\n"
"v_mul_f32 v73, v73, v135\n"
"v_mfma_f32_16x16x32_bf16 v[56:59], v[116:119], v[192:195], v[56:59]\n"
"ds_read_b64_tr_b16 v[108:109], v232 offset:34816\n"
"ds_read_b64_tr_b16 v[110:111], v233 offset:34816\n"
"v_mul_f32 v74, v74, v132\n"
"v_mul_f32 v75, v75, v133\n"
"v_sub_f32 v150, 1.0, v128\n"
"v_mfma_f32_16x16x32_bf16 v[140:143], v[116:119], v[212:215], v[140:143]\n"
"ds_read_b64_tr_b16 v[112:113], v232 offset:35840\n"
"ds_read_b64_tr_b16 v[114:115], v233 offset:35840\n"
"v_add_f32 v162, v128, v128\n"
"v_cvt_pk_bf16_f32 v150, 0, v150\n"
"v_mfma_f32_16x16x32_bf16 v[56:59], v[120:123], v[196:199], v[56:59]\n"
"ds_read_b64_tr_b16 v[116:117], v232 offset:36864\n"
"ds_read_b64_tr_b16 v[118:119], v233 offset:36864\n"
"ds_read_b128 v[132:135], v231 offset:4160\n"
"ds_read_b128 v[136:139], v231 offset:5184\n"
"v_lshlrev_b32 v166, 16, v156\n"
"v_mfma_f32_16x16x32_bf16 v[140:143], v[120:123], v[216:219], v[140:143]\n"
"v_lshlrev_b32 v179, 16, v158\n"
"v_lshlrev_b32 v184, 16, v157\n"
"v_lshlrev_b32 v185, 16, v159\n"
"v_mul_f32 v120, v128, v178\n"
"v_mul_f32 v121, v128, v167\n"
"v_mfma_f32_16x16x32_bf16 v[56:59], v[52:55], v[200:203], v[56:59]\n"
"ds_read_b64_tr_b16 v[144:145], v232 offset:37888\n"
"ds_read_b64_tr_b16 v[146:147], v233 offset:37888\n"
"v_mul_f32 v122, v128, v176\n"
"v_mul_f32 v123, v128, v177\n"
"v_fmac_f32 v120, v150, v166\n"
"v_mfma_f32_16x16x32_bf16 v[140:143], v[52:55], v[220:223], v[140:143]\n"
"v_fmac_f32 v121, v150, v179\n"
"v_fmac_f32 v122, v150, v184\n"
"v_fmac_f32 v123, v150, v185\n"
"s_waitcnt vmcnt(7)\n"
"v_lshlrev_b32 v166, 16, v148\n"
"s_waitcnt lgkmcnt(10)\n"
"v_mfma_f32_16x16x32_bf16 v[52:55], v[96:99], v[8:11], 0\n"
"ds_read_b64_tr_b16 v[8:9], v232 offset:38912\n"
"ds_read_b64_tr_b16 v[10:11], v233 offset:38912\n"
"v_and_b32 v179, 0xffff0000, v148\n"
"v_lshlrev_b32 v184, 16, v149\n"
"v_and_b32 v185, 0xffff0000, v149\n"
"v_mfma_f32_16x16x32_bf16 v[148:151], v[96:99], v[24:27], 0\n"
"v_mul_f32 v120, v120, v166\n"
"v_mul_f32 v121, v121, v179\n"
"v_mul_f32 v122, v122, v184\n"
"v_mul_f32 v123, v123, v185\n"
"v_sub_f32 v98, 1.0, v130\n"
"v_mfma_f32_16x16x32_bf16 v[52:55], v[100:103], v[12:15], v[52:55]\n"
"ds_read_b64_tr_b16 v[12:13], v232 offset:39936\n"
"ds_read_b64_tr_b16 v[14:15], v233 offset:39936\n"
"v_add_f32 v96, v130, v130\n"
"v_cvt_pk_bf16_f32 v98, 0, v98\n"
"v_and_b32 v97, 0xffff0000, v156\n"
"v_mfma_f32_16x16x32_bf16 v[148:151], v[100:103], v[28:31], v[148:151]\n"
"s_waitcnt lgkmcnt(7)\n"
"v_add_f32 v132, v132, v56\n"
"v_add_f32 v133, v133, v57\n"
"v_add_f32 v134, v134, v58\n"
"v_add_f32 v135, v135, v59\n"
"v_mfma_f32_16x16x32_bf16 v[52:55], v[108:111], v[16:19], v[52:55]\n"
"ds_read_b64_tr_b16 v[16:17], v232 offset:40960\n"
"ds_read_b64_tr_b16 v[18:19], v233 offset:40960\n"
"s_waitcnt lgkmcnt(8)\n"
"v_add_f32 v136, v136, v140\n"
"v_add_f32 v137, v137, v141\n"
"v_add_f32 v138, v138, v142\n"
"v_mfma_f32_16x16x32_bf16 v[148:151], v[108:111], v[32:35], v[148:151]\n"
"v_add_f32 v139, v139, v143\n"
"v_and_b32 v33, 0xffff0000, v158\n"
"v_and_b32 v34, 0xffff0000, v157\n"
"v_and_b32 v32, 0xffff0000, v159\n"
"v_mul_f32 v24, v130, v178\n"
"v_mfma_f32_16x16x32_bf16 v[52:55], v[112:115], v[20:23], v[52:55]\n"
"ds_read_b64_tr_b16 v[20:21], v232 offset:41984\n"
"ds_read_b64_tr_b16 v[22:23], v233 offset:41984\n"
"v_mul_f32 v25, v130, v167\n"
"v_mul_f32 v26, v130, v176\n"
"v_mul_f32 v27, v130, v177\n"
"v_mfma_f32_16x16x32_bf16 v[148:151], v[112:115], v[36:39], v[148:151]\n"
"v_fmac_f32 v24, v98, v97\n"
"v_fmac_f32 v25, v98, v33\n"
"v_fmac_f32 v26, v98, v34\n"
"v_fmac_f32 v27, v98, v32\n"
"s_waitcnt lgkmcnt(8)\n"
"v_mfma_f32_16x16x32_bf16 v[52:55], v[116:119], v[0:3], v[52:55]\n"
"ds_read_b64_tr_b16 v[0:1], v232 offset:43008\n"
"ds_read_b64_tr_b16 v[2:3], v233 offset:43008\n"
"global_load_dwordx4 v[28:31], v226, s[60:61] offset:3072 sc1\n"
"s_waitcnt vmcnt(7)\n"
"v_lshlrev_b32 v97, 16, v160\n"
"v_and_b32 v33, 0xffff0000, v160\n"
"v_mfma_f32_16x16x32_bf16 v[148:151], v[116:119], v[40:43], v[148:151]\n"
"v_lshlrev_b32 v34, 16, v161\n"
"v_and_b32 v32, 0xffff0000, v161\n"
"v_mul_f32 v24, v24, v97\n"
"v_mul_f32 v25, v25, v33\n"
"v_mul_f32 v26, v26, v34\n"
"v_mfma_f32_16x16x32_bf16 v[52:55], v[144:147], v[4:7], v[52:55]\n"
"ds_read_b64_tr_b16 v[4:5], v232 offset:44032\n"
"ds_read_b64_tr_b16 v[6:7], v233 offset:44032\n"
"v_mul_f32 v27, v27, v32\n"
"v_mfma_f32_16x16x32_bf16 v[148:151], v[144:147], v[68:71], v[148:151]\n"
"s_waitcnt lgkmcnt(8)\n"
"v_mfma_f32_16x16x32_bf16 v[52:55], v[8:11], v[44:47], v[52:55]\n"
"ds_read_b64_tr_b16 v[32:33], v232 offset:45056\n"
"ds_read_b64_tr_b16 v[34:35], v233 offset:45056\n"
"s_waitcnt vmcnt(1)\n"
"v_lshlrev_b32 v36, 4, v106\n"
"v_bfi_b32 v36, 15, v104, v36\n"
"v_mfma_f32_16x16x32_bf16 v[148:151], v[8:11], v[76:79], v[148:151]\n"
"v_cmp_ne_u32_sdwa vcc, v36, v164 src0_sel:BYTE_0 src1_sel:DWORD\n"
"s_nop 0\n"
"s_cbranch_vccnz .Lr4\n"
".Lb4:\n"
"v_lshlrev_b32 v37, 4, v107\n"
"v_bfi_b32 v37, 15, v105, v37\n"
"v_and_b32 v37, 0xff, v37\n"
"v_mfma_f32_16x16x32_bf16 v[52:55], v[12:15], v[48:51], v[52:55]\n"
"ds_read_b64_tr_b16 v[8:9], v232 offset:46080\n"
"ds_read_b64_tr_b16 v[10:11], v233 offset:46080\n"
"v_subrev_u32 v37, 0x7e, v37\n"
"v_ldexp_f32 v36, s81, v37\n"
"v_cvt_f32_i32_sdwa v39, sext(v104) dst_sel:DWORD dst_unused:UNUSED_PAD src0_sel:WORD_1\n"
"v_mfma_f32_16x16x32_bf16 v[148:151], v[12:15], v[80:83], v[148:151]\n"
"v_cvt_f32_i32_sdwa v40, sext(v105) dst_sel:DWORD dst_unused:UNUSED_PAD src0_sel:WORD_1\n"
"v_cvt_f32_i32_sdwa v38, sext(v106) dst_sel:DWORD dst_unused:UNUSED_PAD src0_sel:WORD_1\n"
"v_cvt_f32_i32_sdwa v37, sext(v107) dst_sel:DWORD dst_unused:UNUSED_PAD src0_sel:WORD_1\n"
"v_and_or_b32 v104, v104, -16, 8\n"
"s_waitcnt lgkmcnt(8)\n"
"v_mfma_f32_16x16x32_bf16 v[52:55], v[16:19], v[60:63], v[52:55]\n"
"ds_read_b64_tr_b16 v[12:13], v232 offset:47104\n"
"ds_read_b64_tr_b16 v[14:15], v233 offset:47104\n"
"v_and_or_b32 v105, v105, -16, 8\n"
"v_and_or_b32 v106, v106, -16, 8\n"
"v_and_or_b32 v107, v107, -16, 8\n"
"v_mfma_f32_16x16x32_bf16 v[148:151], v[16:19], v[124:127], v[148:151]\n"
"v_cvt_f32_i32_sdwa v16, sext(v104) dst_sel:DWORD dst_unused:UNUSED_PAD src0_sel:WORD_0\n"
"v_cvt_f32_i32_sdwa v43, sext(v105) dst_sel:DWORD dst_unused:UNUSED_PAD src0_sel:WORD_0\n"
"v_cvt_f32_i32_sdwa v42, sext(v106) dst_sel:DWORD dst_unused:UNUSED_PAD src0_sel:WORD_0\n"
"v_cvt_f32_i32_sdwa v41, sext(v107) dst_sel:DWORD dst_unused:UNUSED_PAD src0_sel:WORD_0\n"
"v_fmac_f32 v132, v36, v16\n"
"v_mfma_f32_16x16x32_bf16 v[52:55], v[20:23], v[84:87], v[52:55]\n"
"ds_read_b64_tr_b16 v[16:17], v232 offset:48128\n"
"ds_read_b64_tr_b16 v[18:19], v233 offset:48128\n"
"v_fmac_f32 v133, v36, v39\n"
"v_fmac_f32 v134, v36, v43\n"
"v_fmac_f32 v135, v36, v40\n"
"v_mfma_f32_16x16x32_bf16 v[148:151], v[20:23], v[168:171], v[148:151]\n"
"v_fmac_f32 v136, v36, v42\n"
"v_fmac_f32 v137, v36, v38\n"
"v_fmac_f32 v138, v36, v41\n"
"v_fmac_f32 v139, v36, v37\n"
"s_waitcnt lgkmcnt(8)\n"
"v_mfma_f32_16x16x32_bf16 v[52:55], v[0:3], v[88:91], v[52:55]\n"
"v_fmac_f32_dpp v64, v132, v165 quad_perm:[0,1,2,3] row_mask:0xf bank_mask:0x3\n"
"v_fmac_f32_dpp v65, v136, v165 quad_perm:[0,1,2,3] row_mask:0xf bank_mask:0x3\n"
"v_fmac_f32_dpp v66, v132, v165 row_ror:8 row_mask:0xf bank_mask:0x3\n"
"v_fmac_f32_dpp v67, v136, v165 row_ror:8 row_mask:0xf bank_mask:0x3\n"
"v_fmac_f32_dpp v64, v134, v165 row_ror:8 row_mask:0xf bank_mask:0xc\n"
"v_mfma_f32_16x16x32_bf16 v[148:151], v[0:3], v[172:175], v[148:151]\n"
"v_fmac_f32_dpp v65, v138, v165 row_ror:8 row_mask:0xf bank_mask:0xc\n"
"v_fmac_f32_dpp v66, v134, v165 quad_perm:[0,1,2,3] row_mask:0xf bank_mask:0xc\n"
"v_fmac_f32_dpp v67, v138, v165 quad_perm:[0,1,2,3] row_mask:0xf bank_mask:0xc\n"
"global_store_dwordx4 v188, v[64:67], s[90:91] offset:-4096 sc0 sc1\n"
"v_fmac_f32_dpp v72, v133, v163 quad_perm:[0,1,2,3] row_mask:0xf bank_mask:0x3\n"
"v_mfma_f32_16x16x32_bf16 v[52:55], v[4:7], v[92:95], v[52:55]\n"
"v_fmac_f32_dpp v73, v137, v163 quad_perm:[0,1,2,3] row_mask:0xf bank_mask:0x3\n"
"v_fmac_f32_dpp v74, v133, v163 row_ror:8 row_mask:0xf bank_mask:0x3\n"
"v_fmac_f32_dpp v75, v137, v163 row_ror:8 row_mask:0xf bank_mask:0x3\n"
"v_fmac_f32_dpp v72, v135, v163 row_ror:8 row_mask:0xf bank_mask:0xc\n"
"v_fmac_f32_dpp v73, v139, v163 row_ror:8 row_mask:0xf bank_mask:0xc\n"
"v_mfma_f32_16x16x32_bf16 v[148:151], v[4:7], v[180:183], v[148:151]\n"
"v_fmac_f32_dpp v74, v135, v163 quad_perm:[0,1,2,3] row_mask:0xf bank_mask:0xc\n"
"v_fmac_f32_dpp v75, v139, v163 quad_perm:[0,1,2,3] row_mask:0xf bank_mask:0xc\n"
"s_waitcnt lgkmcnt(0)\n"
"s_barrier\n"
"v_mfma_f32_16x16x32_bf16 v[52:55], v[32:35], v[152:155], v[52:55]\n"
"s_waitcnt vmcnt(1)\n"
"v_lshlrev_b32 v0, 4, v30\n"
"v_bfi_b32 v0, 15, v28, v0\n"
"v_cmp_ne_u32_sdwa vcc, v0, v164 src0_sel:BYTE_0 src1_sel:DWORD\n"
"s_nop 0\n"
"s_cbranch_vccnz .Lr5\n"
".Lb5:\n"
"v_lshlrev_b32 v0, 4, v31\n"
"v_mfma_f32_16x16x32_bf16 v[148:151], v[32:35], v[208:211], v[148:151]\n"
"v_bfi_b32 v0, 15, v29, v0\n"
"v_and_b32 v0, 0xff, v0\n"
"v_subrev_u32 v0, 0x7e, v0\n"
"v_ldexp_f32 v20, s81, v0\n"
"v_cvt_f32_i32_sdwa v23, sext(v28) dst_sel:DWORD dst_unused:UNUSED_PAD src0_sel:WORD_1\n"
"v_mfma_f32_16x16x32_bf16 v[52:55], v[8:11], v[192:195], v[52:55]\n"
"v_cvt_f32_i32_sdwa v32, sext(v29) dst_sel:DWORD dst_unused:UNUSED_PAD src0_sel:WORD_1\n"
"v_cvt_f32_i32_sdwa v22, sext(v30) dst_sel:DWORD dst_unused:UNUSED_PAD src0_sel:WORD_1\n"
"v_cvt_f32_i32_sdwa v21, sext(v31) dst_sel:DWORD dst_unused:UNUSED_PAD src0_sel:WORD_1\n"
"v_and_or_b32 v28, v28, -16, 8\n"
"v_and_or_b32 v29, v29, -16, 8\n"
"v_mfma_f32_16x16x32_bf16 v[148:151], v[8:11], v[212:215], v[148:151]\n"
"v_and_or_b32 v30, v30, -16, 8\n"
"v_and_or_b32 v31, v31, -16, 8\n"
"v_cvt_f32_i32_sdwa v9, sext(v28) dst_sel:DWORD dst_unused:UNUSED_PAD src0_sel:WORD_0\n"
"v_cvt_f32_i32_sdwa v8, sext(v29) dst_sel:DWORD dst_unused:UNUSED_PAD src0_sel:WORD_0\n"
"v_mfma_f32_16x16x32_bf16 v[52:55], v[12:15], v[196:199], v[52:55]\n"
"ds_read_b128 v[0:3], v231 offset:6208\n"
"ds_read_b128 v[4:7], v231 offset:7232\n"
"v_cvt_f32_i32_sdwa v11, sext(v30) dst_sel:DWORD dst_unused:UNUSED_PAD src0_sel:WORD_0\n"
"v_cvt_f32_i32_sdwa v10, sext(v31) dst_sel:DWORD dst_unused:UNUSED_PAD src0_sel:WORD_0\n"
"global_store_dwordx4 v188, v[72:75], s[90:91] sc0 sc1\n"
"v_mfma_f32_16x16x32_bf16 v[148:151], v[12:15], v[216:219], v[148:151]\n"
"v_mfma_f32_16x16x32_bf16 v[52:55], v[16:19], v[200:203], v[52:55]\n"
"v_mfma_f32_16x16x32_bf16 v[148:151], v[16:19], v[220:223], v[148:151]\n"
"s_waitcnt lgkmcnt(1)\n"
"v_fmac_f32 v0, v20, v9\n"
"v_fmac_f32 v1, v20, v23\n"
"v_fmac_f32 v2, v20, v8\n"
"v_fmac_f32 v3, v20, v32\n"
"s_waitcnt lgkmcnt(0)\n"
"v_fmac_f32 v4, v20, v11\n"
"v_fmac_f32 v5, v20, v22\n"
"v_fmac_f32 v6, v20, v10\n"
"v_fmac_f32 v7, v20, v21\n"
"v_add_f32 v0, v0, v52\n"
"v_add_f32 v1, v1, v53\n"
"v_add_f32 v2, v2, v54\n"
"v_add_f32 v3, v3, v55\n"
"v_add_f32 v4, v4, v148\n"
"v_add_f32 v5, v5, v149\n"
"v_add_f32 v6, v6, v150\n"
"v_add_f32 v7, v7, v151\n"
"v_fmac_f32_dpp v120, v0, v162 quad_perm:[0,1,2,3] row_mask:0xf bank_mask:0x3\n"
"v_fmac_f32_dpp v121, v4, v162 quad_perm:[0,1,2,3] row_mask:0xf bank_mask:0x3\n"
"v_fmac_f32_dpp v122, v0, v162 row_ror:8 row_mask:0xf bank_mask:0x3\n"
"v_fmac_f32_dpp v123, v4, v162 row_ror:8 row_mask:0xf bank_mask:0x3\n"
"v_fmac_f32_dpp v120, v2, v162 row_ror:8 row_mask:0xf bank_mask:0xc\n"
"v_fmac_f32_dpp v121, v6, v162 row_ror:8 row_mask:0xf bank_mask:0xc\n"
"v_fmac_f32_dpp v122, v2, v162 quad_perm:[0,1,2,3] row_mask:0xf bank_mask:0xc\n"
"v_fmac_f32_dpp v123, v6, v162 quad_perm:[0,1,2,3] row_mask:0xf bank_mask:0xc\n"
"global_store_dwordx4 v188, v[120:123], s[92:93] offset:-4096 sc0 sc1\n"
"v_fmac_f32_dpp v24, v1, v96 quad_perm:[0,1,2,3] row_mask:0xf bank_mask:0x3\n"
"v_fmac_f32_dpp v25, v5, v96 quad_perm:[0,1,2,3] row_mask:0xf bank_mask:0x3\n"
"v_fmac_f32_dpp v26, v1, v96 row_ror:8 row_mask:0xf bank_mask:0x3\n"
"v_fmac_f32_dpp v27, v5, v96 row_ror:8 row_mask:0xf bank_mask:0x3\n"
"v_fmac_f32_dpp v24, v3, v96 row_ror:8 row_mask:0xf bank_mask:0xc\n"
"v_fmac_f32_dpp v25, v7, v96 row_ror:8 row_mask:0xf bank_mask:0xc\n"
"v_fmac_f32_dpp v26, v3, v96 quad_perm:[0,1,2,3] row_mask:0xf bank_mask:0xc\n"
"v_fmac_f32_dpp v27, v7, v96 quad_perm:[0,1,2,3] row_mask:0xf bank_mask:0xc\n"
"global_store_dwordx4 v188, v[24:27], s[92:93] sc0 sc1\n"
"s_lshl2_add_u32 s13, %1, s15\n"
"v_mov_b32 v0, 0\n"
"s_cmp_eq_u32 s13, 0x78\n"
"s_cbranch_scc0 .Lskip\n"
"global_store_dwordx2 v0, v[224:225], s[30:31] sc1\n"
".Lskip:\n"
"s_endpgm\n"
".Lr1:\n"
"s_mov_b32 s12, 0x40000\n"
".Ll1:\n"
"s_sleep 3\n"
"s_nop 7\n"
"buffer_load_dwordx2 v[28:29], v220, s[76:79], 0 offen offset:3584 sc1\n"
"buffer_load_dwordx2 v[26:27], v220, s[76:79], 0 offen offset:3072 sc1\n"
"buffer_load_dwordx2 v[24:25], v220, s[76:79], 0 offen offset:2560 sc1\n"
"buffer_load_dwordx2 v[44:45], v220, s[76:79], 0 offen offset:2048 sc1\n"
"s_waitcnt vmcnt(0)\n"
"v_xor_b32 v28, v28, v244\n"
"v_xor_b32 v29, v29, v244\n"
"v_xor_b32 v26, v26, v243\n"
"v_xor_b32 v27, v27, v243\n"
"v_xor_b32 v24, v24, v242\n"
"v_xor_b32 v25, v25, v242\n"
"v_xor_b32 v44, v44, v241\n"
"v_xor_b32 v45, v45, v241\n"
"v_or3_b32 v30, v28, v29, v26\n"
"v_or3_b32 v30, v30, v27, v24\n"
"v_or3_b32 v30, v30, v25, v44\n"
"v_bitop3_b32 v30, v30, v45, s96 bitop3:0xa8\n"
"v_cmp_ne_u32 vcc, 0, v30\n"
"s_nop 0\n"
"s_cbranch_vccz .Lp1\n"
"s_sub_u32 s12, s12, 1\n"
"s_cbranch_scc0 .Ll1\n"
".Lp1:\n"
"s_nop 7\n"
"s_nop 7\n"
"s_branch .Lb1\n"
".Lr6:\n"
"s_mov_b32 s12, 0x40000\n"
".Ll6:\n"
"s_sleep 2\n"
"s_nop 7\n"
"global_load_dwordx4 v[104:107], v166, s[18:19] sc1\n"
"global_load_dwordx4 v[116:119], v166, s[20:21] sc1\n"
"s_waitcnt vmcnt(0)\n"
"v_xor_b32 v112, s10, v105\n"
"v_bitop3_b32 v112, v112, v107, s10 bitop3:0xf6\n"
"v_bitop3_b32 v112, v112, v117, s10 bitop3:0xf6\n"
"v_bitop3_b32 v112, v112, v119, s10 bitop3:0xf6\n"
"v_cmp_ne_u32 vcc, 0, v112\n"
"s_nop 0\n"
"s_cbranch_vccz .Lp6\n"
"s_sub_u32 s12, s12, 1\n"
"s_cbranch_scc0 .Ll6\n"
".Lp6:\n"
"s_nop 7\n"
"s_nop 7\n"
"s_branch .Lb6\n"
".Lr2:\n"
"s_mov_b32 s12, 0x40000\n"
".Ll2:\n"
"s_sleep 4\n"
"s_nop 7\n"
"global_load_dwordx4 v[108:111], v226, s[60:61] sc1\n"
"s_waitcnt vmcnt(0)\n"
"v_lshlrev_b32 v120, 4, v110\n"
"v_bfi_b32 v120, 15, v108, v120\n"
"v_cmp_ne_u32_sdwa vcc, v120, v164 src0_sel:BYTE_0 src1_sel:DWORD\n"
"s_nop 0\n"
"s_cbranch_vccz .Lp2\n"
"s_sub_u32 s12, s12, 1\n"
"s_cbranch_scc0 .Ll2\n"
".Lp2:\n"
"s_nop 7\n"
"s_nop 7\n"
"s_branch .Lb2\n"
".Lr3:\n"
"s_mov_b32 s12, 0x40000\n"
".Ll3:\n"
"s_sleep 4\n"
"s_nop 7\n"
"global_load_dwordx4 v[108:111], v226, s[60:61] offset:1024 sc1\n"
"s_waitcnt vmcnt(0)\n"
"v_lshlrev_b32 v120, 4, v110\n"
"v_bfi_b32 v120, 15, v108, v120\n"
"v_cmp_ne_u32_sdwa vcc, v120, v164 src0_sel:BYTE_0 src1_sel:DWORD\n"
"s_nop 0\n"
"s_cbranch_vccz .Lp3\n"
"s_sub_u32 s12, s12, 1\n"
"s_cbranch_scc0 .Ll3\n"
".Lp3:\n"
"s_nop 7\n"
"s_nop 7\n"
"s_branch .Lb3\n"
".Lr7:\n"
"s_mov_b32 s12, 0x40000\n"
".Ll7:\n"
"s_sleep 2\n"
"s_nop 7\n"
"global_load_dwordx4 v[108:111], v166, s[22:23] sc1\n"
"global_load_dwordx4 v[128:131], v166, s[24:25] sc1\n"
"s_waitcnt vmcnt(0)\n"
"v_xor_b32 v116, s10, v109\n"
"v_bitop3_b32 v116, v116, v111, s10 bitop3:0xf6\n"
"v_bitop3_b32 v116, v116, v129, s10 bitop3:0xf6\n"
"v_bitop3_b32 v116, v116, v131, s10 bitop3:0xf6\n"
"v_cmp_ne_u32 vcc, 0, v116\n"
"s_nop 0\n"
"s_cbranch_vccz .Lp7\n"
"s_sub_u32 s12, s12, 1\n"
"s_cbranch_scc0 .Ll7\n"
".Lp7:\n"
"s_nop 7\n"
"s_nop 7\n"
"s_branch .Lb7\n"
".Lr4:\n"
"s_mov_b32 s12, 0x40000\n"
".Ll4:\n"
"s_sleep 4\n"
"s_nop 7\n"
"global_load_dwordx4 v[104:107], v226, s[60:61] offset:2048 sc1\n"
"s_waitcnt vmcnt(0)\n"
"v_lshlrev_b32 v36, 4, v106\n"
"v_bfi_b32 v36, 15, v104, v36\n"
"v_cmp_ne_u32_sdwa vcc, v36, v164 src0_sel:BYTE_0 src1_sel:DWORD\n"
"s_nop 0\n"
"s_cbranch_vccz .Lp4\n"
"s_sub_u32 s12, s12, 1\n"
"s_cbranch_scc0 .Ll4\n"
".Lp4:\n"
"s_nop 7\n"
"s_nop 7\n"
"s_branch .Lb4\n"
".Lr5:\n"
"s_mov_b32 s12, 0x40000\n"
".Ll5:\n"
"s_sleep 4\n"
"s_nop 7\n"
"global_load_dwordx4 v[28:31], v226, s[60:61] offset:3072 sc1\n"
"s_waitcnt vmcnt(0)\n"
"v_lshlrev_b32 v0, 4, v30\n"
"v_bfi_b32 v0, 15, v28, v0\n"
"v_cmp_ne_u32_sdwa vcc, v0, v164 src0_sel:BYTE_0 src1_sel:DWORD\n"
"s_nop 0\n"
"s_cbranch_vccz .Lp5\n"
"s_sub_u32 s12, s12, 1\n"
"s_cbranch_scc0 .Ll5\n"
".Lp5:\n"
"s_nop 7\n"
"s_nop 7\n"
"s_branch .Lb5\n"
        :: "s"(__builtin_amdgcn_kernarg_segment_ptr()), "s"(b), "s"(wid), "s"(__builtin_amdgcn_readfirstlane(dlat))
        : "memory", "vcc", "scc", "v0", "v1", "v2", "v3", "v4", "v5", "v6", "v7", "v8", "v9", "v10", "v11", "v12", "v13", "v14", "v15", "v16", "v17", "v18", "v19", "v20", "v21", "v22", "v23", "v24", "v25", "v26", "v27", "v28", "v29", "v30", "v31", "v32", "v33", "v34", "v35", "v36", "v37", "v38", "v39", "v40", "v41", "v42", "v43", "v44", "v45", "v46", "v47", "v48", "v49", "v50", "v51", "v52", "v53", "v54", "v55", "v56", "v57", "v58", "v59", "v60", "v61", "v62", "v63", "v64", "v65", "v66", "v67", "v68", "v69", "v70", "v71", "v72", "v73", "v74", "v75", "v76", "v77", "v78", "v79", "v80", "v81", "v82", "v83", "v84", "v85", "v86", "v87", "v88", "v89", "v90", "v91", "v92", "v93", "v94", "v95", "v96", "v97", "v98", "v99", "v100", "v101", "v102", "v103", "v104", "v105", "v106", "v107", "v108", "v109", "v110", "v111", "v112", "v113", "v114", "v115", "v116", "v117", "v118", "v119", "v120", "v121", "v122", "v123", "v124", "v125", "v126", "v127", "v128", "v129", "v130", "v131", "v132", "v133", "v134", "v135", "v136", "v137", "v138", "v139", "v140", "v141", "v142", "v143", "v144", "v145", "v146", "v147", "v148", "v149", "v150", "v151", "v152", "v153", "v154", "v155", "v156", "v157", "v158", "v159", "v160", "v161", "v162", "v163", "v164", "v165", "v166", "v167", "v168", "v169", "v170", "v171", "v172", "v173", "v174", "v175", "v176", "v177", "v178", "v179", "v180", "v181", "v182", "v183", "v184", "v185", "v186", "v187", "v188", "v189", "v190", "v191", "v192", "v193", "v194", "v195", "v196", "v197", "v198", "v199", "v200", "v201", "v202", "v203", "v204", "v205", "v206", "v207", "v208", "v209", "v210", "v211", "v212", "v213", "v214", "v215", "v216", "v217", "v218", "v219", "v220", "v221", "v222", "v223", "v224", "v225", "v226", "v227", "v228", "v229", "v230", "v231", "v232", "v233", "v234", "v235", "v236", "v237", "v238", "v239", "v240", "v241", "v242", "v243", "v244", "v245", "v246", "v247", "v248", "v249", "v250", "v251", "v252", "v253", "v254", "v255", "s7", "s8", "s9", "s10", "s11", "s12", "s13", "s14", "s15", "s16", "s17", "s18", "s19", "s20", "s21", "s22", "s23", "s24", "s25", "s26", "s27", "s28", "s29", "s30", "s31", "s32", "s33", "s34", "s35", "s36", "s37", "s38", "s39", "s40", "s41", "s42", "s43", "s44", "s45", "s46", "s47", "s48", "s49", "s50", "s51", "s52", "s53", "s54", "s55", "s56", "s57", "s58", "s59", "s60", "s61", "s62", "s63", "s64", "s65", "s66", "s67", "s68", "s69", "s70", "s71", "s72", "s73", "s74", "s75", "s76", "s77", "s78", "s79", "s80", "s81", "s82", "s83", "s84", "s85", "s86", "s87", "s88", "s89", "s90", "s91", "s92", "s93", "s94", "s95", "s96", "s97", "s98", "s99");
}

extern "C" void kernel_launch(void* const* d_in, const int* in_sizes, int n_in, void* d_out, int out_size, void* d_ws, size_t ws_size, hipStream_t stream) {
    hipFuncSetAttribute((const void*)mk_kernel, hipFuncAttributeMaxDynamicSharedMemorySize, 161408);
    mk_kernel<<<256, 512, 161408, stream>>>((const u16*)d_in[0], (const u16*)d_in[3], (const u16*)d_in[4], (const u16*)d_in[10], (const u16*)d_in[11], (const u16*)d_in[15], (const u16*)d_in[13], (char*)d_ws,
                                           (const u16*)d_in[5], (const u16*)d_in[6], (float*)d_out, (const u16*)d_in[8], (const u16*)d_in[12], (const u16*)d_in[14],
                                           (const u16*)d_in[1], (const u16*)d_in[2], (const u16*)d_in[7]);
}
```

```cpp
#include <hip/hip_runtime.h>
#include <hip/hip_bf16.h>
#include <stdint.h>

typedef unsigned short u16;
typedef unsigned long long u64;
typedef __attribute__((ext_vector_type(4))) float f32x4;
typedef __attribute__((address_space(3))) f32x4 lds_f32x4;
struct cf { float r, i; };
__device__ __forceinline__ cf cmul(cf a, cf b) { return {a.r * b.r - a.i * b.i, a.r * b.i + a.i * b.r}; }
__device__ __forceinline__ float bf2f(u16 v) { return __uint_as_float(((unsigned)v) << 16); }
__device__ __forceinline__ float rbf(float f) { __bf16 b = (__bf16)f; return bf2f(__builtin_bit_cast(u16, b)); }

__global__ __launch_bounds__(512, 2) void mk_kernel(const u16* x, const u16* Bre, const u16* Bim, const u16* kin, const u16* vin, const u16* gatew, const u16* ssmk, char* ws,
                                                    const u16* Cre, const u16* Cim, float* out, const u16* dskip, const u16* shiftk, const u16* ssmD,
                                                    const u16* lre_, const u16* lim_, const u16* logdt_) {
    const int lane = threadIdx.x & 63, wid = __builtin_amdgcn_readfirstlane(threadIdx.x >> 6);
    const int b = blockIdx.x;
    {
        u64 pc; unsigned len;
        asm volatile(".subsection 1\n.Lmk_end:\n.subsection 0\n\ts_getpc_b64 %0\n.Lmk_code:\n\ts_mov_b32 %1, .Lmk_end-.Lmk_code-4" : "=s"(pc), "=s"(len));
        unsigned off = (unsigned)(((b >> 3) * 8 + wid) * 2 + (lane >> 5)) * 128u;
        off = off < len ? off : len;
        asm volatile("s_mov_b32 m0, 134656\n\ts_nop 0\n\tglobal_load_lds_dword %0, %1" :: "v"(off), "s"(pc) : "memory");
    }
    asm volatile(
"s_cmp_lt_u32 %2, 4\n"
"s_cbranch_scc1 .LC\n"
"s_load_dwordx16 s[16:31], %0, 0\n"
"s_load_dwordx16 s[32:47], %0, 64\n"
"s_and_b32 s48, %1, 1\n"
"s_bfe_u32 s49, %1, 0x20001\n"
"s_bfe_u32 s13, %1, 0x20003\n"
"s_lshr_b32 s50, %1, 5\n"
"s_lshl2_add_u32 s51, s49, s13\n"
"s_lshl3_add_u32 s52, s48, s50\n"
"s_lshl1_add_u32 s82, s48, s49\n"
"s_lshl_b32 s82, s82, 9\n"
"s_and_b32 s59, s50, 1\n"
"s_lshr_b32 s53, %1, 3\n"
"s_and_b32 s54, %1, 7\n"
"s_and_b32 s15, %2, 3\n"
"s_lshl_b32 s55, s15, 11\n"
"s_lshl_b32 s56, s15, 12\n"
"s_mul_i32 s57, s15, 0x2080\n"
"s_add_u32 s57, s57, 0x18000\n"
"s_lshl_b32 s58, s54, 5\n"
"s_add_u32 s58, s58, s53\n"
"s_lshl4_add_u32 s80, s51, s15\n"
"s_lshl_b32 s80, s80, 13\n"
"v_mbcnt_lo_u32_b32 v152, -1, 0\n"
"v_mbcnt_hi_u32_b32 v152, -1, v152\n"
"v_lshrrev_b32 v0, 4, v152\n"
"v_xor_b32 v0, v0, v152\n"
"v_and_b32 v0, 7, v0\n"
"v_lshlrev_b32 v0, 4, v0\n"
"v_lshrrev_b32 v109, 3, v152\n"
"v_lshl_or_b32 v109, v109, 11, v0\n"
"v_xor_b32 v108, 0x4040, v109\n"
"v_subrev_u32 v108, 0x400, v108\n"
"v_and_b32 v136, 0x1fff, v109\n"
"v_lshrrev_b32 v0, 5, v152\n"
"v_lshl_or_b32 v136, v0, 15, v136\n"
"v_xor_b32 v137, 0x10040, v136\n"
"v_subrev_u32 v137, 0x400, v137\n"
"v_lshlrev_b32 v195, 4, v152\n"
"v_and_b32 v4, 0xf0, v195\n"
"v_lshrrev_b32 v2, 4, v152\n"
"v_lshl_or_b32 v0, v2, 11, v4\n"
"s_lshl_b32 s13, s15, 6\n"
"v_xor_b32 v0, s13, v0\n"
"v_xor_b32 v5, 0x4020, v0\n"
"v_xor_b32 v4, 0xe0, v4\n"
"s_lshl2_add_u32 s81, s52, s15\n"
"s_lshl_b32 s81, s81, 15\n"
"v_and_b32 v1, 3, v152\n"
"v_lshlrev_b32 v1, 7, v1\n"
"v_bfe_u32 v2, v152, 2, 2\n"
"v_lshl_or_b32 v1, v2, 11, v1\n"
"s_waitcnt lgkmcnt(0)\n"
"s_sub_u32 s20, s20, 0x800\n"
"s_subb_u32 s21, s21, 0\n"
"s_add_u32 s13, s82, 0\n"
"s_and_b32 s13, s13, 0x7ff\n"
"s_add_u32 s14, s13, s81\n"
"s_add_u32 s13, s13, s80\n"
"s_add_u32 s60, s16, s13\n"
"s_addc_u32 s61, s17, 0\n"
"s_add_u32 s62, s18, s14\n"
"s_addc_u32 s63, s19, 0\n"
"s_add_u32 s64, s20, s14\n"
"s_addc_u32 s65, s21, 0\n"
"s_lshl2_add_u32 s13, s53, s15\n"
"s_lshl_b32 s13, s13, 15\n"
"s_lshl_b32 s14, s54, 8\n"
"s_add_u32 s13, s13, s14\n"
"s_add_u32 s66, s22, s13\n"
"s_addc_u32 s67, s23, 0\n"
"s_add_u32 s68, s24, s13\n"
"s_addc_u32 s69, s25, 0\n"
"s_add_u32 s13, s82, 0x200\n"
"s_and_b32 s13, s13, 0x7ff\n"
"s_lshl3_add_u32 s14, s51, s50\n"
"s_lshl2_add_u32 s14, s14, s15\n"
"s_lshl_b32 s14, s14, 12\n"
"s_add_u32 s13, s13, s14\n"
"s_add_u32 s70, s16, s13\n"
"s_addc_u32 s71, s17, 0\n"
"s_mov_b64 exec, 0xff\n"
"s_mov_b32 m0, 0x20e00\n"
"s_nop 0\n"
"global_load_lds_dword v1, s[70:71]\n"
"s_mov_b64 exec, -1\n"
"s_add_u32 m0, s56, 0x2000\n"
"s_nop 0\n"
"global_load_lds_dwordx4 v109, s[62:63]\n"
"s_add_u32 m0, s56, 0x2000\n"
"s_nop 0\n"
"global_load_lds_dwordx4 v108, s[62:63] offset:1024\n"
"s_add_u32 m0, s56, 0x18000\n"
"s_nop 0\n"
"global_load_lds_dwordx4 v0, s[66:67] nt\n"
"s_add_u32 m0, s56, 0x2000\n"
"s_nop 0\n"
"global_load_lds_dwordx4 v109, s[64:65] offset:2048\n"
"s_add_u32 m0, s56, 0x2000\n"
"s_nop 0\n"
"global_load_lds_dwordx4 v108, s[64:65] offset:3072\n"
"s_add_u32 m0, s56, 0x1c000\n"
"s_nop 0\n"
"global_load_lds_dwordx4 v0, s[68:69] nt\n"
"s_add_u32 m0, s55, 0\n"
"s_nop 0\n"
"global_load_lds_dwordx4 v136, s[60:61]\n"
"s_add_u32 m0, s55, 0\n"
"s_nop 0\n"
"global_load_lds_dwordx4 v137, s[60:61] offset:1024\n"
"s_add_u32 m0, s56, 0x18800\n"
"s_nop 0\n"
"global_load_lds_dwordx4 v5, s[66:67] nt\n"
"s_add_u32 s30, s30, 0x1fffff\n"
"s_addc_u32 s31, s31, 0\n"
"s_and_b32 s30, s30, 0xffe00000\n"
"s_load_dwordx2 s[10:11], s[30:31], 0\n"
"s_lshl_b32 s13, s15, 9\n"
"s_add_u32 s70, s26, s13\n"
"s_addc_u32 s71, s27, 0\n"
"s_mov_b32 exec_hi, 0\n"
"s_add_u32 m0, s13, 0x20600\n"
"s_nop 0\n"
"global_load_lds_dwordx4 v195, s[70:71]\n"
"s_mov_b64 exec, -1\n"
"s_add_u32 m0, s56, 0x7f80\n"
"s_nop 0\n"
"global_load_lds_dwordx4 v109, s[62:63] offset:128\n"
"s_add_u32 m0, s56, 0x7f80\n"
"s_nop 0\n"
"global_load_lds_dwordx4 v108, s[62:63] offset:1152\n"
"s_add_u32 m0, s56, 0x1c800\n"
"s_nop 0\n"
"global_load_lds_dwordx4 v5, s[68:69] nt\n"
"s_add_u32 m0, s56, 0x7f80\n"
"s_nop 0\n"
"global_load_lds_dwordx4 v109, s[64:65] offset:2176\n"
"s_add_u32 m0, s56, 0x7f80\n"
"s_nop 0\n"
"global_load_lds_dwordx4 v108, s[64:65] offset:3200\n"
"s_add_u32 s66, s66, 0x2000\n"
"s_addc_u32 s67, s67, 0\n"
"s_add_u32 s68, s68, 0x2000\n"
"s_addc_u32 s69, s69, 0\n"
"s_add_u32 m0, s56, 0x18400\n"
"s_nop 0\n"
"global_load_lds_dwordx4 v0, s[66:67] nt\n"
"s_add_u32 m0, s55, 0x5f80\n"
"s_nop 0\n"
"global_load_lds_dwordx4 v136, s[60:61] offset:128\n"
"s_add_u32 m0, s55, 0x5f80\n"
"s_nop 0\n"
"global_load_lds_dwordx4 v137, s[60:61] offset:1152\n"
"s_add_u32 m0, s56, 0x1c400\n"
"s_nop 0\n"
"global_load_lds_dwordx4 v0, s[68:69] nt\n"
"s_waitcnt lgkmcnt(0)\n"
"s_add_u32 s10, s10, 1\n"
"s_addc_u32 s11, s11, 0\n"
"v_bfe_u32 v0, v152, 1, 3\n"
"v_lshrrev_b32 v217, 4, v152\n"
"v_xor_b32 v0, v0, v217\n"
"v_and_b32 v217, 15, v152\n"
"v_lshlrev_b32 v217, 7, v217\n"
"v_lshl_or_b32 v217, v0, 4, v217\n"
"s_add_u32 s13, s56, 0x2000\n"
"v_add_u32 v110, s13, v217\n"
"v_xor_b32 v112, 64, v110\n"
"v_add_u32 v120, 0x10000, v110\n"
"v_add_u32 v128, 0x10000, v112\n"
"v_xor_b32 v228, 64, v217\n"
"v_add_u32 v223, 0x10000, v217\n"
"v_add_u32 v221, 0x10000, v228\n"
"s_mul_i32 s97, s15, 0x11e0\n"
"s_add_u32 s97, s97, 0x20200\n"
"v_add_u32 v194, s57, v195\n"
"v_add_u32 v6, s97, v195\n"
"v_add_u32 v229, s56, v195\n"
"v_mov_b32 v0, 0\n"
"v_mov_b32 v1, 0\n"
"v_mov_b32 v2, 0\n"
"v_mov_b32 v3, 0\n"
"s_mov_b64 exec, 0x3fffffff\n"
"ds_write_b128 v6, v[0:3] offset:4096\n"
"s_mov_b64 exec, -1\n"
"v_mov_b32 v192, s10\n"
"v_mov_b32 v193, s11\n"
"v_lshrrev_b32 v0, 4, v152\n"
"v_and_b32 v212, 0xf0, v195\n"
"v_lshrrev_b32 v207, 1, v212\n"
"s_mul_i32 s13, s15, 0xb00\n"
"s_add_u32 s13, s13, 0x24980\n"
"v_add_u32 v220, s13, v195\n"
"s_add_u32 s13, s13, 0x800\n"
"v_add_u32 v222, s13, v212\n"
"v_lshl_or_b32 v212, v0, 16, v212\n"
"v_xor_b32 v211, 64, v212\n"
"s_mov_b32 s96, 0xff\n"
"s_and_b32 s13, s10, 0xff\n"
"v_mov_b32 v218, s13\n"
"s_sub_u32 s13, 16, s51\n"
"v_add_u32 v1, s13, v0\n"
"v_lshl_or_b32 v219, v1, 7, v207\n"
"v_cmp_gt_u32 vcc, 16, v1\n"
"s_nop 1\n"
"v_cndmask_b32 v224, 0, v218, vcc\n"
"v_cmp_gt_u32 vcc, 12, v1\n"
"s_nop 1\n"
"v_cndmask_b32 v225, 0, v218, vcc\n"
"v_cmp_gt_u32 vcc, 8, v1\n"
"s_nop 1\n"
"v_cndmask_b32 v226, 0, v218, vcc\n"
"v_cmp_gt_u32 vcc, 4, v1\n"
"s_nop 1\n"
"v_cndmask_b32 v227, 0, v218, vcc\n"
"s_lshl2_add_u32 s13, s52, s15\n"
"s_lshl_b32 s13, s13, 12\n"
"s_add_u32 s13, s13, 0x100000\n"
"s_add_u32 s76, s30, s13\n"
"s_addc_u32 s77, s31, 0\n"
"s_sub_u32 s13, 15, s51\n"
"s_lshl_b32 s13, s13, 7\n"
"s_add_u32 s84, s76, s13\n"
"s_addc_u32 s85, s77, 0\n"
"s_and_b32 s77, s77, 0xffff\n"
"s_mov_b32 s78, 0x1000\n"
"s_mov_b32 s79, 0x20000\n"
"s_lshl1_add_u32 s13, s51, s48\n"
"s_lshl2_add_u32 s14, s50, s15\n"
"s_and_b32 s87, s14, 24\n"
"s_add_u32 s87, s87, s14\n"
"s_lshl_b32 s88, s13, 19\n"
"s_lshl_b32 s87, s87, 10\n"
"s_add_u32 s88, s88, s87\n"
"s_add_u32 s88, s88, 0x440000\n"
"s_lshr_b32 s86, s88, 8\n"
"s_and_b32 s86, s86, 0x2000\n"
"s_add_u32 s86, s86, s88\n"
"s_add_u32 s86, s30, s86\n"
"s_addc_u32 s87, s31, 0\n"
"s_lshl_b32 s13, s13, 13\n"
"s_lshl_b32 s14, s14, 7\n"
"s_add_u32 s13, s13, s14\n"
"s_add_u32 s13, s13, 0x21000\n"
"s_add_u32 s88, s30, s13\n"
"s_addc_u32 s89, s31, 0\n"
"v_bfe_u32 v0, v152, 1, 3\n"
"v_bfe_u32 v206, v152, 4, 1\n"
"v_xor_b32 v0, v0, v206\n"
"v_lshrrev_b32 v206, 1, v152\n"
"v_and_b32 v206, 16, v206\n"
"v_and_or_b32 v206, v152, 15, v206\n"
"v_lshlrev_b32 v206, 7, v206\n"
"v_lshl_or_b32 v206, v0, 4, v206\n"
"v_add_u32 v206, s56, v206\n"
"v_add_u32 v206, 0xc000, v206\n"
"v_xor_b32 v208, 32, v206\n"
"v_xor_b32 v209, 64, v206\n"
"v_xor_b32 v210, 0x60, v206\n"
"v_bfe_u32 v0, v152, 1, 1\n"
"v_mul_u32_u24 v198, 0x140, v0\n"
"v_lshrrev_b32 v0, 5, v152\n"
"v_lshl_or_b32 v198, v0, 9, v198\n"
"v_bfe_u32 v0, v152, 4, 1\n"
"v_lshl_or_b32 v198, v0, 7, v198\n"
"v_bfe_u32 v0, v152, 2, 2\n"
"v_lshl_or_b32 v198, v0, 4, v198\n"
"v_and_b32 v0, 1, v152\n"
"v_lshl_or_b32 v198, v0, 3, v198\n"
"v_xor_b32 v199, 64, v198\n"
"v_add_u32 v196, 0x20000, v198\n"
"v_add_u32 v197, 0x20000, v199\n"
"v_add_u32 v200, 0x10000, v198\n"
"v_add_u32 v201, 0x10000, v199\n"
"v_and_b32 v205, 3, v152\n"
"v_bfe_u32 v0, v152, 2, 1\n"
"v_lshl_or_b32 v205, v0, 4, v205\n"
"v_lshlrev_b32 v205, 7, v205\n"
"v_lshrrev_b32 v0, 4, v152\n"
"v_add_u32 v202, 0x8000, v195\n"
"s_lshl_b32 s13, s59, 4\n"
"v_xor_b32 v203, s13, v195\n"
"v_lshlrev_b32 v1, 4, v0\n"
"v_xor_b32 v203, v203, v1\n"
"v_and_b32 v203, 0xf0, v203\n"
"v_lshl_or_b32 v203, v0, 15, v203\n"
"v_and_b32 v204, 31, v152\n"
"v_lshlrev_b32 v204, 7, v204\n"
"v_lshrrev_b32 v0, 4, v152\n"
"v_xor_b32 v0, v0, v152\n"
"v_and_b32 v0, 7, v0\n"
"v_lshlrev_b32 v0, 4, v0\n"
"v_lshrrev_b32 v215, 3, v152\n"
"v_lshl_or_b32 v215, v215, 13, v0\n"
"v_xor_b32 v216, 0xc40, v215\n"
"v_add_u32 v213, 0x800, v215\n"
"v_add_u32 v214, 0x800, v216\n"
"s_lshl2_add_u32 s13, s58, s15\n"
"s_lshl_b32 s13, s13, 12\n"
"s_add_u32 s13, s13, 0x2400000\n"
"s_add_u32 s38, s30, s13\n"
"s_addc_u32 s39, s31, 0\n"
"s_and_b32 s13, s54, 6\n"
"s_lshl_b32 s13, s13, 10\n"
"s_add_u32 s13, s13, 0x1000\n"
"s_add_u32 s26, s30, s13\n"
"s_addc_u32 s27, s31, 0\n"
"s_lshl2_add_u32 s13, s58, s15\n"
"s_lshl_b32 s13, s13, 12\n"
"s_add_u32 s28, s28, s13\n"
"s_addc_u32 s29, s29, 0\n"
"s_lshl2_add_u32 s13, s50, s15\n"
"s_lshl_b32 s13, s13, 16\n"
"s_lshl_b32 s14, s48, 10\n"
"s_add_u32 s13, s13, s14\n"
"s_add_u32 s13, s13, 0x200\n"
"s_add_u32 s72, s32, s13\n"
"s_addc_u32 s73, s33, 0\n"
"s_add_u32 s74, s34, s13\n"
"s_addc_u32 s75, s35, 0\n"
"s_sub_u32 s74, s74, 0x800\n"
"s_subb_u32 s75, s75, 0\n"
"s_lshl1_add_u32 s13, s51, s48\n"
"s_lshl_b32 s13, s13, 19\n"
"s_add_u32 s14, s49, s15\n"
"s_and_b32 s42, s14, 2\n"
"s_and_b32 s14, s14, 3\n"
"s_add_u32 s14, s14, s42\n"
"s_lshl_b32 s14, s14, 12\n"
"s_add_u32 s13, s13, s14\n"
"s_add_u32 s13, s13, 0x400000\n"
"s_lshr_b32 s42, s13, 8\n"
"s_and_b32 s42, s42, 0x2000\n"
"s_add_u32 s42, s42, s13\n"
"s_add_u32 s42, s30, s42\n"
"s_addc_u32 s43, s31, 0\n"
"s_lshl1_add_u32 s46, s48, s59\n"
"s_lshl_b32 s46, s46, 16\n"
"s_xor_b32 s44, s46, 0x20000\n"
"s_xor_b32 s45, s46, 0x30000\n"
"s_xor_b32 s47, s46, 0x10000\n"
"s_lshl1_add_u32 s13, s51, s48\n"
"s_lshl_b32 s13, s13, 13\n"
"s_add_u32 s14, s49, s15\n"
"s_and_b32 s14, s14, 3\n"
"s_lshl_b32 s14, s14, 9\n"
"s_add_u32 s13, s13, s14\n"
"s_add_u32 s13, s13, 0x20000\n"
"s_add_u32 s90, s30, s13\n"
"s_addc_u32 s91, s31, 0\n"
"s_add_u32 m0, s56, 0xdf00\n"
"s_nop 0\n"
"global_load_lds_dwordx4 v109, s[62:63] offset:256\n"
"s_add_u32 m0, s56, 0xdf00\n"
"s_nop 0\n"
"global_load_lds_dwordx4 v108, s[62:63] offset:1280\n"
"s_add_u32 m0, s56, 0x18c00\n"
"s_nop 0\n"
"global_load_lds_dwordx4 v5, s[66:67] nt\n"
"s_add_u32 m0, s56, 0xdf00\n"
"s_nop 0\n"
"global_load_lds_dwordx4 v109, s[64:65] offset:2304\n"
"s_add_u32 m0, s56, 0xdf00\n"
"s_nop 0\n"
"global_load_lds_dwordx4 v108, s[64:65] offset:3328\n"
"s_add_u32 m0, s56, 0x1cc00\n"
"s_nop 0\n"
"global_load_lds_dwordx4 v5, s[68:69] nt\n"
"s_add_u32 m0, s55, 0xbf00\n"
"s_nop 0\n"
"global_load_lds_dwordx4 v136, s[60:61] offset:256\n"
"s_add_u32 m0, s55, 0xbf00\n"
"s_nop 0\n"
"global_load_lds_dwordx4 v137, s[60:61] offset:1280\n"
"s_lshl_b32 s13, s53, 6\n"
"s_cselect_b32 s14, 3, 0\n"
"s_sub_u32 s13, s13, s14\n"
"s_add_u32 s13, s13, s15\n"
"s_lshl_b32 s13, s13, 11\n"
"s_lshl_b32 s14, s54, 8\n"
"s_add_u32 s13, s13, s14\n"
"s_add_u32 s70, s22, s13\n"
"s_addc_u32 s71, s23, 0\n"
"s_lshl_b32 s13, s15, 8\n"
"s_mov_b64 exec, 0xffff\n"
"s_add_u32 m0, s13, 0x20200\n"
"s_nop 0\n"
"global_load_lds_dwordx4 v4, s[70:71]\n"
"s_mov_b64 exec, -1\n"
"s_add_u32 m0, s56, 0x13e80\n"
"s_nop 0\n"
"global_load_lds_dwordx4 v109, s[62:63] offset:384\n"
"global_load_lds_dwordx4 v108, s[62:63] offset:1408\n"
"global_load_lds_dwordx4 v109, s[64:65] offset:2432\n"
"global_load_lds_dwordx4 v108, s[64:65] offset:3456\n"
"s_add_u32 m0, s55, 0x11e80\n"
"s_nop 0\n"
"global_load_lds_dwordx4 v136, s[60:61] offset:384\n"
"global_load_lds_dwordx4 v137, s[60:61] offset:1408\n"
"s_setprio 0\n"
"s_waitcnt vmcnt(26) lgkmcnt(0)\n"
"s_barrier\n"
"s_waitcnt vmcnt(16)\n"
"s_barrier\n"
"s_add_u32 s13, s82, 0x200\n"
"s_and_b32 s13, s13, 0x7ff\n"
"s_add_u32 s14, s13, s81\n"
"s_add_u32 s13, s13, s80\n"
"s_add_u32 s60, s16, s13\n"
"s_addc_u32 s61, s17, 0\n"
"s_add_u32 s62, s18, s14\n"
"s_addc_u32 s63, s19, 0\n"
"s_add_u32 s64, s20, s14\n"
"s_addc_u32 s65, s21, 0\n"
"s_add_u32 m0, s56, 0x2000\n"
"s_nop 0\n"
"global_load_lds_dwordx4 v109, s[62:63]\n"
"global_load_lds_dwordx4 v108, s[62:63] offset:1024\n"
"ds_read_b128 v[0:3], v110 offset:24576\n"
"global_load_lds_dwordx4 v109, s[64:65] offset:2048\n"
"ds_read_b128 v[4:7], v110 offset:26624\n"
"global_load_lds_dwordx4 v108, s[64:65] offset:3072\n"
"s_add_u32 m0, s55, 0\n"
"s_nop 0\n"
"global_load_lds_dwordx4 v136, s[60:61]\n"
"ds_read_b128 v[8:11], v112 offset:24576\n"
"global_load_lds_dwordx4 v137, s[60:61] offset:1024\n"
"ds_read_b128 v[12:15], v112 offset:26624\n"
"s_lshl4_add_u32 s83, s51, s52\n"
"s_lshl2_add_u32 s83, s83, s15\n"
"s_lshl_b32 s13, s83, 11\n"
"s_add_u32 s66, s16, s13\n"
"s_addc_u32 s67, s17, 0\n"
"s_add_u32 s68, s66, 0x200000\n"
"s_addc_u32 s69, s67, 0\n"
"global_load_dwordx4 v[16:19], v195, s[66:67]\n"
"global_load_dwordx4 v[20:23], v195, s[66:67] offset:1024\n"
"global_load_dwordx4 v[24:27], v195, s[68:69]\n"
"global_load_dwordx4 v[28:31], v195, s[68:69] offset:1024\n"
"s_waitcnt vmcnt(17) lgkmcnt(0)\n"
"s_barrier\n"
"s_add_u32 m0, s56, 0x7f80\n"
"s_nop 0\n"
"global_load_lds_dwordx4 v109, s[62:63] offset:128\n"
"global_load_lds_dwordx4 v108, s[62:63] offset:1152\n"
"global_load_lds_dwordx4 v109, s[64:65] offset:2176\n"
"global_load_lds_dwordx4 v108, s[64:65] offset:3200\n"
"s_add_u32 m0, s55, 0x5f80\n"
"s_nop 0\n"
"global_load_lds_dwordx4 v136, s[60:61] offset:128\n"
"global_load_lds_dwordx4 v137, s[60:61] offset:1152\n"
"s_add_u32 s13, s49, 0\n"
"s_and_b32 s13, s13, 3\n"
"s_lshl_b32 s13, s13, 7\n"
"s_add_u32 s68, s72, s13\n"
"s_addc_u32 s69, s73, 0\n"
"s_add_u32 s70, s74, s13\n"
"s_addc_u32 s71, s75, 0\n"
"s_waitcnt vmcnt(16)\n"
"s_barrier\n"
"s_add_u32 m0, s56, 0xdf00\n"
"s_nop 0\n"
"global_load_lds_dwordx4 v109, s[62:63] offset:256\n"
"global_load_lds_dwordx4 v108, s[62:63] offset:1280\n"
"ds_read_b128 v[32:35], v120 offset:8192\n"
"global_load_lds_dwordx4 v109, s[64:65] offset:2304\n"
"ds_read_b128 v[36:39], v120 offset:10240\n"
"global_load_lds_dwordx4 v108, s[64:65] offset:3328\n"
"s_add_u32 m0, s55, 0xbf00\n"
"s_nop 0\n"
"global_load_lds_dwordx4 v136, s[60:61] offset:256\n"
"ds_read_b128 v[40:43], v128 offset:8192\n"
"global_load_lds_dwordx4 v137, s[60:61] offset:1280\n"
"ds_read_b128 v[44:47], v128 offset:10240\n"
"s_waitcnt vmcnt(16) lgkmcnt(0)\n"
"s_barrier\n"
"s_add_u32 m0, s56, 0x13e80\n"
"s_nop 0\n"
"global_load_lds_dwordx4 v109, s[62:63] offset:384\n"
"global_load_lds_dwordx4 v108, s[62:63] offset:1408\n"
"global_load_lds_dwordx4 v109, s[64:65] offset:2432\n"
"global_load_lds_dwordx4 v108, s[64:65] offset:3456\n"
"s_add_u32 m0, s55, 0x11e80\n"
"s_nop 0\n"
"global_load_lds_dwordx4 v136, s[60:61] offset:384\n"
"global_load_lds_dwordx4 v137, s[60:61] offset:1408\n"
"s_waitcnt vmcnt(12)\n"
"s_barrier\n"
"s_add_u32 s13, s82, 0x400\n"
"s_and_b32 s13, s13, 0x7ff\n"
"s_add_u32 s14, s13, s81\n"
"s_add_u32 s13, s13, s80\n"
"s_add_u32 s60, s16, s13\n"
"s_addc_u32 s61, s17, 0\n"
"s_add_u32 s62, s18, s14\n"
"s_addc_u32 s63, s19, 0\n"
"s_add_u32 s64, s20, s14\n"
"s_addc_u32 s65, s21, 0\n"
"s_add_u32 m0, s56, 0x2000\n"
"s_nop 0\n"
"global_load_lds_dwordx4 v109, s[62:63]\n"
"ds_read_b128 v[48:51], v110 offset:24576\n"
"ds_read_b128 v[52:55], v110 offset:26624\n"
"ds_read_b128 v[56:59], v112 offset:24576\n"
"global_load_lds_dwordx4 v108, s[62:63] offset:1024\n"
"ds_read_b128 v[60:63], v112 offset:26624\n"
"v_add_u32 v72, 0x20600, v195\n"
"ds_read_b128 v[64:67], v72\n"
"ds_read_b128 v[68:71], v72 offset:1024\n"
"global_load_lds_dwordx4 v109, s[64:65] offset:2048\n"
"v_mov_b32 v113, 0\n"
"v_mov_b32 v114, 0\n"
"s_waitcnt lgkmcnt(1)\n"
"v_lshlrev_b32 v72, 16, v64\n"
"global_load_lds_dwordx4 v108, s[64:65] offset:3072\n"
"v_and_b32 v73, 0xffff0000, v64\n"
"v_lshlrev_b32 v74, 16, v16\n"
"v_and_b32 v75, 0xffff0000, v16\n"
"v_mul_f32 v74, v74, v72\n"
"s_add_u32 m0, s55, 0\n"
"s_nop 0\n"
"global_load_lds_dwordx4 v136, s[60:61]\n"
"v_mul_f32 v75, v75, v73\n"
"v_cvt_pk_bf16_f32 v74, 0, v74\n"
"v_cvt_pk_bf16_f32 v75, 0, v75\n"
"global_load_lds_dwordx4 v137, s[60:61] offset:1024\n"
"v_add_f32 v113, v113, v74\n"
"v_add_f32 v113, v113, v75\n"
"v_lshlrev_b32 v74, 16, v24\n"
"v_and_b32 v75, 0xffff0000, v24\n"
"s_waitcnt vmcnt(12) lgkmcnt(0)\n"
"s_barrier\n"
"s_add_u32 m0, s56, 0x7f80\n"
"s_nop 0\n"
"global_load_lds_dwordx4 v109, s[62:63] offset:128\n"
"v_mul_f32 v74, v74, v72\n"
"v_mul_f32 v75, v75, v73\n"
"v_cvt_pk_bf16_f32 v74, 0, v74\n"
"global_load_lds_dwordx4 v108, s[62:63] offset:1152\n"
"v_cvt_pk_bf16_f32 v75, 0, v75\n"
"v_add_f32 v114, v114, v74\n"
"v_add_f32 v114, v114, v75\n"
"global_load_lds_dwordx4 v109, s[64:65] offset:2176\n"
"v_lshlrev_b32 v88, 16, v65\n"
"v_and_b32 v89, 0xffff0000, v65\n"
"v_lshlrev_b32 v72, 16, v17\n"
"global_load_lds_dwordx4 v108, s[64:65] offset:3200\n"
"v_and_b32 v73, 0xffff0000, v17\n"
"v_mul_f32 v72, v72, v88\n"
"v_mul_f32 v73, v73, v89\n"
"s_add_u32 m0, s55, 0x5f80\n"
"s_nop 0\n"
"global_load_lds_dwordx4 v136, s[60:61] offset:128\n"
"v_cvt_pk_bf16_f32 v72, 0, v72\n"
"v_cvt_pk_bf16_f32 v73, 0, v73\n"
"v_add_f32 v113, v113, v72\n"
"global_load_lds_dwordx4 v137, s[60:61] offset:1152\n"
"v_add_f32 v113, v113, v73\n"
"v_lshlrev_b32 v90, 16, v25\n"
"v_and_b32 v91, 0xffff0000, v25\n"
"s_waitcnt vmcnt(12)\n"
"s_barrier\n"
"s_add_u32 m0, s56, 0xdf00\n"
"s_nop 0\n"
"global_load_lds_dwordx4 v109, s[62:63] offset:256\n"
"ds_read_b128 v[72:75], v120 offset:8192\n"
"ds_read_b128 v[76:79], v120 offset:10240\n"
"ds_read_b128 v[80:83], v128 offset:8192\n"
"global_load_lds_dwordx4 v108, s[62:63] offset:1280\n"
"ds_read_b128 v[84:87], v128 offset:10240\n"
"v_mul_f32 v90, v90, v88\n"
"v_mul_f32 v91, v91, v89\n"
"v_cvt_pk_bf16_f32 v90, 0, v90\n"
"global_load_lds_dwordx4 v109, s[64:65] offset:2304\n"
"v_cvt_pk_bf16_f32 v91, 0, v91\n"
"v_add_f32 v114, v114, v90\n"
"v_add_f32 v114, v114, v91\n"
"v_lshlrev_b32 v88, 16, v66\n"
"global_load_lds_dwordx4 v108, s[64:65] offset:3328\n"
"v_and_b32 v89, 0xffff0000, v66\n"
"v_lshlrev_b32 v90, 16, v18\n"
"v_and_b32 v91, 0xffff0000, v18\n"
"s_add_u32 m0, s55, 0xbf00\n"
"s_nop 0\n"
"global_load_lds_dwordx4 v136, s[60:61] offset:256\n"
"v_mul_f32 v90, v90, v88\n"
"v_mul_f32 v91, v91, v89\n"
"v_cvt_pk_bf16_f32 v90, 0, v90\n"
"v_cvt_pk_bf16_f32 v91, 0, v91\n"
"global_load_lds_dwordx4 v137, s[60:61] offset:1280\n"
"v_add_f32 v113, v113, v90\n"
"v_add_f32 v113, v113, v91\n"
"v_lshlrev_b32 v90, 16, v26\n"
"v_and_b32 v91, 0xffff0000, v26\n"
"s_waitcnt vmcnt(12) lgkmcnt(0)\n"
"s_barrier\n"
"s_add_u32 m0, s56, 0x13e80\n"
"s_nop 0\n"
"global_load_lds_dwordx4 v109, s[62:63] offset:384\n"
"v_mul_f32 v90, v90, v88\n"
"v_mul_f32 v91, v91, v89\n"
"v_cvt_pk_bf16_f32 v90, 0, v90\n"
"global_load_lds_dwordx4 v108, s[62:63] offset:1408\n"
"v_cvt_pk_bf16_f32 v91, 0, v91\n"
"v_add_f32 v114, v114, v90\n"
"v_add_f32 v114, v114, v91\n"
"global_load_lds_dwordx4 v109, s[64:65] offset:2432\n"
"v_lshlrev_b32 v92, 16, v67\n"
"v_and_b32 v93, 0xffff0000, v67\n"
"v_lshlrev_b32 v64, 16, v19\n"
"global_load_lds_dwordx4 v108, s[64:65] offset:3456\n"
"v_and_b32 v65, 0xffff0000, v19\n"
"v_mul_f32 v64, v64, v92\n"
"v_mul_f32 v65, v65, v93\n"
"s_add_u32 m0, s55, 0x11e80\n"
"s_nop 0\n"
"global_load_lds_dwordx4 v136, s[60:61] offset:384\n"
"v_cvt_pk_bf16_f32 v64, 0, v64\n"
"v_cvt_pk_bf16_f32 v65, 0, v65\n"
"v_add_f32 v113, v113, v64\n"
"global_load_lds_dwordx4 v137, s[60:61] offset:1408\n"
"v_add_f32 v113, v113, v65\n"
"v_lshlrev_b32 v94, 16, v27\n"
"v_and_b32 v95, 0xffff0000, v27\n"
"s_waitcnt vmcnt(12)\n"
"s_barrier\n"
"s_add_u32 s13, s82, 0x600\n"
"s_and_b32 s13, s13, 0x7ff\n"
"s_add_u32 s14, s13, s81\n"
"s_add_u32 s13, s13, s80\n"
"s_add_u32 s60, s16, s13\n"
"s_addc_u32 s61, s17, 0\n"
"s_add_u32 s62, s18, s14\n"
"s_addc_u32 s63, s19, 0\n"
"s_add_u32 s64, s20, s14\n"
"s_addc_u32 s65, s21, 0\n"
"s_add_u32 m0, s56, 0x2000\n"
"s_nop 0\n"
"global_load_lds_dwordx4 v109, s[62:63]\n"
"ds_read_b128 v[16:19], v110 offset:24576\n"
"ds_read_b128 v[24:27], v110 offset:26624\n"
"ds_read_b128 v[64:67], v112 offset:24576\n"
"global_load_lds_dwordx4 v108, s[62:63] offset:1024\n"
"ds_read_b128 v[88:91], v112 offset:26624\n"
"v_mul_f32 v94, v94, v92\n"
"v_mul_f32 v95, v95, v93\n"
"v_cvt_pk_bf16_f32 v94, 0, v94\n"
"global_load_lds_dwordx4 v109, s[64:65] offset:2048\n"
"v_cvt_pk_bf16_f32 v95, 0, v95\n"
"v_add_f32 v114, v114, v94\n"
"v_add_f32 v114, v114, v95\n"
"v_lshlrev_b32 v92, 16, v68\n"
"global_load_lds_dwordx4 v108, s[64:65] offset:3072\n"
"v_and_b32 v93, 0xffff0000, v68\n"
"v_lshlrev_b32 v94, 16, v20\n"
"v_and_b32 v95, 0xffff0000, v20\n"
"s_add_u32 m0, s55, 0\n"
"s_nop 0\n"
"global_load_lds_dwordx4 v136, s[60:61]\n"
"v_mul_f32 v94, v94, v92\n"
"v_mul_f32 v95, v95, v93\n"
"v_cvt_pk_bf16_f32 v94, 0, v94\n"
"v_cvt_pk_bf16_f32 v95, 0, v95\n"
"global_load_lds_dwordx4 v137, s[60:61] offset:1024\n"
"v_add_f32 v113, v113, v94\n"
"v_add_f32 v113, v113, v95\n"
"v_lshlrev_b32 v94, 16, v28\n"
"v_and_b32 v95, 0xffff0000, v28\n"
"s_waitcnt vmcnt(12) lgkmcnt(0)\n"
"s_barrier\n"
"s_add_u32 m0, s56, 0x7f80\n"
"s_nop 0\n"
"global_load_lds_dwordx4 v109, s[62:63] offset:128\n"
"v_mul_f32 v94, v94, v92\n"
"v_mul_f32 v95, v95, v93\n"
"v_cvt_pk_bf16_f32 v94, 0, v94\n"
"global_load_lds_dwordx4 v108, s[62:63] offset:1152\n"
"v_cvt_pk_bf16_f32 v95, 0, v95\n"
"v_add_f32 v114, v114, v94\n"
"v_add_f32 v114, v114, v95\n"
"global_load_lds_dwordx4 v109, s[64:65] offset:2176\n"
"v_lshlrev_b32 v111, 16, v69\n"
"v_and_b32 v115, 0xffff0000, v69\n"
"v_lshlrev_b32 v92, 16, v21\n"
"global_load_lds_dwordx4 v108, s[64:65] offset:3200\n"
"v_and_b32 v93, 0xffff0000, v21\n"
"v_mul_f32 v92, v92, v111\n"
"v_mul_f32 v93, v93, v115\n"
"s_add_u32 m0, s55, 0x5f80\n"
"s_nop 0\n"
"global_load_lds_dwordx4 v136, s[60:61] offset:128\n"
"v_cvt_pk_bf16_f32 v92, 0, v92\n"
"v_cvt_pk_bf16_f32 v93, 0, v93\n"
"v_add_f32 v113, v113, v92\n"
"global_load_lds_dwordx4 v137, s[60:61] offset:1152\n"
"v_add_f32 v113, v113, v93\n"
"v_lshlrev_b32 v116, 16, v29\n"
"v_and_b32 v117, 0xffff0000, v29\n"
"s_waitcnt vmcnt(12)\n"
"s_barrier\n"
"s_add_u32 m0, s56, 0xdf00\n"
"s_nop 0\n"
"global_load_lds_dwordx4 v109, s[62:63] offset:256\n"
"ds_read_b128 v[92:95], v120 offset:8192\n"
"ds_read_b128 v[96:99], v120 offset:10240\n"
"ds_read_b128 v[100:103], v128 offset:8192\n"
"global_load_lds_dwordx4 v108, s[62:63] offset:1280\n"
"ds_read_b128 v[104:107], v128 offset:10240\n"
"v_mul_f32 v116, v116, v111\n"
"v_mul_f32 v117, v117, v115\n"
"v_cvt_pk_bf16_f32 v116, 0, v116\n"
"global_load_lds_dwordx4 v109, s[64:65] offset:2304\n"
"v_cvt_pk_bf16_f32 v117, 0, v117\n"
"v_add_f32 v114, v114, v116\n"
"v_add_f32 v114, v114, v117\n"
"v_lshlrev_b32 v111, 16, v70\n"
"global_load_lds_dwordx4 v108, s[64:65] offset:3328\n"
"v_and_b32 v115, 0xffff0000, v70\n"
"v_lshlrev_b32 v116, 16, v22\n"
"v_and_b32 v117, 0xffff0000, v22\n"
"s_add_u32 m0, s55, 0xbf00\n"
"s_nop 0\n"
"global_load_lds_dwordx4 v136, s[60:61] offset:256\n"
"v_mul_f32 v116, v116, v111\n"
"v_mul_f32 v117, v117, v115\n"
"v_cvt_pk_bf16_f32 v116, 0, v116\n"
"v_cvt_pk_bf16_f32 v117, 0, v117\n"
"global_load_lds_dwordx4 v137, s[60:61] offset:1280\n"
"v_add_f32 v113, v113, v116\n"
"v_add_f32 v113, v113, v117\n"
"v_lshlrev_b32 v116, 16, v30\n"
"v_and_b32 v117, 0xffff0000, v30\n"
"s_waitcnt vmcnt(12) lgkmcnt(0)\n"
"s_barrier\n"
"s_add_u32 m0, s56, 0x13e80\n"
"s_nop 0\n"
"global_load_lds_dwordx4 v109, s[62:63] offset:384\n"
"v_mul_f32 v116, v116, v111\n"
"v_mul_f32 v117, v117, v115\n"
"v_cvt_pk_bf16_f32 v116, 0, v116\n"
"global_load_lds_dwordx4 v108, s[62:63] offset:1408\n"
"v_cvt_pk_bf16_f32 v117, 0, v117\n"
"v_add_f32 v114, v114, v116\n"
"v_add_f32 v114, v114, v117\n"
"global_load_lds_dwordx4 v109, s[64:65] offset:2432\n"
"v_lshlrev_b32 v115, 16, v71\n"
"v_and_b32 v116, 0xffff0000, v71\n"
"v_lshlrev_b32 v68, 16, v23\n"
"global_load_lds_dwordx4 v108, s[64:65] offset:3456\n"
"v_and_b32 v69, 0xffff0000, v23\n"
"v_mul_f32 v68, v68, v115\n"
"v_mul_f32 v69, v69, v116\n"
"s_add_u32 m0, s55, 0x11e80\n"
"s_nop 0\n"
"global_load_lds_dwordx4 v136, s[60:61] offset:384\n"
"v_cvt_pk_bf16_f32 v68, 0, v68\n"
"v_cvt_pk_bf16_f32 v69, 0, v69\n"
"v_add_f32 v113, v113, v68\n"
"global_load_lds_dwordx4 v137, s[60:61] offset:1408\n"
"v_add_f32 v113, v113, v69\n"
"v_lshlrev_b32 v117, 16, v31\n"
"v_and_b32 v118, 0xffff0000, v31\n"
"s_waitcnt vmcnt(12)\n"
"s_barrier\n"
"s_add_u32 s13, s82, 0\n"
"s_and_b32 s13, s13, 0x7ff\n"
"s_add_u32 s13, s13, s80\n"
"s_add_u32 s13, s13, 0x200000\n"
"s_add_u32 s60, s16, s13\n"
"s_addc_u32 s61, s17, 0\n"
"s_add_u32 m0, s97, 0\n"
"s_nop 0\n"
"global_load_lds_dwordx4 v136, s[60:61]\n"
"ds_read_b128 v[20:23], v110 offset:24576\n"
"ds_read_b128 v[28:31], v110 offset:26624\n"
"ds_read_b128 v[68:71], v112 offset:24576\n"
"global_load_lds_dwordx4 v137, s[60:61] offset:1024\n"
"ds_read_b128 v[108:111], v112 offset:26624\n"
"v_mul_f32 v117, v117, v115\n"
"v_mul_f32 v118, v118, v116\n"
"v_cvt_pk_bf16_f32 v117, 0, v117\n"
"s_add_u32 m0, s97, 0x700\n"
"s_nop 0\n"
"global_load_lds_dwordx4 v136, s[60:61] offset:256\n"
"v_cvt_pk_bf16_f32 v118, 0, v118\n"
"v_add_f32 v114, v114, v117\n"
"v_add_f32 v114, v114, v118\n"
"v_add_f32_dpp v113, v113, v113 row_shr:1 row_mask:0xf bank_mask:0xf\n"
"global_load_lds_dwordx4 v137, s[60:61] offset:1280\n"
"v_add_f32_dpp v114, v114, v114 row_shr:1 row_mask:0xf bank_mask:0xf\n"
"v_add_f32_dpp v113, v113, v113 row_shr:2 row_mask:0xf bank_mask:0xf\n"
"s_nop 0\n"
"v_add_f32_dpp v114, v114, v114 row_shr:2 row_mask:0xf bank_mask:0xf\n"
"s_add_u32 s13, s82, 0x200\n"
"s_and_b32 s13, s13, 0x7ff\n"
"s_add_u32 s13, s13, s80\n"
"s_add_u32 s13, s13, 0x200000\n"
"s_add_u32 s60, s16, s13\n"
"s_addc_u32 s61, s17, 0\n"
"s_add_u32 m0, s57, 0x1040\n"
"s_nop 0\n"
"global_load_lds_dwordx4 v136, s[60:61]\n"
"v_add_f32_dpp v113, v113, v113 row_shr:4 row_mask:0xf bank_mask:0xf\n"
"v_add_f32_dpp v114, v114, v114 row_shr:4 row_mask:0xf bank_mask:0xf\n"
"s_nop 0\n"
"v_add_f32_dpp v113, v113, v113 row_shr:8 row_mask:0xf bank_mask:0xf\n"
"v_add_f32_dpp v114, v114, v114 row_shr:8 row_mask:0xf bank_mask:0xf\n"
"global_load_lds_dwordx4 v137, s[60:61] offset:1024\n"
"v_add_f32_dpp v113, v113, v113 row_bcast:15 row_mask:0xa bank_mask:0xf\n"
"v_add_f32_dpp v114, v114, v114 row_bcast:15 row_mask:0xa bank_mask:0xf\n"
"s_nop 0\n"
"v_add_f32_dpp v113, v113, v113 row_bcast:31 row_mask:0xc bank_mask:0xf\n"
"v_add_f32_dpp v114, v114, v114 row_bcast:31 row_mask:0xc bank_mask:0xf\n"
"s_waitcnt vmcnt(12) lgkmcnt(0)\n"
"s_barrier\n"
"s_add_u32 m0, s57, 0x1740\n"
"s_nop 0\n"
"global_load_lds_dwordx4 v136, s[60:61] offset:256\n"
"global_load_lds_dwordx4 v137, s[60:61] offset:1280\n"
"s_add_u32 s13, s82, 0x400\n"
"s_and_b32 s13, s13, 0x7ff\n"
"s_add_u32 s13, s13, s80\n"
"s_add_u32 s13, s13, 0x200000\n"
"s_add_u32 s60, s16, s13\n"
"s_addc_u32 s61, s17, 0\n"
"s_add_u32 m0, s55, 0\n"
"s_nop 0\n"
"global_load_lds_dwordx4 v136, s[60:61]\n"
"global_load_lds_dwordx4 v137, s[60:61] offset:1024\n"
"s_add_u32 m0, s55, 0x1f00\n"
"s_nop 0\n"
"global_load_lds_dwordx4 v136, s[60:61] offset:256\n"
"global_load_lds_dwordx4 v137, s[60:61] offset:1280\n"
"v_readlane_b32 s13, v113, 63\n"
"v_readlane_b32 s14, v114, 63\n"
"v_cmp_lt_u32 vcc, 31, v152\n"
"v_mov_b32 v112, s13\n"
"v_mov_b32 v113, s14\n"
"v_cndmask_b32 v112, v112, v113, vcc\n"
"v_cvt_pk_bf16_f32 v112, 0, v112\n"
"v_mul_f32 v112, 0xbfb8aa3b, v112\n"
"v_exp_f32 v112, v112\n"
"s_nop 0\n"
"v_add_f32 v112, 1.0, v112\n"
"v_rcp_f32 v112, v112\n"
"s_nop 0\n"
"v_cvt_pk_bf16_f32 v112, 0, v112\n"
"v_mov_b32 v113, s10\n"
"s_lshl3_add_u32 s13, s83, 0x140000\n"
"v_lshrrev_b32 v114, 5, v152\n"
"v_lshl_add_u32 v114, v114, 13, s13\n"
"global_store_dwordx2 v114, v[112:113], s[30:31] sc1\n"
"s_waitcnt vmcnt(9)\n"
"s_barrier\n"
"s_setprio 1\n"
"ds_read_b128 v[112:115], v120 offset:8192\n"
"ds_read_b128 v[116:119], v120 offset:10240\n"
"ds_read_b128 v[120:123], v128 offset:8192\n"
"ds_read_b128 v[124:127], v128 offset:10240\n"
"s_add_u32 s13, s82, 0x600\n"
"s_and_b32 s13, s13, 0x7ff\n"
"s_add_u32 s13, s13, s80\n"
"s_add_u32 s13, s13, 0x200000\n"
"s_add_u32 s60, s16, s13\n"
"s_addc_u32 s61, s17, 0\n"
"s_add_u32 m0, s55, 0x4000\n"
"s_nop 0\n"
"global_load_lds_dwordx4 v136, s[60:61]\n"
"global_load_lds_dwordx4 v137, s[60:61] offset:1024\n"
"s_add_u32 m0, s55, 0x5f00\n"
"s_nop 0\n"
"global_load_lds_dwordx4 v136, s[60:61] offset:256\n"
"global_load_lds_dwordx4 v137, s[60:61] offset:1280\n"
"s_add_u32 m0, s57, 32\n"
"s_nop 0\n"
"global_load_lds_dwordx4 v195, s[28:29]\n"
"global_load_lds_dwordx4 v195, s[28:29] offset:1024\n"
"global_load_lds_dwordx4 v195, s[28:29] offset:2048\n"
"global_load_lds_dwordx4 v195, s[28:29] offset:3072\n"
"s_waitcnt vmcnt(11) lgkmcnt(0)\n"
"s_barrier\n"
"global_load_dwordx4 v[128:131], v195, s[26:27] sc1\n"
"global_load_dwordx4 v[132:135], v195, s[26:27] offset:1024 sc1\n"
"s_lshl_b32 s81, s11, 16\n"
"s_add_u32 s13, s82, 0\n"
"s_and_b32 s13, s13, 0x7ff\n"
"s_add_u32 s13, s13, s80\n"
"s_add_u32 s13, s13, 0x200080\n"
"s_add_u32 s60, s16, s13\n"
"s_addc_u32 s61, s17, 0\n"
"s_add_u32 m0, s55, 0x8000\n"
"s_nop 0\n"
"global_load_lds_dwordx4 v136, s[60:61]\n"
"global_load_lds_dwordx4 v137, s[60:61] offset:1024\n"
"s_add_u32 m0, s55, 0x9f00\n"
"s_nop 0\n"
"global_load_lds_dwordx4 v136, s[60:61] offset:256\n"
"global_load_lds_dwordx4 v137, s[60:61] offset:1280\n"
"s_add_u32 s13, s82, 0x200\n"
"s_and_b32 s13, s13, 0x7ff\n"
"s_add_u32 s13, s13, s80\n"
"s_add_u32 s13, s13, 0x200080\n"
"s_add_u32 s60, s16, s13\n"
"s_addc_u32 s61, s17, 0\n"
"s_add_u32 m0, s55, 0xc000\n"
"s_nop 0\n"
"global_load_lds_dwordx4 v136, s[60:61]\n"
"global_load_lds_dwordx4 v137, s[60:61] offset:1024\n"
"s_add_u32 m0, s55, 0xdf00\n"
"s_nop 0\n"
"global_load_lds_dwordx4 v136, s[60:61] offset:256\n"
"global_load_lds_dwordx4 v137, s[60:61] offset:1280\n"
"s_add_u32 s13, s82, 0x400\n"
"s_and_b32 s13, s13, 0x7ff\n"
"s_add_u32 s13, s13, s80\n"
"s_add_u32 s13, s13, 0x200080\n"
"s_add_u32 s60, s16, s13\n"
"s_addc_u32 s61, s17, 0\n"
"s_add_u32 m0, s55, 0x10000\n"
"s_nop 0\n"
"global_load_lds_dwordx4 v136, s[60:61]\n"
"global_load_lds_dwordx4 v137, s[60:61] offset:1024\n"
"s_waitcnt vmcnt(16)\n"
"s_barrier\n"
"s_waitcnt vmcnt(11)\n"
"v_xor_b32 v153, s81, v129\n"
"v_bitop3_b32 v153, v153, v131, s81 bitop3:0xf6\n"
"s_waitcnt vmcnt(10)\n"
"v_bitop3_b32 v153, v153, v133, s81 bitop3:0xf6\n"
"v_bitop3_b32 v153, v153, v135, s81 bitop3:0xf6\n"
"v_lshrrev_b32 v138, 16, v153\n"
"v_bitop3_b32 v138, v138, v128, s10 bitop3:0xf6\n"
"v_bitop3_b32 v138, v138, v130, s10 bitop3:0xf6\n"
"v_bitop3_b32 v138, v138, v132, s10 bitop3:0xf6\n"
"v_bitop3_b32 v138, v138, v134, s10 bitop3:0xf6\n"
"v_cmp_ne_u32 vcc, 0, v138\n"
"s_nop 0\n"
"s_cbranch_vccnz .Lr4\n"
".Lb4:\n"
"s_add_u32 m0, s97, 0\n"
"s_nop 0\n"
"global_load_lds_dwordx4 v195, s[38:39] sc1\n"
"global_load_lds_dwordx4 v195, s[38:39] offset:1024 sc1\n"
"global_load_lds_dwordx4 v195, s[38:39] offset:2048 sc1\n"
"global_load_lds_dwordx4 v195, s[38:39] offset:3072 sc1\n"
"s_add_u32 m0, s55, 0x11f00\n"
"s_nop 0\n"
"global_load_lds_dwordx4 v136, s[60:61] offset:256\n"
"global_load_lds_dwordx4 v137, s[60:61] offset:1280\n"
"s_add_u32 s13, s82, 0x600\n"
"s_and_b32 s13, s13, 0x7ff\n"
"s_add_u32 s13, s13, s80\n"
"s_add_u32 s13, s13, 0x200080\n"
"s_add_u32 s60, s16, s13\n"
"s_addc_u32 s61, s17, 0\n"
"s_add_u32 m0, s55, 0x14000\n"
"s_nop 0\n"
"global_load_lds_dwordx4 v136, s[60:61]\n"
"global_load_lds_dwordx4 v137, s[60:61] offset:1024\n"
"s_add_u32 m0, s55, 0x15f00\n"
"s_nop 0\n"
"global_load_lds_dwordx4 v136, s[60:61] offset:256\n"
"global_load_lds_dwordx4 v137, s[60:61] offset:1280\n"
"ds_read_b128 v[136:139], v194 offset:32\n"
"ds_read_b32 v157, v194 offset:48\n"
"ds_read_b128 v[140:143], v194 offset:1056\n"
"ds_read_b32 v156, v194 offset:1072\n"
"ds_read_b128 v[144:147], v194 offset:2080\n"
"ds_read_b32 v155, v194 offset:2096\n"
"ds_read_b128 v[148:151], v194 offset:3104\n"
"ds_read_b32 v154, v194 offset:3120\n"
"s_waitcnt lgkmcnt(7)\n"
"v_alignbit_b32 v136, v137, v136, 16\n"
"v_alignbit_b32 v137, v138, v137, 16\n"
"v_alignbit_b32 v138, v139, v138, 16\n"
"s_waitcnt lgkmcnt(6)\n"
"v_alignbit_b32 v139, v157, v139, 16\n"
"s_waitcnt lgkmcnt(5)\n"
"v_alignbit_b32 v140, v141, v140, 16\n"
"v_alignbit_b32 v141, v142, v141, 16\n"
"v_alignbit_b32 v142, v143, v142, 16\n"
"s_waitcnt lgkmcnt(4)\n"
"v_alignbit_b32 v143, v156, v143, 16\n"
"s_waitcnt lgkmcnt(3)\n"
"v_alignbit_b32 v144, v145, v144, 16\n"
"v_alignbit_b32 v145, v146, v145, 16\n"
"v_alignbit_b32 v146, v147, v146, 16\n"
"s_waitcnt lgkmcnt(2)\n"
"v_alignbit_b32 v147, v155, v147, 16\n"
"s_waitcnt lgkmcnt(1)\n"
"v_alignbit_b32 v148, v149, v148, 16\n"
"v_alignbit_b32 v149, v150, v149, 16\n"
"v_alignbit_b32 v150, v151, v150, 16\n"
"s_waitcnt lgkmcnt(0)\n"
"v_alignbit_b32 v151, v154, v151, 16\n"
"v_bfe_i32 v176, v129, 0, 16\n"
"v_bfe_i32 v153, v131, 0, 16\n"
"v_add_u32 v176, v176, v153\n"
"v_bfe_i32 v153, v133, 0, 16\n"
"v_add_u32 v176, v176, v153\n"
"v_bfe_i32 v153, v135, 0, 16\n"
"v_add_u32 v176, v176, v153\n"
"s_nop 1\n"
"v_add_u32_dpp v176, v176, v176 row_shr:1 row_mask:0xf bank_mask:0xf\n"
"s_nop 1\n"
"v_add_u32_dpp v176, v176, v176 row_shr:2 row_mask:0xf bank_mask:0xf\n"
"s_nop 1\n"
"v_add_u32_dpp v176, v176, v176 row_shr:4 row_mask:0xf bank_mask:0xf\n"
"s_nop 1\n"
"v_add_u32_dpp v176, v176, v176 row_shr:8 row_mask:0xf bank_mask:0xf\n"
"s_nop 1\n"
"v_add_u32_dpp v176, v176, v176 row_bcast:15 row_mask:0xa bank_mask:0xf\n"
"s_nop 1\n"
"v_add_u32_dpp v176, v176, v176 row_bcast:31 row_mask:0xc bank_mask:0xf\n"
"s_nop 0\n"
"v_readlane_b32 s13, v176, 63\n"
"s_nop 0\n"
"s_cmp_gt_i32 s13, 0\n"
"s_cselect_b32 s7, 0x2000, 0\n"
"s_lshl2_add_u32 s13, s15, 0x27580\n"
"v_mov_b32 v177, s13\n"
"v_mov_b32 v176, s7\n"
"s_xor_b32 s86, s86, s7\n"
"s_bfe_u32 s13, %1, 0x20003\n"
"s_lshl_b32 s13, s13, 14\n"
"v_and_b32 v128, 7, v152\n"
"v_bfe_u32 v230, v152, 3, 1\n"
"v_lshlrev_b32 v128, 11, v128\n"
"v_lshl_or_b32 v128, v230, 9, v128\n"
"v_add_u32 v128, s13, v128\n"
"s_mov_b64 exec, 0xffff\n"
"global_load_dword v230, v128, s[68:69] offset:-512\n"
"global_load_dword v231, v128, s[70:71] offset:1536\n"
"s_mov_b64 exec, -1\n"
"s_waitcnt vmcnt(18)\n"
"s_barrier\n"
"ds_read_b128 v[128:131], v217 offset:32768\n"
"ds_read_b128 v[132:135], v217 offset:34816\n"
"ds_read_b128 v[152:155], v217 offset:36864\n"
"ds_read_b128 v[156:159], v217 offset:38912\n"
"ds_read_b128 v[160:163], v228 offset:32768\n"
"ds_read_b128 v[164:167], v228 offset:34816\n"
"ds_read_b128 v[168:171], v228 offset:36864\n"
"ds_read_b128 v[172:175], v228 offset:38912\n"
"ds_write_b32 v177, v176\n"
"ds_write_b128 v194, v[136:139] offset:4192\n"
"ds_write_b128 v194, v[140:143] offset:5216\n"
"ds_write_b128 v194, v[144:147] offset:6240\n"
"ds_write_b128 v194, v[148:151] offset:7264\n"
"s_waitcnt vmcnt(2) lgkmcnt(0)\n"
"s_setprio 0\n"
"s_barrier\n"
"ds_read_b128 v[136:139], v229\n"
"ds_read_b128 v[140:143], v229 offset:1024\n"
"ds_read_b128 v[144:147], v229 offset:2048\n"
"ds_read_b128 v[148:151], v229 offset:3072\n"
"ds_read_b128 v[176:179], v229 offset:16384\n"
"ds_read_b128 v[180:183], v229 offset:17408\n"
"ds_read_b128 v[184:187], v229 offset:18432\n"
"ds_read_b128 v[188:191], v229 offset:19456\n"
"s_waitcnt lgkmcnt(7)\n"
"v_mfma_f32_16x16x32_bf16 v[136:139], v[128:131], v[0:3], v[136:139]\n"
"s_waitcnt lgkmcnt(6)\n"
"v_mfma_f32_16x16x32_bf16 v[140:143], v[128:131], v[4:7], v[140:143]\n"
"ds_read_b128 v[128:131], v217 offset:40960\n"
"s_waitcnt lgkmcnt(6)\n"
"v_mfma_f32_16x16x32_bf16 v[144:147], v[132:135], v[0:3], v[144:147]\n"
"s_waitcnt lgkmcnt(5)\n"
"v_mfma_f32_16x16x32_bf16 v[148:151], v[132:135], v[4:7], v[148:151]\n"
"ds_read_b128 v[132:135], v217 offset:43008\n"
"s_waitcnt lgkmcnt(5)\n"
"v_mfma_f32_16x16x32_bf16 v[176:179], v[152:155], v[0:3], v[176:179]\n"
"s_waitcnt lgkmcnt(4)\n"
"v_mfma_f32_16x16x32_bf16 v[180:183], v[152:155], v[4:7], v[180:183]\n"
"ds_read_b128 v[152:155], v217 offset:45056\n"
"s_waitcnt lgkmcnt(3)\n"
"s_barrier\n"
"v_mfma_f32_16x16x32_bf16 v[184:187], v[156:159], v[0:3], v[184:187]\n"
"v_mfma_f32_16x16x32_bf16 v[188:191], v[156:159], v[4:7], v[188:191]\n"
"ds_read_b128 v[0:3], v217 offset:47104\n"
"v_mfma_f32_16x16x32_bf16 v[136:139], v[160:163], v[8:11], v[136:139]\n"
"s_xor_b32 s42, s42, s7\n"
"v_mfma_f32_16x16x32_bf16 v[140:143], v[160:163], v[12:15], v[140:143]\n"
"ds_read_b128 v[4:7], v228 offset:40960\n"
"v_mfma_f32_16x16x32_bf16 v[144:147], v[164:167], v[8:11], v[144:147]\n"
"s_add_u32 s62, s42, s44\n"
"s_addc_u32 s63, s43, 0\n"
"v_mfma_f32_16x16x32_bf16 v[148:151], v[164:167], v[12:15], v[148:151]\n"
"ds_read_b128 v[156:159], v228 offset:43008\n"
"v_mfma_f32_16x16x32_bf16 v[176:179], v[168:171], v[8:11], v[176:179]\n"
"s_add_u32 s64, s42, s45\n"
"s_addc_u32 s65, s43, 0\n"
"v_mfma_f32_16x16x32_bf16 v[180:183], v[168:171], v[12:15], v[180:183]\n"
"ds_read_b128 v[160:163], v228 offset:45056\n"
"v_mfma_f32_16x16x32_bf16 v[184:187], v[172:175], v[8:11], v[184:187]\n"
"s_add_u32 s32, s42, s46\n"
"s_addc_u32 s33, s43, 0\n"
"v_mfma_f32_16x16x32_bf16 v[188:191], v[172:175], v[12:15], v[188:191]\n"
"ds_read_b128 v[8:11], v228 offset:47104\n"
"s_waitcnt lgkmcnt(6)\n"
"v_mfma_f32_16x16x32_bf16 v[136:139], v[128:131], v[32:35], v[136:139]\n"
"s_add_u32 s34, s42, s47\n"
"s_addc_u32 s35, s43, 0\n"
"v_mfma_f32_16x16x32_bf16 v[140:143], v[128:131], v[36:39], v[140:143]\n"
"ds_read_b128 v[12:15], v217 offset:49152\n"
"v_mfma_f32_16x16x32_bf16 v[144:147], v[132:135], v[32:35], v[144:147]\n"
"s_add_u32 s18, s62, 0x40000\n"
"s_addc_u32 s19, s63, 0\n"
"v_mfma_f32_16x16x32_bf16 v[148:151], v[132:135], v[36:39], v[148:151]\n"
"ds_read_b128 v[128:131], v217 offset:51200\n"
"s_waitcnt lgkmcnt(6)\n"
"v_mfma_f32_16x16x32_bf16 v[176:179], v[152:155], v[32:35], v[176:179]\n"
"s_add_u32 s20, s64, 0x40000\n"
"s_addc_u32 s21, s65, 0\n"
"v_mfma_f32_16x16x32_bf16 v[180:183], v[152:155], v[36:39], v[180:183]\n"
"ds_read_b128 v[132:135], v217 offset:53248\n"
"v_mfma_f32_16x16x32_bf16 v[184:187], v[0:3], v[32:35], v[184:187]\n"
"s_add_u32 s36, s32, 0x40000\n"
"s_addc_u32 s37, s33, 0\n"
"v_mfma_f32_16x16x32_bf16 v[188:191], v[0:3], v[36:39], v[188:191]\n"
"ds_read_b128 v[0:3], v217 offset:55296\n"
"s_waitcnt lgkmcnt(6)\n"
"v_mfma_f32_16x16x32_bf16 v[136:139], v[4:7], v[40:43], v[136:139]\n"
"s_add_u32 s40, s34, 0x40000\n"
"s_addc_u32 s41, s35, 0\n"
"v_mfma_f32_16x16x32_bf16 v[140:143], v[4:7], v[44:47], v[140:143]\n"
"ds_read_b128 v[4:7], v228 offset:49152\n"
"s_waitcnt lgkmcnt(5)\n"
"s_barrier\n"
"v_mfma_f32_16x16x32_bf16 v[144:147], v[156:159], v[40:43], v[144:147]\n"
"s_lshl2_add_u32 s13, %1, s15\n"
"s_lshl_b32 s13, s13, 14\n"
"v_mfma_f32_16x16x32_bf16 v[148:151], v[156:159], v[44:47], v[148:151]\n"
"ds_read_b128 v[32:35], v228 offset:51200\n"
"v_mfma_f32_16x16x32_bf16 v[176:179], v[160:163], v[40:43], v[176:179]\n"
"s_add_u32 s13, s13, 0x1400000\n"
"s_lshr_b32 s16, s13, 8\n"
"v_mfma_f32_16x16x32_bf16 v[180:183], v[160:163], v[44:47], v[180:183]\n"
"ds_read_b128 v[36:39], v228 offset:53248\n"
"v_mfma_f32_16x16x32_bf16 v[184:187], v[8:11], v[40:43], v[184:187]\n"
"s_and_b32 s16, s16, 0x2000\n"
"s_add_u32 s16, s16, s13\n"
"v_mfma_f32_16x16x32_bf16 v[188:191], v[8:11], v[44:47], v[188:191]\n"
"ds_read_b128 v[8:11], v228 offset:55296\n"
"s_waitcnt lgkmcnt(6)\n"
"v_mfma_f32_16x16x32_bf16 v[136:139], v[12:15], v[48:51], v[136:139]\n"
"s_add_u32 s16, s30, s16\n"
"s_addc_u32 s17, s31, 0\n"
"v_mfma_f32_16x16x32_bf16 v[140:143], v[12:15], v[52:55], v[140:143]\n"
"ds_read_b128 v[12:15], v217 offset:57344\n"
"v_mfma_f32_16x16x32_bf16 v[144:147], v[128:131], v[48:51], v[144:147]\n"
"s_xor_b32 s16, s16, s7\n"
"s_lshr_b32 s13, s10, 4\n"
"v_mfma_f32_16x16x32_bf16 v[148:151], v[128:131], v[52:55], v[148:151]\n"
"ds_read_b128 v[40:43], v217 offset:59392\n"
"s_waitcnt lgkmcnt(6)\n"
"v_mfma_f32_16x16x32_bf16 v[176:179], v[132:135], v[48:51], v[176:179]\n"
"s_add_u32 s13, s13, s10\n"
"s_and_b32 s13, s13, 15\n"
"v_mfma_f32_16x16x32_bf16 v[180:183], v[132:135], v[52:55], v[180:183]\n"
"ds_read_b128 v[44:47], v217 offset:61440\n"
"v_mfma_f32_16x16x32_bf16 v[184:187], v[0:3], v[48:51], v[184:187]\n"
"s_and_b32 s14, s10, 15\n"
"s_lshl4_add_u32 s13, s13, s14\n"
"v_mfma_f32_16x16x32_bf16 v[188:191], v[0:3], v[52:55], v[188:191]\n"
"ds_read_b128 v[0:3], v217 offset:63488\n"
"s_waitcnt lgkmcnt(6)\n"
"v_mfma_f32_16x16x32_bf16 v[136:139], v[4:7], v[56:59], v[136:139]\n"
"v_mov_b32 v217, s13\n"
"s_mov_b32 s82, 0x38000100\n"
"v_mfma_f32_16x16x32_bf16 v[140:143], v[4:7], v[60:63], v[140:143]\n"
"ds_read_b128 v[4:7], v228 offset:57344\n"
"buffer_load_dwordx2 v[54:55], v219, s[76:79], 0 offen offset:3584 sc1\n"
"v_mfma_f32_16x16x32_bf16 v[144:147], v[32:35], v[56:59], v[144:147]\n"
"s_lshl2_add_u32 s14, s50, s15\n"
"s_lshl_b32 s13, s14, 12\n"
"v_mfma_f32_16x16x32_bf16 v[148:151], v[32:35], v[60:63], v[148:151]\n"
"ds_read_b128 v[32:35], v228 offset:59392\n"
"s_waitcnt lgkmcnt(6)\n"
"v_mfma_f32_16x16x32_bf16 v[176:179], v[36:39], v[56:59], v[176:179]\n"
"s_add_u32 s13, s13, 0x60000\n"
"buffer_load_dwordx2 v[52:53], v219, s[76:79], 0 offen offset:3072 sc1\n"
"v_mfma_f32_16x16x32_bf16 v[180:183], v[36:39], v[60:63], v[180:183]\n"
"ds_read_b128 v[36:39], v228 offset:61440\n"
"buffer_load_dwordx2 v[50:51], v219, s[76:79], 0 offen offset:2560 sc1\n"
"v_mfma_f32_16x16x32_bf16 v[184:187], v[8:11], v[56:59], v[184:187]\n"
"s_add_u32 s66, s30, s13\n"
"s_addc_u32 s67, s31, 0\n"
"v_mfma_f32_16x16x32_bf16 v[188:191], v[8:11], v[60:63], v[188:191]\n"
"ds_read_b128 v[8:11], v228 offset:63488\n"
"s_waitcnt lgkmcnt(6)\n"
"v_mfma_f32_16x16x32_bf16 v[136:139], v[12:15], v[72:75], v[136:139]\n"
"s_lshl_b32 s14, s14, 18\n"
"s_lshl1_add_u32 s13, s51, s48\n"
"v_mfma_f32_16x16x32_bf16 v[140:143], v[12:15], v[76:79], v[140:143]\n"
"ds_read_b128 v[12:15], v223\n"
"buffer_load_dwordx2 v[48:49], v219, s[76:79], 0 offen offset:2048 sc1\n"
"v_mfma_f32_16x16x32_bf16 v[144:147], v[40:43], v[72:75], v[144:147]\n"
"s_lshl_b32 s13, s13, 8\n"
"s_add_u32 s13, s13, s14\n"
"v_mfma_f32_16x16x32_bf16 v[148:151], v[40:43], v[76:79], v[148:151]\n"
"ds_read_b128 v[40:43], v223 offset:2048\n"
"s_waitcnt lgkmcnt(6)\n"
"v_mfma_f32_16x16x32_bf16 v[176:179], v[44:47], v[72:75], v[176:179]\n"
"s_add_u32 s13, s13, 0x2c00000\n"
"v_mfma_f32_16x16x32_bf16 v[180:183], v[44:47], v[76:79], v[180:183]\n"
"ds_read_b128 v[44:47], v223 offset:4096\n"
"v_mfma_f32_16x16x32_bf16 v[184:187], v[0:3], v[72:75], v[184:187]\n"
"s_add_u32 s92, s30, s13\n"
"s_addc_u32 s93, s31, 0\n"
"v_mfma_f32_16x16x32_bf16 v[188:191], v[0:3], v[76:79], v[188:191]\n"
"ds_read_b128 v[0:3], v223 offset:6144\n"
"s_waitcnt lgkmcnt(6)\n"
"v_mfma_f32_16x16x32_bf16 v[136:139], v[4:7], v[80:83], v[136:139]\n"
"s_lshr_b32 s13, s13, 8\n"
"s_and_b32 s13, s13, 0x2000\n"
"v_mfma_f32_16x16x32_bf16 v[140:143], v[4:7], v[84:87], v[140:143]\n"
"ds_read_b128 v[4:7], v221\n"
"v_or_b32 v203, s13, v203\n"
"v_mfma_f32_16x16x32_bf16 v[144:147], v[32:35], v[80:83], v[144:147]\n"
"s_add_u32 s94, s92, 0x20000\n"
"s_addc_u32 s95, s93, 0\n"
"v_mfma_f32_16x16x32_bf16 v[148:151], v[32:35], v[84:87], v[148:151]\n"
"ds_read_b128 v[32:35], v221 offset:2048\n"
"s_waitcnt lgkmcnt(6)\n"
"v_mfma_f32_16x16x32_bf16 v[176:179], v[36:39], v[80:83], v[176:179]\n"
"s_add_u32 s22, s92, 0x4000\n"
"s_addc_u32 s23, s93, 0\n"
"v_mfma_f32_16x16x32_bf16 v[180:183], v[36:39], v[84:87], v[180:183]\n"
"ds_read_b128 v[36:39], v221 offset:4096\n"
"v_mfma_f32_16x16x32_bf16 v[184:187], v[8:11], v[80:83], v[184:187]\n"
"s_add_u32 s24, s94, 0x4000\n"
"s_addc_u32 s25, s95, 0\n"
"v_mfma_f32_16x16x32_bf16 v[188:191], v[8:11], v[84:87], v[188:191]\n"
"ds_read_b128 v[8:11], v221 offset:6144\n"
"s_waitcnt lgkmcnt(6)\n"
"v_mfma_f32_16x16x32_bf16 v[136:139], v[12:15], v[16:19], v[136:139]\n"
"v_mfma_f32_16x16x32_bf16 v[140:143], v[12:15], v[24:27], v[140:143]\n"
"ds_read_b128 v[12:15], v223 offset:8192\n"
"v_mfma_f32_16x16x32_bf16 v[144:147], v[40:43], v[16:19], v[144:147]\n"
"v_mfma_f32_16x16x32_bf16 v[148:151], v[40:43], v[24:27], v[148:151]\n"
"ds_read_b128 v[40:43], v223 offset:10240\n"
"s_waitcnt lgkmcnt(6)\n"
"v_mfma_f32_16x16x32_bf16 v[176:179], v[44:47], v[16:19], v[176:179]\n"
"v_mfma_f32_16x16x32_bf16 v[180:183], v[44:47], v[24:27], v[180:183]\n"
"ds_read_b128 v[44:47], v223 offset:12288\n"
"v_mfma_f32_16x16x32_bf16 v[184:187], v[0:3], v[16:19], v[184:187]\n"
"v_mfma_f32_16x16x32_bf16 v[188:191], v[0:3], v[24:27], v[188:191]\n"
"ds_read_b128 v[0:3], v223 offset:14336\n"
"s_waitcnt lgkmcnt(6)\n"
"v_mfma_f32_16x16x32_bf16 v[136:139], v[4:7], v[64:67], v[136:139]\n"
"v_mfma_f32_16x16x32_bf16 v[140:143], v[4:7], v[88:91], v[140:143]\n"
"ds_read_b128 v[4:7], v221 offset:8192\n"
"v_mfma_f32_16x16x32_bf16 v[144:147], v[32:35], v[64:67], v[144:147]\n"
"v_mfma_f32_16x16x32_bf16 v[148:151], v[32:35], v[88:91], v[148:151]\n"
"ds_read_b128 v[16:19], v221 offset:10240\n"
"s_waitcnt lgkmcnt(6)\n"
"v_mfma_f32_16x16x32_bf16 v[176:179], v[36:39], v[64:67], v[176:179]\n"
"v_mfma_f32_16x16x32_bf16 v[180:183], v[36:39], v[88:91], v[180:183]\n"
"ds_read_b128 v[24:27], v221 offset:12288\n"
"v_mfma_f32_16x16x32_bf16 v[184:187], v[8:11], v[64:67], v[184:187]\n"
"v_mfma_f32_16x16x32_bf16 v[188:191], v[8:11], v[88:91], v[188:191]\n"
"ds_read_b128 v[8:11], v221 offset:14336\n"
"s_waitcnt lgkmcnt(6)\n"
"v_mfma_f32_16x16x32_bf16 v[136:139], v[12:15], v[92:95], v[136:139]\n"
"v_mfma_f32_16x16x32_bf16 v[140:143], v[12:15], v[96:99], v[140:143]\n"
"ds_read_b128 v[12:15], v223 offset:16384\n"
"v_mfma_f32_16x16x32_bf16 v[144:147], v[40:43], v[92:95], v[144:147]\n"
"v_mfma_f32_16x16x32_bf16 v[148:151], v[40:43], v[96:99], v[148:151]\n"
"ds_read_b128 v[32:35], v223 offset:18432\n"
"s_waitcnt lgkmcnt(6)\n"
"v_mfma_f32_16x16x32_bf16 v[176:179], v[44:47], v[92:95], v[176:179]\n"
"ds_read_b128 v[36:39], v222 offset:512\n"
"v_mfma_f32_16x16x32_bf16 v[180:183], v[44:47], v[96:99], v[180:183]\n"
"ds_read_b128 v[40:43], v223 offset:20480\n"
"v_mfma_f32_16x16x32_bf16 v[184:187], v[0:3], v[92:95], v[184:187]\n"
"v_mfma_f32_16x16x32_bf16 v[188:191], v[0:3], v[96:99], v[188:191]\n"
"ds_read_b128 v[0:3], v223 offset:22528\n"
"s_waitcnt lgkmcnt(7)\n"
"v_mfma_f32_16x16x32_bf16 v[136:139], v[4:7], v[100:103], v[136:139]\n"
"v_mfma_f32_16x16x32_bf16 v[140:143], v[4:7], v[104:107], v[140:143]\n"
"ds_read_b128 v[4:7], v221 offset:16384\n"
"v_mfma_f32_16x16x32_bf16 v[144:147], v[16:19], v[100:103], v[144:147]\n"
"s_waitcnt vmcnt(3)\n"
"v_xor_b32 v54, v54, v227\n"
"v_xor_b32 v55, v55, v227\n"
"v_mfma_f32_16x16x32_bf16 v[148:151], v[16:19], v[104:107], v[148:151]\n"
"ds_read_b128 v[16:19], v221 offset:18432\n"
"s_waitcnt lgkmcnt(7)\n"
"v_mfma_f32_16x16x32_bf16 v[176:179], v[24:27], v[100:103], v[176:179]\n"
"s_waitcnt vmcnt(2)\n"
"v_xor_b32 v52, v52, v226\n"
"v_xor_b32 v53, v53, v226\n"
"v_mfma_f32_16x16x32_bf16 v[180:183], v[24:27], v[104:107], v[180:183]\n"
"ds_read_b128 v[24:27], v221 offset:20480\n"
"v_mfma_f32_16x16x32_bf16 v[184:187], v[8:11], v[100:103], v[184:187]\n"
"s_waitcnt vmcnt(1)\n"
"v_xor_b32 v50, v50, v225\n"
"v_xor_b32 v51, v51, v225\n"
"v_mfma_f32_16x16x32_bf16 v[188:191], v[8:11], v[104:107], v[188:191]\n"
"ds_read_b128 v[8:11], v221 offset:22528\n"
"s_waitcnt lgkmcnt(7)\n"
"v_mfma_f32_16x16x32_bf16 v[136:139], v[12:15], v[20:23], v[136:139]\n"
"s_waitcnt vmcnt(0)\n"
"v_xor_b32 v48, v48, v224\n"
"v_xor_b32 v49, v49, v224\n"
"v_mfma_f32_16x16x32_bf16 v[140:143], v[12:15], v[28:31], v[140:143]\n"
"ds_read_b128 v[12:15], v223 offset:24576\n"
"v_mfma_f32_16x16x32_bf16 v[144:147], v[32:35], v[20:23], v[144:147]\n"
"v_or3_b32 v44, v54, v55, v52\n"
"v_or3_b32 v44, v44, v53, v50\n"
"v_mfma_f32_16x16x32_bf16 v[148:151], v[32:35], v[28:31], v[148:151]\n"
"ds_read_b128 v[32:35], v223 offset:26624\n"
"s_waitcnt lgkmcnt(6)\n"
"v_mfma_f32_16x16x32_bf16 v[176:179], v[40:43], v[20:23], v[176:179]\n"
"v_or3_b32 v44, v44, v51, v48\n"
"v_bitop3_b32 v44, v44, v49, s96 bitop3:0xa8\n"
"v_xor_b32 v230, v230, v231\n"
"v_mfma_f32_16x16x32_bf16 v[180:183], v[40:43], v[28:31], v[180:183]\n"
"ds_read_b128 v[40:43], v223 offset:28672\n"
"v_mfma_f32_16x16x32_bf16 v[184:187], v[0:3], v[20:23], v[184:187]\n"
"v_cmp_ne_u32 vcc, 0, v44\n"
"s_nop 0\n"
"s_cbranch_vccnz .Lr5\n"
".Lb5:\n"
"v_mfma_f32_16x16x32_bf16 v[188:191], v[0:3], v[28:31], v[188:191]\n"
"ds_read_b128 v[0:3], v223 offset:30720\n"
"s_waitcnt lgkmcnt(6)\n"
"v_mfma_f32_16x16x32_bf16 v[136:139], v[4:7], v[68:71], v[136:139]\n"
"v_fmac_f32 v52, v38, v54\n"
"v_fmac_f32 v53, v38, v55\n"
"v_mfma_f32_16x16x32_bf16 v[140:143], v[4:7], v[108:111], v[140:143]\n"
"ds_read_b128 v[4:7], v221 offset:24576\n"
"v_mfma_f32_16x16x32_bf16 v[144:147], v[16:19], v[68:71], v[144:147]\n"
"v_fma_f32 v52, -v39, v55, v52\n"
"v_fmac_f32 v53, v39, v54\n"
"v_mfma_f32_16x16x32_bf16 v[148:151], v[16:19], v[108:111], v[148:151]\n"
"ds_read_b128 v[16:19], v221 offset:26624\n"
"s_waitcnt lgkmcnt(6)\n"
"v_mfma_f32_16x16x32_bf16 v[176:179], v[24:27], v[68:71], v[176:179]\n"
"v_fmac_f32 v50, v38, v52\n"
"v_fmac_f32 v51, v38, v53\n"
"v_mfma_f32_16x16x32_bf16 v[180:183], v[24:27], v[108:111], v[180:183]\n"
"ds_read_b128 v[20:23], v221 offset:28672\n"
"v_mfma_f32_16x16x32_bf16 v[184:187], v[8:11], v[68:71], v[184:187]\n"
"v_fma_f32 v50, -v39, v53, v50\n"
"v_fmac_f32 v51, v39, v52\n"
"v_mfma_f32_16x16x32_bf16 v[188:191], v[8:11], v[108:111], v[188:191]\n"
"ds_read_b128 v[8:11], v221 offset:30720\n"
"s_waitcnt lgkmcnt(6)\n"
"v_mfma_f32_16x16x32_bf16 v[136:139], v[12:15], v[112:115], v[136:139]\n"
"v_fmac_f32 v48, v38, v50\n"
"v_fmac_f32 v49, v38, v51\n"
"v_mfma_f32_16x16x32_bf16 v[140:143], v[12:15], v[116:119], v[140:143]\n"
"v_fma_f32 v48, -v39, v51, v48\n"
"v_fmac_f32 v49, v39, v50\n"
"v_mfma_f32_16x16x32_bf16 v[144:147], v[32:35], v[112:115], v[144:147]\n"
"v_mfma_f32_16x16x32_bf16 v[148:151], v[32:35], v[116:119], v[148:151]\n"
"s_waitcnt lgkmcnt(4)\n"
"v_mfma_f32_16x16x32_bf16 v[176:179], v[40:43], v[112:115], v[176:179]\n"
"v_mfma_f32_16x16x32_bf16 v[180:183], v[40:43], v[116:119], v[180:183]\n"
"v_mfma_f32_16x16x32_bf16 v[184:187], v[0:3], v[112:115], v[184:187]\n"
"ds_read_b128 v[12:15], v222\n"
"ds_read_b128 v[24:27], v222 offset:256\n"
"v_mfma_f32_16x16x32_bf16 v[188:191], v[0:3], v[116:119], v[188:191]\n"
"ds_read_b128 v[0:3], v220\n"
"s_waitcnt lgkmcnt(5)\n"
"v_mfma_f32_16x16x32_bf16 v[136:139], v[4:7], v[120:123], v[136:139]\n"
"v_mfma_f32_16x16x32_bf16 v[140:143], v[4:7], v[124:127], v[140:143]\n"
"v_mfma_f32_16x16x32_bf16 v[144:147], v[16:19], v[120:123], v[144:147]\n"
"v_mfma_f32_16x16x32_bf16 v[148:151], v[16:19], v[124:127], v[148:151]\n"
"s_waitcnt lgkmcnt(3)\n"
"v_mfma_f32_16x16x32_bf16 v[176:179], v[20:23], v[120:123], v[176:179]\n"
"v_mfma_f32_16x16x32_bf16 v[180:183], v[20:23], v[124:127], v[180:183]\n"
"v_mfma_f32_16x16x32_bf16 v[184:187], v[8:11], v[120:123], v[184:187]\n"
"v_mfma_f32_16x16x32_bf16 v[188:191], v[8:11], v[124:127], v[188:191]\n"
"s_setprio 1\n"
"s_waitcnt lgkmcnt(2)\n"
"v_fma_f32 v4, v12, v136, v137\n"
"v_fma_f32 v6, v12, v140, v141\n"
"v_fma_f32 v4, -v13, v140, v4\n"
"v_fmac_f32 v6, v13, v136\n"
"v_fma_f32 v5, v12, v4, v138\n"
"v_fma_f32 v7, v12, v6, v142\n"
"v_fma_f32 v5, -v13, v6, v5\n"
"v_fmac_f32 v7, v13, v4\n"
"v_fma_f32 v4, v12, v5, v139\n"
"v_fma_f32 v6, v12, v7, v143\n"
"v_fma_f32 v4, -v13, v7, v4\n"
"v_fmac_f32 v6, v13, v5\n"
"v_fma_f32 v5, v12, v4, v144\n"
"v_fma_f32 v7, v12, v6, v148\n"
"v_fma_f32 v5, -v13, v6, v5\n"
"v_fmac_f32 v7, v13, v4\n"
"v_fma_f32 v4, v12, v5, v145\n"
"v_fma_f32 v6, v12, v7, v149\n"
"v_fma_f32 v4, -v13, v7, v4\n"
"v_fmac_f32 v6, v13, v5\n"
"v_fma_f32 v5, v12, v4, v146\n"
"v_fma_f32 v7, v12, v6, v150\n"
"v_fma_f32 v5, -v13, v6, v5\n"
"v_fmac_f32 v7, v13, v4\n"
"v_fma_f32 v4, v12, v5, v147\n"
"v_fma_f32 v6, v12, v7, v151\n"
"v_fma_f32 v4, -v13, v7, v4\n"
"v_fmac_f32 v6, v13, v5\n"
"v_fma_f32 v5, v12, v4, v176\n"
"v_fma_f32 v7, v12, v6, v180\n"
"v_fma_f32 v5, -v13, v6, v5\n"
"v_fmac_f32 v7, v13, v4\n"
"v_fma_f32 v4, v12, v5, v177\n"
"v_fma_f32 v6, v12, v7, v181\n"
"v_fma_f32 v4, -v13, v7, v4\n"
"v_fmac_f32 v6, v13, v5\n"
"v_fma_f32 v5, v12, v4, v178\n"
"v_fma_f32 v7, v12, v6, v182\n"
"v_fma_f32 v5, -v13, v6, v5\n"
"v_fmac_f32 v7, v13, v4\n"
"v_fma_f32 v4, v12, v5, v179\n"
"v_fma_f32 v6, v12, v7, v183\n"
"v_fma_f32 v4, -v13, v7, v4\n"
"v_fmac_f32 v6, v13, v5\n"
"v_fma_f32 v5, v12, v4, v184\n"
"v_fma_f32 v7, v12, v6, v188\n"
"v_fma_f32 v5, -v13, v6, v5\n"
"v_fmac_f32 v7, v13, v4\n"
"v_fma_f32 v4, v12, v5, v185\n"
"v_fma_f32 v6, v12, v7, v189\n"
"v_fma_f32 v4, -v13, v7, v4\n"
"v_fmac_f32 v6, v13, v5\n"
"v_fma_f32 v5, v12, v4, v186\n"
"v_fma_f32 v7, v12, v6, v190\n"
"v_fma_f32 v5, -v13, v6, v5\n"
"v_fmac_f32 v7, v13, v4\n"
"v_fma_f32 v55, v12, v5, v187\n"
"v_fma_f32 v56, v12, v7, v191\n"
"v_fma_f32 v55, -v13, v7, v55\n"
"v_fmac_f32 v56, v13, v5\n"
"v_mov_b32 v6, v55\n"
"v_mov_b32 v7, v56\n"
"v_mul_f32 v54, v14, v136\n"
"v_fma_f32 v54, -v15, v140, v54\n"
"v_permlane16_swap_b32 v55, v6\n"
"v_permlane16_swap_b32 v56, v7\n"
"s_waitcnt lgkmcnt(1)\n"
"v_fmac_f32 v6, v24, v55\n"
"v_fmac_f32 v7, v24, v56\n"
"v_fma_f32 v6, -v25, v56, v6\n"
"v_fmac_f32 v7, v25, v55\n"
"v_mov_b32 v8, v6\n"
"v_mov_b32 v9, v7\n"
"v_mul_f32_e64 v140, -v14, v140\n"
"v_fma_f32 v140, -v15, v136, v140\n"
"v_permlane32_swap_b32 v6, v8\n"
"v_permlane32_swap_b32 v7, v9\n"
"v_fmac_f32 v8, v26, v6\n"
"v_fmac_f32 v9, v26, v7\n"
"v_fma_f32 v8, -v27, v7, v8\n"
"v_fmac_f32 v9, v27, v6\n"
"v_bfi_b32 v4, s96, v218, v8\n"
"v_bfi_b32 v5, s96, v218, v9\n"
"global_store_dwordx2 v207, v[4:5], s[84:85] sc1\n"
"s_waitcnt lgkmcnt(0)\n"
"v_mul_f32 v55, v2, v55\n"
"v_mul_f32 v56, v2, v56\n"
"v_fmac_f32 v55, v0, v6\n"
"v_fmac_f32 v56, v0, v7\n"
"v_fma_f32 v55, -v1, v7, v55\n"
"v_fmac_f32 v56, v1, v6\n"
"v_mul_f32 v71, v14, v137\n"
"v_fma_f32 v71, -v15, v141, v71\n"
"v_mul_f32_e64 v141, -v14, v141\n"
"v_fma_f32 v141, -v15, v137, v141\n"
"v_mul_f32 v67, v14, v138\n"
"v_fma_f32 v67, -v15, v142, v67\n"
"v_mul_f32_e64 v142, -v14, v142\n"
"v_fma_f32 v142, -v15, v138, v142\n"
"v_mul_f32 v70, v14, v139\n"
"v_fma_f32 v70, -v15, v143, v70\n"
"v_mul_f32_e64 v143, -v14, v143\n"
"v_fma_f32 v143, -v15, v139, v143\n"
"v_mul_f32 v66, v14, v144\n"
"v_fma_f32 v66, -v15, v148, v66\n"
"v_mul_f32_e64 v148, -v14, v148\n"
"v_fma_f32 v148, -v15, v144, v148\n"
"v_mul_f32 v69, v14, v145\n"
"v_fma_f32 v69, -v15, v149, v69\n"
"v_mul_f32_e64 v149, -v14, v149\n"
"v_fma_f32 v149, -v15, v145, v149\n"
"v_mul_f32 v64, v14, v146\n"
"v_fma_f32 v64, -v15, v150, v64\n"
"v_mul_f32_e64 v150, -v14, v150\n"
"v_fma_f32 v150, -v15, v146, v150\n"
"v_mul_f32 v68, v14, v147\n"
"v_fma_f32 v68, -v15, v151, v68\n"
"v_mul_f32_e64 v151, -v14, v151\n"
"v_fma_f32 v151, -v15, v147, v151\n"
"v_mul_f32 v60, v14, v176\n"
"v_fma_f32 v60, -v15, v180, v60\n"
"v_mul_f32_e64 v180, -v14, v180\n"
"v_fma_f32 v180, -v15, v176, v180\n"
"v_mul_f32 v65, v14, v177\n"
"v_fma_f32 v65, -v15, v181, v65\n"
"v_mul_f32_e64 v181, -v14, v181\n"
"v_fma_f32 v181, -v15, v177, v181\n"
"v_mul_f32 v58, v14, v178\n"
"v_fma_f32 v58, -v15, v182, v58\n"
"v_mul_f32_e64 v182, -v14, v182\n"
"v_fma_f32 v182, -v15, v178, v182\n"
"v_mul_f32 v62, v14, v179\n"
"v_fma_f32 v62, -v15, v183, v62\n"
"v_mul_f32_e64 v183, -v14, v183\n"
"v_fma_f32 v183, -v15, v179, v183\n"
"v_mul_f32 v57, v14, v184\n"
"v_fma_f32 v57, -v15, v188, v57\n"
"v_mul_f32_e64 v188, -v14, v188\n"
"v_fma_f32 v188, -v15, v184, v188\n"
"v_mul_f32 v61, v14, v185\n"
"v_fma_f32 v61, -v15, v189, v61\n"
"v_mul_f32_e64 v189, -v14, v189\n"
"v_fma_f32 v189, -v15, v185, v189\n"
"v_mul_f32 v59, v14, v186\n"
"v_fma_f32 v59, -v15, v190, v59\n"
"v_mul_f32_e64 v190, -v14, v190\n"
"v_fma_f32 v190, -v15, v186, v190\n"
"v_mul_f32 v63, v14, v187\n"
"v_fma_f32 v63, -v15, v191, v63\n"
"v_mul_f32_e64 v191, -v14, v191\n"
"v_fma_f32 v191, -v15, v187, v191\n"
"s_barrier\n"
"s_add_u32 m0, s56, 0xc000\n"
"s_nop 0\n"
"global_load_lds_dwordx4 v215, s[68:69]\n"
"global_load_lds_dwordx4 v216, s[68:69] offset:1024\n"
"global_load_lds_dwordx4 v215, s[70:71] offset:2048\n"
"global_load_lds_dwordx4 v216, s[70:71] offset:3072\n"
"s_add_u32 m0, s56, 0x10000\n"
"s_nop 0\n"
"global_load_lds_dwordx4 v213, s[68:69]\n"
"global_load_lds_dwordx4 v214, s[68:69] offset:1024\n"
"global_load_lds_dwordx4 v213, s[70:71] offset:2048\n"
"global_load_lds_dwordx4 v214, s[70:71] offset:3072\n"
"s_add_u32 s13, s49, 1\n"
"s_and_b32 s13, s13, 3\n"
"s_lshl_b32 s13, s13, 7\n"
"s_add_u32 s68, s72, s13\n"
"s_addc_u32 s69, s73, 0\n"
"s_add_u32 s70, s74, s13\n"
"s_addc_u32 s71, s75, 0\n"
"s_add_u32 m0, s56, 0x14000\n"
"s_nop 0\n"
"global_load_lds_dwordx4 v215, s[68:69]\n"
"global_load_lds_dwordx4 v216, s[68:69] offset:1024\n"
"global_load_lds_dwordx4 v215, s[70:71] offset:2048\n"
"global_load_lds_dwordx4 v216, s[70:71] offset:3072\n"
"buffer_load_dwordx2 v[52:53], v219, s[76:79], 0 offen offset:1536 sc1\n"
"buffer_load_dwordx2 v[50:51], v219, s[76:79], 0 offen offset:1024 sc1\n"
"buffer_load_dwordx2 v[46:47], v219, s[76:79], 0 offen offset:512 sc1\n"
"buffer_load_dwordx2 v[44:45], v219, s[76:79], 0 offen offset:0 sc1\n"
"ds_read_b128 v[0:3], v220 offset:1024\n"
"s_waitcnt vmcnt(12)\n"
"ds_read_b128 v[4:7], v206\n"
"ds_read_b128 v[8:11], v208\n"
"ds_read_b128 v[16:19], v209\n"
"ds_read_b128 v[20:23], v210\n"
"s_add_u32 m0, s56, 0xc000\n"
"s_waitcnt lgkmcnt(0)\n"
"global_load_lds_dwordx4 v213, s[68:69]\n"
"global_load_lds_dwordx4 v214, s[68:69] offset:1024\n"
"global_load_lds_dwordx4 v213, s[70:71] offset:2048\n"
"global_load_lds_dwordx4 v214, s[70:71] offset:3072\n"
"s_waitcnt vmcnt(12)\n"
"ds_read_b128 v[24:27], v206 offset:16384\n"
"ds_read_b128 v[28:31], v208 offset:16384\n"
"ds_read_b128 v[32:35], v209 offset:16384\n"
"ds_read_b128 v[40:43], v210 offset:16384\n"
"s_add_u32 s13, s49, 2\n"
"s_and_b32 s13, s13, 3\n"
"s_lshl_b32 s13, s13, 7\n"
"s_add_u32 s68, s72, s13\n"
"s_addc_u32 s69, s73, 0\n"
"s_add_u32 s70, s74, s13\n"
"s_addc_u32 s71, s75, 0\n"
"s_add_u32 m0, s56, 0x10000\n"
"s_waitcnt lgkmcnt(0)\n"
"global_load_lds_dwordx4 v215, s[68:69]\n"
"global_load_lds_dwordx4 v216, s[68:69] offset:1024\n"
"global_load_lds_dwordx4 v215, s[70:71] offset:2048\n"
"global_load_lds_dwordx4 v216, s[70:71] offset:3072\n"
"s_waitcnt vmcnt(11)\n"
"v_xor_b32 v52, v52, v218\n"
"v_xor_b32 v53, v53, v218\n"
"s_waitcnt vmcnt(10)\n"
"v_xor_b32 v50, v50, v218\n"
"v_xor_b32 v51, v51, v218\n"
"s_waitcnt vmcnt(9)\n"
"v_xor_b32 v46, v46, v218\n"
"v_xor_b32 v47, v47, v218\n"
"s_waitcnt vmcnt(8)\n"
"v_xor_b32 v44, v44, v218\n"
"v_xor_b32 v45, v45, v218\n"
"v_or3_b32 v72, v52, v53, v50\n"
"v_or3_b32 v72, v72, v51, v46\n"
"v_or3_b32 v72, v72, v47, v44\n"
"v_bitop3_b32 v72, v72, v45, s96 bitop3:0xa8\n"
"v_cmp_ne_u32 vcc, 0, v72\n"
"s_nop 0\n"
"s_cbranch_vccnz .Lr6\n"
".Lb6:\n"
"v_fmac_f32 v52, v38, v48\n"
"v_fmac_f32 v53, v38, v49\n"
"v_fma_f32 v52, -v39, v49, v52\n"
"v_fmac_f32 v53, v39, v48\n"
"v_fmac_f32 v50, v38, v52\n"
"v_fmac_f32 v51, v38, v53\n"
"v_fma_f32 v50, -v39, v53, v50\n"
"v_fmac_f32 v51, v39, v52\n"
"v_fmac_f32 v46, v38, v50\n"
"v_fmac_f32 v47, v38, v51\n"
"v_fma_f32 v46, -v39, v51, v46\n"
"v_fmac_f32 v47, v39, v50\n"
"v_fmac_f32 v44, v38, v46\n"
"v_fmac_f32 v45, v38, v47\n"
"v_fma_f32 v44, -v39, v47, v44\n"
"v_fmac_f32 v45, v39, v46\n"
"v_mul_f32 v46, v2, v44\n"
"v_mul_f32 v47, v2, v45\n"
"v_fma_f32 v46, -v3, v45, v46\n"
"v_fmac_f32 v47, v3, v44\n"
"s_nop 1\n"
"v_permlane16_swap_b32 v46, v47\n"
"v_add_f32 v46, v46, v47\n"
"v_mov_b32 v47, v46\n"
"s_nop 1\n"
"v_permlane32_swap_b32 v46, v47\n"
"v_add_f32 v46, v46, v47\n"
"v_mov_b32 v47, v46\n"
"s_nop 1\n"
"v_permlane16_swap_b32 v46, v47\n"
"v_fmac_f32 v55, v0, v46\n"
"v_fmac_f32 v56, v0, v47\n"
"v_fma_f32 v55, -v1, v47, v55\n"
"v_fmac_f32 v56, v1, v46\n"
"v_fmac_f32 v54, v36, v55\n"
"v_fma_f32 v140, -v36, v56, v140\n"
"v_fma_f32 v54, -v37, v56, v54\n"
"v_fma_f32 v140, -v37, v55, v140\n"
"v_fmac_f32 v71, v12, v54\n"
"v_fmac_f32 v141, v12, v140\n"
"v_fmac_f32 v71, v13, v140\n"
"v_fma_f32 v141, -v13, v54, v141\n"
"v_fmac_f32 v67, v12, v71\n"
"v_fmac_f32 v142, v12, v141\n"
"v_fmac_f32 v67, v13, v141\n"
"v_fma_f32 v142, -v13, v71, v142\n"
"v_cvt_pk_bf16_f32 v0, v54, v71\n"
"v_cvt_pk_bf16_f32 v36, v140, v141\n"
"v_fmac_f32 v70, v12, v67\n"
"v_fmac_f32 v143, v12, v142\n"
"v_fmac_f32 v70, v13, v142\n"
"v_fma_f32 v143, -v13, v67, v143\n"
"v_fmac_f32 v66, v12, v70\n"
"v_fmac_f32 v148, v12, v143\n"
"v_fmac_f32 v66, v13, v143\n"
"v_fma_f32 v148, -v13, v70, v148\n"
"v_cvt_pk_bf16_f32 v1, v67, v70\n"
"v_cvt_pk_bf16_f32 v37, v142, v143\n"
"v_fmac_f32 v69, v12, v66\n"
"v_fmac_f32 v149, v12, v148\n"
"v_fmac_f32 v69, v13, v148\n"
"v_fma_f32 v149, -v13, v66, v149\n"
"v_fmac_f32 v64, v12, v69\n"
"v_fmac_f32 v150, v12, v149\n"
"v_fmac_f32 v64, v13, v149\n"
"v_fma_f32 v150, -v13, v69, v150\n"
"v_cvt_pk_bf16_f32 v2, v66, v69\n"
"v_cvt_pk_bf16_f32 v38, v148, v149\n"
"v_fmac_f32 v68, v12, v64\n"
"v_fmac_f32 v151, v12, v150\n"
"v_fmac_f32 v68, v13, v150\n"
"v_fma_f32 v151, -v13, v64, v151\n"
"v_fmac_f32 v60, v12, v68\n"
"v_fmac_f32 v180, v12, v151\n"
"v_fmac_f32 v60, v13, v151\n"
"v_fma_f32 v180, -v13, v68, v180\n"
"v_cvt_pk_bf16_f32 v3, v64, v68\n"
"v_cvt_pk_bf16_f32 v39, v150, v151\n"
"global_store_dwordx4 v212, v[0:3], s[86:87] sc1\n"
"global_store_dwordx4 v212, v[36:39], s[86:87] offset:512 sc1\n"
"v_fmac_f32 v65, v12, v60\n"
"v_fmac_f32 v181, v12, v180\n"
"v_fmac_f32 v65, v13, v180\n"
"v_fma_f32 v181, -v13, v60, v181\n"
"v_fmac_f32 v58, v12, v65\n"
"v_fmac_f32 v182, v12, v181\n"
"v_fmac_f32 v58, v13, v181\n"
"v_fma_f32 v182, -v13, v65, v182\n"
"v_cvt_pk_bf16_f32 v0, v60, v65\n"
"v_cvt_pk_bf16_f32 v36, v180, v181\n"
"v_fmac_f32 v62, v12, v58\n"
"v_fmac_f32 v183, v12, v182\n"
"v_fmac_f32 v62, v13, v182\n"
"v_fma_f32 v183, -v13, v58, v183\n"
"v_fmac_f32 v57, v12, v62\n"
"v_fmac_f32 v188, v12, v183\n"
"v_fmac_f32 v57, v13, v183\n"
"v_fma_f32 v188, -v13, v62, v188\n"
"v_cvt_pk_bf16_f32 v1, v58, v62\n"
"v_cvt_pk_bf16_f32 v37, v182, v183\n"
"v_fmac_f32 v61, v12, v57\n"
"v_fmac_f32 v189, v12, v188\n"
"v_fmac_f32 v61, v13, v188\n"
"v_fma_f32 v189, -v13, v57, v189\n"
"v_fmac_f32 v59, v12, v61\n"
"v_fmac_f32 v190, v12, v189\n"
"v_fmac_f32 v59, v13, v189\n"
"v_fma_f32 v190, -v13, v61, v190\n"
"v_cvt_pk_bf16_f32 v2, v57, v61\n"
"v_cvt_pk_bf16_f32 v38, v188, v189\n"
"v_fmac_f32 v63, v12, v59\n"
"v_fmac_f32 v191, v12, v190\n"
"v_fmac_f32 v63, v13, v190\n"
"v_fma_f32 v191, -v13, v59, v191\n"
"v_cvt_pk_bf16_f32 v3, v59, v63\n"
"v_cvt_pk_bf16_f32 v39, v190, v191\n"
"global_store_dwordx4 v211, v[0:3], s[86:87] offset:256 sc1\n"
"global_store_dwordx4 v211, v[36:39], s[86:87] offset:768 sc1\n"
"global_load_dwordx2 v[96:97], v205, s[90:91] sc1\n"
"ds_read_b128 v[0:3], v206 offset:32768\n"
"ds_read_b128 v[12:15], v208 offset:32768\n"
"ds_read_b128 v[36:39], v209 offset:32768\n"
"ds_read_b128 v[44:47], v210 offset:32768\n"
"s_add_u32 m0, s56, 0x14000\n"
"s_waitcnt lgkmcnt(0)\n"
"global_load_lds_dwordx4 v213, s[68:69]\n"
"global_load_lds_dwordx4 v214, s[68:69] offset:1024\n"
"global_load_lds_dwordx4 v213, s[70:71] offset:2048\n"
"global_load_lds_dwordx4 v214, s[70:71] offset:3072\n"
"s_waitcnt vmcnt(5)\n"
"global_store_dwordx2 v207, v[192:193], s[88:89] sc1\n"
"ds_read_b128 v[48:51], v206\n"
"ds_read_b128 v[52:55], v208\n"
"ds_read_b128 v[56:59], v209\n"
"ds_read_b128 v[60:63], v210\n"
"s_add_u32 s13, s49, 3\n"
"s_and_b32 s13, s13, 3\n"
"s_lshl_b32 s13, s13, 7\n"
"s_add_u32 s68, s72, s13\n"
"s_addc_u32 s69, s73, 0\n"
"s_add_u32 s70, s74, s13\n"
"s_addc_u32 s71, s75, 0\n"
"s_add_u32 m0, s56, 0xc000\n"
"s_waitcnt lgkmcnt(0)\n"
"global_load_lds_dwordx4 v215, s[68:69]\n"
"global_load_lds_dwordx4 v216, s[68:69] offset:1024\n"
"global_load_lds_dwordx4 v215, s[70:71] offset:2048\n"
"global_load_lds_dwordx4 v216, s[70:71] offset:3072\n"
"ds_read_b128 v[64:67], v206 offset:16384\n"
"ds_read_b128 v[68:71], v208 offset:16384\n"
"ds_read_b128 v[72:75], v209 offset:16384\n"
"ds_read_b128 v[76:79], v210 offset:16384\n"
"s_add_u32 m0, s56, 0x10000\n"
"s_waitcnt lgkmcnt(0)\n"
"global_load_lds_dwordx4 v213, s[68:69]\n"
"global_load_lds_dwordx4 v214, s[68:69] offset:1024\n"
"global_load_lds_dwordx4 v213, s[70:71] offset:2048\n"
"global_load_lds_dwordx4 v214, s[70:71] offset:3072\n"
"s_waitcnt vmcnt(9)\n"
"ds_read_b128 v[80:83], v206 offset:32768\n"
"ds_read_b128 v[84:87], v208 offset:32768\n"
"ds_read_b128 v[88:91], v209 offset:32768\n"
"ds_read_b128 v[92:95], v210 offset:32768\n"
"v_cmp_ne_u64 vcc, s[10:11], v[96:97]\n"
"s_nop 0\n"
"s_cbranch_vccnz .Lr7\n"
".Lb7:\n"
"s_add_u32 s90, s90, 0x1000\n"
"s_addc_u32 s91, s91, 0\n"
"s_waitcnt lgkmcnt(0)\n"
"s_add_u32 m0, s56, 0x14000\n"
"s_nop 0\n"
"global_load_lds_dwordx4 v202, s[62:63] sc1\n"
"global_load_lds_dwordx4 v202, s[62:63] offset:1024 sc1\n"
"global_load_lds_dwordx4 v202, s[62:63] offset:2048 sc1\n"
"global_load_lds_dwordx4 v202, s[62:63] offset:3072 sc1\n"
"s_waitcnt vmcnt(8)\n"
"ds_read_b128 v[96:99], v206\n"
"ds_read_b128 v[100:103], v208\n"
"ds_read_b128 v[104:107], v209\n"
"ds_read_b128 v[108:111], v210\n"
"s_waitcnt lgkmcnt(0)\n"
"s_add_u32 m0, s56, 0xc000\n"
"s_nop 0\n"
"global_load_lds_dwordx4 v195, s[62:63] sc1\n"
"global_load_lds_dwordx4 v195, s[62:63] offset:1024 sc1\n"
"global_load_lds_dwordx4 v195, s[62:63] offset:2048 sc1\n"
"global_load_lds_dwordx4 v195, s[62:63] offset:3072 sc1\n"
"s_waitcnt vmcnt(8)\n"
"ds_read_b128 v[112:115], v206 offset:16384\n"
"ds_read_b128 v[116:119], v208 offset:16384\n"
"ds_read_b128 v[120:123], v209 offset:16384\n"
"ds_read_b128 v[124:127], v210 offset:16384\n"
"s_waitcnt lgkmcnt(0)\n"
"s_add_u32 m0, s56, 0x10000\n"
"s_nop 0\n"
"global_load_lds_dwordx4 v202, s[64:65] sc1\n"
"global_load_lds_dwordx4 v202, s[64:65] offset:1024 sc1\n"
"global_load_lds_dwordx4 v202, s[64:65] offset:2048 sc1\n"
"global_load_lds_dwordx4 v202, s[64:65] offset:3072 sc1\n"
"s_waitcnt vmcnt(8)\n"
"s_barrier\n"
"ds_read_b64_tr_b16 v[128:129], v200 offset:16384\n"
"ds_read_b64_tr_b16 v[130:131], v201 offset:16384\n"
"ds_read_b64_tr_b16 v[132:133], v200 offset:17408\n"
"ds_read_b64_tr_b16 v[134:135], v201 offset:17408\n"
"ds_read_b64_tr_b16 v[136:137], v200 offset:18432\n"
"ds_read_b64_tr_b16 v[138:139], v201 offset:18432\n"
"ds_read_b64_tr_b16 v[140:141], v200 offset:19456\n"
"ds_read_b64_tr_b16 v[142:143], v201 offset:19456\n"
"ds_read_b64_tr_b16 v[144:145], v200 offset:20480\n"
"ds_read_b64_tr_b16 v[146:147], v201 offset:20480\n"
"ds_read_b64_tr_b16 v[148:149], v200 offset:21504\n"
"ds_read_b64_tr_b16 v[150:151], v201 offset:21504\n"
"s_add_u32 m0, s56, 0x8000\n"
"s_nop 0\n"
"global_load_lds_dwordx4 v195, s[64:65] sc1\n"
"global_load_lds_dwordx4 v195, s[64:65] offset:1024 sc1\n"
"global_load_lds_dwordx4 v195, s[64:65] offset:2048 sc1\n"
"global_load_lds_dwordx4 v195, s[64:65] offset:3072 sc1\n"
"s_setprio 0\n"
"s_waitcnt lgkmcnt(8)\n"
"v_mfma_f32_16x16x32_bf16 v[152:155], v[128:131], v[4:7], 0\n"
"ds_read_b64_tr_b16 v[156:157], v200 offset:22528\n"
"ds_read_b64_tr_b16 v[158:159], v201 offset:22528\n"
"v_mfma_f32_16x16x32_bf16 v[160:163], v[128:131], v[24:27], 0\n"
"v_mfma_f32_16x16x32_bf16 v[152:155], v[132:135], v[8:11], v[152:155]\n"
"ds_read_b64_tr_b16 v[128:129], v200 offset:23552\n"
"ds_read_b64_tr_b16 v[130:131], v201 offset:23552\n"
"v_mfma_f32_16x16x32_bf16 v[160:163], v[132:135], v[28:31], v[160:163]\n"
"s_waitcnt lgkmcnt(8)\n"
"v_mfma_f32_16x16x32_bf16 v[152:155], v[136:139], v[16:19], v[152:155]\n"
"ds_read_b64_tr_b16 v[132:133], v200 offset:24576\n"
"ds_read_b64_tr_b16 v[134:135], v201 offset:24576\n"
"v_mfma_f32_16x16x32_bf16 v[160:163], v[136:139], v[32:35], v[160:163]\n"
"v_mfma_f32_16x16x32_bf16 v[152:155], v[140:143], v[20:23], v[152:155]\n"
"ds_read_b64_tr_b16 v[136:137], v200 offset:25600\n"
"ds_read_b64_tr_b16 v[138:139], v201 offset:25600\n"
"v_mfma_f32_16x16x32_bf16 v[160:163], v[140:143], v[40:43], v[160:163]\n"
"s_waitcnt lgkmcnt(8)\n"
"v_mfma_f32_16x16x32_bf16 v[152:155], v[144:147], v[0:3], v[152:155]\n"
"ds_read_b64_tr_b16 v[140:141], v200 offset:26624\n"
"ds_read_b64_tr_b16 v[142:143], v201 offset:26624\n"
"v_mfma_f32_16x16x32_bf16 v[160:163], v[144:147], v[48:51], v[160:163]\n"
"v_mfma_f32_16x16x32_bf16 v[152:155], v[148:151], v[12:15], v[152:155]\n"
"ds_read_b64_tr_b16 v[144:145], v200 offset:27648\n"
"ds_read_b64_tr_b16 v[146:147], v201 offset:27648\n"
"v_mfma_f32_16x16x32_bf16 v[160:163], v[148:151], v[52:55], v[160:163]\n"
"global_load_dwordx2 v[164:165], v205, s[90:91] sc1\n"
"s_waitcnt lgkmcnt(8)\n"
"v_mfma_f32_16x16x32_bf16 v[152:155], v[156:159], v[36:39], v[152:155]\n"
"ds_read_b64_tr_b16 v[148:149], v200 offset:28672\n"
"ds_read_b64_tr_b16 v[150:151], v201 offset:28672\n"
"v_mfma_f32_16x16x32_bf16 v[160:163], v[156:159], v[56:59], v[160:163]\n"
"v_mfma_f32_16x16x32_bf16 v[152:155], v[128:131], v[44:47], v[152:155]\n"
"ds_read_b64_tr_b16 v[156:157], v200 offset:29696\n"
"ds_read_b64_tr_b16 v[158:159], v201 offset:29696\n"
"v_mfma_f32_16x16x32_bf16 v[160:163], v[128:131], v[60:63], v[160:163]\n"
"s_waitcnt lgkmcnt(8)\n"
"v_mfma_f32_16x16x32_bf16 v[152:155], v[132:135], v[64:67], v[152:155]\n"
"ds_read_b64_tr_b16 v[128:129], v200 offset:30720\n"
"ds_read_b64_tr_b16 v[130:131], v201 offset:30720\n"
"v_mfma_f32_16x16x32_bf16 v[160:163], v[132:135], v[80:83], v[160:163]\n"
"v_mfma_f32_16x16x32_bf16 v[152:155], v[136:139], v[68:71], v[152:155]\n"
"ds_read_b64_tr_b16 v[132:133], v200 offset:31744\n"
"ds_read_b64_tr_b16 v[134:135], v201 offset:31744\n"
"v_mfma_f32_16x16x32_bf16 v[160:163], v[136:139], v[84:87], v[160:163]\n"
"s_waitcnt vmcnt(5) lgkmcnt(8)\n"
"s_barrier\n"
"s_waitcnt vmcnt(0)\n"
"v_cmp_ne_u64 vcc, s[10:11], v[164:165]\n"
"s_nop 0\n"
"s_cbranch_vccnz .Lr12\n"
".Lb12:\n"
"s_add_u32 m0, s56, 0x4000\n"
"s_nop 0\n"
"global_load_lds_dwordx4 v202, s[18:19] sc1\n"
"global_load_lds_dwordx4 v202, s[18:19] offset:1024 sc1\n"
"global_load_lds_dwordx4 v202, s[18:19] offset:2048 sc1\n"
"global_load_lds_dwordx4 v202, s[18:19] offset:3072 sc1\n"
"s_add_u32 m0, s56, 0\n"
"s_nop 0\n"
"global_load_lds_dwordx4 v195, s[18:19] sc1\n"
"global_load_lds_dwordx4 v195, s[18:19] offset:1024 sc1\n"
"global_load_lds_dwordx4 v195, s[18:19] offset:2048 sc1\n"
"global_load_lds_dwordx4 v195, s[18:19] offset:3072 sc1\n"
"v_mfma_f32_16x16x32_bf16 v[152:155], v[140:143], v[72:75], v[152:155]\n"
"ds_read_b64_tr_b16 v[136:137], v200\n"
"ds_read_b64_tr_b16 v[138:139], v201\n"
"v_mfma_f32_16x16x32_bf16 v[160:163], v[140:143], v[88:91], v[160:163]\n"
"v_mfma_f32_16x16x32_bf16 v[152:155], v[144:147], v[76:79], v[152:155]\n"
"ds_read_b64_tr_b16 v[140:141], v200 offset:1024\n"
"ds_read_b64_tr_b16 v[142:143], v201 offset:1024\n"
"v_mfma_f32_16x16x32_bf16 v[160:163], v[144:147], v[92:95], v[160:163]\n"
"s_waitcnt lgkmcnt(8)\n"
"v_mfma_f32_16x16x32_bf16 v[152:155], v[148:151], v[96:99], v[152:155]\n"
"ds_read_b64_tr_b16 v[144:145], v200 offset:2048\n"
"ds_read_b64_tr_b16 v[146:147], v201 offset:2048\n"
"v_mfma_f32_16x16x32_bf16 v[160:163], v[148:151], v[112:115], v[160:163]\n"
"v_mfma_f32_16x16x32_bf16 v[152:155], v[156:159], v[100:103], v[152:155]\n"
"ds_read_b64_tr_b16 v[148:149], v200 offset:3072\n"
"ds_read_b64_tr_b16 v[150:151], v201 offset:3072\n"
"v_mfma_f32_16x16x32_bf16 v[160:163], v[156:159], v[116:119], v[160:163]\n"
"s_waitcnt lgkmcnt(8)\n"
"v_mfma_f32_16x16x32_bf16 v[152:155], v[128:131], v[104:107], v[152:155]\n"
"ds_read_b64_tr_b16 v[156:157], v200 offset:4096\n"
"ds_read_b64_tr_b16 v[158:159], v201 offset:4096\n"
"v_mfma_f32_16x16x32_bf16 v[160:163], v[128:131], v[120:123], v[160:163]\n"
"v_mfma_f32_16x16x32_bf16 v[152:155], v[132:135], v[108:111], v[152:155]\n"
"ds_read_b64_tr_b16 v[128:129], v200 offset:5120\n"
"ds_read_b64_tr_b16 v[130:131], v201 offset:5120\n"
"v_mfma_f32_16x16x32_bf16 v[160:163], v[132:135], v[124:127], v[160:163]\n"
"s_waitcnt lgkmcnt(8)\n"
"v_mfma_f32_16x16x32_bf16 v[132:135], v[136:139], v[4:7], 0\n"
"ds_read_b64_tr_b16 v[164:165], v200 offset:6144\n"
"ds_read_b64_tr_b16 v[166:167], v201 offset:6144\n"
"v_mfma_f32_16x16x32_bf16 v[168:171], v[136:139], v[24:27], 0\n"
"v_mfma_f32_16x16x32_bf16 v[132:135], v[140:143], v[8:11], v[132:135]\n"
"ds_read_b64_tr_b16 v[136:137], v200 offset:7168\n"
"ds_read_b64_tr_b16 v[138:139], v201 offset:7168\n"
"v_mfma_f32_16x16x32_bf16 v[168:171], v[140:143], v[28:31], v[168:171]\n"
"ds_write_b128 v194, v[152:155] offset:4160\n"
"s_waitcnt lgkmcnt(9)\n"
"v_mfma_f32_16x16x32_bf16 v[132:135], v[144:147], v[16:19], v[132:135]\n"
"ds_read_b64_tr_b16 v[140:141], v200 offset:8192\n"
"ds_read_b64_tr_b16 v[142:143], v201 offset:8192\n"
"v_mfma_f32_16x16x32_bf16 v[168:171], v[144:147], v[32:35], v[168:171]\n"
"ds_write_b128 v194, v[160:163] offset:5184\n"
"v_mfma_f32_16x16x32_bf16 v[132:135], v[148:151], v[20:23], v[132:135]\n"
"ds_read_b64_tr_b16 v[144:145], v200 offset:9216\n"
"ds_read_b64_tr_b16 v[146:147], v201 offset:9216\n"
"v_mfma_f32_16x16x32_bf16 v[168:171], v[148:151], v[40:43], v[168:171]\n"
"s_waitcnt lgkmcnt(10)\n"
"v_mfma_f32_16x16x32_bf16 v[132:135], v[156:159], v[0:3], v[132:135]\n"
"ds_read_b64_tr_b16 v[148:149], v200 offset:10240\n"
"ds_read_b64_tr_b16 v[150:151], v201 offset:10240\n"
"v_mfma_f32_16x16x32_bf16 v[168:171], v[156:159], v[48:51], v[168:171]\n"
"s_waitcnt vmcnt(4) lgkmcnt(4)\n"
"s_barrier\n"
"s_add_u32 m0, s56, 0x20200\n"
"s_nop 0\n"
"global_load_lds_dwordx4 v202, s[20:21] sc1\n"
"global_load_lds_dwordx4 v202, s[20:21] offset:1024 sc1\n"
"global_load_lds_dwordx4 v202, s[20:21] offset:2048 sc1\n"
"global_load_lds_dwordx4 v202, s[20:21] offset:3072 sc1\n"
"s_add_u32 m0, s56, 0xc000\n"
"s_nop 0\n"
"global_load_lds_dwordx4 v195, s[20:21] sc1\n"
"global_load_lds_dwordx4 v195, s[20:21] offset:1024 sc1\n"
"global_load_lds_dwordx4 v195, s[20:21] offset:2048 sc1\n"
"global_load_lds_dwordx4 v195, s[20:21] offset:3072 sc1\n"
"v_mfma_f32_16x16x32_bf16 v[132:135], v[128:131], v[12:15], v[132:135]\n"
"ds_read_b64_tr_b16 v[152:153], v200 offset:11264\n"
"ds_read_b64_tr_b16 v[154:155], v201 offset:11264\n"
"v_mfma_f32_16x16x32_bf16 v[168:171], v[128:131], v[52:55], v[168:171]\n"
"v_mfma_f32_16x16x32_bf16 v[132:135], v[164:167], v[36:39], v[132:135]\n"
"ds_read_b64_tr_b16 v[128:129], v200 offset:12288\n"
"ds_read_b64_tr_b16 v[130:131], v201 offset:12288\n"
"v_mfma_f32_16x16x32_bf16 v[168:171], v[164:167], v[56:59], v[168:171]\n"
"v_mfma_f32_16x16x32_bf16 v[132:135], v[136:139], v[44:47], v[132:135]\n"
"ds_read_b64_tr_b16 v[156:157], v200 offset:13312\n"
"ds_read_b64_tr_b16 v[158:159], v201 offset:13312\n"
"v_mfma_f32_16x16x32_bf16 v[168:171], v[136:139], v[60:63], v[168:171]\n"
"s_waitcnt lgkmcnt(8)\n"
"v_mfma_f32_16x16x32_bf16 v[132:135], v[140:143], v[64:67], v[132:135]\n"
"ds_read_b64_tr_b16 v[136:137], v200 offset:14336\n"
"ds_read_b64_tr_b16 v[138:139], v201 offset:14336\n"
"v_mfma_f32_16x16x32_bf16 v[168:171], v[140:143], v[80:83], v[168:171]\n"
"v_mfma_f32_16x16x32_bf16 v[132:135], v[144:147], v[68:71], v[132:135]\n"
"ds_read_b64_tr_b16 v[140:141], v200 offset:15360\n"
"ds_read_b64_tr_b16 v[142:143], v201 offset:15360\n"
"v_mfma_f32_16x16x32_bf16 v[168:171], v[144:147], v[84:87], v[168:171]\n"
"s_waitcnt lgkmcnt(8)\n"
"v_mfma_f32_16x16x32_bf16 v[132:135], v[148:151], v[72:75], v[132:135]\n"
"ds_read_b64_tr_b16 v[144:145], v198 offset:16384\n"
"ds_read_b64_tr_b16 v[146:147], v199 offset:16384\n"
"v_mfma_f32_16x16x32_bf16 v[168:171], v[148:151], v[88:91], v[168:171]\n"
"v_mfma_f32_16x16x32_bf16 v[132:135], v[152:155], v[76:79], v[132:135]\n"
"ds_read_b64_tr_b16 v[148:149], v198 offset:17408\n"
"ds_read_b64_tr_b16 v[150:151], v199 offset:17408\n"
"v_mfma_f32_16x16x32_bf16 v[168:171], v[152:155], v[92:95], v[168:171]\n"
"s_waitcnt lgkmcnt(8)\n"
"v_mfma_f32_16x16x32_bf16 v[132:135], v[128:131], v[96:99], v[132:135]\n"
"ds_read_b64_tr_b16 v[152:153], v198 offset:18432\n"
"ds_read_b64_tr_b16 v[154:155], v199 offset:18432\n"
"v_mfma_f32_16x16x32_bf16 v[168:171], v[128:131], v[112:115], v[168:171]\n"
"v_mfma_f32_16x16x32_bf16 v[132:135], v[156:159], v[100:103], v[132:135]\n"
"ds_read_b64_tr_b16 v[128:129], v198 offset:19456\n"
"ds_read_b64_tr_b16 v[130:131], v199 offset:19456\n"
"v_mfma_f32_16x16x32_bf16 v[168:171], v[156:159], v[116:119], v[168:171]\n"
"s_waitcnt lgkmcnt(8)\n"
"v_mfma_f32_16x16x32_bf16 v[132:135], v[136:139], v[104:107], v[132:135]\n"
"ds_read_b64_tr_b16 v[156:157], v198 offset:20480\n"
"ds_read_b64_tr_b16 v[158:159], v199 offset:20480\n"
"v_mfma_f32_16x16x32_bf16 v[168:171], v[136:139], v[120:123], v[168:171]\n"
"v_mfma_f32_16x16x32_bf16 v[132:135], v[140:143], v[108:111], v[132:135]\n"
"ds_read_b64_tr_b16 v[136:137], v198 offset:21504\n"
"ds_read_b64_tr_b16 v[138:139], v199 offset:21504\n"
"v_mfma_f32_16x16x32_bf16 v[168:171], v[140:143], v[124:127], v[168:171]\n"
"s_waitcnt lgkmcnt(8)\n"
"v_mfma_f32_16x16x32_bf16 v[140:143], v[144:147], v[4:7], 0\n"
"ds_read_b64_tr_b16 v[160:161], v198 offset:22528\n"
"ds_read_b64_tr_b16 v[162:163], v199 offset:22528\n"
"v_mfma_f32_16x16x32_bf16 v[164:167], v[144:147], v[24:27], 0\n"
"v_mfma_f32_16x16x32_bf16 v[140:143], v[148:151], v[8:11], v[140:143]\n"
"ds_read_b64_tr_b16 v[144:145], v198 offset:23552\n"
"ds_read_b64_tr_b16 v[146:147], v199 offset:23552\n"
"v_mfma_f32_16x16x32_bf16 v[164:167], v[148:151], v[28:31], v[164:167]\n"
"ds_write_b128 v194, v[132:135] offset:6208\n"
"s_waitcnt lgkmcnt(9)\n"
"v_mfma_f32_16x16x32_bf16 v[140:143], v[152:155], v[16:19], v[140:143]\n"
"ds_read_b64_tr_b16 v[132:133], v198 offset:24576\n"
"ds_read_b64_tr_b16 v[134:135], v199 offset:24576\n"
"v_mfma_f32_16x16x32_bf16 v[164:167], v[152:155], v[32:35], v[164:167]\n"
"ds_write_b128 v194, v[168:171] offset:7232\n"
"v_mfma_f32_16x16x32_bf16 v[140:143], v[128:131], v[20:23], v[140:143]\n"
"ds_read_b64_tr_b16 v[148:149], v198 offset:25600\n"
"ds_read_b64_tr_b16 v[150:151], v199 offset:25600\n"
"v_mfma_f32_16x16x32_bf16 v[164:167], v[128:131], v[40:43], v[164:167]\n"
"s_waitcnt lgkmcnt(10)\n"
"v_mfma_f32_16x16x32_bf16 v[140:143], v[156:159], v[0:3], v[140:143]\n"
"ds_read_b64_tr_b16 v[128:129], v198 offset:26624\n"
"ds_read_b64_tr_b16 v[130:131], v199 offset:26624\n"
"v_mfma_f32_16x16x32_bf16 v[164:167], v[156:159], v[48:51], v[164:167]\n"
"s_waitcnt vmcnt(4) lgkmcnt(4)\n"
"s_barrier\n"
"s_add_u32 m0, s56, 0x14000\n"
"s_nop 0\n"
"global_load_lds_dwordx4 v202, s[32:33] sc1\n"
"global_load_lds_dwordx4 v202, s[32:33] offset:1024 sc1\n"
"global_load_lds_dwordx4 v202, s[32:33] offset:2048 sc1\n"
"global_load_lds_dwordx4 v202, s[32:33] offset:3072 sc1\n"
"s_add_u32 m0, s56, 0x8000\n"
"s_nop 0\n"
"global_load_lds_dwordx4 v195, s[32:33] sc1\n"
"global_load_lds_dwordx4 v195, s[32:33] offset:1024 sc1\n"
"global_load_lds_dwordx4 v195, s[32:33] offset:2048 sc1\n"
"global_load_lds_dwordx4 v195, s[32:33] offset:3072 sc1\n"
"v_mfma_f32_16x16x32_bf16 v[140:143], v[136:139], v[12:15], v[140:143]\n"
"ds_read_b64_tr_b16 v[152:153], v198 offset:27648\n"
"ds_read_b64_tr_b16 v[154:155], v199 offset:27648\n"
"v_mfma_f32_16x16x32_bf16 v[164:167], v[136:139], v[52:55], v[164:167]\n"
"v_mfma_f32_16x16x32_bf16 v[140:143], v[160:163], v[36:39], v[140:143]\n"
"ds_read_b64_tr_b16 v[136:137], v198 offset:28672\n"
"ds_read_b64_tr_b16 v[138:139], v199 offset:28672\n"
"v_mfma_f32_16x16x32_bf16 v[164:167], v[160:163], v[56:59], v[164:167]\n"
"v_mfma_f32_16x16x32_bf16 v[140:143], v[144:147], v[44:47], v[140:143]\n"
"ds_read_b64_tr_b16 v[156:157], v198 offset:29696\n"
"ds_read_b64_tr_b16 v[158:159], v199 offset:29696\n"
"v_mfma_f32_16x16x32_bf16 v[164:167], v[144:147], v[60:63], v[164:167]\n"
"s_waitcnt lgkmcnt(8)\n"
"v_mfma_f32_16x16x32_bf16 v[140:143], v[132:135], v[64:67], v[140:143]\n"
"ds_read_b64_tr_b16 v[144:145], v198 offset:30720\n"
"ds_read_b64_tr_b16 v[146:147], v199 offset:30720\n"
"v_mfma_f32_16x16x32_bf16 v[164:167], v[132:135], v[80:83], v[164:167]\n"
"v_mfma_f32_16x16x32_bf16 v[140:143], v[148:151], v[68:71], v[140:143]\n"
"ds_read_b64_tr_b16 v[132:133], v198 offset:31744\n"
"ds_read_b64_tr_b16 v[134:135], v199 offset:31744\n"
"v_mfma_f32_16x16x32_bf16 v[164:167], v[148:151], v[84:87], v[164:167]\n"
"s_waitcnt lgkmcnt(8)\n"
"v_mfma_f32_16x16x32_bf16 v[140:143], v[128:131], v[72:75], v[140:143]\n"
"ds_read_b64_tr_b16 v[148:149], v196 offset:512\n"
"ds_read_b64_tr_b16 v[150:151], v197 offset:512\n"
"v_mfma_f32_16x16x32_bf16 v[164:167], v[128:131], v[88:91], v[164:167]\n"
"v_mfma_f32_16x16x32_bf16 v[140:143], v[152:155], v[76:79], v[140:143]\n"
"ds_read_b64_tr_b16 v[128:129], v196 offset:1536\n"
"ds_read_b64_tr_b16 v[130:131], v197 offset:1536\n"
"v_mfma_f32_16x16x32_bf16 v[164:167], v[152:155], v[92:95], v[164:167]\n"
"s_waitcnt lgkmcnt(8)\n"
"v_mfma_f32_16x16x32_bf16 v[140:143], v[136:139], v[96:99], v[140:143]\n"
"ds_read_b64_tr_b16 v[152:153], v196 offset:2560\n"
"ds_read_b64_tr_b16 v[154:155], v197 offset:2560\n"
"v_mfma_f32_16x16x32_bf16 v[164:167], v[136:139], v[112:115], v[164:167]\n"
"v_mfma_f32_16x16x32_bf16 v[140:143], v[156:159], v[100:103], v[140:143]\n"
"ds_read_b64_tr_b16 v[136:137], v196 offset:3584\n"
"ds_read_b64_tr_b16 v[138:139], v197 offset:3584\n"
"v_mfma_f32_16x16x32_bf16 v[164:167], v[156:159], v[116:119], v[164:167]\n"
"s_waitcnt lgkmcnt(8)\n"
"v_mfma_f32_16x16x32_bf16 v[140:143], v[144:147], v[104:107], v[140:143]\n"
"ds_read_b64_tr_b16 v[156:157], v196 offset:4608\n"
"ds_read_b64_tr_b16 v[158:159], v197 offset:4608\n"
"v_mfma_f32_16x16x32_bf16 v[164:167], v[144:147], v[120:123], v[164:167]\n"
"v_mfma_f32_16x16x32_bf16 v[140:143], v[132:135], v[108:111], v[140:143]\n"
"ds_read_b64_tr_b16 v[144:145], v196 offset:5632\n"
"ds_read_b64_tr_b16 v[146:147], v197 offset:5632\n"
"v_mfma_f32_16x16x32_bf16 v[164:167], v[132:135], v[124:127], v[164:167]\n"
"s_waitcnt lgkmcnt(8)\n"
"v_mfma_f32_16x16x32_bf16 v[132:135], v[148:151], v[4:7], 0\n"
"ds_read_b64_tr_b16 v[160:161], v196 offset:6656\n"
"ds_read_b64_tr_b16 v[162:163], v197 offset:6656\n"
"v_mfma_f32_16x16x32_bf16 v[168:171], v[148:151], v[24:27], 0\n"
"v_mfma_f32_16x16x32_bf16 v[132:135], v[128:131], v[8:11], v[132:135]\n"
"ds_read_b64_tr_b16 v[148:149], v196 offset:7680\n"
"ds_read_b64_tr_b16 v[150:151], v197 offset:7680\n"
"v_mfma_f32_16x16x32_bf16 v[168:171], v[128:131], v[28:31], v[168:171]\n"
"ds_write_b128 v194, v[140:143] offset:4160\n"
"s_waitcnt lgkmcnt(9)\n"
"v_mfma_f32_16x16x32_bf16 v[132:135], v[152:155], v[16:19], v[132:135]\n"
"ds_read_b64_tr_b16 v[128:129], v196 offset:8704\n"
"ds_read_b64_tr_b16 v[130:131], v197 offset:8704\n"
"v_mfma_f32_16x16x32_bf16 v[168:171], v[152:155], v[32:35], v[168:171]\n"
"ds_write_b128 v194, v[164:167] offset:5184\n"
"global_load_dwordx2 v[164:165], v204, s[66:67] sc1\n"
"v_mfma_f32_16x16x32_bf16 v[132:135], v[136:139], v[20:23], v[132:135]\n"
"ds_read_b64_tr_b16 v[140:141], v196 offset:9728\n"
"ds_read_b64_tr_b16 v[142:143], v197 offset:9728\n"
"v_mfma_f32_16x16x32_bf16 v[168:171], v[136:139], v[40:43], v[168:171]\n"
"s_waitcnt lgkmcnt(10)\n"
"v_mfma_f32_16x16x32_bf16 v[132:135], v[156:159], v[0:3], v[132:135]\n"
"ds_read_b64_tr_b16 v[136:137], v196 offset:10752\n"
"ds_read_b64_tr_b16 v[138:139], v197 offset:10752\n"
"v_mfma_f32_16x16x32_bf16 v[168:171], v[156:159], v[48:51], v[168:171]\n"
"s_waitcnt vmcnt(5) lgkmcnt(4)\n"
"s_barrier\n"
"global_load_dwordx4 v[152:155], v195, s[16:17] sc1\n"
"s_add_u32 m0, s56, 0x10000\n"
"s_nop 0\n"
"global_load_lds_dwordx4 v202, s[34:35] sc1\n"
"global_load_lds_dwordx4 v202, s[34:35] offset:1024 sc1\n"
"global_load_lds_dwordx4 v202, s[34:35] offset:2048 sc1\n"
"global_load_lds_dwordx4 v202, s[34:35] offset:3072 sc1\n"
"s_add_u32 m0, s56, 0\n"
"s_nop 0\n"
"global_load_lds_dwordx4 v195, s[34:35] sc1\n"
"global_load_lds_dwordx4 v195, s[34:35] offset:1024 sc1\n"
"global_load_lds_dwordx4 v195, s[34:35] offset:2048 sc1\n"
"global_load_lds_dwordx4 v195, s[34:35] offset:3072 sc1\n"
"s_waitcnt vmcnt(9)\n"
"v_xor_b32 v156, s11, v165\n"
"v_and_b32 v156, 0xffffdfff, v156\n"
"v_bitop3_b32 v156, v156, v164, s10 bitop3:0xf6\n"
"v_cmp_ne_u32 vcc, 0, v156\n"
"s_nop 0\n"
"s_cbranch_vccnz .Lr13\n"
".Lb13:\n"
"v_and_b32 v156, 0x2000, v165\n"
"v_xor_b32 v203, v203, v156\n"
"v_xor_b32 v156, 64, v203\n"
"s_add_u32 m0, s57, 0\n"
"s_nop 0\n"
"global_load_lds_dwordx4 v203, s[92:93] sc1\n"
"s_add_u32 m0, s57, 0x400\n"
"s_nop 0\n"
"global_load_lds_dwordx4 v156, s[94:95] sc1\n"
"s_add_u32 m0, s57, 0x800\n"
"s_nop 0\n"
"global_load_lds_dwordx4 v203, s[22:23] sc1\n"
"s_add_u32 m0, s57, 0xc00\n"
"s_nop 0\n"
"global_load_lds_dwordx4 v156, s[24:25] sc1\n"
"v_mfma_f32_16x16x32_bf16 v[132:135], v[144:147], v[12:15], v[132:135]\n"
"ds_read_b64_tr_b16 v[156:157], v196 offset:11776\n"
"ds_read_b64_tr_b16 v[158:159], v197 offset:11776\n"
"v_mfma_f32_16x16x32_bf16 v[168:171], v[144:147], v[52:55], v[168:171]\n"
"v_mfma_f32_16x16x32_bf16 v[132:135], v[160:163], v[36:39], v[132:135]\n"
"ds_read_b64_tr_b16 v[144:145], v196 offset:12800\n"
"ds_read_b64_tr_b16 v[146:147], v197 offset:12800\n"
"v_mfma_f32_16x16x32_bf16 v[168:171], v[160:163], v[56:59], v[168:171]\n"
"v_mfma_f32_16x16x32_bf16 v[132:135], v[148:151], v[44:47], v[132:135]\n"
"ds_read_b64_tr_b16 v[160:161], v196 offset:13824\n"
"ds_read_b64_tr_b16 v[162:163], v197 offset:13824\n"
"v_mfma_f32_16x16x32_bf16 v[168:171], v[148:151], v[60:63], v[168:171]\n"
"s_waitcnt lgkmcnt(8)\n"
"v_mfma_f32_16x16x32_bf16 v[132:135], v[128:131], v[64:67], v[132:135]\n"
"ds_read_b64_tr_b16 v[148:149], v196 offset:14848\n"
"ds_read_b64_tr_b16 v[150:151], v197 offset:14848\n"
"v_mfma_f32_16x16x32_bf16 v[168:171], v[128:131], v[80:83], v[168:171]\n"
"v_mfma_f32_16x16x32_bf16 v[132:135], v[140:143], v[68:71], v[132:135]\n"
"ds_read_b64_tr_b16 v[128:129], v196 offset:15872\n"
"ds_read_b64_tr_b16 v[130:131], v197 offset:15872\n"
"s_waitcnt vmcnt(12)\n"
"v_lshlrev_b32 v164, 4, v154\n"
"v_bfi_b32 v164, 15, v152, v164\n"
"v_mfma_f32_16x16x32_bf16 v[168:171], v[140:143], v[84:87], v[168:171]\n"
"v_cmp_ne_u32_sdwa vcc, v164, v217 src0_sel:BYTE_0 src1_sel:DWORD\n"
"s_nop 0\n"
"s_cbranch_vccnz .Lr8\n"
".Lb8:\n"
"v_lshlrev_b32 v164, 4, v155\n"
"s_waitcnt lgkmcnt(8)\n"
"v_mfma_f32_16x16x32_bf16 v[132:135], v[136:139], v[72:75], v[132:135]\n"
"ds_read_b64_tr_b16 v[140:141], v200 offset:16384\n"
"ds_read_b64_tr_b16 v[142:143], v201 offset:16384\n"
"v_bfi_b32 v164, 15, v153, v164\n"
"v_and_b32 v164, 0xff, v164\n"
"v_mfma_f32_16x16x32_bf16 v[168:171], v[136:139], v[88:91], v[168:171]\n"
"v_subrev_u32 v164, 0x7e, v164\n"
"v_ldexp_f32 v176, s82, v164\n"
"v_cvt_f32_i32_sdwa v137, sext(v152) dst_sel:DWORD dst_unused:UNUSED_PAD src0_sel:WORD_1\n"
"v_cvt_f32_i32_sdwa v139, sext(v153) dst_sel:DWORD dst_unused:UNUSED_PAD src0_sel:WORD_1\n"
"v_mfma_f32_16x16x32_bf16 v[132:135], v[156:159], v[76:79], v[132:135]\n"
"ds_read_b64_tr_b16 v[164:165], v200 offset:17408\n"
"ds_read_b64_tr_b16 v[166:167], v201 offset:17408\n"
"v_cvt_f32_i32_sdwa v173, sext(v154) dst_sel:DWORD dst_unused:UNUSED_PAD src0_sel:WORD_1\n"
"v_cvt_f32_i32_sdwa v175, sext(v155) dst_sel:DWORD dst_unused:UNUSED_PAD src0_sel:WORD_1\n"
"v_mfma_f32_16x16x32_bf16 v[168:171], v[156:159], v[92:95], v[168:171]\n"
"v_and_or_b32 v152, v152, -16, 8\n"
"v_and_or_b32 v153, v153, -16, 8\n"
"v_and_or_b32 v154, v154, -16, 8\n"
"s_waitcnt lgkmcnt(8)\n"
"v_mfma_f32_16x16x32_bf16 v[132:135], v[144:147], v[96:99], v[132:135]\n"
"ds_read_b64_tr_b16 v[156:157], v200 offset:18432\n"
"ds_read_b64_tr_b16 v[158:159], v201 offset:18432\n"
"v_and_or_b32 v155, v155, -16, 8\n"
"v_cvt_f32_i32_sdwa v136, sext(v152) dst_sel:DWORD dst_unused:UNUSED_PAD src0_sel:WORD_0\n"
"v_mfma_f32_16x16x32_bf16 v[168:171], v[144:147], v[112:115], v[168:171]\n"
"v_cvt_f32_i32_sdwa v138, sext(v153) dst_sel:DWORD dst_unused:UNUSED_PAD src0_sel:WORD_0\n"
"v_cvt_f32_i32_sdwa v172, sext(v154) dst_sel:DWORD dst_unused:UNUSED_PAD src0_sel:WORD_0\n"
"v_cvt_f32_i32_sdwa v174, sext(v155) dst_sel:DWORD dst_unused:UNUSED_PAD src0_sel:WORD_0\n"
"v_mul_f32 v136, v176, v136\n"
"v_mfma_f32_16x16x32_bf16 v[132:135], v[160:163], v[100:103], v[132:135]\n"
"ds_read_b64_tr_b16 v[144:145], v200 offset:19456\n"
"ds_read_b64_tr_b16 v[146:147], v201 offset:19456\n"
"v_mul_f32 v137, v176, v137\n"
"v_mul_f32 v138, v176, v138\n"
"v_mfma_f32_16x16x32_bf16 v[168:171], v[160:163], v[116:119], v[168:171]\n"
"v_mul_f32 v139, v176, v139\n"
"v_mul_f32 v172, v176, v172\n"
"v_mul_f32 v173, v176, v173\n"
"s_waitcnt lgkmcnt(8)\n"
"v_mfma_f32_16x16x32_bf16 v[132:135], v[148:151], v[104:107], v[132:135]\n"
"ds_read_b64_tr_b16 v[152:153], v200 offset:20480\n"
"ds_read_b64_tr_b16 v[154:155], v201 offset:20480\n"
"v_mul_f32 v174, v176, v174\n"
"v_mul_f32 v175, v176, v175\n"
"v_mfma_f32_16x16x32_bf16 v[168:171], v[148:151], v[120:123], v[168:171]\n"
"v_mfma_f32_16x16x32_bf16 v[132:135], v[128:131], v[108:111], v[132:135]\n"
"ds_read_b64_tr_b16 v[148:149], v200 offset:21504\n"
"ds_read_b64_tr_b16 v[150:151], v201 offset:21504\n"
"v_mfma_f32_16x16x32_bf16 v[168:171], v[128:131], v[124:127], v[168:171]\n"
"s_waitcnt lgkmcnt(8)\n"
"v_mfma_f32_16x16x32_bf16 v[136:139], v[140:143], v[4:7], v[136:139]\n"
"ds_read_b64_tr_b16 v[128:129], v200 offset:22528\n"
"ds_read_b64_tr_b16 v[130:131], v201 offset:22528\n"
"v_mfma_f32_16x16x32_bf16 v[172:175], v[140:143], v[24:27], v[172:175]\n"
"v_mfma_f32_16x16x32_bf16 v[136:139], v[164:167], v[8:11], v[136:139]\n"
"ds_read_b64_tr_b16 v[140:141], v200 offset:23552\n"
"ds_read_b64_tr_b16 v[142:143], v201 offset:23552\n"
"v_mfma_f32_16x16x32_bf16 v[172:175], v[164:167], v[28:31], v[172:175]\n"
"ds_write_b128 v194, v[132:135] offset:6208\n"
"s_waitcnt lgkmcnt(9)\n"
"v_mfma_f32_16x16x32_bf16 v[136:139], v[156:159], v[16:19], v[136:139]\n"
"ds_read_b64_tr_b16 v[132:133], v200 offset:24576\n"
"ds_read_b64_tr_b16 v[134:135], v201 offset:24576\n"
"v_mfma_f32_16x16x32_bf16 v[172:175], v[156:159], v[32:35], v[172:175]\n"
"ds_write_b128 v194, v[168:171] offset:7232\n"
"v_mfma_f32_16x16x32_bf16 v[136:139], v[144:147], v[20:23], v[136:139]\n"
"ds_read_b64_tr_b16 v[156:157], v200 offset:25600\n"
"ds_read_b64_tr_b16 v[158:159], v201 offset:25600\n"
"v_mfma_f32_16x16x32_bf16 v[172:175], v[144:147], v[40:43], v[172:175]\n"
"s_waitcnt lgkmcnt(10)\n"
"v_mfma_f32_16x16x32_bf16 v[136:139], v[152:155], v[0:3], v[136:139]\n"
"ds_read_b64_tr_b16 v[144:145], v200 offset:26624\n"
"ds_read_b64_tr_b16 v[146:147], v201 offset:26624\n"
"v_mfma_f32_16x16x32_bf16 v[172:175], v[152:155], v[48:51], v[172:175]\n"
"s_waitcnt vmcnt(0) lgkmcnt(4)\n"
"s_barrier\n"
"global_load_dwordx4 v[152:155], v195, s[16:17] offset:1024 sc1\n"
"s_add_u32 m0, s56, 0x4000\n"
"s_nop 0\n"
"global_load_lds_dwordx4 v202, s[36:37] sc1\n"
"global_load_lds_dwordx4 v202, s[36:37] offset:1024 sc1\n"
"global_load_lds_dwordx4 v202, s[36:37] offset:2048 sc1\n"
"global_load_lds_dwordx4 v202, s[36:37] offset:3072 sc1\n"
"s_add_u32 m0, s56, 0xc000\n"
"s_nop 0\n"
"global_load_lds_dwordx4 v195, s[36:37] sc1\n"
"global_load_lds_dwordx4 v195, s[36:37] offset:1024 sc1\n"
"global_load_lds_dwordx4 v195, s[36:37] offset:2048 sc1\n"
"global_load_lds_dwordx4 v195, s[36:37] offset:3072 sc1\n"
"v_mfma_f32_16x16x32_bf16 v[136:139], v[148:151], v[12:15], v[136:139]\n"
"ds_read_b64_tr_b16 v[160:161], v200 offset:27648\n"
"ds_read_b64_tr_b16 v[162:163], v201 offset:27648\n"
"v_mfma_f32_16x16x32_bf16 v[172:175], v[148:151], v[52:55], v[172:175]\n"
"v_mfma_f32_16x16x32_bf16 v[136:139], v[128:131], v[36:39], v[136:139]\n"
"ds_read_b64_tr_b16 v[148:149], v200 offset:28672\n"
"ds_read_b64_tr_b16 v[150:151], v201 offset:28672\n"
"v_mfma_f32_16x16x32_bf16 v[172:175], v[128:131], v[56:59], v[172:175]\n"
"v_mfma_f32_16x16x32_bf16 v[136:139], v[140:143], v[44:47], v[136:139]\n"
"ds_read_b64_tr_b16 v[128:129], v200 offset:29696\n"
"ds_read_b64_tr_b16 v[130:131], v201 offset:29696\n"
"v_mfma_f32_16x16x32_bf16 v[172:175], v[140:143], v[60:63], v[172:175]\n"
"s_waitcnt lgkmcnt(8)\n"
"v_mfma_f32_16x16x32_bf16 v[136:139], v[132:135], v[64:67], v[136:139]\n"
"ds_read_b64_tr_b16 v[140:141], v200 offset:30720\n"
"ds_read_b64_tr_b16 v[142:143], v201 offset:30720\n"
"v_mfma_f32_16x16x32_bf16 v[172:175], v[132:135], v[80:83], v[172:175]\n"
"v_mfma_f32_16x16x32_bf16 v[136:139], v[156:159], v[68:71], v[136:139]\n"
"ds_read_b64_tr_b16 v[132:133], v200 offset:31744\n"
"ds_read_b64_tr_b16 v[134:135], v201 offset:31744\n"
"s_waitcnt vmcnt(8)\n"
"v_lshlrev_b32 v164, 4, v154\n"
"v_bfi_b32 v164, 15, v152, v164\n"
"v_mfma_f32_16x16x32_bf16 v[172:175], v[156:159], v[84:87], v[172:175]\n"
"v_cmp_ne_u32_sdwa vcc, v164, v217 src0_sel:BYTE_0 src1_sel:DWORD\n"
"s_nop 0\n"
"s_cbranch_vccnz .Lr9\n"
".Lb9:\n"
"v_lshlrev_b32 v164, 4, v155\n"
"s_waitcnt lgkmcnt(8)\n"
"v_mfma_f32_16x16x32_bf16 v[136:139], v[144:147], v[72:75], v[136:139]\n"
"ds_read_b64_tr_b16 v[156:157], v200\n"
"ds_read_b64_tr_b16 v[158:159], v201\n"
"v_bfi_b32 v164, 15, v153, v164\n"
"v_and_b32 v164, 0xff, v164\n"
"v_mfma_f32_16x16x32_bf16 v[172:175], v[144:147], v[88:91], v[172:175]\n"
"v_subrev_u32 v164, 0x7e, v164\n"
"v_ldexp_f32 v176, s82, v164\n"
"v_cvt_f32_i32_sdwa v145, sext(v152) dst_sel:DWORD dst_unused:UNUSED_PAD src0_sel:WORD_1\n"
"v_cvt_f32_i32_sdwa v147, sext(v153) dst_sel:DWORD dst_unused:UNUSED_PAD src0_sel:WORD_1\n"
"v_mfma_f32_16x16x32_bf16 v[136:139], v[160:163], v[76:79], v[136:139]\n"
"ds_read_b64_tr_b16 v[164:165], v200 offset:1024\n"
"ds_read_b64_tr_b16 v[166:167], v201 offset:1024\n"
"v_cvt_f32_i32_sdwa v169, sext(v154) dst_sel:DWORD dst_unused:UNUSED_PAD src0_sel:WORD_1\n"
"v_cvt_f32_i32_sdwa v171, sext(v155) dst_sel:DWORD dst_unused:UNUSED_PAD src0_sel:WORD_1\n"
"v_mfma_f32_16x16x32_bf16 v[172:175], v[160:163], v[92:95], v[172:175]\n"
"v_and_or_b32 v152, v152, -16, 8\n"
"v_and_or_b32 v153, v153, -16, 8\n"
"v_and_or_b32 v154, v154, -16, 8\n"
"s_waitcnt lgkmcnt(8)\n"
"v_mfma_f32_16x16x32_bf16 v[136:139], v[148:151], v[96:99], v[136:139]\n"
"ds_read_b64_tr_b16 v[160:161], v200 offset:2048\n"
"ds_read_b64_tr_b16 v[162:163], v201 offset:2048\n"
"v_and_or_b32 v155, v155, -16, 8\n"
"v_cvt_f32_i32_sdwa v144, sext(v152) dst_sel:DWORD dst_unused:UNUSED_PAD src0_sel:WORD_0\n"
"v_mfma_f32_16x16x32_bf16 v[172:175], v[148:151], v[112:115], v[172:175]\n"
"v_cvt_f32_i32_sdwa v146, sext(v153) dst_sel:DWORD dst_unused:UNUSED_PAD src0_sel:WORD_0\n"
"v_cvt_f32_i32_sdwa v168, sext(v154) dst_sel:DWORD dst_unused:UNUSED_PAD src0_sel:WORD_0\n"
"v_cvt_f32_i32_sdwa v170, sext(v155) dst_sel:DWORD dst_unused:UNUSED_PAD src0_sel:WORD_0\n"
"v_mul_f32 v144, v176, v144\n"
"v_mfma_f32_16x16x32_bf16 v[136:139], v[128:131], v[100:103], v[136:139]\n"
"ds_read_b64_tr_b16 v[148:149], v200 offset:3072\n"
"ds_read_b64_tr_b16 v[150:151], v201 offset:3072\n"
"v_mul_f32 v145, v176, v145\n"
"v_mul_f32 v146, v176, v146\n"
"v_mfma_f32_16x16x32_bf16 v[172:175], v[128:131], v[116:119], v[172:175]\n"
"v_mul_f32 v147, v176, v147\n"
"v_mul_f32 v168, v176, v168\n"
"v_mul_f32 v169, v176, v169\n"
"s_waitcnt lgkmcnt(8)\n"
"v_mfma_f32_16x16x32_bf16 v[136:139], v[140:143], v[104:107], v[136:139]\n"
"ds_read_b64_tr_b16 v[128:129], v200 offset:4096\n"
"ds_read_b64_tr_b16 v[130:131], v201 offset:4096\n"
"v_mul_f32 v170, v176, v170\n"
"v_mul_f32 v171, v176, v171\n"
"v_mfma_f32_16x16x32_bf16 v[172:175], v[140:143], v[120:123], v[172:175]\n"
"v_mfma_f32_16x16x32_bf16 v[136:139], v[132:135], v[108:111], v[136:139]\n"
"ds_read_b64_tr_b16 v[140:141], v200 offset:5120\n"
"ds_read_b64_tr_b16 v[142:143], v201 offset:5120\n"
"v_mfma_f32_16x16x32_bf16 v[172:175], v[132:135], v[124:127], v[172:175]\n"
"s_waitcnt lgkmcnt(8)\n"
"v_mfma_f32_16x16x32_bf16 v[144:147], v[156:159], v[4:7], v[144:147]\n"
"ds_read_b64_tr_b16 v[132:133], v200 offset:6144\n"
"ds_read_b64_tr_b16 v[134:135], v201 offset:6144\n"
"v_mfma_f32_16x16x32_bf16 v[168:171], v[156:159], v[24:27], v[168:171]\n"
"v_mfma_f32_16x16x32_bf16 v[144:147], v[164:167], v[8:11], v[144:147]\n"
"ds_read_b64_tr_b16 v[152:153], v200 offset:7168\n"
"ds_read_b64_tr_b16 v[154:155], v201 offset:7168\n"
"v_mfma_f32_16x16x32_bf16 v[168:171], v[164:167], v[28:31], v[168:171]\n"
"ds_write_b128 v194, v[136:139] offset:4160\n"
"s_waitcnt lgkmcnt(9)\n"
"v_mfma_f32_16x16x32_bf16 v[144:147], v[160:163], v[16:19], v[144:147]\n"
"ds_read_b64_tr_b16 v[136:137], v200 offset:8192\n"
"ds_read_b64_tr_b16 v[138:139], v201 offset:8192\n"
"v_mfma_f32_16x16x32_bf16 v[168:171], v[160:163], v[32:35], v[168:171]\n"
"ds_write_b128 v194, v[172:175] offset:5184\n"
"v_mfma_f32_16x16x32_bf16 v[144:147], v[148:151], v[20:23], v[144:147]\n"
"ds_read_b64_tr_b16 v[156:157], v200 offset:9216\n"
"ds_read_b64_tr_b16 v[158:159], v201 offset:9216\n"
"v_mfma_f32_16x16x32_bf16 v[168:171], v[148:151], v[40:43], v[168:171]\n"
"s_waitcnt lgkmcnt(10)\n"
"v_mfma_f32_16x16x32_bf16 v[144:147], v[128:131], v[0:3], v[144:147]\n"
"ds_read_b64_tr_b16 v[148:149], v200 offset:10240\n"
"ds_read_b64_tr_b16 v[150:151], v201 offset:10240\n"
"v_mfma_f32_16x16x32_bf16 v[168:171], v[128:131], v[48:51], v[168:171]\n"
"s_waitcnt vmcnt(4) lgkmcnt(4)\n"
"s_barrier\n"
"global_load_dwordx4 v[128:131], v195, s[16:17] offset:2048 sc1\n"
"s_add_u32 m0, s56, 0x20200\n"
"s_nop 0\n"
"global_load_lds_dwordx4 v202, s[40:41] sc1\n"
"global_load_lds_dwordx4 v202, s[40:41] offset:1024 sc1\n"
"global_load_lds_dwordx4 v202, s[40:41] offset:2048 sc1\n"
"global_load_lds_dwordx4 v202, s[40:41] offset:3072 sc1\n"
"s_add_u32 m0, s56, 0x8000\n"
"s_nop 0\n"
"global_load_lds_dwordx4 v195, s[40:41] sc1\n"
"global_load_lds_dwordx4 v195, s[40:41] offset:1024 sc1\n"
"global_load_lds_dwordx4 v195, s[40:41] offset:2048 sc1\n"
"global_load_lds_dwordx4 v195, s[40:41] offset:3072 sc1\n"
"v_mfma_f32_16x16x32_bf16 v[144:147], v[140:143], v[12:15], v[144:147]\n"
"ds_read_b64_tr_b16 v[160:161], v200 offset:11264\n"
"ds_read_b64_tr_b16 v[162:163], v201 offset:11264\n"
"v_mfma_f32_16x16x32_bf16 v[168:171], v[140:143], v[52:55], v[168:171]\n"
"v_mfma_f32_16x16x32_bf16 v[144:147], v[132:135], v[36:39], v[144:147]\n"
"ds_read_b64_tr_b16 v[140:141], v200 offset:12288\n"
"ds_read_b64_tr_b16 v[142:143], v201 offset:12288\n"
"v_mfma_f32_16x16x32_bf16 v[168:171], v[132:135], v[56:59], v[168:171]\n"
"v_mfma_f32_16x16x32_bf16 v[144:147], v[152:155], v[44:47], v[144:147]\n"
"ds_read_b64_tr_b16 v[132:133], v200 offset:13312\n"
"ds_read_b64_tr_b16 v[134:135], v201 offset:13312\n"
"v_mfma_f32_16x16x32_bf16 v[168:171], v[152:155], v[60:63], v[168:171]\n"
"s_waitcnt lgkmcnt(8)\n"
"v_mfma_f32_16x16x32_bf16 v[144:147], v[136:139], v[64:67], v[144:147]\n"
"ds_read_b64_tr_b16 v[152:153], v200 offset:14336\n"
"ds_read_b64_tr_b16 v[154:155], v201 offset:14336\n"
"v_mfma_f32_16x16x32_bf16 v[168:171], v[136:139], v[80:83], v[168:171]\n"
"v_mfma_f32_16x16x32_bf16 v[144:147], v[156:159], v[68:71], v[144:147]\n"
"ds_read_b64_tr_b16 v[136:137], v200 offset:15360\n"
"ds_read_b64_tr_b16 v[138:139], v201 offset:15360\n"
"s_waitcnt vmcnt(8)\n"
"v_lshlrev_b32 v164, 4, v130\n"
"v_bfi_b32 v164, 15, v128, v164\n"
"v_mfma_f32_16x16x32_bf16 v[168:171], v[156:159], v[84:87], v[168:171]\n"
"v_cmp_ne_u32_sdwa vcc, v164, v217 src0_sel:BYTE_0 src1_sel:DWORD\n"
"s_nop 0\n"
"s_cbranch_vccnz .Lr10\n"
".Lb10:\n"
"v_lshlrev_b32 v164, 4, v131\n"
"s_waitcnt lgkmcnt(8)\n"
"v_mfma_f32_16x16x32_bf16 v[144:147], v[148:151], v[72:75], v[144:147]\n"
"ds_read_b64_tr_b16 v[156:157], v198 offset:16384\n"
"ds_read_b64_tr_b16 v[158:159], v199 offset:16384\n"
"v_bfi_b32 v164, 15, v129, v164\n"
"v_and_b32 v164, 0xff, v164\n"
"v_mfma_f32_16x16x32_bf16 v[168:171], v[148:151], v[88:91], v[168:171]\n"
"v_subrev_u32 v164, 0x7e, v164\n"
"v_ldexp_f32 v176, s82, v164\n"
"v_cvt_f32_i32_sdwa v149, sext(v128) dst_sel:DWORD dst_unused:UNUSED_PAD src0_sel:WORD_1\n"
"v_cvt_f32_i32_sdwa v151, sext(v129) dst_sel:DWORD dst_unused:UNUSED_PAD src0_sel:WORD_1\n"
"v_mfma_f32_16x16x32_bf16 v[144:147], v[160:163], v[76:79], v[144:147]\n"
"ds_read_b64_tr_b16 v[164:165], v198 offset:17408\n"
"ds_read_b64_tr_b16 v[166:167], v199 offset:17408\n"
"v_cvt_f32_i32_sdwa v173, sext(v130) dst_sel:DWORD dst_unused:UNUSED_PAD src0_sel:WORD_1\n"
"v_cvt_f32_i32_sdwa v175, sext(v131) dst_sel:DWORD dst_unused:UNUSED_PAD src0_sel:WORD_1\n"
"v_mfma_f32_16x16x32_bf16 v[168:171], v[160:163], v[92:95], v[168:171]\n"
"v_and_or_b32 v128, v128, -16, 8\n"
"v_and_or_b32 v129, v129, -16, 8\n"
"v_and_or_b32 v130, v130, -16, 8\n"
"s_waitcnt lgkmcnt(8)\n"
"v_mfma_f32_16x16x32_bf16 v[144:147], v[140:143], v[96:99], v[144:147]\n"
"ds_read_b64_tr_b16 v[160:161], v198 offset:18432\n"
"ds_read_b64_tr_b16 v[162:163], v199 offset:18432\n"
"v_and_or_b32 v131, v131, -16, 8\n"
"v_cvt_f32_i32_sdwa v148, sext(v128) dst_sel:DWORD dst_unused:UNUSED_PAD src0_sel:WORD_0\n"
"v_mfma_f32_16x16x32_bf16 v[168:171], v[140:143], v[112:115], v[168:171]\n"
"v_cvt_f32_i32_sdwa v150, sext(v129) dst_sel:DWORD dst_unused:UNUSED_PAD src0_sel:WORD_0\n"
"v_cvt_f32_i32_sdwa v172, sext(v130) dst_sel:DWORD dst_unused:UNUSED_PAD src0_sel:WORD_0\n"
"v_cvt_f32_i32_sdwa v174, sext(v131) dst_sel:DWORD dst_unused:UNUSED_PAD src0_sel:WORD_0\n"
"v_mul_f32 v148, v176, v148\n"
"v_mfma_f32_16x16x32_bf16 v[144:147], v[132:135], v[100:103], v[144:147]\n"
"ds_read_b64_tr_b16 v[128:129], v198 offset:19456\n"
"ds_read_b64_tr_b16 v[130:131], v199 offset:19456\n"
"v_mul_f32 v149, v176, v149\n"
"v_mul_f32 v150, v176, v150\n"
"v_mfma_f32_16x16x32_bf16 v[168:171], v[132:135], v[116:119], v[168:171]\n"
"v_mul_f32 v151, v176, v151\n"
"v_mul_f32 v172, v176, v172\n"
"v_mul_f32 v173, v176, v173\n"
"s_waitcnt lgkmcnt(8)\n"
"v_mfma_f32_16x16x32_bf16 v[144:147], v[152:155], v[104:107], v[144:147]\n"
"ds_read_b64_tr_b16 v[132:133], v198 offset:20480\n"
"ds_read_b64_tr_b16 v[134:135], v199 offset:20480\n"
"v_mul_f32 v174, v176, v174\n"
"v_mul_f32 v175, v176, v175\n"
"v_mfma_f32_16x16x32_bf16 v[168:171], v[152:155], v[120:123], v[168:171]\n"
"v_mfma_f32_16x16x32_bf16 v[144:147], v[136:139], v[108:111], v[144:147]\n"
"ds_read_b64_tr_b16 v[140:141], v198 offset:21504\n"
"ds_read_b64_tr_b16 v[142:143], v199 offset:21504\n"
"v_mfma_f32_16x16x32_bf16 v[168:171], v[136:139], v[124:127], v[168:171]\n"
"s_waitcnt lgkmcnt(8)\n"
"v_mfma_f32_16x16x32_bf16 v[148:151], v[156:159], v[4:7], v[148:151]\n"
"ds_read_b64_tr_b16 v[136:137], v198 offset:22528\n"
"ds_read_b64_tr_b16 v[138:139], v199 offset:22528\n"
"v_mfma_f32_16x16x32_bf16 v[172:175], v[156:159], v[24:27], v[172:175]\n"
"v_mfma_f32_16x16x32_bf16 v[148:151], v[164:167], v[8:11], v[148:151]\n"
"ds_read_b64_tr_b16 v[152:153], v198 offset:23552\n"
"ds_read_b64_tr_b16 v[154:155], v199 offset:23552\n"
"v_mfma_f32_16x16x32_bf16 v[172:175], v[164:167], v[28:31], v[172:175]\n"
"ds_write_b128 v194, v[144:147] offset:6208\n"
"s_waitcnt lgkmcnt(9)\n"
"v_mfma_f32_16x16x32_bf16 v[148:151], v[160:163], v[16:19], v[148:151]\n"
"ds_read_b64_tr_b16 v[144:145], v198 offset:24576\n"
"ds_read_b64_tr_b16 v[146:147], v199 offset:24576\n"
"v_mfma_f32_16x16x32_bf16 v[172:175], v[160:163], v[32:35], v[172:175]\n"
"ds_write_b128 v194, v[168:171] offset:7232\n"
"v_mfma_f32_16x16x32_bf16 v[148:151], v[128:131], v[20:23], v[148:151]\n"
"ds_read_b64_tr_b16 v[156:157], v198 offset:25600\n"
"ds_read_b64_tr_b16 v[158:159], v199 offset:25600\n"
"v_mfma_f32_16x16x32_bf16 v[172:175], v[128:131], v[40:43], v[172:175]\n"
"s_waitcnt lgkmcnt(10)\n"
"v_mfma_f32_16x16x32_bf16 v[148:151], v[132:135], v[0:3], v[148:151]\n"
"ds_read_b64_tr_b16 v[128:129], v198 offset:26624\n"
"ds_read_b64_tr_b16 v[130:131], v199 offset:26624\n"
"v_mfma_f32_16x16x32_bf16 v[172:175], v[132:135], v[48:51], v[172:175]\n"
"s_waitcnt vmcnt(4) lgkmcnt(4)\n"
"s_barrier\n"
"global_load_dwordx4 v[132:135], v195, s[16:17] offset:3072 sc1\n"
"v_mfma_f32_16x16x32_bf16 v[148:151], v[140:143], v[12:15], v[148:151]\n"
"ds_read_b64_tr_b16 v[160:161], v198 offset:27648\n"
"ds_read_b64_tr_b16 v[162:163], v199 offset:27648\n"
"v_mfma_f32_16x16x32_bf16 v[172:175], v[140:143], v[52:55], v[172:175]\n"
"v_mfma_f32_16x16x32_bf16 v[148:151], v[136:139], v[36:39], v[148:151]\n"
"ds_read_b64_tr_b16 v[140:141], v198 offset:28672\n"
"ds_read_b64_tr_b16 v[142:143], v199 offset:28672\n"
"v_mfma_f32_16x16x32_bf16 v[172:175], v[136:139], v[56:59], v[172:175]\n"
"v_mfma_f32_16x16x32_bf16 v[148:151], v[152:155], v[44:47], v[148:151]\n"
"ds_read_b64_tr_b16 v[136:137], v198 offset:29696\n"
"ds_read_b64_tr_b16 v[138:139], v199 offset:29696\n"
"v_mfma_f32_16x16x32_bf16 v[172:175], v[152:155], v[60:63], v[172:175]\n"
"s_waitcnt lgkmcnt(8)\n"
"v_mfma_f32_16x16x32_bf16 v[148:151], v[144:147], v[64:67], v[148:151]\n"
"ds_read_b64_tr_b16 v[152:153], v198 offset:30720\n"
"ds_read_b64_tr_b16 v[154:155], v199 offset:30720\n"
"v_mfma_f32_16x16x32_bf16 v[172:175], v[144:147], v[80:83], v[172:175]\n"
"v_mfma_f32_16x16x32_bf16 v[148:151], v[156:159], v[68:71], v[148:151]\n"
"ds_read_b64_tr_b16 v[144:145], v198 offset:31744\n"
"ds_read_b64_tr_b16 v[146:147], v199 offset:31744\n"
"s_waitcnt vmcnt(0)\n"
"v_lshlrev_b32 v164, 4, v134\n"
"v_bfi_b32 v164, 15, v132, v164\n"
"v_mfma_f32_16x16x32_bf16 v[172:175], v[156:159], v[84:87], v[172:175]\n"
"v_cmp_ne_u32_sdwa vcc, v164, v217 src0_sel:BYTE_0 src1_sel:DWORD\n"
"s_nop 0\n"
"s_cbranch_vccnz .Lr11\n"
".Lb11:\n"
"v_lshlrev_b32 v164, 4, v135\n"
"s_waitcnt lgkmcnt(8)\n"
"v_mfma_f32_16x16x32_bf16 v[148:151], v[128:131], v[72:75], v[148:151]\n"
"ds_read_b64_tr_b16 v[156:157], v196 offset:512\n"
"ds_read_b64_tr_b16 v[158:159], v197 offset:512\n"
"v_bfi_b32 v164, 15, v133, v164\n"
"v_and_b32 v164, 0xff, v164\n"
"v_mfma_f32_16x16x32_bf16 v[172:175], v[128:131], v[88:91], v[172:175]\n"
"v_subrev_u32 v164, 0x7e, v164\n"
"v_ldexp_f32 v176, s82, v164\n"
"v_cvt_f32_i32_sdwa v129, sext(v132) dst_sel:DWORD dst_unused:UNUSED_PAD src0_sel:WORD_1\n"
"v_cvt_f32_i32_sdwa v131, sext(v133) dst_sel:DWORD dst_unused:UNUSED_PAD src0_sel:WORD_1\n"
"v_mfma_f32_16x16x32_bf16 v[148:151], v[160:163], v[76:79], v[148:151]\n"
"ds_read_b64_tr_b16 v[164:165], v196 offset:1536\n"
"ds_read_b64_tr_b16 v[166:167], v197 offset:1536\n"
"v_cvt_f32_i32_sdwa v169, sext(v134) dst_sel:DWORD dst_unused:UNUSED_PAD src0_sel:WORD_1\n"
"v_cvt_f32_i32_sdwa v171, sext(v135) dst_sel:DWORD dst_unused:UNUSED_PAD src0_sel:WORD_1\n"
"v_mfma_f32_16x16x32_bf16 v[172:175], v[160:163], v[92:95], v[172:175]\n"
"v_and_or_b32 v132, v132, -16, 8\n"
"v_and_or_b32 v133, v133, -16, 8\n"
"v_and_or_b32 v134, v134, -16, 8\n"
"s_waitcnt lgkmcnt(8)\n"
"v_mfma_f32_16x16x32_bf16 v[148:151], v[140:143], v[96:99], v[148:151]\n"
"ds_read_b64_tr_b16 v[160:161], v196 offset:2560\n"
"ds_read_b64_tr_b16 v[162:163], v197 offset:2560\n"
"v_and_or_b32 v135, v135, -16, 8\n"
"v_cvt_f32_i32_sdwa v128, sext(v132) dst_sel:DWORD dst_unused:UNUSED_PAD src0_sel:WORD_0\n"
"v_mfma_f32_16x16x32_bf16 v[172:175], v[140:143], v[112:115], v[172:175]\n"
"v_cvt_f32_i32_sdwa v130, sext(v133) dst_sel:DWORD dst_unused:UNUSED_PAD src0_sel:WORD_0\n"
"v_cvt_f32_i32_sdwa v168, sext(v134) dst_sel:DWORD dst_unused:UNUSED_PAD src0_sel:WORD_0\n"
"v_cvt_f32_i32_sdwa v170, sext(v135) dst_sel:DWORD dst_unused:UNUSED_PAD src0_sel:WORD_0\n"
"v_mul_f32 v128, v176, v128\n"
"v_mfma_f32_16x16x32_bf16 v[148:151], v[136:139], v[100:103], v[148:151]\n"
"ds_read_b64_tr_b16 v[132:133], v196 offset:3584\n"
"ds_read_b64_tr_b16 v[134:135], v197 offset:3584\n"
"v_mul_f32 v129, v176, v129\n"
"v_mul_f32 v130, v176, v130\n"
"v_mfma_f32_16x16x32_bf16 v[172:175], v[136:139], v[116:119], v[172:175]\n"
"v_mul_f32 v131, v176, v131\n"
"v_mul_f32 v168, v176, v168\n"
"v_mul_f32 v169, v176, v169\n"
"s_waitcnt lgkmcnt(8)\n"
"v_mfma_f32_16x16x32_bf16 v[148:151], v[152:155], v[104:107], v[148:151]\n"
"ds_read_b64_tr_b16 v[136:137], v196 offset:4608\n"
"ds_read_b64_tr_b16 v[138:139], v197 offset:4608\n"
"v_mul_f32 v170, v176, v170\n"
"v_mul_f32 v171, v176, v171\n"
"v_mfma_f32_16x16x32_bf16 v[172:175], v[152:155], v[120:123], v[172:175]\n"
"v_mfma_f32_16x16x32_bf16 v[148:151], v[144:147], v[108:111], v[148:151]\n"
"ds_read_b64_tr_b16 v[140:141], v196 offset:5632\n"
"ds_read_b64_tr_b16 v[142:143], v197 offset:5632\n"
"v_mfma_f32_16x16x32_bf16 v[172:175], v[144:147], v[124:127], v[172:175]\n"
"s_waitcnt lgkmcnt(8)\n"
"v_mfma_f32_16x16x32_bf16 v[128:131], v[156:159], v[4:7], v[128:131]\n"
"ds_read_b64_tr_b16 v[4:5], v196 offset:6656\n"
"ds_read_b64_tr_b16 v[6:7], v197 offset:6656\n"
"v_mfma_f32_16x16x32_bf16 v[168:171], v[156:159], v[24:27], v[168:171]\n"
"v_mfma_f32_16x16x32_bf16 v[128:131], v[164:167], v[8:11], v[128:131]\n"
"ds_read_b64_tr_b16 v[8:9], v196 offset:7680\n"
"ds_read_b64_tr_b16 v[10:11], v197 offset:7680\n"
"v_mfma_f32_16x16x32_bf16 v[168:171], v[164:167], v[28:31], v[168:171]\n"
"ds_write_b128 v194, v[148:151] offset:4160\n"
"s_waitcnt lgkmcnt(9)\n"
"v_mfma_f32_16x16x32_bf16 v[128:131], v[160:163], v[16:19], v[128:131]\n"
"ds_read_b64_tr_b16 v[16:17], v196 offset:8704\n"
"ds_read_b64_tr_b16 v[18:19], v197 offset:8704\n"
"v_mfma_f32_16x16x32_bf16 v[168:171], v[160:163], v[32:35], v[168:171]\n"
"ds_write_b128 v194, v[172:175] offset:5184\n"
"v_mfma_f32_16x16x32_bf16 v[128:131], v[132:135], v[20:23], v[128:131]\n"
"ds_read_b64_tr_b16 v[20:21], v196 offset:9728\n"
"ds_read_b64_tr_b16 v[22:23], v197 offset:9728\n"
"v_mfma_f32_16x16x32_bf16 v[168:171], v[132:135], v[40:43], v[168:171]\n"
"s_waitcnt lgkmcnt(10)\n"
"v_mfma_f32_16x16x32_bf16 v[128:131], v[136:139], v[0:3], v[128:131]\n"
"ds_read_b64_tr_b16 v[0:1], v196 offset:10752\n"
"ds_read_b64_tr_b16 v[2:3], v197 offset:10752\n"
"v_mfma_f32_16x16x32_bf16 v[168:171], v[136:139], v[48:51], v[168:171]\n"
"s_waitcnt lgkmcnt(4)\n"
"s_barrier\n"
"v_mfma_f32_16x16x32_bf16 v[128:131], v[140:143], v[12:15], v[128:131]\n"
"ds_read_b64_tr_b16 v[12:13], v196 offset:11776\n"
"ds_read_b64_tr_b16 v[14:15], v197 offset:11776\n"
"v_mfma_f32_16x16x32_bf16 v[168:171], v[140:143], v[52:55], v[168:171]\n"
"v_mfma_f32_16x16x32_bf16 v[128:131], v[4:7], v[36:39], v[128:131]\n"
"ds_read_b64_tr_b16 v[24:25], v196 offset:12800\n"
"ds_read_b64_tr_b16 v[26:27], v197 offset:12800\n"
"v_mfma_f32_16x16x32_bf16 v[168:171], v[4:7], v[56:59], v[168:171]\n"
"v_mfma_f32_16x16x32_bf16 v[128:131], v[8:11], v[44:47], v[128:131]\n"
"ds_read_b64_tr_b16 v[4:5], v196 offset:13824\n"
"ds_read_b64_tr_b16 v[6:7], v197 offset:13824\n"
"v_mfma_f32_16x16x32_bf16 v[168:171], v[8:11], v[60:63], v[168:171]\n"
"s_waitcnt lgkmcnt(8)\n"
"v_mfma_f32_16x16x32_bf16 v[128:131], v[16:19], v[64:67], v[128:131]\n"
"ds_read_b64_tr_b16 v[8:9], v196 offset:14848\n"
"ds_read_b64_tr_b16 v[10:11], v197 offset:14848\n"
"v_mfma_f32_16x16x32_bf16 v[168:171], v[16:19], v[80:83], v[168:171]\n"
"v_mfma_f32_16x16x32_bf16 v[128:131], v[20:23], v[68:71], v[128:131]\n"
"ds_read_b64_tr_b16 v[16:17], v196 offset:15872\n"
"ds_read_b64_tr_b16 v[18:19], v197 offset:15872\n"
"v_mfma_f32_16x16x32_bf16 v[168:171], v[20:23], v[84:87], v[168:171]\n"
"s_waitcnt lgkmcnt(8)\n"
"v_mfma_f32_16x16x32_bf16 v[128:131], v[0:3], v[72:75], v[128:131]\n"
"v_mfma_f32_16x16x32_bf16 v[168:171], v[0:3], v[88:91], v[168:171]\n"
"v_mfma_f32_16x16x32_bf16 v[128:131], v[12:15], v[76:79], v[128:131]\n"
"v_mfma_f32_16x16x32_bf16 v[168:171], v[12:15], v[92:95], v[168:171]\n"
"s_waitcnt lgkmcnt(4)\n"
"v_mfma_f32_16x16x32_bf16 v[128:131], v[24:27], v[96:99], v[128:131]\n"
"v_mfma_f32_16x16x32_bf16 v[168:171], v[24:27], v[112:115], v[168:171]\n"
"v_mfma_f32_16x16x32_bf16 v[128:131], v[4:7], v[100:103], v[128:131]\n"
"v_mfma_f32_16x16x32_bf16 v[168:171], v[4:7], v[116:119], v[168:171]\n"
"s_waitcnt lgkmcnt(0)\n"
"v_mfma_f32_16x16x32_bf16 v[128:131], v[8:11], v[104:107], v[128:131]\n"
"v_mfma_f32_16x16x32_bf16 v[168:171], v[8:11], v[120:123], v[168:171]\n"
"v_mfma_f32_16x16x32_bf16 v[128:131], v[16:19], v[108:111], v[128:131]\n"
"v_mfma_f32_16x16x32_bf16 v[168:171], v[16:19], v[124:127], v[168:171]\n"
"s_nop 6\n"
"ds_write_b128 v194, v[128:131] offset:6208\n"
"ds_write_b128 v194, v[168:171] offset:7232\n"
"s_waitcnt lgkmcnt(0)\n"
"s_barrier\n"
"s_endpgm\n"
".Lr4:\n"
"s_mov_b32 s12, 0x40000\n"
".Ll4:\n"
"s_sleep 8\n"
"s_nop 7\n"
"global_load_dwordx4 v[128:131], v195, s[26:27] sc1\n"
"global_load_dwordx4 v[132:135], v195, s[26:27] offset:1024 sc1\n"
"s_waitcnt vmcnt(0)\n"
"v_xor_b32 v153, s81, v129\n"
"v_bitop3_b32 v153, v153, v131, s81 bitop3:0xf6\n"
"v_bitop3_b32 v153, v153, v133, s81 bitop3:0xf6\n"
"v_bitop3_b32 v153, v153, v135, s81 bitop3:0xf6\n"
"v_lshrrev_b32 v138, 16, v153\n"
"v_bitop3_b32 v138, v138, v128, s10 bitop3:0xf6\n"
"v_bitop3_b32 v138, v138, v130, s10 bitop3:0xf6\n"
"v_bitop3_b32 v138, v138, v132, s10 bitop3:0xf6\n"
"v_bitop3_b32 v138, v138, v134, s10 bitop3:0xf6\n"
"v_cmp_ne_u32 vcc, 0, v138\n"
"s_nop 0\n"
"s_cbranch_vccz .Lp4\n"
"s_sub_u32 s12, s12, 1\n"
"s_cbranch_scc0 .Ll4\n"
".Lp4:\n"
"s_nop 7\n"
"s_nop 7\n"
"s_branch .Lb4\n"
".Lr5:\n"
"s_mov_b32 s12, 0x40000\n"
".Ll5:\n"
"s_sleep 3\n"
"s_nop 7\n"
"buffer_load_dwordx2 v[54:55], v219, s[76:79], 0 offen offset:3584 sc1\n"
"buffer_load_dwordx2 v[52:53], v219, s[76:79], 0 offen offset:3072 sc1\n"
"buffer_load_dwordx2 v[50:51], v219, s[76:79], 0 offen offset:2560 sc1\n"
"buffer_load_dwordx2 v[48:49], v219, s[76:79], 0 offen offset:2048 sc1\n"
"s_waitcnt vmcnt(0)\n"
"v_xor_b32 v54, v54, v227\n"
"v_xor_b32 v55, v55, v227\n"
"v_xor_b32 v52, v52, v226\n"
"v_xor_b32 v53, v53, v226\n"
"v_xor_b32 v50, v50, v225\n"
"v_xor_b32 v51, v51, v225\n"
"v_xor_b32 v48, v48, v224\n"
"v_xor_b32 v49, v49, v224\n"
"v_or3_b32 v44, v54, v55, v52\n"
"v_or3_b32 v44, v44, v53, v50\n"
"v_or3_b32 v44, v44, v51, v48\n"
"v_bitop3_b32 v44, v44, v49, s96 bitop3:0xa8\n"
"v_cmp_ne_u32 vcc, 0, v44\n"
"s_nop 0\n"
"s_cbranch_vccz .Lp5\n"
"s_sub_u32 s12, s12, 1\n"
"s_cbranch_scc0 .Ll5\n"
".Lp5:\n"
"s_nop 7\n"
"s_nop 7\n"
"s_branch .Lb5\n"
".Lr6:\n"
"s_mov_b32 s12, 0x40000\n"
".Ll6:\n"
"s_sleep 3\n"
"s_nop 7\n"
"buffer_load_dwordx2 v[52:53], v219, s[76:79], 0 offen offset:1536 sc1\n"
"buffer_load_dwordx2 v[50:51], v219, s[76:79], 0 offen offset:1024 sc1\n"
"buffer_load_dwordx2 v[46:47], v219, s[76:79], 0 offen offset:512 sc1\n"
"buffer_load_dwordx2 v[44:45], v219, s[76:79], 0 offen offset:0 sc1\n"
"s_waitcnt vmcnt(0)\n"
"v_xor_b32 v52, v52, v218\n"
"v_xor_b32 v53, v53, v218\n"
"v_xor_b32 v50, v50, v218\n"
"v_xor_b32 v51, v51, v218\n"
"v_xor_b32 v46, v46, v218\n"
"v_xor_b32 v47, v47, v218\n"
"v_xor_b32 v44, v44, v218\n"
"v_xor_b32 v45, v45, v218\n"
"v_or3_b32 v72, v52, v53, v50\n"
"v_or3_b32 v72, v72, v51, v46\n"
"v_or3_b32 v72, v72, v47, v44\n"
"v_bitop3_b32 v72, v72, v45, s96 bitop3:0xa8\n"
"v_cmp_ne_u32 vcc, 0, v72\n"
"s_nop 0\n"
"s_cbranch_vccz .Lp6\n"
"s_sub_u32 s12, s12, 1\n"
"s_cbranch_scc0 .Ll6\n"
".Lp6:\n"
"s_nop 7\n"
"s_nop 7\n"
"s_branch .Lb6\n"
".Lr7:\n"
"s_mov_b32 s12, 0x40000\n"
".Ll7:\n"
"s_sleep 2\n"
"s_nop 7\n"
"global_load_dwordx2 v[96:97], v205, s[90:91] sc1\n"
"s_waitcnt vmcnt(0)\n"
"v_cmp_ne_u64 vcc, s[10:11], v[96:97]\n"
"s_nop 0\n"
"s_cbranch_vccz .Lp7\n"
"s_sub_u32 s12, s12, 1\n"
"s_cbranch_scc0 .Ll7\n"
".Lp7:\n"
"s_nop 7\n"
"s_nop 7\n"
"s_branch .Lb7\n"
".Lr12:\n"
"s_mov_b32 s12, 0x40000\n"
".Ll12:\n"
"s_sleep 2\n"
"s_nop 7\n"
"global_load_dwordx2 v[164:165], v205, s[90:91] sc1\n"
"s_waitcnt vmcnt(0)\n"
"v_cmp_ne_u64 vcc, s[10:11], v[164:165]\n"
"s_nop 0\n"
"s_cbranch_vccz .Lp12\n"
"s_sub_u32 s12, s12, 1\n"
"s_cbranch_scc0 .Ll12\n"
".Lp12:\n"
"s_nop 7\n"
"s_nop 7\n"
"s_branch .Lb12\n"
".Lr13:\n"
"s_mov_b32 s12, 0x40000\n"
".Ll13:\n"
"s_sleep 6\n"
"s_nop 7\n"
"global_load_dwordx2 v[164:165], v204, s[66:67] sc1\n"
"s_waitcnt vmcnt(0)\n"
"v_xor_b32 v156, s11, v165\n"
"v_and_b32 v156, 0xffffdfff, v156\n"
"v_bitop3_b32 v156, v156, v164, s10 bitop3:0xf6\n"
"v_cmp_ne_u32 vcc, 0, v156\n"
"s_nop 0\n"
"s_cbranch_vccz .Lp13\n"
"s_sub_u32 s12, s12, 1\n"
"s_cbranch_scc0 .Ll13\n"
".Lp13:\n"
"s_nop 7\n"
"s_nop 7\n"
"s_branch .Lb13\n"
".Lr8:\n"
"s_mov_b32 s12, 0x40000\n"
".Ll8:\n"
"s_sleep 4\n"
"s_nop 7\n"
"global_load_dwordx4 v[152:155], v195, s[16:17] sc1\n"
"s_waitcnt vmcnt(0)\n"
"v_lshlrev_b32 v164, 4, v154\n"
"v_bfi_b32 v164, 15, v152, v164\n"
"v_cmp_ne_u32_sdwa vcc, v164, v217 src0_sel:BYTE_0 src1_sel:DWORD\n"
"s_nop 0\n"
"s_cbranch_vccz .Lp8\n"
"s_sub_u32 s12, s12, 1\n"
"s_cbranch_scc0 .Ll8\n"
".Lp8:\n"
"s_nop 7\n"
"s_nop 7\n"
"s_branch .Lb8\n"
".Lr9:\n"
"s_mov_b32 s12, 0x40000\n"
".Ll9:\n"
"s_sleep 4\n"
"s_nop 7\n"
"global_load_dwordx4 v[152:155], v195, s[16:17] offset:1024 sc1\n"
"s_waitcnt vmcnt(0)\n"
"v_lshlrev_b32 v164, 4, v154\n"
"v_bfi_b32 v164, 15, v152, v164\n"
"v_cmp_ne_u32_sdwa vcc, v164, v217 src0_sel:BYTE_0 src1_sel:DWORD\n"
"s_nop 0\n"
"s_cbranch_vccz .Lp9\n"
"s_sub_u32 s12, s12, 1\n"
"s_cbranch_scc0 .Ll9\n"
".Lp9:\n"
"s_nop 7\n"
"s_nop 7\n"
"s_branch .Lb9\n"
".Lr10:\n"
"s_mov_b32 s12, 0x40000\n"
".Ll10:\n"
"s_sleep 4\n"
"s_nop 7\n"
"global_load_dwordx4 v[128:131], v195, s[16:17] offset:2048 sc1\n"
"s_waitcnt vmcnt(0)\n"
"v_lshlrev_b32 v164, 4, v130\n"
"v_bfi_b32 v164, 15, v128, v164\n"
"v_cmp_ne_u32_sdwa vcc, v164, v217 src0_sel:BYTE_0 src1_sel:DWORD\n"
"s_nop 0\n"
"s_cbranch_vccz .Lp10\n"
"s_sub_u32 s12, s12, 1\n"
"s_cbranch_scc0 .Ll10\n"
".Lp10:\n"
"s_nop 7\n"
"s_nop 7\n"
"s_branch .Lb10\n"
".Lr11:\n"
"s_mov_b32 s12, 0x40000\n"
".Ll11:\n"
"s_sleep 4\n"
"s_nop 7\n"
"global_load_dwordx4 v[132:135], v195, s[16:17] offset:3072 sc1\n"
"s_waitcnt vmcnt(0)\n"
"v_lshlrev_b32 v164, 4, v134\n"
"v_bfi_b32 v164, 15, v132, v164\n"
"v_cmp_ne_u32_sdwa vcc, v164, v217 src0_sel:BYTE_0 src1_sel:DWORD\n"
"s_nop 0\n"
"s_cbranch_vccz .Lp11\n"
"s_sub_u32 s12, s12, 1\n"
"s_cbranch_scc0 .Ll11\n"
".Lp11:\n"
"s_nop 7\n"
"s_nop 7\n"
"s_branch .Lb11\n"
".LC:\n"
        :: "{s[0:1]}"(__builtin_amdgcn_kernarg_segment_ptr()), "{s2}"(b), "{s3}"(wid)
        : "memory", "scc");
    unsigned dlat;
    {
        typedef const __attribute__((address_space(1))) u16* gp;
        const __attribute__((address_space(4))) gp* ka = (const __attribute__((address_space(4))) gp*)__builtin_amdgcn_kernarg_segment_ptr();
        asm volatile("" : "+s"(ka));
        const gp lre = ka[14], lim = ka[15], logdt = ka[16];
        const int q = lane >> 4, p = ((b & 1) * 8 + (b >> 5)) * 64 + wid * 16 + (lane & 15);
        unsigned ldt, lr, li;
        {
            const u64 wa = (((u64)ka[7] + 0x1fffffull) & ~0x1fffffull) + (54525952 + (unsigned)((b >> 4) * 16384 + ((b & 15) * 4 + wid) * 128 + (wid & 1) * 8192));
            const u64 wb = wa ^ 8192ull;
            u64 t0, t1, t2; unsigned junk;
            asm volatile("global_load_ushort %0, %7, %8\n\tglobal_load_ushort %1, %7, %9\n\tglobal_load_ushort %2, %7, %10\n\t"
                         "s_memtime %3\n\ts_waitcnt lgkmcnt(0)\n\ts_load_dword %6, %11, 0x0\n\ts_waitcnt lgkmcnt(0)\n\ts_memtime %4\n\ts_waitcnt lgkmcnt(0)\n\t"
                         "s_load_dword %6, %12, 0x0\n\ts_waitcnt lgkmcnt(0)\n\ts_memtime %5\n\ts_waitcnt vmcnt(0) lgkmcnt(0)"
                         : "=&v"(ldt), "=&v"(lr), "=&v"(li), "=&s"(t0), "=&s"(t1), "=&s"(t2), "=&s"(junk)
                         : "v"(p * 2), "s"(logdt), "s"(lre), "s"(lim), "s"(wa), "s"(wb) : "memory");
            const int d = 2 * (int)(unsigned)t1 - (int)(unsigned)t0 - (int)(unsigned)t2;
            dlat = (unsigned)min(max((wid & 1) ? -d : d, -2000), 2000) & 0xffffu;
        }
        const float dt = rbf(expf(bf2f(ldt)));
        const float re = bf2f(lr), im = bf2f(li);
        const float ea = expf(dt * re);
        float sn, cs;
        sincosf(dt * im, &sn, &cs);
        const cf L1 = {ea * cs, ea * sn}, one = {1.f, 0.f};
        const float nr = L1.r - 1.0f, ni = L1.i, den = re * re + im * im;
        const cf cc = {(nr * re + ni * im) / den, (ni * re - nr * im) / den}, g = cmul(cc, L1);
        cf w = L1;
        for (int i = 0; i < 4; ++i) w = cmul(w, w);
        const cf w16 = w, w32 = cmul(w, w), w64 = cmul(w32, w32), w128 = cmul(w64, w64), w256 = cmul(w128, w128);
        const cf wq = cmul((q & 1) ? w16 : one, (q & 2) ? w32 : one), qq = cmul((q & 1) ? w64 : one, (q & 2) ? w128 : one);
        const cf al = q == 3 ? w16 : cf{q == 2 ? 1.f : 0.f, 0.f};
        lds_f32x4* tp = (lds_f32x4*)(size_t)(unsigned)(149888 + wid * 2816 + lane * 16);
        tp[0] = f32x4{al.r, al.i, (float)(q & 1), 0.f};
        tp[64] = f32x4{wq.r, wq.i, qq.r, qq.i};
        if (q < 3) tp[128] = q == 0 ? f32x4{L1.r, L1.i, cc.r, cc.i} : q == 1 ? f32x4{w16.r, w16.i, w32.r, w32.i} : f32x4{g.r, g.i, w256.r, w256.i};
    }
    asm volatile(
"s_load_dwordx16 s[16:31], %0, 0\n"
"s_load_dwordx16 s[32:47], %0, 64\n"
"s_and_b32 s48, %1, 1\n"
"s_bfe_u32 s49, %1, 0x20001\n"
"s_bfe_u32 s13, %1, 0x20003\n"
"s_lshr_b32 s50, %1, 5\n"
"s_lshl2_add_u32 s51, s49, s13\n"
"s_lshl3_add_u32 s52, s48, s50\n"
"s_lshl1_add_u32 s82, s48, s49\n"
"s_lshl_b32 s82, s82, 9\n"
"s_and_b32 s59, s50, 1\n"
"s_lshr_b32 s53, %1, 3\n"
"s_and_b32 s54, %1, 7\n"
"s_and_b32 s15, %2, 3\n"
"s_lshl_b32 s55, s15, 11\n"
"s_lshl_b32 s56, s15, 12\n"
"s_mul_i32 s57, s15, 0x2080\n"
"s_add_u32 s57, s57, 0x18000\n"
"s_lshl_b32 s58, s54, 5\n"
"s_add_u32 s58, s58, s53\n"
"s_lshl4_add_u32 s80, s51, s15\n"
"s_lshl_b32 s80, s80, 13\n"
"v_mbcnt_lo_u32_b32 v227, -1, 0\n"
"v_mbcnt_hi_u32_b32 v227, -1, v227\n"
"v_bfe_u32 v0, v227, 1, 3\n"
"v_lshrrev_b32 v218, 4, v227\n"
"v_xor_b32 v0, v0, v218\n"
"v_and_b32 v218, 15, v227\n"
"v_lshlrev_b32 v218, 7, v218\n"
"v_lshl_or_b32 v218, v0, 4, v218\n"
"s_add_u32 s13, s56, 0x2000\n"
"v_add_u32 v182, s13, v218\n"
"v_xor_b32 v181, 64, v182\n"
"v_xor_b32 v219, 64, v218\n"
"v_add_u32 v180, 0x10000, v182\n"
"v_add_u32 v188, 0x10000, v181\n"
"v_add_u32 v235, 0x10000, v218\n"
"v_add_u32 v234, 0x10000, v219\n"
"v_lshrrev_b32 v52, 3, v227\n"
"v_lshlrev_b32 v52, 5, v52\n"
"s_lshl2_add_u32 s14, s54, s15\n"
"s_lshl_b32 s13, s14, 8\n"
"s_waitcnt lgkmcnt(0)\n"
"s_add_u32 s60, s40, s13\n"
"s_addc_u32 s61, s41, 0\n"
"global_load_dwordx4 v[0:3], v52, s[60:61]\n"
"global_load_dwordx4 v[4:7], v52, s[60:61] offset:16\n"
"s_add_u32 s30, s30, 0x1fffff\n"
"s_addc_u32 s31, s31, 0\n"
"s_and_b32 s30, s30, 0xffe00000\n"
"s_load_dwordx2 s[10:11], s[30:31], 0\n"
"s_lshl_b32 s14, s14, 17\n"
"s_lshl_b32 s13, s53, 7\n"
"s_sub_u32 s14, s14, s13\n"
"s_add_u32 s14, s14, 0x2400f80\n"
"s_add_u32 s60, s30, s14\n"
"s_addc_u32 s61, s31, 0\n"
"s_waitcnt lgkmcnt(0)\n"
"s_add_u32 s10, s10, 1\n"
"s_addc_u32 s11, s11, 0\n"
"s_barrier\n"
"ds_read_b128 v[8:11], v182\n"
"ds_read_b128 v[12:15], v182 offset:2048\n"
"ds_read_b128 v[16:19], v218\n"
"ds_read_b128 v[20:23], v218 offset:2048\n"
"ds_read_b128 v[24:27], v218 offset:4096\n"
"ds_read_b128 v[28:31], v218 offset:6144\n"
"s_waitcnt lgkmcnt(3)\n"
"v_mfma_f32_16x16x32_bf16 v[32:35], v[16:19], v[8:11], 0\n"
"ds_read_b128 v[36:39], v181\n"
"v_and_b32 v93, 7, v227\n"
"v_mfma_f32_16x16x32_bf16 v[40:43], v[16:19], v[12:15], 0\n"
"ds_read_b128 v[16:19], v181 offset:2048\n"
"v_sub_u32 v94, 7, v93\n"
"s_waitcnt lgkmcnt(4)\n"
"v_mfma_f32_16x16x32_bf16 v[44:47], v[20:23], v[8:11], 0\n"
"ds_read_b128 v[48:51], v219\n"
"v_lshlrev_b32 v178, 9, v52\n"
"v_mfma_f32_16x16x32_bf16 v[52:55], v[20:23], v[12:15], 0\n"
"ds_read_b128 v[20:23], v219 offset:2048\n"
"v_lshl_or_b32 v178, v93, 4, v178\n"
"s_waitcnt lgkmcnt(5)\n"
"v_mfma_f32_16x16x32_bf16 v[56:59], v[24:27], v[8:11], 0\n"
"ds_read_b128 v[60:63], v219 offset:4096\n"
"v_lshrrev_b32 v84, 4, v227\n"
"v_mfma_f32_16x16x32_bf16 v[64:67], v[24:27], v[12:15], 0\n"
"ds_read_b128 v[24:27], v219 offset:6144\n"
"s_lshl_b32 s13, s15, 2\n"
"v_add_u32 v84, s13, v84\n"
"s_waitcnt lgkmcnt(6)\n"
"v_mfma_f32_16x16x32_bf16 v[68:71], v[28:31], v[8:11], 0\n"
"v_lshlrev_b32 v84, 4, v84\n"
"v_mfma_f32_16x16x32_bf16 v[72:75], v[28:31], v[12:15], 0\n"
"v_and_or_b32 v84, v227, 8, v84\n"
"s_waitcnt lgkmcnt(0)\n"
"s_barrier\n"
"v_mfma_f32_16x16x32_bf16 v[32:35], v[48:51], v[36:39], v[32:35]\n"
"ds_read_b128 v[28:31], v182 offset:24576\n"
"v_lshlrev_b32 v93, 5, v94\n"
"v_mfma_f32_16x16x32_bf16 v[40:43], v[48:51], v[16:19], v[40:43]\n"
"ds_read_b128 v[48:51], v182 offset:26624\n"
"v_xor_b32 v100, v84, v93\n"
"v_mfma_f32_16x16x32_bf16 v[44:47], v[20:23], v[36:39], v[44:47]\n"
"ds_read_b128 v[76:79], v218 offset:24576\n"
"v_lshl_add_u32 v100, v94, 11, v100\n"
"v_mfma_f32_16x16x32_bf16 v[52:55], v[20:23], v[16:19], v[52:55]\n"
"ds_read_b128 v[20:23], v218 offset:26624\n"
"v_add_u32 v100, 0x18000, v100\n"
"v_mfma_f32_16x16x32_bf16 v[56:59], v[60:63], v[36:39], v[56:59]\n"
"ds_read_b128 v[80:83], v218 offset:28672\n"
"v_add_u32 v93, 0xe0, v93\n"
"v_mfma_f32_16x16x32_bf16 v[64:67], v[60:63], v[16:19], v[64:67]\n"
"ds_read_b128 v[60:63], v218 offset:30720\n"
"v_mfma_f32_16x16x32_bf16 v[68:71], v[24:27], v[36:39], v[68:71]\n"
"v_and_b32 v93, 0xe0, v93\n"
"v_mfma_f32_16x16x32_bf16 v[72:75], v[24:27], v[16:19], v[72:75]\n"
"v_xor_b32 v92, v84, v93\n"
"s_waitcnt lgkmcnt(3)\n"
"v_mfma_f32_16x16x32_bf16 v[32:35], v[76:79], v[28:31], v[32:35]\n"
"ds_read_b128 v[24:27], v181 offset:24576\n"
"v_lshlrev_b32 v93, 11, v94\n"
"v_mfma_f32_16x16x32_bf16 v[40:43], v[76:79], v[48:51], v[40:43]\n"
"ds_read_b128 v[76:79], v181 offset:26624\n"
"v_cmp_eq_u32 vcc, 0, v94\n"
"s_waitcnt lgkmcnt(4)\n"
"v_mfma_f32_16x16x32_bf16 v[44:47], v[20:23], v[28:31], v[44:47]\n"
"ds_read_b128 v[84:87], v219 offset:24576\n"
"v_add_u32 v93, 0x17d00, v93\n"
"v_mfma_f32_16x16x32_bf16 v[52:55], v[20:23], v[48:51], v[52:55]\n"
"ds_read_b128 v[20:23], v219 offset:26624\n"
"v_mov_b32 v95, 0x20200\n"
"s_waitcnt lgkmcnt(5)\n"
"v_mfma_f32_16x16x32_bf16 v[56:59], v[80:83], v[28:31], v[56:59]\n"
"ds_read_b128 v[88:91], v219 offset:28672\n"
"v_cndmask_b32 v93, v93, v95, vcc\n"
"v_mfma_f32_16x16x32_bf16 v[64:67], v[80:83], v[48:51], v[64:67]\n"
"ds_read_b128 v[80:83], v219 offset:30720\n"
"v_or_b32 v95, s53, v94\n"
"s_waitcnt lgkmcnt(6)\n"
"v_mfma_f32_16x16x32_bf16 v[68:71], v[60:63], v[28:31], v[68:71]\n"
"v_cmp_ne_u32 vcc, 0, v95\n"
"v_mfma_f32_16x16x32_bf16 v[72:75], v[60:63], v[48:51], v[72:75]\n"
"v_add_u32 v92, v92, v93\n"
"s_waitcnt lgkmcnt(0)\n"
"s_barrier\n"
"v_mfma_f32_16x16x32_bf16 v[32:35], v[84:87], v[24:27], v[32:35]\n"
"ds_read_b128 v[28:31], v182 offset:49152\n"
"v_lshlrev_b32 v226, 4, v227\n"
"v_mfma_f32_16x16x32_bf16 v[40:43], v[84:87], v[76:79], v[40:43]\n"
"ds_read_b128 v[48:51], v182 offset:51200\n"
"v_cndmask_b32_e64 v108, 0, -1, vcc\n"
"v_mfma_f32_16x16x32_bf16 v[44:47], v[20:23], v[24:27], v[44:47]\n"
"ds_read_b128 v[60:63], v218 offset:49152\n"
"v_mfma_f32_16x16x32_bf16 v[52:55], v[20:23], v[76:79], v[52:55]\n"
"ds_read_b128 v[20:23], v218 offset:51200\n"
"v_mov_b32 v224, s10\n"
"v_mfma_f32_16x16x32_bf16 v[56:59], v[88:91], v[24:27], v[56:59]\n"
"ds_read_b128 v[84:87], v218 offset:53248\n"
"v_mov_b32 v225, s11\n"
"v_mfma_f32_16x16x32_bf16 v[64:67], v[88:91], v[76:79], v[64:67]\n"
"ds_read_b128 v[88:91], v218 offset:55296\n"
"s_lshl2_add_u32 s13, s58, s15\n"
"s_lshl3_add_u32 s14, s13, 0x1000\n"
"v_mov_b32 v179, s14\n"
"s_lshl_b32 s13, s13, 1\n"
"v_mov_b32 v223, s13\n"
"v_lshlrev_b32 v183, 11, v223\n"
"global_load_ushort v223, v223, s[42:43]\n"
"global_load_ushort v183, v183, s[28:29]\n"
"v_mfma_f32_16x16x32_bf16 v[68:71], v[80:83], v[24:27], v[68:71]\n"
"v_lshrrev_b32 v93, 4, v227\n"
"v_mfma_f32_16x16x32_bf16 v[72:75], v[80:83], v[76:79], v[72:75]\n"
"v_and_b32 v217, 0xf0, v226\n"
"s_waitcnt lgkmcnt(3)\n"
"v_mfma_f32_16x16x32_bf16 v[32:35], v[60:63], v[28:31], v[32:35]\n"
"ds_read_b128 v[24:27], v181 offset:49152\n"
"v_lshrrev_b32 v228, 1, v217\n"
"v_mfma_f32_16x16x32_bf16 v[40:43], v[60:63], v[48:51], v[40:43]\n"
"ds_read_b128 v[60:63], v181 offset:51200\n"
"s_mul_i32 s13, s15, 0xb00\n"
"s_add_u32 s13, s13, 0x24980\n"
"v_add_u32 v221, s13, v226\n"
"s_add_u32 s13, s13, 0x800\n"
"v_add_u32 v222, s13, v217\n"
"s_waitcnt lgkmcnt(4)\n"
"v_mfma_f32_16x16x32_bf16 v[44:47], v[20:23], v[28:31], v[44:47]\n"
"ds_read_b128 v[76:79], v219 offset:49152\n"
"v_lshl_or_b32 v217, v93, 16, v217\n"
"v_mfma_f32_16x16x32_bf16 v[52:55], v[20:23], v[48:51], v[52:55]\n"
"ds_read_b128 v[20:23], v219 offset:51200\n"
"v_xor_b32 v216, 64, v217\n"
"s_waitcnt lgkmcnt(5)\n"
"v_mfma_f32_16x16x32_bf16 v[56:59], v[84:87], v[28:31], v[56:59]\n"
"ds_read_b128 v[80:83], v219 offset:53248\n"
"s_mov_b32 s96, 0xff\n"
"s_and_b32 s13, s10, 0xff\n"
"v_mov_b32 v236, s13\n"
"v_mfma_f32_16x16x32_bf16 v[64:67], v[84:87], v[48:51], v[64:67]\n"
"ds_read_b128 v[84:87], v219 offset:55296\n"
"s_sub_u32 s13, 16, s51\n"
"v_add_u32 v184, s13, v93\n"
"s_waitcnt lgkmcnt(6)\n"
"v_mfma_f32_16x16x32_bf16 v[68:71], v[88:91], v[28:31], v[68:71]\n"
"v_lshl_or_b32 v220, v184, 7, v228\n"
"v_mfma_f32_16x16x32_bf16 v[72:75], v[88:91], v[48:51], v[72:75]\n"
"s_waitcnt lgkmcnt(0)\n"
"s_barrier\n"
"v_mfma_f32_16x16x32_bf16 v[32:35], v[76:79], v[24:27], v[32:35]\n"
"ds_read_b128 v[88:91], v180 offset:8192\n"
"ds_read_b64 v[170:171], v92\n"
"ds_read_b64 v[140:141], v92 offset:256\n"
"ds_read_b64 v[142:143], v92 offset:512\n"
"v_mfma_f32_16x16x32_bf16 v[40:43], v[76:79], v[60:63], v[40:43]\n"
"ds_read_b128 v[76:79], v180 offset:10240\n"
"ds_read_b64 v[172:173], v100\n"
"ds_read_b64 v[144:145], v100 offset:256\n"
"v_mfma_f32_16x16x32_bf16 v[44:47], v[20:23], v[24:27], v[44:47]\n"
"ds_read_b128 v[92:95], v235 offset:8192\n"
"ds_read_b64 v[174:175], v100 offset:512\n"
"ds_read_b64 v[146:147], v100 offset:768\n"
"ds_read_b64 v[164:165], v100 offset:1024\n"
"v_mfma_f32_16x16x32_bf16 v[52:55], v[20:23], v[60:63], v[52:55]\n"
"ds_read_b128 v[20:23], v235 offset:10240\n"
"ds_read_b64 v[166:167], v100 offset:1280\n"
"ds_read_b64 v[168:169], v100 offset:1536\n"
"v_mfma_f32_16x16x32_bf16 v[56:59], v[80:83], v[24:27], v[56:59]\n"
"ds_read_b128 v[96:99], v235 offset:12288\n"
"ds_read_b64 v[176:177], v100 offset:1792\n"
"ds_read_b64 v[154:155], v100 offset:16384\n"
"v_mfma_f32_16x16x32_bf16 v[64:67], v[80:83], v[60:63], v[64:67]\n"
"ds_read_b128 v[80:83], v235 offset:14336\n"
"ds_read_b64 v[162:163], v100 offset:16640\n"
"ds_read_b64 v[156:157], v100 offset:16896\n"
"ds_read_b64 v[158:159], v100 offset:17152\n"
"v_mfma_f32_16x16x32_bf16 v[68:71], v[84:87], v[24:27], v[68:71]\n"
"ds_read_b64 v[148:149], v100 offset:17408\n"
"ds_read_b64 v[150:151], v100 offset:17664\n"
"v_mfma_f32_16x16x32_bf16 v[72:75], v[84:87], v[60:63], v[72:75]\n"
"ds_read_b64 v[160:161], v100 offset:17920\n"
"ds_read_b64 v[152:153], v100 offset:18176\n"
"s_waitcnt lgkmcnt(15)\n"
"v_mfma_f32_16x16x32_bf16 v[32:35], v[92:95], v[88:91], v[32:35]\n"
"ds_read_b128 v[84:87], v188 offset:8192\n"
"v_mfma_f32_16x16x32_bf16 v[40:43], v[92:95], v[76:79], v[40:43]\n"
"ds_read_b128 v[92:95], v188 offset:10240\n"
"s_waitcnt lgkmcnt(15)\n"
"v_mfma_f32_16x16x32_bf16 v[44:47], v[20:23], v[88:91], v[44:47]\n"
"ds_read_b128 v[100:103], v234 offset:8192\n"
"v_and_b32 v170, v170, v108\n"
"v_and_b32 v171, v171, v108\n"
"v_and_b32 v140, v140, v108\n"
"v_mfma_f32_16x16x32_bf16 v[52:55], v[20:23], v[76:79], v[52:55]\n"
"ds_read_b128 v[20:23], v234 offset:10240\n"
"v_and_b32 v141, v141, v108\n"
"v_and_b32 v142, v142, v108\n"
"s_waitcnt lgkmcnt(14)\n"
"v_mfma_f32_16x16x32_bf16 v[56:59], v[96:99], v[88:91], v[56:59]\n"
"ds_read_b128 v[104:107], v234 offset:12288\n"
"v_and_b32 v143, v143, v108\n"
"s_waitcnt vmcnt(3)\n"
"v_lshlrev_b32 v108, 16, v0\n"
"v_and_b32 v109, 0xffff0000, v0\n"
"v_mfma_f32_16x16x32_bf16 v[64:67], v[96:99], v[76:79], v[64:67]\n"
"ds_read_b128 v[96:99], v234 offset:14336\n"
"v_lshlrev_b32 v118, 16, v1\n"
"v_and_b32 v128, 0xffff0000, v1\n"
"s_waitcnt lgkmcnt(13)\n"
"v_mfma_f32_16x16x32_bf16 v[68:71], v[80:83], v[88:91], v[68:71]\n"
"v_lshlrev_b32 v121, 16, v170\n"
"v_lshlrev_b32 v129, 16, v140\n"
"v_lshlrev_b32 v119, 16, v142\n"
"v_mfma_f32_16x16x32_bf16 v[72:75], v[80:83], v[76:79], v[72:75]\n"
"v_lshlrev_b32 v110, 16, v172\n"
"v_lshlrev_b32 v111, 16, v144\n"
"s_waitcnt lgkmcnt(0)\n"
"s_barrier\n"
"v_mfma_f32_16x16x32_bf16 v[32:35], v[100:103], v[84:87], v[32:35]\n"
"ds_read_b128 v[76:79], v182\n"
"v_lshlrev_b32 v113, 16, v174\n"
"v_lshlrev_b32 v114, 16, v146\n"
"v_lshlrev_b32 v112, 16, v164\n"
"v_mfma_f32_16x16x32_bf16 v[40:43], v[100:103], v[92:95], v[40:43]\n"
"ds_read_b128 v[80:83], v182 offset:2048\n"
"v_lshlrev_b32 v115, 16, v166\n"
"v_lshlrev_b32 v116, 16, v168\n"
"v_mfma_f32_16x16x32_bf16 v[44:47], v[20:23], v[84:87], v[44:47]\n"
"ds_read_b128 v[88:91], v218\n"
"v_lshlrev_b32 v117, 16, v176\n"
"v_mul_f32 v120, v121, v128\n"
"v_mfma_f32_16x16x32_bf16 v[52:55], v[20:23], v[92:95], v[52:55]\n"
"ds_read_b128 v[20:23], v218 offset:2048\n"
"v_mul_f32 v121, v129, v128\n"
"v_mul_f32 v122, v119, v128\n"
"v_mul_f32 v123, v110, v128\n"
"v_mfma_f32_16x16x32_bf16 v[56:59], v[104:107], v[84:87], v[56:59]\n"
"ds_read_b128 v[100:103], v218 offset:4096\n"
"v_mul_f32 v124, v111, v128\n"
"v_mul_f32 v125, v113, v128\n"
"v_mfma_f32_16x16x32_bf16 v[64:67], v[104:107], v[92:95], v[64:67]\n"
"ds_read_b128 v[104:107], v218 offset:6144\n"
"v_mul_f32 v126, v114, v128\n"
"v_mul_f32 v127, v112, v128\n"
"v_fmac_f32 v120, v129, v118\n"
"v_mfma_f32_16x16x32_bf16 v[68:71], v[96:99], v[84:87], v[68:71]\n"
"v_fmac_f32 v121, v119, v118\n"
"v_fmac_f32 v122, v110, v118\n"
"v_mfma_f32_16x16x32_bf16 v[72:75], v[96:99], v[92:95], v[72:75]\n"
"v_fmac_f32 v123, v111, v118\n"
"v_fmac_f32 v124, v113, v118\n"
"v_fmac_f32 v125, v114, v118\n"
"s_waitcnt lgkmcnt(3)\n"
"v_mfma_f32_16x16x32_bf16 v[32:35], v[88:91], v[76:79], v[32:35]\n"
"ds_read_b128 v[84:87], v181\n"
"v_fmac_f32 v126, v112, v118\n"
"v_fmac_f32 v127, v115, v118\n"
"v_mfma_f32_16x16x32_bf16 v[40:43], v[88:91], v[80:83], v[40:43]\n"
"ds_read_b128 v[88:91], v181 offset:2048\n"
"v_fmac_f32 v120, v119, v109\n"
"v_fmac_f32 v121, v110, v109\n"
"v_fmac_f32 v122, v111, v109\n"
"s_waitcnt lgkmcnt(4)\n"
"v_mfma_f32_16x16x32_bf16 v[44:47], v[20:23], v[76:79], v[44:47]\n"
"ds_read_b128 v[92:95], v219\n"
"v_fmac_f32 v123, v113, v109\n"
"v_fmac_f32 v124, v114, v109\n"
"v_mfma_f32_16x16x32_bf16 v[52:55], v[20:23], v[80:83], v[52:55]\n"
"ds_read_b128 v[20:23], v219 offset:2048\n"
"v_fmac_f32 v125, v112, v109\n"
"v_fmac_f32 v126, v115, v109\n"
"s_waitcnt lgkmcnt(5)\n"
"v_mfma_f32_16x16x32_bf16 v[56:59], v[100:103], v[76:79], v[56:59]\n"
"ds_read_b128 v[96:99], v219 offset:4096\n"
"v_fmac_f32 v127, v116, v109\n"
"v_fmac_f32 v120, v110, v108\n"
"v_fmac_f32 v121, v111, v108\n"
"v_mfma_f32_16x16x32_bf16 v[64:67], v[100:103], v[80:83], v[64:67]\n"
"ds_read_b128 v[100:103], v219 offset:6144\n"
"v_fmac_f32 v122, v113, v108\n"
"v_fmac_f32 v123, v114, v108\n"
"s_waitcnt lgkmcnt(6)\n"
"v_mfma_f32_16x16x32_bf16 v[68:71], v[104:107], v[76:79], v[68:71]\n"
"v_fmac_f32 v124, v112, v108\n"
"v_fmac_f32 v125, v115, v108\n"
"v_fmac_f32 v126, v116, v108\n"
"v_mfma_f32_16x16x32_bf16 v[72:75], v[104:107], v[80:83], v[72:75]\n"
"v_fmac_f32 v127, v117, v108\n"
"v_lshlrev_b32 v112, 16, v154\n"
"s_waitcnt lgkmcnt(0)\n"
"s_barrier\n"
"v_mfma_f32_16x16x32_bf16 v[32:35], v[92:95], v[84:87], v[32:35]\n"
"ds_read_b128 v[104:107], v182 offset:24576\n"
"v_lshlrev_b32 v113, 16, v162\n"
"v_lshlrev_b32 v114, 16, v156\n"
"v_lshlrev_b32 v116, 16, v158\n"
"v_mfma_f32_16x16x32_bf16 v[40:43], v[92:95], v[88:91], v[40:43]\n"
"ds_read_b128 v[92:95], v182 offset:26624\n"
"v_lshlrev_b32 v118, 16, v148\n"
"v_lshlrev_b32 v117, 16, v150\n"
"v_mfma_f32_16x16x32_bf16 v[44:47], v[20:23], v[84:87], v[44:47]\n"
"ds_read_b128 v[108:111], v218 offset:24576\n"
"v_lshlrev_b32 v119, 16, v160\n"
"v_lshlrev_b32 v128, 16, v152\n"
"v_mfma_f32_16x16x32_bf16 v[52:55], v[20:23], v[88:91], v[52:55]\n"
"ds_read_b128 v[20:23], v218 offset:26624\n"
"v_mul_f32 v120, v120, v112\n"
"v_mul_f32 v121, v121, v113\n"
"v_mul_f32 v122, v122, v114\n"
"v_mfma_f32_16x16x32_bf16 v[56:59], v[96:99], v[84:87], v[56:59]\n"
"ds_read_b128 v[112:115], v218 offset:28672\n"
"v_mul_f32 v123, v123, v116\n"
"v_mul_f32 v124, v124, v118\n"
"v_mfma_f32_16x16x32_bf16 v[64:67], v[96:99], v[88:91], v[64:67]\n"
"ds_read_b128 v[96:99], v218 offset:30720\n"
"v_mul_f32 v125, v125, v117\n"
"v_mul_f32 v126, v126, v119\n"
"v_mul_f32 v127, v127, v128\n"
"v_mfma_f32_16x16x32_bf16 v[68:71], v[100:103], v[84:87], v[68:71]\n"
"v_cvt_pk_bf16_f32 v119, v121, v120\n"
"v_cvt_pk_bf16_f32 v118, v123, v122\n"
"v_mfma_f32_16x16x32_bf16 v[72:75], v[100:103], v[88:91], v[72:75]\n"
"v_cvt_pk_bf16_f32 v117, v125, v124\n"
"v_cvt_pk_bf16_f32 v116, v127, v126\n"
"global_store_dwordx4 v178, v[116:119], s[60:61] sc1\n"
"s_add_u32 s60, s60, 0x1000\n"
"s_addc_u32 s61, s61, 0\n"
"s_waitcnt lgkmcnt(3)\n"
"v_mfma_f32_16x16x32_bf16 v[32:35], v[108:111], v[104:107], v[32:35]\n"
"ds_read_b128 v[100:103], v181 offset:24576\n"
"v_lshlrev_b32 v120, 16, v2\n"
"v_and_b32 v121, 0xffff0000, v2\n"
"v_mfma_f32_16x16x32_bf16 v[40:43], v[108:111], v[92:95], v[40:43]\n"
"ds_read_b128 v[108:111], v181 offset:26624\n"
"v_lshlrev_b32 v138, 16, v3\n"
"v_and_b32 v185, 0xffff0000, v3\n"
"v_and_b32 v129, 0xffff0000, v170\n"
"s_waitcnt lgkmcnt(4)\n"
"v_mfma_f32_16x16x32_bf16 v[44:47], v[20:23], v[104:107], v[44:47]\n"
"ds_read_b128 v[0:3], v219 offset:24576\n"
"v_and_b32 v186, 0xffff0000, v140\n"
"v_and_b32 v139, 0xffff0000, v142\n"
"v_mfma_f32_16x16x32_bf16 v[52:55], v[20:23], v[92:95], v[52:55]\n"
"ds_read_b128 v[20:23], v219 offset:26624\n"
"v_and_b32 v123, 0xffff0000, v172\n"
"v_and_b32 v124, 0xffff0000, v144\n"
"s_waitcnt lgkmcnt(5)\n"
"v_mfma_f32_16x16x32_bf16 v[56:59], v[112:115], v[104:107], v[56:59]\n"
"ds_read_b128 v[116:119], v219 offset:28672\n"
"v_and_b32 v125, 0xffff0000, v174\n"
"v_and_b32 v126, 0xffff0000, v146\n"
"v_and_b32 v127, 0xffff0000, v164\n"
"v_mfma_f32_16x16x32_bf16 v[64:67], v[112:115], v[92:95], v[64:67]\n"
"ds_read_b128 v[112:115], v219 offset:30720\n"
"v_and_b32 v136, 0xffff0000, v166\n"
"v_and_b32 v137, 0xffff0000, v168\n"
"s_waitcnt lgkmcnt(6)\n"
"v_mfma_f32_16x16x32_bf16 v[68:71], v[96:99], v[104:107], v[68:71]\n"
"v_and_b32 v122, 0xffff0000, v176\n"
"v_mul_f32 v128, v129, v185\n"
"v_mul_f32 v129, v186, v185\n"
"v_mfma_f32_16x16x32_bf16 v[72:75], v[96:99], v[92:95], v[72:75]\n"
"v_mul_f32 v130, v139, v185\n"
"v_mul_f32 v131, v123, v185\n"
"s_waitcnt lgkmcnt(0)\n"
"s_barrier\n"
"v_mfma_f32_16x16x32_bf16 v[32:35], v[0:3], v[100:103], v[32:35]\n"
"ds_read_b128 v[92:95], v182 offset:49152\n"
"v_mul_f32 v132, v124, v185\n"
"v_mul_f32 v133, v125, v185\n"
"v_mul_f32 v134, v126, v185\n"
"v_mfma_f32_16x16x32_bf16 v[40:43], v[0:3], v[108:111], v[40:43]\n"
"ds_read_b128 v[0:3], v182 offset:51200\n"
"v_mul_f32 v135, v127, v185\n"
"v_fmac_f32 v128, v186, v138\n"
"v_mfma_f32_16x16x32_bf16 v[44:47], v[20:23], v[100:103], v[44:47]\n"
"ds_read_b128 v[96:99], v218 offset:49152\n"
"v_fmac_f32 v129, v139, v138\n"
"v_fmac_f32 v130, v123, v138\n"
"v_mfma_f32_16x16x32_bf16 v[52:55], v[20:23], v[108:111], v[52:55]\n"
"ds_read_b128 v[20:23], v218 offset:51200\n"
"v_fmac_f32 v131, v124, v138\n"
"v_fmac_f32 v132, v125, v138\n"
"v_fmac_f32 v133, v126, v138\n"
"v_mfma_f32_16x16x32_bf16 v[56:59], v[116:119], v[100:103], v[56:59]\n"
"ds_read_b128 v[104:107], v218 offset:53248\n"
"v_fmac_f32 v134, v127, v138\n"
"v_fmac_f32 v135, v136, v138\n"
"v_mfma_f32_16x16x32_bf16 v[64:67], v[116:119], v[108:111], v[64:67]\n"
"ds_read_b128 v[116:119], v218 offset:55296\n"
"v_fmac_f32 v128, v139, v121\n"
"v_fmac_f32 v129, v123, v121\n"
"v_fmac_f32 v130, v124, v121\n"
"v_mfma_f32_16x16x32_bf16 v[68:71], v[112:115], v[100:103], v[68:71]\n"
"v_fmac_f32 v131, v125, v121\n"
"v_fmac_f32 v132, v126, v121\n"
"v_mfma_f32_16x16x32_bf16 v[72:75], v[112:115], v[108:111], v[72:75]\n"
"v_fmac_f32 v133, v127, v121\n"
"v_fmac_f32 v134, v136, v121\n"
"v_fmac_f32 v135, v137, v121\n"
"s_waitcnt lgkmcnt(3)\n"
"v_mfma_f32_16x16x32_bf16 v[32:35], v[96:99], v[92:95], v[32:35]\n"
"ds_read_b128 v[100:103], v181 offset:49152\n"
"v_fmac_f32 v128, v123, v120\n"
"v_fmac_f32 v129, v124, v120\n"
"v_mfma_f32_16x16x32_bf16 v[40:43], v[96:99], v[0:3], v[40:43]\n"
"ds_read_b128 v[96:99], v181 offset:51200\n"
"v_fmac_f32 v130, v125, v120\n"
"v_fmac_f32 v131, v126, v120\n"
"v_fmac_f32 v132, v127, v120\n"
"s_waitcnt lgkmcnt(4)\n"
"v_mfma_f32_16x16x32_bf16 v[44:47], v[20:23], v[92:95], v[44:47]\n"
"ds_read_b128 v[108:111], v219 offset:49152\n"
"v_fmac_f32 v133, v136, v120\n"
"v_fmac_f32 v134, v137, v120\n"
"v_mfma_f32_16x16x32_bf16 v[52:55], v[20:23], v[0:3], v[52:55]\n"
"ds_read_b128 v[20:23], v219 offset:51200\n"
"v_fmac_f32 v135, v122, v120\n"
"v_and_b32 v125, 0xffff0000, v154\n"
"s_waitcnt lgkmcnt(5)\n"
"v_mfma_f32_16x16x32_bf16 v[56:59], v[104:107], v[92:95], v[56:59]\n"
"ds_read_b128 v[112:115], v219 offset:53248\n"
"v_and_b32 v126, 0xffff0000, v162\n"
"v_and_b32 v127, 0xffff0000, v156\n"
"v_and_b32 v120, 0xffff0000, v158\n"
"v_mfma_f32_16x16x32_bf16 v[64:67], v[104:107], v[0:3], v[64:67]\n"
"ds_read_b128 v[104:107], v219 offset:55296\n"
"v_and_b32 v123, 0xffff0000, v148\n"
"v_and_b32 v124, 0xffff0000, v150\n"
"s_waitcnt lgkmcnt(6)\n"
"v_mfma_f32_16x16x32_bf16 v[68:71], v[116:119], v[92:95], v[68:71]\n"
"v_and_b32 v121, 0xffff0000, v160\n"
"v_and_b32 v122, 0xffff0000, v152\n"
"v_mul_f32 v128, v128, v125\n"
"v_mfma_f32_16x16x32_bf16 v[72:75], v[116:119], v[0:3], v[72:75]\n"
"v_mul_f32 v129, v129, v126\n"
"v_mul_f32 v130, v130, v127\n"
"s_waitcnt lgkmcnt(0)\n"
"s_barrier\n"
"v_mfma_f32_16x16x32_bf16 v[32:35], v[108:111], v[100:103], v[32:35]\n"
"ds_read_b128 v[116:119], v180 offset:8192\n"
"v_mul_f32 v131, v131, v120\n"
"v_mul_f32 v132, v132, v123\n"
"v_mul_f32 v133, v133, v124\n"
"v_mfma_f32_16x16x32_bf16 v[40:43], v[108:111], v[96:99], v[40:43]\n"
"ds_read_b128 v[108:111], v180 offset:10240\n"
"v_mul_f32 v134, v134, v121\n"
"v_mul_f32 v135, v135, v122\n"
"v_mfma_f32_16x16x32_bf16 v[44:47], v[20:23], v[100:103], v[44:47]\n"
"ds_read_b128 v[120:123], v235 offset:8192\n"
"v_cvt_pk_bf16_f32 v127, v129, v128\n"
"v_cvt_pk_bf16_f32 v126, v131, v130\n"
"v_mfma_f32_16x16x32_bf16 v[52:55], v[20:23], v[96:99], v[52:55]\n"
"ds_read_b128 v[20:23], v235 offset:10240\n"
"v_cvt_pk_bf16_f32 v125, v133, v132\n"
"v_cvt_pk_bf16_f32 v124, v135, v134\n"
"global_store_dwordx4 v178, v[124:127], s[60:61] sc1\n"
"s_add_u32 s60, s60, 0x1000\n"
"s_addc_u32 s61, s61, 0\n"
"v_mfma_f32_16x16x32_bf16 v[56:59], v[112:115], v[100:103], v[56:59]\n"
"ds_read_b128 v[124:127], v235 offset:12288\n"
"s_waitcnt vmcnt(4)\n"
"v_lshlrev_b32 v136, 16, v4\n"
"v_and_b32 v137, 0xffff0000, v4\n"
"v_mfma_f32_16x16x32_bf16 v[64:67], v[112:115], v[96:99], v[64:67]\n"
"ds_read_b128 v[112:115], v235 offset:14336\n"
"v_lshlrev_b32 v200, 16, v5\n"
"v_and_b32 v202, 0xffff0000, v5\n"
"v_lshlrev_b32 v132, 16, v171\n"
"v_mfma_f32_16x16x32_bf16 v[68:71], v[104:107], v[100:103], v[68:71]\n"
"v_lshlrev_b32 v203, 16, v141\n"
"v_lshlrev_b32 v201, 16, v143\n"
"v_mfma_f32_16x16x32_bf16 v[72:75], v[104:107], v[96:99], v[72:75]\n"
"v_lshlrev_b32 v139, 16, v173\n"
"v_lshlrev_b32 v194, 16, v145\n"
"v_lshlrev_b32 v138, 16, v175\n"
"s_waitcnt lgkmcnt(3)\n"
"v_mfma_f32_16x16x32_bf16 v[32:35], v[120:123], v[116:119], v[32:35]\n"
"ds_read_b128 v[104:107], v188 offset:8192\n"
"v_lshlrev_b32 v195, 16, v147\n"
"v_lshlrev_b32 v196, 16, v165\n"
"v_mfma_f32_16x16x32_bf16 v[40:43], v[120:123], v[108:111], v[40:43]\n"
"ds_read_b128 v[120:123], v188 offset:10240\n"
"v_lshlrev_b32 v198, 16, v167\n"
"v_lshlrev_b32 v199, 16, v169\n"
"v_lshlrev_b32 v197, 16, v177\n"
"s_waitcnt lgkmcnt(4)\n"
"v_mfma_f32_16x16x32_bf16 v[44:47], v[20:23], v[116:119], v[44:47]\n"
"ds_read_b128 v[128:131], v234 offset:8192\n"
"v_mul_f32 v185, v132, v202\n"
"v_mul_f32 v186, v203, v202\n"
"v_mfma_f32_16x16x32_bf16 v[52:55], v[20:23], v[108:111], v[52:55]\n"
"ds_read_b128 v[20:23], v234 offset:10240\n"
"v_mul_f32 v187, v201, v202\n"
"v_mul_f32 v189, v139, v202\n"
"s_waitcnt lgkmcnt(5)\n"
"v_mfma_f32_16x16x32_bf16 v[56:59], v[124:127], v[116:119], v[56:59]\n"
"ds_read_b128 v[132:135], v234 offset:12288\n"
"v_mul_f32 v190, v194, v202\n"
"v_mul_f32 v192, v138, v202\n"
"v_mul_f32 v191, v195, v202\n"
"v_mfma_f32_16x16x32_bf16 v[64:67], v[124:127], v[108:111], v[64:67]\n"
"ds_read_b128 v[124:127], v234 offset:14336\n"
"v_mul_f32 v193, v196, v202\n"
"v_fmac_f32 v185, v203, v200\n"
"s_waitcnt lgkmcnt(6)\n"
"v_mfma_f32_16x16x32_bf16 v[68:71], v[112:115], v[116:119], v[68:71]\n"
"v_fmac_f32 v186, v201, v200\n"
"v_fmac_f32 v187, v139, v200\n"
"v_fmac_f32 v189, v194, v200\n"
"v_mfma_f32_16x16x32_bf16 v[72:75], v[112:115], v[108:111], v[72:75]\n"
"v_fmac_f32 v190, v138, v200\n"
"v_fmac_f32 v192, v195, v200\n"
"s_waitcnt lgkmcnt(0)\n"
"s_barrier\n"
"v_mfma_f32_16x16x32_bf16 v[32:35], v[128:131], v[104:107], v[32:35]\n"
"ds_read_b128 v[108:111], v182\n"
"v_fmac_f32 v191, v196, v200\n"
"v_fmac_f32 v193, v198, v200\n"
"v_fmac_f32 v185, v201, v137\n"
"v_mfma_f32_16x16x32_bf16 v[40:43], v[128:131], v[120:123], v[40:43]\n"
"ds_read_b128 v[112:115], v182 offset:2048\n"
"v_fmac_f32 v186, v139, v137\n"
"v_fmac_f32 v187, v194, v137\n"
"v_mfma_f32_16x16x32_bf16 v[44:47], v[20:23], v[104:107], v[44:47]\n"
"ds_read_b128 v[116:119], v218\n"
"v_fmac_f32 v189, v138, v137\n"
"v_fmac_f32 v190, v195, v137\n"
"v_fmac_f32 v192, v196, v137\n"
"v_mfma_f32_16x16x32_bf16 v[52:55], v[20:23], v[120:123], v[52:55]\n"
"ds_read_b128 v[20:23], v218 offset:2048\n"
"v_fmac_f32 v191, v198, v137\n"
"v_fmac_f32 v193, v199, v137\n"
"v_mfma_f32_16x16x32_bf16 v[56:59], v[132:135], v[104:107], v[56:59]\n"
"ds_read_b128 v[128:131], v218 offset:4096\n"
"v_fmac_f32 v185, v139, v136\n"
"v_fmac_f32 v186, v194, v136\n"
"v_mfma_f32_16x16x32_bf16 v[64:67], v[132:135], v[120:123], v[64:67]\n"
"ds_read_b128 v[132:135], v218 offset:6144\n"
"v_fmac_f32 v187, v138, v136\n"
"v_fmac_f32 v189, v195, v136\n"
"v_fmac_f32 v190, v196, v136\n"
"v_mfma_f32_16x16x32_bf16 v[68:71], v[124:127], v[104:107], v[68:71]\n"
"v_fmac_f32 v192, v198, v136\n"
"v_fmac_f32 v191, v199, v136\n"
"v_mfma_f32_16x16x32_bf16 v[72:75], v[124:127], v[120:123], v[72:75]\n"
"v_fmac_f32 v193, v197, v136\n"
"v_lshlrev_b32 v125, 16, v155\n"
"v_lshlrev_b32 v124, 16, v163\n"
"s_waitcnt lgkmcnt(3)\n"
"v_mfma_f32_16x16x32_bf16 v[32:35], v[116:119], v[108:111], v[32:35]\n"
"ds_read_b128 v[104:107], v181\n"
"v_lshlrev_b32 v126, 16, v157\n"
"v_lshlrev_b32 v136, 16, v159\n"
"v_mfma_f32_16x16x32_bf16 v[40:43], v[116:119], v[112:115], v[40:43]\n"
"ds_read_b128 v[116:119], v181 offset:2048\n"
"v_lshlrev_b32 v138, 16, v149\n"
"v_lshlrev_b32 v139, 16, v151\n"
"v_lshlrev_b32 v137, 16, v161\n"
"s_waitcnt lgkmcnt(4)\n"
"v_mfma_f32_16x16x32_bf16 v[44:47], v[20:23], v[108:111], v[44:47]\n"
"ds_read_b128 v[120:123], v219\n"
"v_lshlrev_b32 v194, 16, v153\n"
"v_mul_f32 v185, v185, v125\n"
"v_mfma_f32_16x16x32_bf16 v[52:55], v[20:23], v[112:115], v[52:55]\n"
"ds_read_b128 v[20:23], v219 offset:2048\n"
"v_mul_f32 v186, v186, v124\n"
"v_mul_f32 v187, v187, v126\n"
"s_waitcnt lgkmcnt(5)\n"
"v_mfma_f32_16x16x32_bf16 v[56:59], v[128:131], v[108:111], v[56:59]\n"
"ds_read_b128 v[124:127], v219 offset:4096\n"
"v_mul_f32 v189, v189, v136\n"
"v_mul_f32 v190, v190, v138\n"
"v_mul_f32 v192, v192, v139\n"
"v_mfma_f32_16x16x32_bf16 v[64:67], v[128:131], v[112:115], v[64:67]\n"
"ds_read_b128 v[128:131], v219 offset:6144\n"
"v_mul_f32 v191, v191, v137\n"
"v_mul_f32 v193, v193, v194\n"
"s_waitcnt lgkmcnt(6)\n"
"v_mfma_f32_16x16x32_bf16 v[68:71], v[132:135], v[108:111], v[68:71]\n"
"v_cvt_pk_bf16_f32 v139, v186, v185\n"
"v_cvt_pk_bf16_f32 v138, v189, v187\n"
"v_cvt_pk_bf16_f32 v137, v192, v190\n"
"v_mfma_f32_16x16x32_bf16 v[72:75], v[132:135], v[112:115], v[72:75]\n"
"v_cvt_pk_bf16_f32 v136, v193, v191\n"
"global_store_dwordx4 v178, v[136:139], s[60:61] sc1\n"
"s_add_u32 s60, s60, 0x1000\n"
"s_addc_u32 s61, s61, 0\n"
"s_waitcnt lgkmcnt(0)\n"
"s_barrier\n"
"v_mfma_f32_16x16x32_bf16 v[32:35], v[120:123], v[104:107], v[32:35]\n"
"ds_read_b128 v[132:135], v182 offset:24576\n"
"v_lshlrev_b32 v185, 16, v6\n"
"v_and_b32 v186, 0xffff0000, v6\n"
"v_lshlrev_b32 v192, 16, v7\n"
"v_mfma_f32_16x16x32_bf16 v[40:43], v[120:123], v[116:119], v[40:43]\n"
"ds_read_b128 v[120:123], v182 offset:26624\n"
"v_and_b32 v194, 0xffff0000, v7\n"
"v_and_b32 v196, 0xffff0000, v171\n"
"v_mfma_f32_16x16x32_bf16 v[44:47], v[20:23], v[104:107], v[44:47]\n"
"ds_read_b128 v[4:7], v218 offset:24576\n"
"v_and_b32 v195, 0xffff0000, v141\n"
"v_and_b32 v193, 0xffff0000, v143\n"
"v_and_b32 v187, 0xffff0000, v173\n"
"v_mfma_f32_16x16x32_bf16 v[52:55], v[20:23], v[116:119], v[52:55]\n"
"ds_read_b128 v[20:23], v218 offset:26624\n"
"v_and_b32 v172, 0xffff0000, v145\n"
"v_and_b32 v173, 0xffff0000, v175\n"
"v_mfma_f32_16x16x32_bf16 v[56:59], v[124:127], v[104:107], v[56:59]\n"
"ds_read_b128 v[136:139], v218 offset:28672\n"
"v_and_b32 v174, 0xffff0000, v147\n"
"v_and_b32 v175, 0xffff0000, v165\n"
"v_mfma_f32_16x16x32_bf16 v[64:67], v[124:127], v[116:119], v[64:67]\n"
"ds_read_b128 v[124:127], v218 offset:30720\n"
"v_and_b32 v189, 0xffff0000, v167\n"
"v_and_b32 v190, 0xffff0000, v169\n"
"v_and_b32 v191, 0xffff0000, v177\n"
"v_mfma_f32_16x16x32_bf16 v[68:71], v[128:131], v[104:107], v[68:71]\n"
"v_mul_f32 v164, v196, v194\n"
"v_mul_f32 v166, v195, v194\n"
"v_mfma_f32_16x16x32_bf16 v[72:75], v[128:131], v[116:119], v[72:75]\n"
"v_mul_f32 v165, v193, v194\n"
"v_mul_f32 v167, v187, v194\n"
"v_mul_f32 v168, v172, v194\n"
"s_waitcnt lgkmcnt(3)\n"
"v_mfma_f32_16x16x32_bf16 v[32:35], v[4:7], v[132:135], v[32:35]\n"
"ds_read_b128 v[128:131], v181 offset:24576\n"
"v_mul_f32 v170, v173, v194\n"
"v_mul_f32 v169, v174, v194\n"
"v_mfma_f32_16x16x32_bf16 v[40:43], v[4:7], v[120:123], v[40:43]\n"
"ds_read_b128 v[4:7], v181 offset:26624\n"
"v_mul_f32 v171, v175, v194\n"
"v_fmac_f32 v164, v195, v192\n"
"v_fmac_f32 v166, v193, v192\n"
"s_waitcnt lgkmcnt(4)\n"
"v_mfma_f32_16x16x32_bf16 v[44:47], v[20:23], v[132:135], v[44:47]\n"
"ds_read_b128 v[140:143], v219 offset:24576\n"
"v_fmac_f32 v165, v187, v192\n"
"v_fmac_f32 v167, v172, v192\n"
"v_mfma_f32_16x16x32_bf16 v[52:55], v[20:23], v[120:123], v[52:55]\n"
"ds_read_b128 v[20:23], v219 offset:26624\n"
"v_fmac_f32 v168, v173, v192\n"
"v_fmac_f32 v170, v174, v192\n"
"s_waitcnt lgkmcnt(5)\n"
"v_mfma_f32_16x16x32_bf16 v[56:59], v[136:139], v[132:135], v[56:59]\n"
"ds_read_b128 v[144:147], v219 offset:28672\n"
"v_fmac_f32 v169, v175, v192\n"
"v_fmac_f32 v171, v189, v192\n"
"v_fmac_f32 v164, v193, v186\n"
"v_mfma_f32_16x16x32_bf16 v[64:67], v[136:139], v[120:123], v[64:67]\n"
"ds_read_b128 v[136:139], v219 offset:30720\n"
"v_fmac_f32 v166, v187, v186\n"
"v_fmac_f32 v165, v172, v186\n"
"s_waitcnt lgkmcnt(6)\n"
"v_mfma_f32_16x16x32_bf16 v[68:71], v[124:127], v[132:135], v[68:71]\n"
"v_fmac_f32 v167, v173, v186\n"
"v_fmac_f32 v168, v174, v186\n"
"v_fmac_f32 v170, v175, v186\n"
"v_mfma_f32_16x16x32_bf16 v[72:75], v[124:127], v[120:123], v[72:75]\n"
"v_fmac_f32 v169, v189, v186\n"
"v_fmac_f32 v171, v190, v186\n"
"s_waitcnt lgkmcnt(0)\n"
"s_barrier\n"
"v_mfma_f32_16x16x32_bf16 v[32:35], v[140:143], v[128:131], v[32:35]\n"
"ds_read_b128 v[120:123], v182 offset:49152\n"
"v_fmac_f32 v164, v187, v185\n"
"v_fmac_f32 v166, v172, v185\n"
"v_fmac_f32 v165, v173, v185\n"
"v_mfma_f32_16x16x32_bf16 v[40:43], v[140:143], v[4:7], v[40:43]\n"
"ds_read_b128 v[124:127], v182 offset:51200\n"
"v_fmac_f32 v167, v174, v185\n"
"v_fmac_f32 v168, v175, v185\n"
"v_mfma_f32_16x16x32_bf16 v[44:47], v[20:23], v[128:131], v[44:47]\n"
"ds_read_b128 v[132:135], v218 offset:49152\n"
"v_fmac_f32 v170, v189, v185\n"
"v_fmac_f32 v169, v190, v185\n"
"v_fmac_f32 v171, v191, v185\n"
"v_mfma_f32_16x16x32_bf16 v[52:55], v[20:23], v[4:7], v[52:55]\n"
"ds_read_b128 v[20:23], v218 offset:51200\n"
"v_and_b32 v172, 0xffff0000, v155\n"
"v_and_b32 v154, 0xffff0000, v163\n"
"v_mfma_f32_16x16x32_bf16 v[56:59], v[144:147], v[128:131], v[56:59]\n"
"ds_read_b128 v[140:143], v218 offset:53248\n"
"v_and_b32 v155, 0xffff0000, v157\n"
"v_and_b32 v156, 0xffff0000, v159\n"
"v_mfma_f32_16x16x32_bf16 v[64:67], v[144:147], v[4:7], v[64:67]\n"
"ds_read_b128 v[144:147], v218 offset:55296\n"
"v_and_b32 v157, 0xffff0000, v149\n"
"v_and_b32 v158, 0xffff0000, v151\n"
"v_and_b32 v148, 0xffff0000, v161\n"
"v_mfma_f32_16x16x32_bf16 v[68:71], v[136:139], v[128:131], v[68:71]\n"
"v_and_b32 v149, 0xffff0000, v153\n"
"v_mul_f32 v164, v164, v172\n"
"v_mfma_f32_16x16x32_bf16 v[72:75], v[136:139], v[4:7], v[72:75]\n"
"v_mul_f32 v166, v166, v154\n"
"v_mul_f32 v165, v165, v155\n"
"v_mul_f32 v167, v167, v156\n"
"s_waitcnt lgkmcnt(3)\n"
"v_mfma_f32_16x16x32_bf16 v[32:35], v[132:135], v[120:123], v[32:35]\n"
"ds_read_b128 v[4:7], v181 offset:49152\n"
"v_mul_f32 v168, v168, v157\n"
"v_mul_f32 v170, v170, v158\n"
"v_mfma_f32_16x16x32_bf16 v[40:43], v[132:135], v[124:127], v[40:43]\n"
"ds_read_b128 v[128:131], v181 offset:51200\n"
"v_mul_f32 v169, v169, v148\n"
"v_mul_f32 v171, v171, v149\n"
"v_cvt_pk_bf16_f32 v135, v166, v164\n"
"s_waitcnt lgkmcnt(4)\n"
"v_mfma_f32_16x16x32_bf16 v[44:47], v[20:23], v[120:123], v[44:47]\n"
"ds_read_b128 v[136:139], v219 offset:49152\n"
"v_cvt_pk_bf16_f32 v134, v167, v165\n"
"v_cvt_pk_bf16_f32 v133, v170, v168\n"
"v_mfma_f32_16x16x32_bf16 v[52:55], v[20:23], v[124:127], v[52:55]\n"
"ds_read_b128 v[20:23], v219 offset:51200\n"
"v_cvt_pk_bf16_f32 v132, v171, v169\n"
"global_store_dwordx4 v178, v[132:135], s[60:61] sc1\n"
"s_add_u32 s60, s60, 0x1000\n"
"s_addc_u32 s61, s61, 0\n"
"s_waitcnt lgkmcnt(5)\n"
"v_mfma_f32_16x16x32_bf16 v[56:59], v[140:143], v[120:123], v[56:59]\n"
"ds_read_b128 v[132:135], v219 offset:53248\n"
"v_cmp_gt_u32 vcc, 16, v184\n"
"s_nop 1\n"
"v_cndmask_b32 v241, 0, v236, vcc\n"
"v_mfma_f32_16x16x32_bf16 v[64:67], v[140:143], v[124:127], v[64:67]\n"
"ds_read_b128 v[140:143], v219 offset:55296\n"
"s_waitcnt lgkmcnt(6)\n"
"v_mfma_f32_16x16x32_bf16 v[68:71], v[144:147], v[120:123], v[68:71]\n"
"v_mfma_f32_16x16x32_bf16 v[72:75], v[144:147], v[124:127], v[72:75]\n"
"s_waitcnt lgkmcnt(0)\n"
"s_barrier\n"
"v_mfma_f32_16x16x32_bf16 v[32:35], v[136:139], v[4:7], v[32:35]\n"
"ds_read_b128 v[144:147], v180 offset:8192\n"
"v_mfma_f32_16x16x32_bf16 v[40:43], v[136:139], v[128:131], v[40:43]\n"
"ds_read_b128 v[136:139], v180 offset:10240\n"
"v_cmp_gt_u32 vcc, 12, v184\n"
"s_nop 1\n"
"v_cndmask_b32 v242, 0, v236, vcc\n"
"v_mfma_f32_16x16x32_bf16 v[44:47], v[20:23], v[4:7], v[44:47]\n"
"ds_read_b128 v[148:151], v235 offset:8192\n"
"v_mfma_f32_16x16x32_bf16 v[52:55], v[20:23], v[128:131], v[52:55]\n"
"ds_read_b128 v[20:23], v235 offset:10240\n"
"v_mfma_f32_16x16x32_bf16 v[56:59], v[132:135], v[4:7], v[56:59]\n"
"ds_read_b128 v[152:155], v235 offset:12288\n"
"v_mfma_f32_16x16x32_bf16 v[64:67], v[132:135], v[128:131], v[64:67]\n"
"ds_read_b128 v[132:135], v235 offset:14336\n"
"v_mfma_f32_16x16x32_bf16 v[68:71], v[140:143], v[4:7], v[68:71]\n"
"v_mfma_f32_16x16x32_bf16 v[72:75], v[140:143], v[128:131], v[72:75]\n"
"v_cmp_gt_u32 vcc, 8, v184\n"
"s_nop 1\n"
"v_cndmask_b32 v243, 0, v236, vcc\n"
"s_waitcnt lgkmcnt(3)\n"
"v_mfma_f32_16x16x32_bf16 v[32:35], v[148:151], v[144:147], v[32:35]\n"
"ds_read_b128 v[140:143], v188 offset:8192\n"
"v_mfma_f32_16x16x32_bf16 v[40:43], v[148:151], v[136:139], v[40:43]\n"
"ds_read_b128 v[148:151], v188 offset:10240\n"
"s_waitcnt lgkmcnt(4)\n"
"v_mfma_f32_16x16x32_bf16 v[44:47], v[20:23], v[144:147], v[44:47]\n"
"ds_read_b128 v[156:159], v234 offset:8192\n"
"v_mfma_f32_16x16x32_bf16 v[52:55], v[20:23], v[136:139], v[52:55]\n"
"ds_read_b128 v[20:23], v234 offset:10240\n"
"s_waitcnt lgkmcnt(5)\n"
"v_mfma_f32_16x16x32_bf16 v[56:59], v[152:155], v[144:147], v[56:59]\n"
"ds_read_b128 v[160:163], v234 offset:12288\n"
"v_cmp_gt_u32 vcc, 4, v184\n"
"s_nop 1\n"
"v_cndmask_b32 v244, 0, v236, vcc\n"
"v_mfma_f32_16x16x32_bf16 v[64:67], v[152:155], v[136:139], v[64:67]\n"
"ds_read_b128 v[152:155], v234 offset:14336\n"
"s_waitcnt lgkmcnt(6)\n"
"v_mfma_f32_16x16x32_bf16 v[68:71], v[132:135], v[144:147], v[68:71]\n"
"v_mfma_f32_16x16x32_bf16 v[72:75], v[132:135], v[136:139], v[72:75]\n"
"s_waitcnt lgkmcnt(0)\n"
"s_barrier\n"
"v_mfma_f32_16x16x32_bf16 v[32:35], v[156:159], v[140:143], v[32:35]\n"
"ds_read_b128 v[132:135], v182\n"
"v_mfma_f32_16x16x32_bf16 v[40:43], v[156:159], v[148:151], v[40:43]\n"
"ds_read_b128 v[136:139], v182 offset:2048\n"
"v_mfma_f32_16x16x32_bf16 v[44:47], v[20:23], v[140:143], v[44:47]\n"
"ds_read_b128 v[144:147], v218\n"
"s_lshl2_add_u32 s13, s52, s15\n"
"s_lshl_b32 s13, s13, 12\n"
"s_add_u32 s13, s13, 0x100000\n"
"s_add_u32 s76, s30, s13\n"
"s_addc_u32 s77, s31, 0\n"
"v_mfma_f32_16x16x32_bf16 v[52:55], v[20:23], v[148:151], v[52:55]\n"
"ds_read_b128 v[20:23], v218 offset:2048\n"
"v_mfma_f32_16x16x32_bf16 v[56:59], v[160:163], v[140:143], v[56:59]\n"
"ds_read_b128 v[156:159], v218 offset:4096\n"
"v_mfma_f32_16x16x32_bf16 v[64:67], v[160:163], v[148:151], v[64:67]\n"
"ds_read_b128 v[160:163], v218 offset:6144\n"
"v_mfma_f32_16x16x32_bf16 v[68:71], v[152:155], v[140:143], v[68:71]\n"
"s_lshl_b32 s12, s11, 16\n"
"s_or_b32 s12, s12, %3\n"
"v_mov_b32 v140, s10\n"
"v_mov_b32 v141, s12\n"
"s_waitcnt vmcnt(0)\n"
"global_store_dwordx2 v179, v[140:141], s[30:31] sc1\n"
"v_mfma_f32_16x16x32_bf16 v[72:75], v[152:155], v[148:151], v[72:75]\n"
"s_sub_u32 s13, 15, s51\n"
"s_lshl_b32 s13, s13, 7\n"
"s_add_u32 s64, s76, s13\n"
"s_addc_u32 s65, s77, 0\n"
"s_waitcnt lgkmcnt(3)\n"
"v_mfma_f32_16x16x32_bf16 v[32:35], v[144:147], v[132:135], v[32:35]\n"
"ds_read_b128 v[140:143], v181\n"
"v_mfma_f32_16x16x32_bf16 v[40:43], v[144:147], v[136:139], v[40:43]\n"
"ds_read_b128 v[144:147], v181 offset:2048\n"
"s_waitcnt lgkmcnt(4)\n"
"v_mfma_f32_16x16x32_bf16 v[44:47], v[20:23], v[132:135], v[44:47]\n"
"ds_read_b128 v[148:151], v219\n"
"v_mfma_f32_16x16x32_bf16 v[52:55], v[20:23], v[136:139], v[52:55]\n"
"ds_read_b128 v[20:23], v219 offset:2048\n"
"s_waitcnt lgkmcnt(5)\n"
"v_mfma_f32_16x16x32_bf16 v[56:59], v[156:159], v[132:135], v[56:59]\n"
"ds_read_b128 v[152:155], v219 offset:4096\n"
"v_mfma_f32_16x16x32_bf16 v[64:67], v[156:159], v[136:139], v[64:67]\n"
"ds_read_b128 v[156:159], v219 offset:6144\n"
"s_and_b32 s77, s77, 0xffff\n"
"s_mov_b32 s78, 0x1000\n"
"s_mov_b32 s79, 0x20000\n"
"s_waitcnt lgkmcnt(6)\n"
"v_mfma_f32_16x16x32_bf16 v[68:71], v[160:163], v[132:135], v[68:71]\n"
"v_mfma_f32_16x16x32_bf16 v[72:75], v[160:163], v[136:139], v[72:75]\n"
"s_waitcnt lgkmcnt(0)\n"
"s_barrier\n"
"v_mfma_f32_16x16x32_bf16 v[32:35], v[148:151], v[140:143], v[32:35]\n"
"ds_read_b128 v[160:163], v182 offset:24576\n"
"v_mfma_f32_16x16x32_bf16 v[40:43], v[148:151], v[144:147], v[40:43]\n"
"ds_read_b128 v[148:151], v182 offset:26624\n"
"v_mfma_f32_16x16x32_bf16 v[44:47], v[20:23], v[140:143], v[44:47]\n"
"ds_read_b128 v[164:167], v218 offset:24576\n"
"s_lshl1_add_u32 s13, s51, s48\n"
"s_lshl2_add_u32 s14, s50, s15\n"
"s_and_b32 s67, s14, 24\n"
"s_add_u32 s67, s67, s14\n"
"s_lshl_b32 s62, s13, 19\n"
"s_lshl_b32 s67, s67, 10\n"
"s_add_u32 s62, s62, s67\n"
"s_add_u32 s62, s62, 0x400000\n"
"s_lshr_b32 s66, s62, 8\n"
"s_and_b32 s66, s66, 0x2000\n"
"s_add_u32 s66, s66, s62\n"
"s_add_u32 s66, s30, s66\n"
"s_addc_u32 s67, s31, 0\n"
"s_lshl_b32 s13, s13, 13\n"
"s_lshl_b32 s14, s14, 7\n"
"s_add_u32 s13, s13, s14\n"
"s_add_u32 s13, s13, 0x20000\n"
"s_add_u32 s62, s30, s13\n"
"s_addc_u32 s63, s31, 0\n"
"v_mfma_f32_16x16x32_bf16 v[52:55], v[20:23], v[144:147], v[52:55]\n"
"ds_read_b128 v[20:23], v218 offset:26624\n"
"v_mfma_f32_16x16x32_bf16 v[56:59], v[152:155], v[140:143], v[56:59]\n"
"ds_read_b128 v[168:171], v218 offset:28672\n"
"v_mfma_f32_16x16x32_bf16 v[64:67], v[152:155], v[144:147], v[64:67]\n"
"ds_read_b128 v[152:155], v218 offset:30720\n"
"v_mfma_f32_16x16x32_bf16 v[68:71], v[156:159], v[140:143], v[68:71]\n"
"v_mfma_f32_16x16x32_bf16 v[72:75], v[156:159], v[144:147], v[72:75]\n"
"s_waitcnt lgkmcnt(3)\n"
"v_mfma_f32_16x16x32_bf16 v[32:35], v[164:167], v[160:163], v[32:35]\n"
"ds_read_b128 v[156:159], v181 offset:24576\n"
"s_lshl2_add_u32 s13, s50, s15\n"
"s_lshl_b32 s13, s13, 16\n"
"s_lshl_b32 s14, s48, 10\n"
"s_add_u32 s13, s13, s14\n"
"s_add_u32 s13, s13, 0\n"
"s_add_u32 s72, s32, s13\n"
"s_addc_u32 s73, s33, 0\n"
"s_add_u32 s74, s34, s13\n"
"s_addc_u32 s75, s35, 0\n"
"s_sub_u32 s74, s74, 0x800\n"
"s_subb_u32 s75, s75, 0\n"
"v_mfma_f32_16x16x32_bf16 v[40:43], v[164:167], v[148:151], v[40:43]\n"
"ds_read_b128 v[164:167], v181 offset:26624\n"
"s_waitcnt lgkmcnt(4)\n"
"v_mfma_f32_16x16x32_bf16 v[44:47], v[20:23], v[160:163], v[44:47]\n"
"ds_read_b128 v[172:175], v219 offset:24576\n"
"v_mfma_f32_16x16x32_bf16 v[52:55], v[20:23], v[148:151], v[52:55]\n"
"ds_read_b128 v[20:23], v219 offset:26624\n"
"s_waitcnt lgkmcnt(5)\n"
"v_mfma_f32_16x16x32_bf16 v[56:59], v[168:171], v[160:163], v[56:59]\n"
"ds_read_b128 v[176:179], v219 offset:28672\n"
"v_mfma_f32_16x16x32_bf16 v[64:67], v[168:171], v[148:151], v[64:67]\n"
"ds_read_b128 v[168:171], v219 offset:30720\n"
"s_add_u32 s13, s49, 0\n"
"s_and_b32 s13, s13, 3\n"
"s_lshl_b32 s13, s13, 7\n"
"s_add_u32 s68, s72, s13\n"
"s_addc_u32 s69, s73, 0\n"
"s_add_u32 s70, s74, s13\n"
"s_addc_u32 s71, s75, 0\n"
"s_waitcnt lgkmcnt(6)\n"
"v_mfma_f32_16x16x32_bf16 v[68:71], v[152:155], v[160:163], v[68:71]\n"
"v_mfma_f32_16x16x32_bf16 v[72:75], v[152:155], v[148:151], v[72:75]\n"
"s_waitcnt lgkmcnt(0)\n"
"s_barrier\n"
"v_mfma_f32_16x16x32_bf16 v[32:35], v[172:175], v[156:159], v[32:35]\n"
"ds_read_b128 v[148:151], v182 offset:49152\n"
"v_mfma_f32_16x16x32_bf16 v[40:43], v[172:175], v[164:167], v[40:43]\n"
"ds_read_b128 v[152:155], v182 offset:51200\n"
"v_mfma_f32_16x16x32_bf16 v[44:47], v[20:23], v[156:159], v[44:47]\n"
"ds_read_b128 v[160:163], v218 offset:49152\n"
"v_mfma_f32_16x16x32_bf16 v[52:55], v[20:23], v[164:167], v[52:55]\n"
"ds_read_b128 v[20:23], v218 offset:51200\n"
"v_lshlrev_b32 v223, 16, v223\n"
"v_lshlrev_b32 v183, 16, v183\n"
"v_add_f32 v223, v223, v183\n"
"v_cvt_pk_bf16_f32 v223, v223, 0\n"
"v_mfma_f32_16x16x32_bf16 v[56:59], v[176:179], v[156:159], v[56:59]\n"
"ds_read_b128 v[172:175], v218 offset:53248\n"
"v_mfma_f32_16x16x32_bf16 v[64:67], v[176:179], v[164:167], v[64:67]\n"
"ds_read_b128 v[176:179], v218 offset:55296\n"
"v_mfma_f32_16x16x32_bf16 v[68:71], v[168:171], v[156:159], v[68:71]\n"
"v_mfma_f32_16x16x32_bf16 v[72:75], v[168:171], v[164:167], v[72:75]\n"
"s_waitcnt lgkmcnt(3)\n"
"v_mfma_f32_16x16x32_bf16 v[32:35], v[160:163], v[148:151], v[32:35]\n"
"ds_read_b128 v[156:159], v181 offset:49152\n"
"s_mul_i32 s97, s15, 0x11e0\n"
"s_add_u32 s97, s97, 0x20200\n"
"v_mfma_f32_16x16x32_bf16 v[40:43], v[160:163], v[152:155], v[40:43]\n"
"ds_read_b128 v[160:163], v181 offset:51200\n"
"s_waitcnt lgkmcnt(4)\n"
"v_mfma_f32_16x16x32_bf16 v[44:47], v[20:23], v[148:151], v[44:47]\n"
"ds_read_b128 v[164:167], v219 offset:49152\n"
"v_mfma_f32_16x16x32_bf16 v[52:55], v[20:23], v[152:155], v[52:55]\n"
"ds_read_b128 v[20:23], v219 offset:51200\n"
"s_waitcnt lgkmcnt(5)\n"
"v_mfma_f32_16x16x32_bf16 v[56:59], v[172:175], v[148:151], v[56:59]\n"
"ds_read_b128 v[168:171], v219 offset:53248\n"
"v_mfma_f32_16x16x32_bf16 v[64:67], v[172:175], v[152:155], v[64:67]\n"
"ds_read_b128 v[172:175], v219 offset:55296\n"
"s_waitcnt lgkmcnt(6)\n"
"v_mfma_f32_16x16x32_bf16 v[68:71], v[176:179], v[148:151], v[68:71]\n"
"v_bfe_u32 v184, v227, 1, 3\n"
"v_bfe_u32 v237, v227, 4, 1\n"
"v_mfma_f32_16x16x32_bf16 v[72:75], v[176:179], v[152:155], v[72:75]\n"
"v_xor_b32 v184, v184, v237\n"
"v_lshrrev_b32 v237, 1, v227\n"
"s_waitcnt lgkmcnt(0)\n"
"s_barrier\n"
"v_mfma_f32_16x16x32_bf16 v[32:35], v[164:167], v[156:159], v[32:35]\n"
"ds_read_b128 v[176:179], v180 offset:8192\n"
"v_and_b32 v237, 16, v237\n"
"v_and_or_b32 v237, v227, 15, v237\n"
"v_mfma_f32_16x16x32_bf16 v[40:43], v[164:167], v[160:163], v[40:43]\n"
"ds_read_b128 v[164:167], v180 offset:10240\n"
"v_lshlrev_b32 v237, 7, v237\n"
"v_mfma_f32_16x16x32_bf16 v[44:47], v[20:23], v[156:159], v[44:47]\n"
"ds_read_b128 v[180:183], v235 offset:8192\n"
"v_lshl_or_b32 v237, v184, 4, v237\n"
"v_add_u32 v237, s56, v237\n"
"v_mfma_f32_16x16x32_bf16 v[52:55], v[20:23], v[160:163], v[52:55]\n"
"ds_read_b128 v[20:23], v235 offset:10240\n"
"v_xor_b32 v238, 32, v237\n"
"v_xor_b32 v239, 64, v237\n"
"v_mfma_f32_16x16x32_bf16 v[56:59], v[168:171], v[156:159], v[56:59]\n"
"ds_read_b128 v[184:187], v235 offset:12288\n"
"v_xor_b32 v240, 0x60, v237\n"
"v_mfma_f32_16x16x32_bf16 v[64:67], v[168:171], v[160:163], v[64:67]\n"
"ds_read_b128 v[168:171], v235 offset:14336\n"
"v_and_b32 v192, 15, v227\n"
"v_lshrrev_b32 v189, 4, v227\n"
"v_mfma_f32_16x16x32_bf16 v[68:71], v[172:175], v[156:159], v[68:71]\n"
"v_lshl_add_u32 v229, v189, 3, v192\n"
"v_lshlrev_b32 v230, 4, v189\n"
"v_mfma_f32_16x16x32_bf16 v[72:75], v[172:175], v[160:163], v[72:75]\n"
"v_lshlrev_b32 v189, 5, v192\n"
"v_sub_u32 v230, v230, v189\n"
"s_waitcnt lgkmcnt(3)\n"
"v_mfma_f32_16x16x32_bf16 v[32:35], v[180:183], v[176:179], v[32:35]\n"
"ds_read_b128 v[172:175], v188 offset:8192\n"
"v_add_u32 v230, s97, v230\n"
"v_mfma_f32_16x16x32_bf16 v[40:43], v[180:183], v[164:167], v[40:43]\n"
"ds_read_b128 v[180:183], v188 offset:10240\n"
"v_add_u32 v230, 0x1e0, v230\n"
"v_and_b32 v192, 1, v192\n"
"s_waitcnt lgkmcnt(4)\n"
"v_mfma_f32_16x16x32_bf16 v[44:47], v[20:23], v[176:179], v[44:47]\n"
"ds_read_b128 v[188:191], v234 offset:8192\n"
"v_mul_u32_u24 v192, 0x103e, v192\n"
"v_lshlrev_b32 v229, 1, v229\n"
"v_mfma_f32_16x16x32_bf16 v[52:55], v[20:23], v[164:167], v[52:55]\n"
"ds_read_b128 v[20:23], v234 offset:10240\n"
"v_sub_u32 v229, v229, v192\n"
"s_waitcnt lgkmcnt(5)\n"
"v_mfma_f32_16x16x32_bf16 v[56:59], v[184:187], v[176:179], v[56:59]\n"
"ds_read_b128 v[192:195], v234 offset:12288\n"
"v_add_u32 v246, 0x20000, v218\n"
"v_add_u32 v245, 0x20000, v219\n"
"v_add_u32 v229, s57, v229\n"
"v_add_u32 v229, 0x1040, v229\n"
"v_mfma_f32_16x16x32_bf16 v[64:67], v[184:187], v[164:167], v[64:67]\n"
"ds_read_b128 v[184:187], v234 offset:14336\n"
"v_add_u32 v231, 0x400, v229\n"
"v_add_u32 v232, 0x800, v229\n"
"s_waitcnt lgkmcnt(6)\n"
"v_mfma_f32_16x16x32_bf16 v[68:71], v[168:171], v[176:179], v[68:71]\n"
"v_add_u32 v233, 0xc00, v229\n"
"v_mfma_f32_16x16x32_bf16 v[72:75], v[168:171], v[164:167], v[72:75]\n"
"s_waitcnt lgkmcnt(3)\n"
"v_mfma_f32_16x16x32_bf16 v[32:35], v[188:191], v[172:175], v[32:35]\n"
"ds_read_b128 v[164:167], v246 offset:512\n"
"v_mfma_f32_16x16x32_bf16 v[40:43], v[188:191], v[180:183], v[40:43]\n"
"ds_read_b128 v[168:171], v246 offset:5088\n"
"s_waitcnt lgkmcnt(4)\n"
"v_mfma_f32_16x16x32_bf16 v[44:47], v[20:23], v[172:175], v[44:47]\n"
"ds_read_b128 v[176:179], v246 offset:9664\n"
"v_mfma_f32_16x16x32_bf16 v[52:55], v[20:23], v[180:183], v[52:55]\n"
"ds_read_b128 v[20:23], v246 offset:14240\n"
"s_waitcnt lgkmcnt(5)\n"
"v_mfma_f32_16x16x32_bf16 v[56:59], v[192:195], v[172:175], v[56:59]\n"
"ds_read_b128 v[188:191], v222\n"
"v_mfma_f32_16x16x32_bf16 v[64:67], v[192:195], v[180:183], v[64:67]\n"
"ds_read_b128 v[192:195], v222 offset:256\n"
"s_waitcnt lgkmcnt(6)\n"
"v_mfma_f32_16x16x32_bf16 v[68:71], v[184:187], v[172:175], v[68:71]\n"
"ds_read_b128 v[172:175], v221\n"
"v_mfma_f32_16x16x32_bf16 v[72:75], v[184:187], v[180:183], v[72:75]\n"
"s_waitcnt lgkmcnt(6)\n"
"v_mfma_f32_16x16x32_bf16 v[180:183], v[164:167], v[8:11], 0\n"
"v_mfma_f32_16x16x32_bf16 v[184:187], v[164:167], v[12:15], 0\n"
"ds_read_b128 v[164:167], v245 offset:512\n"
"s_waitcnt lgkmcnt(6)\n"
"v_mfma_f32_16x16x32_bf16 v[196:199], v[168:171], v[8:11], 0\n"
"v_mfma_f32_16x16x32_bf16 v[200:203], v[168:171], v[12:15], 0\n"
"ds_read_b128 v[168:171], v245 offset:5088\n"
"s_waitcnt lgkmcnt(6)\n"
"v_mfma_f32_16x16x32_bf16 v[204:207], v[176:179], v[8:11], 0\n"
"s_waitcnt lgkmcnt(4)\n"
"v_fma_f32 v247, v188, v32, v33\n"
"v_fma_f32 v249, v188, v40, v41\n"
"v_mfma_f32_16x16x32_bf16 v[208:211], v[176:179], v[12:15], 0\n"
"ds_read_b128 v[176:179], v245 offset:9664\n"
"v_mfma_f32_16x16x32_bf16 v[212:215], v[20:23], v[8:11], 0\n"
"v_fma_f32 v247, -v189, v40, v247\n"
"v_fmac_f32 v249, v189, v32\n"
"v_mfma_f32_16x16x32_bf16 v[8:11], v[20:23], v[12:15], 0\n"
"ds_read_b128 v[12:15], v245 offset:14240\n"
"s_waitcnt lgkmcnt(3)\n"
"v_mfma_f32_16x16x32_bf16 v[180:183], v[164:167], v[36:39], v[180:183]\n"
"v_fma_f32 v248, v188, v247, v34\n"
"v_fma_f32 v250, v188, v249, v42\n"
"v_mfma_f32_16x16x32_bf16 v[184:187], v[164:167], v[16:19], v[184:187]\n"
"ds_read_b128 v[20:23], v246 offset:2560\n"
"s_waitcnt lgkmcnt(3)\n"
"v_mfma_f32_16x16x32_bf16 v[196:199], v[168:171], v[36:39], v[196:199]\n"
"v_fma_f32 v248, -v189, v249, v248\n"
"v_fmac_f32 v250, v189, v247\n"
"v_mfma_f32_16x16x32_bf16 v[200:203], v[168:171], v[16:19], v[200:203]\n"
"ds_read_b128 v[164:167], v246 offset:7136\n"
"s_waitcnt lgkmcnt(3)\n"
"v_mfma_f32_16x16x32_bf16 v[204:207], v[176:179], v[36:39], v[204:207]\n"
"v_fma_f32 v247, v188, v248, v35\n"
"v_fma_f32 v249, v188, v250, v43\n"
"v_mfma_f32_16x16x32_bf16 v[208:211], v[176:179], v[16:19], v[208:211]\n"
"ds_read_b128 v[168:171], v246 offset:11712\n"
"s_waitcnt lgkmcnt(3)\n"
"v_mfma_f32_16x16x32_bf16 v[212:215], v[12:15], v[36:39], v[212:215]\n"
"v_fma_f32 v247, -v189, v250, v247\n"
"v_fmac_f32 v249, v189, v248\n"
"v_mfma_f32_16x16x32_bf16 v[8:11], v[12:15], v[16:19], v[8:11]\n"
"ds_read_b128 v[12:15], v246 offset:16288\n"
"s_waitcnt lgkmcnt(3)\n"
"v_mfma_f32_16x16x32_bf16 v[180:183], v[20:23], v[28:31], v[180:183]\n"
"ds_read_b128 v[16:19], v245 offset:2560\n"
"v_fma_f32 v176, v188, v247, v44\n"
"v_fma_f32 v178, v188, v249, v52\n"
"v_mfma_f32_16x16x32_bf16 v[184:187], v[20:23], v[48:51], v[184:187]\n"
"ds_read_b128 v[20:23], v245 offset:7136\n"
"s_waitcnt lgkmcnt(4)\n"
"v_mfma_f32_16x16x32_bf16 v[196:199], v[164:167], v[28:31], v[196:199]\n"
"v_fma_f32 v176, -v189, v249, v176\n"
"v_fmac_f32 v178, v189, v247\n"
"v_mfma_f32_16x16x32_bf16 v[200:203], v[164:167], v[48:51], v[200:203]\n"
"ds_read_b128 v[36:39], v245 offset:11712\n"
"s_waitcnt lgkmcnt(4)\n"
"s_barrier\n"
"v_mfma_f32_16x16x32_bf16 v[204:207], v[168:171], v[28:31], v[204:207]\n"
"ds_read_b128 v[164:167], v245 offset:16288\n"
"v_fma_f32 v177, v188, v176, v45\n"
"v_fma_f32 v179, v188, v178, v53\n"
"v_mfma_f32_16x16x32_bf16 v[208:211], v[168:171], v[48:51], v[208:211]\n"
"ds_read_b128 v[168:171], v235 offset:36928\n"
"s_waitcnt lgkmcnt(5)\n"
"v_mfma_f32_16x16x32_bf16 v[212:215], v[12:15], v[28:31], v[212:215]\n"
"v_fma_f32 v177, -v189, v178, v177\n"
"v_fmac_f32 v179, v189, v176\n"
"v_mfma_f32_16x16x32_bf16 v[8:11], v[12:15], v[48:51], v[8:11]\n"
"ds_read_b128 v[12:15], v235 offset:45248\n"
"s_waitcnt lgkmcnt(5)\n"
"v_mfma_f32_16x16x32_bf16 v[180:183], v[16:19], v[24:27], v[180:183]\n"
"ds_read_b128 v[28:31], v235 offset:53568\n"
"v_fma_f32 v176, v188, v177, v46\n"
"v_fma_f32 v178, v188, v179, v54\n"
"v_mfma_f32_16x16x32_bf16 v[184:187], v[16:19], v[60:63], v[184:187]\n"
"ds_read_b128 v[16:19], v235 offset:61888\n"
"s_waitcnt lgkmcnt(6)\n"
"v_mfma_f32_16x16x32_bf16 v[196:199], v[20:23], v[24:27], v[196:199]\n"
"v_fma_f32 v176, -v189, v179, v176\n"
"v_fmac_f32 v178, v189, v177\n"
"v_mfma_f32_16x16x32_bf16 v[200:203], v[20:23], v[60:63], v[200:203]\n"
"ds_read_b128 v[20:23], v234 offset:36928\n"
"s_waitcnt lgkmcnt(5)\n"
"v_mfma_f32_16x16x32_bf16 v[204:207], v[36:39], v[24:27], v[204:207]\n"
"ds_read_b128 v[48:51], v234 offset:45248\n"
"v_fma_f32 v177, v188, v176, v47\n"
"v_fma_f32 v179, v188, v178, v55\n"
"v_mfma_f32_16x16x32_bf16 v[208:211], v[36:39], v[60:63], v[208:211]\n"
"ds_read_b128 v[36:39], v234 offset:53568\n"
"v_fma_f32 v177, -v189, v178, v177\n"
"v_fmac_f32 v179, v189, v176\n"
"v_mfma_f32_16x16x32_bf16 v[212:215], v[164:167], v[24:27], v[212:215]\n"
"v_fma_f32 v176, v188, v177, v56\n"
"v_fma_f32 v178, v188, v179, v64\n"
"v_mfma_f32_16x16x32_bf16 v[8:11], v[164:167], v[60:63], v[8:11]\n"
"ds_read_b128 v[24:27], v234 offset:61888\n"
"s_waitcnt lgkmcnt(6)\n"
"v_mfma_f32_16x16x32_bf16 v[180:183], v[168:171], v[76:79], v[180:183]\n"
"v_fma_f32 v176, -v189, v179, v176\n"
"v_fmac_f32 v178, v189, v177\n"
"v_mfma_f32_16x16x32_bf16 v[184:187], v[168:171], v[80:83], v[184:187]\n"
"ds_read_b128 v[60:63], v235 offset:38976\n"
"v_fma_f32 v164, v188, v176, v57\n"
"v_fma_f32 v166, v188, v178, v65\n"
"v_mfma_f32_16x16x32_bf16 v[196:199], v[12:15], v[76:79], v[196:199]\n"
"v_fma_f32 v164, -v189, v178, v164\n"
"v_fmac_f32 v166, v189, v176\n"
"v_mfma_f32_16x16x32_bf16 v[200:203], v[12:15], v[80:83], v[200:203]\n"
"ds_read_b128 v[12:15], v235 offset:47296\n"
"s_waitcnt lgkmcnt(6)\n"
"v_mfma_f32_16x16x32_bf16 v[204:207], v[28:31], v[76:79], v[204:207]\n"
"v_fma_f32 v165, v188, v164, v58\n"
"v_fma_f32 v167, v188, v166, v66\n"
"v_mfma_f32_16x16x32_bf16 v[208:211], v[28:31], v[80:83], v[208:211]\n"
"ds_read_b128 v[28:31], v235 offset:55616\n"
"v_fma_f32 v165, -v189, v166, v165\n"
"v_fmac_f32 v167, v189, v164\n"
"v_mfma_f32_16x16x32_bf16 v[212:215], v[16:19], v[76:79], v[212:215]\n"
"v_fma_f32 v76, v188, v165, v59\n"
"v_fma_f32 v78, v188, v167, v67\n"
"v_mfma_f32_16x16x32_bf16 v[8:11], v[16:19], v[80:83], v[8:11]\n"
"ds_read_b128 v[16:19], v235 offset:63936\n"
"s_waitcnt lgkmcnt(6)\n"
"v_mfma_f32_16x16x32_bf16 v[180:183], v[20:23], v[84:87], v[180:183]\n"
"v_fma_f32 v76, -v189, v167, v76\n"
"v_fmac_f32 v78, v189, v165\n"
"v_mfma_f32_16x16x32_bf16 v[184:187], v[20:23], v[88:91], v[184:187]\n"
"ds_read_b128 v[20:23], v234 offset:38976\n"
"v_fma_f32 v77, v188, v76, v68\n"
"v_fma_f32 v79, v188, v78, v72\n"
"v_mfma_f32_16x16x32_bf16 v[196:199], v[48:51], v[84:87], v[196:199]\n"
"v_fma_f32 v77, -v189, v78, v77\n"
"v_fmac_f32 v79, v189, v76\n"
"v_mfma_f32_16x16x32_bf16 v[200:203], v[48:51], v[88:91], v[200:203]\n"
"ds_read_b128 v[48:51], v234 offset:47296\n"
"s_waitcnt lgkmcnt(6)\n"
"v_mfma_f32_16x16x32_bf16 v[204:207], v[36:39], v[84:87], v[204:207]\n"
"v_fma_f32 v76, v188, v77, v69\n"
"v_fma_f32 v80, v188, v79, v73\n"
"v_mfma_f32_16x16x32_bf16 v[208:211], v[36:39], v[88:91], v[208:211]\n"
"ds_read_b128 v[36:39], v234 offset:55616\n"
"v_fma_f32 v76, -v189, v79, v76\n"
"v_fmac_f32 v80, v189, v77\n"
"v_mfma_f32_16x16x32_bf16 v[212:215], v[24:27], v[84:87], v[212:215]\n"
"v_fma_f32 v78, v188, v76, v70\n"
"v_fma_f32 v79, v188, v80, v74\n"
"v_mfma_f32_16x16x32_bf16 v[8:11], v[24:27], v[88:91], v[8:11]\n"
"ds_read_b128 v[24:27], v234 offset:63936\n"
"s_waitcnt lgkmcnt(6)\n"
"v_mfma_f32_16x16x32_bf16 v[180:183], v[60:63], v[92:95], v[180:183]\n"
"v_fma_f32 v78, -v189, v80, v78\n"
"v_fmac_f32 v79, v189, v76\n"
"v_mfma_f32_16x16x32_bf16 v[184:187], v[60:63], v[0:3], v[184:187]\n"
"ds_read_b128 v[60:63], v218\n"
"v_fma_f32 v76, v188, v78, v71\n"
"v_fma_f32 v77, v188, v79, v75\n"
"v_mfma_f32_16x16x32_bf16 v[196:199], v[12:15], v[92:95], v[196:199]\n"
"v_fma_f32 v76, -v189, v79, v76\n"
"v_fmac_f32 v77, v189, v78\n"
"v_mfma_f32_16x16x32_bf16 v[200:203], v[12:15], v[0:3], v[200:203]\n"
"ds_read_b128 v[12:15], v218 offset:2048\n"
"s_waitcnt lgkmcnt(6)\n"
"v_mfma_f32_16x16x32_bf16 v[204:207], v[28:31], v[92:95], v[204:207]\n"
"v_mov_b32 v79, v76\n"
"v_mov_b32 v80, v77\n"
"v_mfma_f32_16x16x32_bf16 v[208:211], v[28:31], v[0:3], v[208:211]\n"
"ds_read_b128 v[28:31], v218 offset:4096\n"
"s_waitcnt lgkmcnt(3)\n"
"s_barrier\n"
"v_mfma_f32_16x16x32_bf16 v[212:215], v[16:19], v[92:95], v[212:215]\n"
"v_mul_f32 v78, v190, v32\n"
"v_fma_f32 v78, -v191, v40, v78\n"
"v_mfma_f32_16x16x32_bf16 v[8:11], v[16:19], v[0:3], v[8:11]\n"
"ds_read_b128 v[0:3], v218 offset:6144\n"
"v_mfma_f32_16x16x32_bf16 v[180:183], v[20:23], v[100:103], v[180:183]\n"
"v_permlane16_swap_b32 v76, v79\n"
"v_permlane16_swap_b32 v77, v80\n"
"v_mfma_f32_16x16x32_bf16 v[184:187], v[20:23], v[96:99], v[184:187]\n"
"ds_read_b128 v[16:19], v219\n"
"v_fmac_f32 v79, v192, v76\n"
"v_fmac_f32 v80, v192, v77\n"
"v_mfma_f32_16x16x32_bf16 v[196:199], v[48:51], v[100:103], v[196:199]\n"
"v_fma_f32 v79, -v193, v77, v79\n"
"v_fmac_f32 v80, v193, v76\n"
"v_mfma_f32_16x16x32_bf16 v[200:203], v[48:51], v[96:99], v[200:203]\n"
"ds_read_b128 v[20:23], v219 offset:2048\n"
"v_mfma_f32_16x16x32_bf16 v[204:207], v[36:39], v[100:103], v[204:207]\n"
"v_mov_b32 v81, v79\n"
"v_mov_b32 v82, v80\n"
"v_mfma_f32_16x16x32_bf16 v[208:211], v[36:39], v[96:99], v[208:211]\n"
"ds_read_b128 v[36:39], v219 offset:4096\n"
"v_mul_f32_e64 v40, -v190, v40\n"
"v_fma_f32 v40, -v191, v32, v40\n"
"v_mfma_f32_16x16x32_bf16 v[212:215], v[24:27], v[100:103], v[212:215]\n"
"v_permlane32_swap_b32 v79, v81\n"
"v_permlane32_swap_b32 v80, v82\n"
"v_mfma_f32_16x16x32_bf16 v[8:11], v[24:27], v[96:99], v[8:11]\n"
"ds_read_b128 v[24:27], v219 offset:6144\n"
"s_waitcnt lgkmcnt(6)\n"
"v_mfma_f32_16x16x32_bf16 v[180:183], v[60:63], v[108:111], v[180:183]\n"
"v_fmac_f32 v81, v194, v79\n"
"v_fmac_f32 v82, v194, v80\n"
"v_mfma_f32_16x16x32_bf16 v[184:187], v[60:63], v[112:115], v[184:187]\n"
"ds_read_b128 v[48:51], v218 offset:8192\n"
"v_fma_f32 v81, -v195, v80, v81\n"
"v_fmac_f32 v82, v195, v79\n"
"v_mfma_f32_16x16x32_bf16 v[196:199], v[12:15], v[108:111], v[196:199]\n"
"v_bfi_b32 v60, s96, v236, v81\n"
"v_bfi_b32 v61, s96, v236, v82\n"
"v_mfma_f32_16x16x32_bf16 v[200:203], v[12:15], v[112:115], v[200:203]\n"
"ds_read_b128 v[12:15], v218 offset:10240\n"
"s_waitcnt lgkmcnt(6)\n"
"v_mfma_f32_16x16x32_bf16 v[204:207], v[28:31], v[108:111], v[204:207]\n"
"s_setprio 3\n"
"global_store_dwordx2 v228, v[60:61], s[64:65] offset:2048 sc1\n"
"s_setprio 0\n"
"v_mfma_f32_16x16x32_bf16 v[208:211], v[28:31], v[112:115], v[208:211]\n"
"ds_read_b128 v[28:31], v218 offset:12288\n"
"v_mfma_f32_16x16x32_bf16 v[212:215], v[0:3], v[108:111], v[212:215]\n"
"v_mul_f32 v76, v174, v76\n"
"v_mul_f32 v77, v174, v77\n"
"v_mfma_f32_16x16x32_bf16 v[8:11], v[0:3], v[112:115], v[8:11]\n"
"ds_read_b128 v[0:3], v218 offset:14336\n"
"s_waitcnt lgkmcnt(6)\n"
"v_mfma_f32_16x16x32_bf16 v[180:183], v[16:19], v[104:107], v[180:183]\n"
"v_fmac_f32 v76, v172, v79\n"
"v_fmac_f32 v77, v172, v80\n"
"v_mfma_f32_16x16x32_bf16 v[184:187], v[16:19], v[116:119], v[184:187]\n"
"ds_read_b128 v[16:19], v219 offset:8192\n"
"v_mfma_f32_16x16x32_bf16 v[196:199], v[20:23], v[104:107], v[196:199]\n"
"v_fma_f32 v76, -v173, v80, v76\n"
"v_fmac_f32 v77, v173, v79\n"
"v_mfma_f32_16x16x32_bf16 v[200:203], v[20:23], v[116:119], v[200:203]\n"
"ds_read_b128 v[20:23], v219 offset:10240\n"
"s_waitcnt lgkmcnt(6)\n"
"v_mfma_f32_16x16x32_bf16 v[204:207], v[36:39], v[104:107], v[204:207]\n"
"v_mul_f32 v60, v190, v33\n"
"v_fma_f32 v60, -v191, v41, v60\n"
"v_mfma_f32_16x16x32_bf16 v[208:211], v[36:39], v[116:119], v[208:211]\n"
"ds_read_b128 v[36:39], v219 offset:12288\n"
"v_mfma_f32_16x16x32_bf16 v[212:215], v[24:27], v[104:107], v[212:215]\n"
"v_mul_f32_e64 v41, -v190, v41\n"
"v_fma_f32 v41, -v191, v33, v41\n"
"v_mfma_f32_16x16x32_bf16 v[8:11], v[24:27], v[116:119], v[8:11]\n"
"ds_read_b128 v[24:27], v219 offset:14336\n"
"s_waitcnt lgkmcnt(6)\n"
"v_mfma_f32_16x16x32_bf16 v[180:183], v[48:51], v[120:123], v[180:183]\n"
"v_mul_f32 v79, v190, v34\n"
"v_fma_f32 v79, -v191, v42, v79\n"
"v_mfma_f32_16x16x32_bf16 v[184:187], v[48:51], v[124:127], v[184:187]\n"
"ds_read_b128 v[48:51], v218 offset:16384\n"
"v_mfma_f32_16x16x32_bf16 v[196:199], v[12:15], v[120:123], v[196:199]\n"
"v_mul_f32_e64 v42, -v190, v42\n"
"v_fma_f32 v42, -v191, v34, v42\n"
"v_mfma_f32_16x16x32_bf16 v[200:203], v[12:15], v[124:127], v[200:203]\n"
"ds_read_b128 v[12:15], v218 offset:18432\n"
"s_waitcnt lgkmcnt(6)\n"
"v_mfma_f32_16x16x32_bf16 v[204:207], v[28:31], v[120:123], v[204:207]\n"
"v_mul_f32 v80, v190, v35\n"
"v_fma_f32 v80, -v191, v43, v80\n"
"v_mfma_f32_16x16x32_bf16 v[208:211], v[28:31], v[124:127], v[208:211]\n"
"ds_read_b128 v[28:31], v218 offset:20480\n"
"v_mfma_f32_16x16x32_bf16 v[212:215], v[0:3], v[120:123], v[212:215]\n"
"v_mul_f32_e64 v43, -v190, v43\n"
"v_fma_f32 v43, -v191, v35, v43\n"
"v_mfma_f32_16x16x32_bf16 v[8:11], v[0:3], v[124:127], v[8:11]\n"
"ds_read_b128 v[0:3], v218 offset:22528\n"
"s_waitcnt lgkmcnt(6)\n"
"v_mfma_f32_16x16x32_bf16 v[180:183], v[16:19], v[4:7], v[180:183]\n"
"v_mul_f32 v81, v190, v44\n"
"v_fma_f32 v81, -v191, v52, v81\n"
"v_mfma_f32_16x16x32_bf16 v[184:187], v[16:19], v[128:131], v[184:187]\n"
"ds_read_b128 v[16:19], v219 offset:16384\n"
"v_mfma_f32_16x16x32_bf16 v[196:199], v[20:23], v[4:7], v[196:199]\n"
"v_mul_f32_e64 v52, -v190, v52\n"
"v_fma_f32 v52, -v191, v44, v52\n"
"v_mfma_f32_16x16x32_bf16 v[200:203], v[20:23], v[128:131], v[200:203]\n"
"ds_read_b128 v[20:23], v219 offset:18432\n"
"s_waitcnt lgkmcnt(6)\n"
"v_mfma_f32_16x16x32_bf16 v[204:207], v[36:39], v[4:7], v[204:207]\n"
"v_mul_f32 v82, v190, v45\n"
"v_fma_f32 v82, -v191, v53, v82\n"
"v_mfma_f32_16x16x32_bf16 v[208:211], v[36:39], v[128:131], v[208:211]\n"
"ds_read_b128 v[32:35], v219 offset:20480\n"
"v_mfma_f32_16x16x32_bf16 v[212:215], v[24:27], v[4:7], v[212:215]\n"
"v_mul_f32_e64 v53, -v190, v53\n"
"v_fma_f32 v53, -v191, v45, v53\n"
"v_mfma_f32_16x16x32_bf16 v[8:11], v[24:27], v[128:131], v[8:11]\n"
"ds_read_b128 v[4:7], v219 offset:22528\n"
"s_waitcnt lgkmcnt(6)\n"
"v_mfma_f32_16x16x32_bf16 v[180:183], v[48:51], v[132:135], v[180:183]\n"
"v_mul_f32 v84, v190, v46\n"
"v_fma_f32 v84, -v191, v54, v84\n"
"v_mfma_f32_16x16x32_bf16 v[184:187], v[48:51], v[136:139], v[184:187]\n"
"ds_read_b128 v[24:27], v218 offset:24576\n"
"v_mfma_f32_16x16x32_bf16 v[196:199], v[12:15], v[132:135], v[196:199]\n"
"v_mul_f32_e64 v54, -v190, v54\n"
"v_fma_f32 v54, -v191, v46, v54\n"
"v_mfma_f32_16x16x32_bf16 v[200:203], v[12:15], v[136:139], v[200:203]\n"
"ds_read_b128 v[12:15], v219 offset:24576\n"
"s_waitcnt lgkmcnt(6)\n"
"v_mfma_f32_16x16x32_bf16 v[204:207], v[28:31], v[132:135], v[204:207]\n"
"v_add_u32 v36, s56, v226\n"
"v_lshrrev_b32 v37, 4, v227\n"
"v_mfma_f32_16x16x32_bf16 v[208:211], v[28:31], v[136:139], v[208:211]\n"
"ds_read_b128 v[28:31], v218 offset:26624\n"
"v_mfma_f32_16x16x32_bf16 v[212:215], v[0:3], v[132:135], v[212:215]\n"
"v_xor_b32 v37, v37, v227\n"
"v_and_b32 v37, 7, v37\n"
"v_mfma_f32_16x16x32_bf16 v[8:11], v[0:3], v[136:139], v[8:11]\n"
"ds_read_b128 v[0:3], v219 offset:26624\n"
"s_waitcnt lgkmcnt(6)\n"
"v_mfma_f32_16x16x32_bf16 v[180:183], v[16:19], v[140:143], v[180:183]\n"
"v_lshlrev_b32 v37, 4, v37\n"
"v_lshrrev_b32 v85, 3, v227\n"
"v_mfma_f32_16x16x32_bf16 v[184:187], v[16:19], v[144:147], v[184:187]\n"
"ds_read_b128 v[16:19], v218 offset:28672\n"
"v_mfma_f32_16x16x32_bf16 v[196:199], v[20:23], v[140:143], v[196:199]\n"
"v_lshl_or_b32 v85, v85, 13, v37\n"
"v_xor_b32 v86, 0xc40, v85\n"
"v_mfma_f32_16x16x32_bf16 v[200:203], v[20:23], v[144:147], v[200:203]\n"
"ds_read_b128 v[20:23], v219 offset:28672\n"
"s_waitcnt lgkmcnt(6)\n"
"v_mfma_f32_16x16x32_bf16 v[204:207], v[32:35], v[140:143], v[204:207]\n"
"v_add_u32 v97, 0x800, v85\n"
"v_add_u32 v96, 0x800, v86\n"
"v_mfma_f32_16x16x32_bf16 v[208:211], v[32:35], v[144:147], v[208:211]\n"
"ds_read_b128 v[32:35], v218 offset:30720\n"
"v_mfma_f32_16x16x32_bf16 v[212:215], v[4:7], v[140:143], v[212:215]\n"
"v_mul_f32 v89, v190, v47\n"
"v_fma_f32 v89, -v191, v55, v89\n"
"v_mfma_f32_16x16x32_bf16 v[8:11], v[4:7], v[144:147], v[8:11]\n"
"ds_read_b128 v[4:7], v219 offset:30720\n"
"s_waitcnt lgkmcnt(6)\n"
"v_mfma_f32_16x16x32_bf16 v[180:183], v[24:27], v[148:151], v[180:183]\n"
"v_mul_f32_e64 v55, -v190, v55\n"
"v_fma_f32 v55, -v191, v47, v55\n"
"v_mfma_f32_16x16x32_bf16 v[184:187], v[24:27], v[152:155], v[184:187]\n"
"v_mul_f32 v87, v190, v56\n"
"v_fma_f32 v87, -v191, v64, v87\n"
"v_mfma_f32_16x16x32_bf16 v[180:183], v[12:15], v[156:159], v[180:183]\n"
"v_mul_f32_e64 v64, -v190, v64\n"
"v_fma_f32 v64, -v191, v56, v64\n"
"v_mfma_f32_16x16x32_bf16 v[184:187], v[12:15], v[160:163], v[184:187]\n"
"s_waitcnt lgkmcnt(4)\n"
"v_mfma_f32_16x16x32_bf16 v[196:199], v[28:31], v[148:151], v[196:199]\n"
"v_mul_f32 v88, v190, v57\n"
"v_fma_f32 v88, -v191, v65, v88\n"
"v_mfma_f32_16x16x32_bf16 v[200:203], v[28:31], v[152:155], v[200:203]\n"
"s_waitcnt lgkmcnt(0)\n"
"s_barrier\n"
"v_mfma_f32_16x16x32_bf16 v[196:199], v[0:3], v[156:159], v[196:199]\n"
"v_mul_f32_e64 v65, -v190, v65\n"
"v_fma_f32 v65, -v191, v57, v65\n"
"v_mfma_f32_16x16x32_bf16 v[200:203], v[0:3], v[160:163], v[200:203]\n"
"v_mfma_f32_16x16x32_bf16 v[204:207], v[16:19], v[148:151], v[204:207]\n"
"ds_write_b128 v36, v[180:183]\n"
"v_mfma_f32_16x16x32_bf16 v[208:211], v[16:19], v[152:155], v[208:211]\n"
"v_mul_f32 v98, v190, v58\n"
"v_fma_f32 v98, -v191, v66, v98\n"
"v_mfma_f32_16x16x32_bf16 v[204:207], v[20:23], v[156:159], v[204:207]\n"
"ds_write_b128 v36, v[184:187] offset:1024\n"
"v_mfma_f32_16x16x32_bf16 v[208:211], v[20:23], v[160:163], v[208:211]\n"
"v_mfma_f32_16x16x32_bf16 v[212:215], v[32:35], v[148:151], v[212:215]\n"
"ds_write_b128 v36, v[196:199] offset:2048\n"
"v_mfma_f32_16x16x32_bf16 v[8:11], v[32:35], v[152:155], v[8:11]\n"
"v_mul_f32_e64 v66, -v190, v66\n"
"v_fma_f32 v66, -v191, v58, v66\n"
"v_mfma_f32_16x16x32_bf16 v[212:215], v[4:7], v[156:159], v[212:215]\n"
"ds_write_b128 v36, v[200:203] offset:3072\n"
"v_mfma_f32_16x16x32_bf16 v[8:11], v[4:7], v[160:163], v[8:11]\n"
"v_mul_f32 v99, v190, v59\n"
"v_fma_f32 v99, -v191, v67, v99\n"
"ds_write_b128 v36, v[204:207] offset:16384\n"
"ds_write_b128 v36, v[208:211] offset:17408\n"
"s_nop 1\n"
"ds_write_b128 v36, v[212:215] offset:18432\n"
"s_nop 0\n"
"ds_write_b128 v36, v[8:11] offset:19456\n"
"s_waitcnt lgkmcnt(0)\n"
"s_barrier\n"
"buffer_load_dwordx2 v[28:29], v220, s[76:79], 0 offen offset:3584 sc1\n"
"buffer_load_dwordx2 v[26:27], v220, s[76:79], 0 offen offset:3072 sc1\n"
"buffer_load_dwordx2 v[24:25], v220, s[76:79], 0 offen offset:2560 sc1\n"
"buffer_load_dwordx2 v[44:45], v220, s[76:79], 0 offen offset:2048 sc1\n"
"v_mul_f32_e64 v67, -v190, v67\n"
"v_fma_f32 v67, -v191, v59, v67\n"
"v_mul_f32 v100, v190, v68\n"
"v_fma_f32 v100, -v191, v72, v100\n"
"s_barrier\n"
"v_mul_f32_e64 v72, -v190, v72\n"
"v_fma_f32 v72, -v191, v68, v72\n"
"v_mul_f32 v101, v190, v69\n"
"v_fma_f32 v101, -v191, v73, v101\n"
"s_add_u32 m0, s56, 0\n"
"s_nop 0\n"
"global_load_lds_dwordx4 v85, s[68:69]\n"
"global_load_lds_dwordx4 v86, s[68:69] offset:1024\n"
"v_mul_f32_e64 v73, -v190, v73\n"
"v_fma_f32 v73, -v191, v69, v73\n"
"global_load_lds_dwordx4 v85, s[70:71] offset:2048\n"
"v_mul_f32 v116, v190, v70\n"
"v_fma_f32 v116, -v191, v74, v116\n"
"global_load_lds_dwordx4 v86, s[70:71] offset:3072\n"
"v_mul_f32_e64 v74, -v190, v74\n"
"v_fma_f32 v74, -v191, v70, v74\n"
"v_mul_f32 v117, v190, v71\n"
"v_fma_f32 v117, -v191, v75, v117\n"
"s_add_u32 m0, s56, 0x4000\n"
"s_nop 0\n"
"global_load_lds_dwordx4 v97, s[68:69]\n"
"global_load_lds_dwordx4 v96, s[68:69] offset:1024\n"
"v_mul_f32_e64 v75, -v190, v75\n"
"v_fma_f32 v75, -v191, v71, v75\n"
"global_load_lds_dwordx4 v97, s[70:71] offset:2048\n"
"v_and_b32 v8, 15, v227\n"
"v_lshrrev_b32 v9, 4, v227\n"
"v_lshl_add_u32 v8, v9, 3, v8\n"
"v_sub_u32 v8, 15, v8\n"
"s_lshl2_add_u32 s12, s15, 0x27580\n"
"v_mov_b32 v10, s12\n"
"ds_read_b32 v10, v10\n"
"global_load_lds_dwordx4 v96, s[70:71] offset:3072\n"
"v_and_b32 v9, 1, v8\n"
"v_lshlrev_b32 v9, 4, v9\n"
"v_mov_b32 v11, 0xffff0000\n"
"v_lshlrev_b32 v11, v9, v11\n"
"v_lshlrev_b32 v9, v9, v223\n"
"v_ashrrev_i32 v8, 1, v8\n"
"v_cmp_eq_u32_e64 s[92:93], 0, v8\n"
"v_cmp_gt_i32_e64 s[94:95], 0, v8\n"
"s_barrier\n"
"s_add_u32 s13, s49, 1\n"
"s_and_b32 s13, s13, 3\n"
"v_cndmask_b32_e64 v184, 0, v9, s[92:93]\n"
"v_cndmask_b32_e64 v185, 0, v11, s[92:93]\n"
"s_lshl_b32 s13, s13, 7\n"
"v_cndmask_b32_e64 v185, v185, -1, s[94:95]\n"
"v_cmp_eq_u32_e64 s[92:93], 1, v8\n"
"s_add_u32 s68, s72, s13\n"
"s_addc_u32 s69, s73, 0\n"
"v_cmp_gt_i32_e64 s[94:95], 1, v8\n"
"v_cndmask_b32_e64 v186, 0, v9, s[92:93]\n"
"s_add_u32 s70, s74, s13\n"
"s_addc_u32 s71, s75, 0\n"
"v_cndmask_b32_e64 v187, 0, v11, s[92:93]\n"
"v_cndmask_b32_e64 v187, v187, -1, s[94:95]\n"
"s_add_u32 m0, s56, 0x8000\n"
"s_nop 0\n"
"global_load_lds_dwordx4 v85, s[68:69]\n"
"global_load_lds_dwordx4 v86, s[68:69] offset:1024\n"
"global_load_lds_dwordx4 v85, s[70:71] offset:2048\n"
"global_load_lds_dwordx4 v86, s[70:71] offset:3072\n"
"v_cmp_eq_u32_e64 s[92:93], 2, v8\n"
"v_cmp_gt_i32_e64 s[94:95], 2, v8\n"
"s_nop 0\n"
"v_cndmask_b32_e64 v192, 0, v9, s[92:93]\n"
"v_cndmask_b32_e64 v193, 0, v11, s[92:93]\n"
"ds_read_b128 v[0:3], v222 offset:512\n"
"v_cndmask_b32_e64 v193, v193, -1, s[94:95]\n"
"v_cmp_eq_u32_e64 s[92:93], 3, v8\n"
"ds_read_b128 v[4:7], v221 offset:1024\n"
"s_waitcnt lgkmcnt(2)\n"
"v_readfirstlane_b32 s7, v10\n"
"s_nop 0\n"
"s_xor_b32 s66, s66, s7\n"
"v_cmp_gt_i32_e64 s[94:95], 3, v8\n"
"v_cndmask_b32_e64 v194, 0, v9, s[92:93]\n"
"v_cndmask_b32_e64 v195, 0, v11, s[92:93]\n"
"v_cndmask_b32_e64 v195, v195, -1, s[94:95]\n"
"s_waitcnt vmcnt(15)\n"
"v_xor_b32 v28, v28, v244\n"
"v_xor_b32 v29, v29, v244\n"
"s_waitcnt vmcnt(8)\n"
"ds_read_b128 v[8:11], v237\n"
"ds_read_b128 v[12:15], v238\n"
"ds_read_b128 v[16:19], v239\n"
"ds_read_b128 v[20:23], v240\n"
"v_xor_b32 v26, v26, v243\n"
"v_xor_b32 v27, v27, v243\n"
"v_xor_b32 v24, v24, v242\n"
"v_xor_b32 v25, v25, v242\n"
"s_add_u32 m0, s56, 0\n"
"s_waitcnt lgkmcnt(0)\n"
"global_load_lds_dwordx4 v97, s[68:69]\n"
"global_load_lds_dwordx4 v96, s[68:69] offset:1024\n"
"v_xor_b32 v44, v44, v241\n"
"v_xor_b32 v45, v45, v241\n"
"global_load_lds_dwordx4 v97, s[70:71] offset:2048\n"
"v_or3_b32 v30, v28, v29, v26\n"
"v_or3_b32 v30, v30, v27, v24\n"
"global_load_lds_dwordx4 v96, s[70:71] offset:3072\n"
"v_or3_b32 v30, v30, v25, v44\n"
"v_bitop3_b32 v30, v30, v45, s96 bitop3:0xa8\n"
"v_cmp_ne_u32 vcc, 0, v30\n"
"s_nop 0\n"
"s_cbranch_vccnz .Lr1\n"
".Lb1:\n"
"v_fmac_f32 v26, v2, v28\n"
"v_fmac_f32 v27, v2, v29\n"
"v_fma_f32 v26, -v3, v29, v26\n"
"v_fmac_f32 v27, v3, v28\n"
"v_fmac_f32 v24, v2, v26\n"
"v_fmac_f32 v25, v2, v27\n"
"v_fma_f32 v24, -v3, v27, v24\n"
"v_fmac_f32 v25, v3, v26\n"
"v_fmac_f32 v44, v2, v24\n"
"v_fmac_f32 v45, v2, v25\n"
"v_fma_f32 v44, -v3, v25, v44\n"
"v_fmac_f32 v45, v3, v24\n"
"v_mul_f32 v46, v6, v44\n"
"v_mul_f32 v47, v6, v45\n"
"s_waitcnt vmcnt(8)\n"
"ds_read_b128 v[24:27], v237 offset:16384\n"
"ds_read_b128 v[28:31], v238 offset:16384\n"
"ds_read_b128 v[32:35], v239 offset:16384\n"
"ds_read_b128 v[36:39], v240 offset:16384\n"
"v_fma_f32 v46, -v7, v45, v46\n"
"v_fmac_f32 v47, v7, v44\n"
"s_nop 1\n"
"v_permlane16_swap_b32 v46, v47\n"
"v_add_f32 v46, v46, v47\n"
"s_add_u32 s13, s49, 2\n"
"s_and_b32 s13, s13, 3\n"
"v_mov_b32 v47, v46\n"
"s_nop 1\n"
"v_permlane32_swap_b32 v46, v47\n"
"s_lshl_b32 s13, s13, 7\n"
"v_add_f32 v46, v46, v47\n"
"v_mov_b32 v47, v46\n"
"s_add_u32 s68, s72, s13\n"
"s_addc_u32 s69, s73, 0\n"
"v_permlane16_swap_b32 v46, v47\n"
"v_fmac_f32 v76, v4, v46\n"
"s_add_u32 s70, s74, s13\n"
"s_addc_u32 s71, s75, 0\n"
"v_fmac_f32 v77, v4, v47\n"
"v_fma_f32 v76, -v5, v47, v76\n"
"s_add_u32 m0, s56, 0x4000\n"
"s_waitcnt lgkmcnt(0)\n"
"global_load_lds_dwordx4 v85, s[68:69]\n"
"global_load_lds_dwordx4 v86, s[68:69] offset:1024\n"
"global_load_lds_dwordx4 v85, s[70:71] offset:2048\n"
"global_load_lds_dwordx4 v86, s[70:71] offset:3072\n"
"v_fmac_f32 v77, v5, v46\n"
"v_fmac_f32 v78, v0, v76\n"
"v_fma_f32 v40, -v0, v77, v40\n"
"v_fma_f32 v78, -v1, v77, v78\n"
"v_fma_f32 v40, -v1, v76, v40\n"
"v_fmac_f32 v60, v188, v78\n"
"v_fmac_f32 v41, v188, v40\n"
"v_fmac_f32 v60, v189, v40\n"
"v_fma_f32 v41, -v189, v78, v41\n"
"v_fmac_f32 v79, v188, v60\n"
"v_fmac_f32 v42, v188, v41\n"
"v_fmac_f32 v79, v189, v41\n"
"s_waitcnt vmcnt(8)\n"
"ds_read_b128 v[0:3], v237 offset:32768\n"
"ds_read_b128 v[4:7], v238 offset:32768\n"
"ds_read_b128 v[44:47], v239 offset:32768\n"
"ds_read_b128 v[48:51], v240 offset:32768\n"
"v_fma_f32 v42, -v189, v60, v42\n"
"v_cvt_pk_bf16_f32 v56, v78, v60\n"
"v_cvt_pk_bf16_f32 v60, v40, v41\n"
"v_fmac_f32 v80, v188, v79\n"
"s_add_u32 m0, s56, 0x8000\n"
"s_waitcnt lgkmcnt(0)\n"
"global_load_lds_dwordx4 v97, s[68:69]\n"
"global_load_lds_dwordx4 v96, s[68:69] offset:1024\n"
"v_fmac_f32 v43, v188, v42\n"
"v_fmac_f32 v80, v189, v42\n"
"global_load_lds_dwordx4 v97, s[70:71] offset:2048\n"
"v_fma_f32 v43, -v189, v79, v43\n"
"v_fmac_f32 v81, v188, v80\n"
"global_load_lds_dwordx4 v96, s[70:71] offset:3072\n"
"v_fmac_f32 v52, v188, v43\n"
"v_fmac_f32 v81, v189, v43\n"
"v_fma_f32 v52, -v189, v80, v52\n"
"v_cvt_pk_bf16_f32 v57, v79, v80\n"
"v_cvt_pk_bf16_f32 v61, v42, v43\n"
"v_fmac_f32 v82, v188, v81\n"
"v_fmac_f32 v53, v188, v52\n"
"v_fmac_f32 v82, v189, v52\n"
"v_fma_f32 v53, -v189, v81, v53\n"
"v_fmac_f32 v84, v188, v82\n"
"v_fmac_f32 v54, v188, v53\n"
"v_fmac_f32 v84, v189, v53\n"
"v_fma_f32 v54, -v189, v82, v54\n"
"v_cvt_pk_bf16_f32 v58, v81, v82\n"
"v_cvt_pk_bf16_f32 v62, v52, v53\n"
"v_fmac_f32 v89, v188, v84\n"
"s_waitcnt vmcnt(8)\n"
"ds_read_b128 v[40:43], v237\n"
"ds_read_b128 v[68:71], v238\n"
"ds_read_b128 v[76:79], v239\n"
"ds_read_b128 v[80:83], v240\n"
"v_fmac_f32 v55, v188, v54\n"
"v_fmac_f32 v89, v189, v54\n"
"v_fma_f32 v55, -v189, v84, v55\n"
"v_fmac_f32 v87, v188, v89\n"
"s_add_u32 s13, s49, 3\n"
"s_and_b32 s13, s13, 3\n"
"v_fmac_f32 v64, v188, v55\n"
"v_fmac_f32 v87, v189, v55\n"
"s_lshl_b32 s13, s13, 7\n"
"v_fma_f32 v64, -v189, v89, v64\n"
"v_cvt_pk_bf16_f32 v59, v84, v89\n"
"s_add_u32 s68, s72, s13\n"
"s_addc_u32 s69, s73, 0\n"
"v_cvt_pk_bf16_f32 v63, v54, v55\n"
"global_store_dwordx4 v217, v[56:59], s[66:67] sc1\n"
"s_add_u32 s70, s74, s13\n"
"s_addc_u32 s71, s75, 0\n"
"global_store_dwordx4 v217, v[60:63], s[66:67] offset:512 sc1\n"
"s_add_u32 m0, s56, 0\n"
"s_waitcnt lgkmcnt(0)\n"
"global_load_lds_dwordx4 v85, s[68:69]\n"
"global_load_lds_dwordx4 v86, s[68:69] offset:1024\n"
"global_load_lds_dwordx4 v85, s[70:71] offset:2048\n"
"global_load_lds_dwordx4 v86, s[70:71] offset:3072\n"
"v_fmac_f32 v88, v188, v87\n"
"v_fmac_f32 v65, v188, v64\n"
"v_fmac_f32 v88, v189, v64\n"
"v_fma_f32 v65, -v189, v87, v65\n"
"v_fmac_f32 v98, v188, v88\n"
"v_fmac_f32 v66, v188, v65\n"
"v_fmac_f32 v98, v189, v65\n"
"v_fma_f32 v66, -v189, v88, v66\n"
"v_cvt_pk_bf16_f32 v52, v87, v88\n"
"v_cvt_pk_bf16_f32 v56, v64, v65\n"
"v_fmac_f32 v99, v188, v98\n"
"v_fmac_f32 v67, v188, v66\n"
"s_waitcnt vmcnt(10)\n"
"ds_read_b128 v[60:63], v237 offset:16384\n"
"ds_read_b128 v[84:87], v238 offset:16384\n"
"ds_read_b128 v[88:91], v239 offset:16384\n"
"ds_read_b128 v[92:95], v240 offset:16384\n"
"v_fmac_f32 v99, v189, v66\n"
"v_fma_f32 v67, -v189, v98, v67\n"
"v_fmac_f32 v100, v188, v99\n"
"v_fmac_f32 v72, v188, v67\n"
"s_add_u32 m0, s56, 0x4000\n"
"s_waitcnt lgkmcnt(0)\n"
"global_load_lds_dwordx4 v97, s[68:69]\n"
"global_load_lds_dwordx4 v96, s[68:69] offset:1024\n"
"v_fmac_f32 v100, v189, v67\n"
"v_fma_f32 v72, -v189, v99, v72\n"
"global_load_lds_dwordx4 v97, s[70:71] offset:2048\n"
"v_cvt_pk_bf16_f32 v53, v98, v99\n"
"v_cvt_pk_bf16_f32 v57, v66, v67\n"
"global_load_lds_dwordx4 v96, s[70:71] offset:3072\n"
"v_fmac_f32 v101, v188, v100\n"
"v_fmac_f32 v73, v188, v72\n"
"v_fmac_f32 v101, v189, v72\n"
"v_fma_f32 v73, -v189, v100, v73\n"
"v_fmac_f32 v116, v188, v101\n"
"v_fmac_f32 v74, v188, v73\n"
"ds_read2_b32 v[64:65], v233 offset0:128 offset1:129\n"
"v_fmac_f32 v116, v189, v73\n"
"v_fma_f32 v74, -v189, v101, v74\n"
"ds_read2_b32 v[66:67], v233 offset0:130 offset1:131\n"
"ds_read_b128 v[96:99], v230 offset:3584\n"
"v_cvt_pk_bf16_f32 v54, v100, v101\n"
"v_cvt_pk_bf16_f32 v58, v72, v73\n"
"ds_read2_b32 v[100:101], v233 offset0:0 offset1:1\n"
"ds_read2_b32 v[102:103], v233 offset0:2 offset1:3\n"
"v_fmac_f32 v117, v188, v116\n"
"v_fmac_f32 v75, v188, v74\n"
"ds_read_b128 v[104:107], v230 offset:3072\n"
"ds_read2_b32 v[108:109], v232 offset0:128 offset1:129\n"
"v_fmac_f32 v117, v189, v74\n"
"v_fma_f32 v75, -v189, v116, v75\n"
"ds_read2_b32 v[110:111], v232 offset0:130 offset1:131\n"
"ds_read_b128 v[112:115], v230 offset:2560\n"
"v_cvt_pk_bf16_f32 v55, v116, v117\n"
"v_cvt_pk_bf16_f32 v59, v74, v75\n"
"global_store_dwordx4 v216, v[52:55], s[66:67] offset:256 sc1\n"
"global_store_dwordx4 v216, v[56:59], s[66:67] offset:768 sc1\n"
"s_barrier\n"
"s_waitcnt lgkmcnt(6)\n"
"v_mfma_f32_16x16x32_bf16 v[52:55], v[64:67], v[96:99], 0\n"
"ds_read_b128 v[56:59], v230\n"
"ds_read2_b32 v[64:65], v232 offset0:0 offset1:1\n"
"ds_read2_b32 v[66:67], v232 offset0:2 offset1:3\n"
"s_waitcnt lgkmcnt(7)\n"
"v_mfma_f32_16x16x32_bf16 v[72:75], v[100:103], v[96:99], 0\n"
"ds_read2_b32 v[116:117], v229 offset0:0 offset1:1\n"
"ds_read_b128 v[120:123], v230 offset:2048\n"
"ds_read2_b32 v[124:125], v231 offset0:128 offset1:129\n"
"s_waitcnt lgkmcnt(9)\n"
"v_mfma_f32_16x16x32_bf16 v[52:55], v[100:103], v[104:107], v[52:55]\n"
"ds_read2_b32 v[118:119], v229 offset0:2 offset1:3\n"
"ds_read2_b32 v[126:127], v231 offset0:130 offset1:131\n"
"ds_read_b128 v[100:103], v230 offset:1536\n"
"s_waitcnt lgkmcnt(10)\n"
"v_mfma_f32_16x16x32_bf16 v[128:131], v[108:111], v[96:99], 0\n"
"ds_read2_b32 v[132:133], v233 offset0:144 offset1:145\n"
"ds_read2_b32 v[136:137], v231 offset0:0 offset1:1\n"
"ds_read2_b32 v[138:139], v231 offset0:2 offset1:3\n"
"v_mfma_f32_16x16x32_bf16 v[72:75], v[108:111], v[104:107], v[72:75]\n"
"ds_read2_b32 v[134:135], v233 offset0:146 offset1:147\n"
"ds_read_b128 v[140:143], v230 offset:1024\n"
"ds_read_b128 v[144:147], v230 offset:512\n"
"s_waitcnt lgkmcnt(15)\n"
"v_mfma_f32_16x16x32_bf16 v[52:55], v[108:111], v[112:115], v[52:55]\n"
"ds_read2_b32 v[108:109], v233 offset0:16 offset1:17\n"
"s_waitcnt lgkmcnt(13)\n"
"v_mfma_f32_16x16x32_bf16 v[148:151], v[64:67], v[96:99], 0\n"
"ds_read2_b32 v[110:111], v233 offset0:18 offset1:19\n"
"v_mfma_f32_16x16x32_bf16 v[128:131], v[64:67], v[104:107], v[128:131]\n"
"ds_read2_b32 v[152:153], v232 offset0:144 offset1:145\n"
"v_mfma_f32_16x16x32_bf16 v[72:75], v[64:67], v[112:115], v[72:75]\n"
"ds_read2_b32 v[154:155], v232 offset0:146 offset1:147\n"
"s_waitcnt lgkmcnt(14)\n"
"v_mfma_f32_16x16x32_bf16 v[52:55], v[64:67], v[120:123], v[52:55]\n"
"ds_read2_b32 v[64:65], v229 offset0:128 offset1:129\n"
"s_waitcnt lgkmcnt(12)\n"
"v_mfma_f32_16x16x32_bf16 v[156:159], v[124:127], v[96:99], 0\n"
"ds_read2_b32 v[66:67], v229 offset0:130 offset1:131\n"
"v_mfma_f32_16x16x32_bf16 v[148:151], v[124:127], v[104:107], v[148:151]\n"
"ds_read2_b32 v[160:161], v232 offset0:16 offset1:17\n"
"v_mfma_f32_16x16x32_bf16 v[128:131], v[124:127], v[112:115], v[128:131]\n"
"ds_read2_b32 v[162:163], v232 offset0:18 offset1:19\n"
"v_mfma_f32_16x16x32_bf16 v[72:75], v[124:127], v[120:123], v[72:75]\n"
"s_waitcnt lgkmcnt(14)\n"
"v_mfma_f32_16x16x32_bf16 v[52:55], v[124:127], v[100:103], v[52:55]\n"
"s_waitcnt vmcnt(12)\n"
"ds_read_b128 v[124:127], v237 offset:32768\n"
"s_waitcnt lgkmcnt(12)\n"
"v_mfma_f32_16x16x32_bf16 v[164:167], v[136:139], v[96:99], 0\n"
"ds_read_b128 v[168:171], v238 offset:32768\n"
"ds_read_b128 v[172:175], v239 offset:32768\n"
"v_mfma_f32_16x16x32_bf16 v[156:159], v[136:139], v[104:107], v[156:159]\n"
"ds_read2_b32 v[176:177], v231 offset0:144 offset1:145\n"
"ds_read_b128 v[180:183], v240 offset:32768\n"
"v_mfma_f32_16x16x32_bf16 v[148:151], v[136:139], v[112:115], v[148:151]\n"
"ds_read2_b32 v[178:179], v231 offset0:146 offset1:147\n"
"v_mfma_f32_16x16x32_bf16 v[128:131], v[136:139], v[120:123], v[128:131]\n"
"v_and_or_b32 v116, v116, v185, v184\n"
"v_and_or_b32 v117, v117, v187, v186\n"
"v_mfma_f32_16x16x32_bf16 v[72:75], v[136:139], v[100:103], v[72:75]\n"
"v_and_or_b32 v118, v118, v193, v192\n"
"v_and_or_b32 v119, v119, v195, v194\n"
"s_waitcnt lgkmcnt(15)\n"
"v_mfma_f32_16x16x32_bf16 v[52:55], v[136:139], v[140:143], v[52:55]\n"
"s_waitcnt lgkmcnt(8)\n"
"v_mfma_f32_16x16x32_bf16 v[136:139], v[64:67], v[96:99], 0\n"
"v_mfma_f32_16x16x32_bf16 v[164:167], v[64:67], v[104:107], v[164:167]\n"
"ds_read2_b32 v[184:185], v231 offset0:16 offset1:17\n"
"v_mfma_f32_16x16x32_bf16 v[156:159], v[64:67], v[112:115], v[156:159]\n"
"ds_read2_b32 v[186:187], v231 offset0:18 offset1:19\n"
"v_mfma_f32_16x16x32_bf16 v[148:151], v[64:67], v[120:123], v[148:151]\n"
"v_mfma_f32_16x16x32_bf16 v[128:131], v[64:67], v[100:103], v[128:131]\n"
"v_mfma_f32_16x16x32_bf16 v[72:75], v[64:67], v[140:143], v[72:75]\n"
"v_mfma_f32_16x16x32_bf16 v[52:55], v[64:67], v[144:147], v[52:55]\n"
"v_mfma_f32_16x16x32_bf16 v[64:67], v[116:119], v[96:99], 0\n"
"v_mfma_f32_16x16x32_bf16 v[136:139], v[116:119], v[104:107], v[136:139]\n"
"ds_read_b128 v[96:99], v230 offset:3648\n"
"v_mfma_f32_16x16x32_bf16 v[164:167], v[116:119], v[112:115], v[164:167]\n"
"ds_read_b128 v[104:107], v230 offset:3136\n"
"v_mfma_f32_16x16x32_bf16 v[156:159], v[116:119], v[120:123], v[156:159]\n"
"ds_read_b128 v[112:115], v230 offset:2624\n"
"v_mfma_f32_16x16x32_bf16 v[148:151], v[116:119], v[100:103], v[148:151]\n"
"ds_read_b128 v[100:103], v230 offset:2112\n"
"v_mfma_f32_16x16x32_bf16 v[128:131], v[116:119], v[140:143], v[128:131]\n"
"ds_read_b128 v[120:123], v230 offset:1600\n"
"v_mfma_f32_16x16x32_bf16 v[72:75], v[116:119], v[144:147], v[72:75]\n"
"ds_read_b128 v[140:143], v230 offset:1088\n"
"v_mfma_f32_16x16x32_bf16 v[52:55], v[116:119], v[56:59], v[52:55]\n"
"ds_read_b128 v[56:59], v230 offset:576\n"
"s_waitcnt lgkmcnt(6)\n"
"v_mfma_f32_16x16x32_bf16 v[52:55], v[132:135], v[96:99], v[52:55]\n"
"ds_read_b128 v[116:119], v230 offset:64\n"
"v_mfma_f32_16x16x32_bf16 v[72:75], v[108:111], v[96:99], v[72:75]\n"
"ds_read2_b32 v[132:133], v229 offset0:16 offset1:17\n"
"s_waitcnt lgkmcnt(7)\n"
"v_mfma_f32_16x16x32_bf16 v[52:55], v[108:111], v[104:107], v[52:55]\n"
"ds_read2_b32 v[134:135], v229 offset0:18 offset1:19\n"
"v_mfma_f32_16x16x32_bf16 v[128:131], v[152:155], v[96:99], v[128:131]\n"
"ds_read2_b32 v[108:109], v233 offset0:160 offset1:161\n"
"v_mfma_f32_16x16x32_bf16 v[72:75], v[152:155], v[104:107], v[72:75]\n"
"ds_read2_b32 v[110:111], v233 offset0:162 offset1:163\n"
"s_waitcnt lgkmcnt(9)\n"
"v_mfma_f32_16x16x32_bf16 v[52:55], v[152:155], v[112:115], v[52:55]\n"
"ds_read2_b32 v[144:145], v233 offset0:32 offset1:33\n"
"s_waitcnt vmcnt(6)\n"
"v_mfma_f32_16x16x32_bf16 v[148:151], v[160:163], v[96:99], v[148:151]\n"
"ds_read2_b32 v[146:147], v233 offset0:34 offset1:35\n"
"ds_read_b128 v[152:155], v237\n"
"v_mfma_f32_16x16x32_bf16 v[128:131], v[160:163], v[104:107], v[128:131]\n"
"ds_read2_b32 v[188:189], v232 offset0:160 offset1:161\n"
"ds_read_b128 v[192:195], v238\n"
"v_mfma_f32_16x16x32_bf16 v[72:75], v[160:163], v[112:115], v[72:75]\n"
"ds_read2_b32 v[190:191], v232 offset0:162 offset1:163\n"
"s_waitcnt lgkmcnt(14)\n"
"v_mfma_f32_16x16x32_bf16 v[52:55], v[160:163], v[100:103], v[52:55]\n"
"ds_read2_b32 v[160:161], v229 offset0:144 offset1:145\n"
"ds_read_b128 v[196:199], v239\n"
"v_mfma_f32_16x16x32_bf16 v[156:159], v[176:179], v[96:99], v[156:159]\n"
"ds_read2_b32 v[162:163], v229 offset0:146 offset1:147\n"
"ds_read_b128 v[200:203], v240\n"
"v_mfma_f32_16x16x32_bf16 v[148:151], v[176:179], v[104:107], v[148:151]\n"
"ds_read2_b32 v[204:205], v232 offset0:32 offset1:33\n"
"v_mfma_f32_16x16x32_bf16 v[128:131], v[176:179], v[112:115], v[128:131]\n"
"ds_read2_b32 v[206:207], v232 offset0:34 offset1:35\n"
"v_mfma_f32_16x16x32_bf16 v[72:75], v[176:179], v[100:103], v[72:75]\n"
"s_waitcnt lgkmcnt(15)\n"
"v_mfma_f32_16x16x32_bf16 v[52:55], v[176:179], v[120:123], v[52:55]\n"
"s_setprio 3\n"
"s_waitcnt vmcnt(0)\n"
"global_store_dwordx2 v228, v[224:225], s[62:63] sc1\n"
"s_setprio 0\n"
"v_mfma_f32_16x16x32_bf16 v[164:167], v[184:187], v[96:99], v[164:167]\n"
"v_mfma_f32_16x16x32_bf16 v[156:159], v[184:187], v[104:107], v[156:159]\n"
"ds_read2_b32 v[176:177], v231 offset0:160 offset1:161\n"
"v_mfma_f32_16x16x32_bf16 v[148:151], v[184:187], v[112:115], v[148:151]\n"
"ds_read2_b32 v[178:179], v231 offset0:162 offset1:163\n"
"v_mfma_f32_16x16x32_bf16 v[128:131], v[184:187], v[100:103], v[128:131]\n"
"v_mfma_f32_16x16x32_bf16 v[72:75], v[184:187], v[120:123], v[72:75]\n"
"v_mfma_f32_16x16x32_bf16 v[52:55], v[184:187], v[140:143], v[52:55]\n"
"s_waitcnt lgkmcnt(5)\n"
"v_mfma_f32_16x16x32_bf16 v[136:139], v[160:163], v[96:99], v[136:139]\n"
"v_mfma_f32_16x16x32_bf16 v[164:167], v[160:163], v[104:107], v[164:167]\n"
"ds_read2_b32 v[184:185], v231 offset0:32 offset1:33\n"
"v_mfma_f32_16x16x32_bf16 v[156:159], v[160:163], v[112:115], v[156:159]\n"
"ds_read2_b32 v[186:187], v231 offset0:34 offset1:35\n"
"v_mfma_f32_16x16x32_bf16 v[148:151], v[160:163], v[100:103], v[148:151]\n"
"ds_read_b128 v[208:211], v237 offset:16384\n"
"v_mfma_f32_16x16x32_bf16 v[128:131], v[160:163], v[120:123], v[128:131]\n"
"ds_read_b128 v[212:215], v238 offset:16384\n"
"ds_read_b128 v[216:219], v239 offset:16384\n"
"v_mfma_f32_16x16x32_bf16 v[72:75], v[160:163], v[140:143], v[72:75]\n"
"ds_read_b128 v[220:223], v240 offset:16384\n"
"s_lshl_b32 s94, s58, 9\n"
"v_mfma_f32_16x16x32_bf16 v[52:55], v[160:163], v[56:59], v[52:55]\n"
"s_add_u32 s94, s94, s15\n"
"s_lshl_b32 s94, s94, 6\n"
"v_mfma_f32_16x16x32_bf16 v[64:67], v[132:135], v[96:99], v[64:67]\n"
"s_add_u32 s94, s94, 0x2c01000\n"
"s_lshr_b32 s26, s94, 8\n"
"v_mfma_f32_16x16x32_bf16 v[136:139], v[132:135], v[104:107], v[136:139]\n"
"ds_read_b128 v[96:99], v230 offset:3712\n"
"s_and_b32 s26, s26, 0x2000\n"
"v_mfma_f32_16x16x32_bf16 v[164:167], v[132:135], v[112:115], v[164:167]\n"
"ds_read_b128 v[104:107], v230 offset:3200\n"
"s_add_u32 s26, s26, s94\n"
"v_mfma_f32_16x16x32_bf16 v[156:159], v[132:135], v[100:103], v[156:159]\n"
"ds_read_b128 v[100:103], v230 offset:2688\n"
"v_mfma_f32_16x16x32_bf16 v[148:151], v[132:135], v[120:123], v[148:151]\n"
"ds_read_b128 v[112:115], v230 offset:2176\n"
"v_mfma_f32_16x16x32_bf16 v[128:131], v[132:135], v[140:143], v[128:131]\n"
"ds_read_b128 v[120:123], v230 offset:1664\n"
"v_mfma_f32_16x16x32_bf16 v[72:75], v[132:135], v[56:59], v[72:75]\n"
"ds_read_b128 v[56:59], v230 offset:1152\n"
"v_mfma_f32_16x16x32_bf16 v[52:55], v[132:135], v[116:119], v[52:55]\n"
"ds_read_b128 v[116:119], v230 offset:640\n"
"s_waitcnt lgkmcnt(6)\n"
"v_mfma_f32_16x16x32_bf16 v[52:55], v[108:111], v[96:99], v[52:55]\n"
"ds_read_b128 v[108:111], v230 offset:128\n"
"v_mfma_f32_16x16x32_bf16 v[72:75], v[144:147], v[96:99], v[72:75]\n"
"ds_read2_b32 v[132:133], v229 offset0:32 offset1:33\n"
"s_waitcnt lgkmcnt(7)\n"
"v_mfma_f32_16x16x32_bf16 v[52:55], v[144:147], v[104:107], v[52:55]\n"
"ds_read2_b32 v[134:135], v229 offset0:34 offset1:35\n"
"v_mfma_f32_16x16x32_bf16 v[128:131], v[188:191], v[96:99], v[128:131]\n"
"ds_read2_b32 v[140:141], v233 offset0:176 offset1:177\n"
"v_mfma_f32_16x16x32_bf16 v[72:75], v[188:191], v[104:107], v[72:75]\n"
"ds_read2_b32 v[142:143], v233 offset0:178 offset1:179\n"
"s_waitcnt lgkmcnt(9)\n"
"v_mfma_f32_16x16x32_bf16 v[52:55], v[188:191], v[100:103], v[52:55]\n"
"ds_read2_b32 v[144:145], v233 offset0:48 offset1:49\n"
"v_mfma_f32_16x16x32_bf16 v[148:151], v[204:207], v[96:99], v[148:151]\n"
"ds_read2_b32 v[146:147], v233 offset0:50 offset1:51\n"
"v_mfma_f32_16x16x32_bf16 v[128:131], v[204:207], v[104:107], v[128:131]\n"
"ds_read2_b32 v[160:161], v232 offset0:176 offset1:177\n"
"v_mfma_f32_16x16x32_bf16 v[72:75], v[204:207], v[100:103], v[72:75]\n"
"ds_read2_b32 v[162:163], v232 offset0:178 offset1:179\n"
"s_waitcnt lgkmcnt(12)\n"
"v_mfma_f32_16x16x32_bf16 v[52:55], v[204:207], v[112:115], v[52:55]\n"
"ds_read2_b32 v[188:189], v229 offset0:160 offset1:161\n"
"v_mfma_f32_16x16x32_bf16 v[156:159], v[176:179], v[96:99], v[156:159]\n"
"ds_read2_b32 v[190:191], v229 offset0:162 offset1:163\n"
"v_mfma_f32_16x16x32_bf16 v[148:151], v[176:179], v[104:107], v[148:151]\n"
"ds_read2_b32 v[204:205], v232 offset0:48 offset1:49\n"
"v_mfma_f32_16x16x32_bf16 v[128:131], v[176:179], v[100:103], v[128:131]\n"
"ds_read2_b32 v[206:207], v232 offset0:50 offset1:51\n"
"v_mfma_f32_16x16x32_bf16 v[72:75], v[176:179], v[112:115], v[72:75]\n"
"s_waitcnt lgkmcnt(15)\n"
"v_mfma_f32_16x16x32_bf16 v[52:55], v[176:179], v[120:123], v[52:55]\n"
"s_add_u32 s26, s30, s26\n"
"s_addc_u32 s27, s31, 0\n"
"v_mfma_f32_16x16x32_bf16 v[164:167], v[184:187], v[96:99], v[164:167]\n"
"s_lshl2_add_u32 s95, s58, s15\n"
"s_lshl_b32 s95, s95, 7\n"
"v_mfma_f32_16x16x32_bf16 v[156:159], v[184:187], v[104:107], v[156:159]\n"
"ds_read2_b32 v[176:177], v231 offset0:176 offset1:177\n"
"s_add_u32 s95, s95, 0x60000\n"
"v_mfma_f32_16x16x32_bf16 v[148:151], v[184:187], v[100:103], v[148:151]\n"
"ds_read2_b32 v[178:179], v231 offset0:178 offset1:179\n"
"v_mfma_f32_16x16x32_bf16 v[128:131], v[184:187], v[112:115], v[128:131]\n"
"s_add_u32 s40, s30, s95\n"
"s_addc_u32 s41, s31, 0\n"
"v_mfma_f32_16x16x32_bf16 v[72:75], v[184:187], v[120:123], v[72:75]\n"
"s_lshl2_add_u32 s94, %1, s15\n"
"s_waitcnt lgkmcnt(15)\n"
"v_mfma_f32_16x16x32_bf16 v[52:55], v[184:187], v[56:59], v[52:55]\n"
"s_lshl_b32 s94, s94, 14\n"
"s_xor_b32 s94, s94, 0x10000\n"
"s_waitcnt lgkmcnt(4)\n"
"v_mfma_f32_16x16x32_bf16 v[136:139], v[188:191], v[96:99], v[136:139]\n"
"s_add_u32 s94, s94, 0x1400000\n"
"s_lshr_b32 s42, s94, 8\n"
"v_mfma_f32_16x16x32_bf16 v[164:167], v[188:191], v[104:107], v[164:167]\n"
"ds_read2_b32 v[184:185], v231 offset0:48 offset1:49\n"
"s_and_b32 s42, s42, 0x2000\n"
"v_mfma_f32_16x16x32_bf16 v[156:159], v[188:191], v[100:103], v[156:159]\n"
"ds_read2_b32 v[186:187], v231 offset0:50 offset1:51\n"
"s_add_u32 s42, s42, s94\n"
"v_mfma_f32_16x16x32_bf16 v[148:151], v[188:191], v[112:115], v[148:151]\n"
"s_add_u32 s42, s30, s42\n"
"s_addc_u32 s43, s31, 0\n"
"v_mfma_f32_16x16x32_bf16 v[128:131], v[188:191], v[120:123], v[128:131]\n"
"s_xor_b32 s26, s26, s7\n"
"s_xor_b32 s42, s42, s7\n"
"v_mfma_f32_16x16x32_bf16 v[72:75], v[188:191], v[56:59], v[72:75]\n"
"s_add_u32 s68, s26, 0x4000\n"
"s_addc_u32 s69, s27, 0\n"
"v_mfma_f32_16x16x32_bf16 v[52:55], v[188:191], v[116:119], v[52:55]\n"
"s_lshl1_add_u32 s46, s48, s59\n"
"s_lshl_b32 s46, s46, 16\n"
"v_mfma_f32_16x16x32_bf16 v[64:67], v[132:135], v[96:99], v[64:67]\n"
"s_xor_b32 s44, s46, 0x20000\n"
"s_xor_b32 s45, s46, 0x30000\n"
"v_mfma_f32_16x16x32_bf16 v[136:139], v[132:135], v[104:107], v[136:139]\n"
"ds_read_b128 v[96:99], v230 offset:3776\n"
"s_xor_b32 s47, s46, 0x10000\n"
"v_mfma_f32_16x16x32_bf16 v[164:167], v[132:135], v[100:103], v[164:167]\n"
"ds_read_b128 v[100:103], v230 offset:3264\n"
"s_lshl2_add_u32 s95, s50, s15\n"
"v_mfma_f32_16x16x32_bf16 v[156:159], v[132:135], v[112:115], v[156:159]\n"
"ds_read_b128 v[104:107], v230 offset:2752\n"
"s_lshl_b32 s95, s95, 6\n"
"v_mfma_f32_16x16x32_bf16 v[148:151], v[132:135], v[120:123], v[148:151]\n"
"ds_read_b128 v[112:115], v230 offset:2240\n"
"s_lshl_b32 s94, s51, 17\n"
"v_mfma_f32_16x16x32_bf16 v[128:131], v[132:135], v[56:59], v[128:131]\n"
"ds_read_b128 v[56:59], v230 offset:1728\n"
"s_add_u32 s94, s94, s95\n"
"v_mfma_f32_16x16x32_bf16 v[72:75], v[132:135], v[116:119], v[72:75]\n"
"ds_read_b128 v[116:119], v230 offset:1216\n"
"v_mfma_f32_16x16x32_bf16 v[52:55], v[132:135], v[108:111], v[52:55]\n"
"ds_read_b128 v[108:111], v230 offset:704\n"
"s_waitcnt lgkmcnt(6)\n"
"v_mfma_f32_16x16x32_bf16 v[52:55], v[140:143], v[96:99], v[52:55]\n"
"ds_read_b128 v[120:123], v230 offset:192\n"
"v_mfma_f32_16x16x32_bf16 v[72:75], v[144:147], v[96:99], v[72:75]\n"
"ds_read2_b32 v[132:133], v229 offset0:48 offset1:49\n"
"s_waitcnt lgkmcnt(7)\n"
"v_mfma_f32_16x16x32_bf16 v[52:55], v[144:147], v[100:103], v[52:55]\n"
"ds_read2_b32 v[134:135], v229 offset0:50 offset1:51\n"
"v_mfma_f32_16x16x32_bf16 v[128:131], v[160:163], v[96:99], v[128:131]\n"
"ds_read2_b32 v[140:141], v233 offset0:192 offset1:193\n"
"v_mfma_f32_16x16x32_bf16 v[72:75], v[160:163], v[100:103], v[72:75]\n"
"ds_read2_b32 v[142:143], v233 offset0:194 offset1:195\n"
"s_waitcnt lgkmcnt(9)\n"
"v_mfma_f32_16x16x32_bf16 v[52:55], v[160:163], v[104:107], v[52:55]\n"
"ds_read2_b32 v[144:145], v233 offset0:64 offset1:65\n"
"v_mfma_f32_16x16x32_bf16 v[148:151], v[204:207], v[96:99], v[148:151]\n"
"ds_read2_b32 v[146:147], v233 offset0:66 offset1:67\n"
"v_mfma_f32_16x16x32_bf16 v[128:131], v[204:207], v[100:103], v[128:131]\n"
"ds_read2_b32 v[160:161], v232 offset0:192 offset1:193\n"
"v_mfma_f32_16x16x32_bf16 v[72:75], v[204:207], v[104:107], v[72:75]\n"
"ds_read2_b32 v[162:163], v232 offset0:194 offset1:195\n"
"s_waitcnt lgkmcnt(12)\n"
"v_mfma_f32_16x16x32_bf16 v[52:55], v[204:207], v[112:115], v[52:55]\n"
"ds_read2_b32 v[188:189], v229 offset0:176 offset1:177\n"
"v_mfma_f32_16x16x32_bf16 v[156:159], v[176:179], v[96:99], v[156:159]\n"
"ds_read2_b32 v[190:191], v229 offset0:178 offset1:179\n"
"v_mfma_f32_16x16x32_bf16 v[148:151], v[176:179], v[100:103], v[148:151]\n"
"ds_read2_b32 v[204:205], v232 offset0:64 offset1:65\n"
"v_mfma_f32_16x16x32_bf16 v[128:131], v[176:179], v[104:107], v[128:131]\n"
"ds_read2_b32 v[206:207], v232 offset0:66 offset1:67\n"
"v_mfma_f32_16x16x32_bf16 v[72:75], v[176:179], v[112:115], v[72:75]\n"
"s_waitcnt lgkmcnt(15)\n"
"v_mfma_f32_16x16x32_bf16 v[52:55], v[176:179], v[56:59], v[52:55]\n"
"s_add_u32 s16, s16, s94\n"
"s_addc_u32 s17, s17, 0\n"
"v_mfma_f32_16x16x32_bf16 v[164:167], v[184:187], v[96:99], v[164:167]\n"
"s_lshl1_add_u32 s94, s94, 0x1000\n"
"v_mfma_f32_16x16x32_bf16 v[156:159], v[184:187], v[100:103], v[156:159]\n"
"ds_read2_b32 v[176:177], v231 offset0:192 offset1:193\n"
"v_mfma_f32_16x16x32_bf16 v[148:151], v[184:187], v[104:107], v[148:151]\n"
"ds_read2_b32 v[178:179], v231 offset0:194 offset1:195\n"
"v_mfma_f32_16x16x32_bf16 v[128:131], v[184:187], v[112:115], v[128:131]\n"
"s_add_u32 s36, s36, s94\n"
"s_addc_u32 s37, s37, 0\n"
"v_mfma_f32_16x16x32_bf16 v[72:75], v[184:187], v[56:59], v[72:75]\n"
"s_waitcnt lgkmcnt(15)\n"
"v_mfma_f32_16x16x32_bf16 v[52:55], v[184:187], v[116:119], v[52:55]\n"
"s_add_u32 s38, s38, s95\n"
"s_addc_u32 s39, s39, 0\n"
"s_waitcnt lgkmcnt(4)\n"
"v_mfma_f32_16x16x32_bf16 v[136:139], v[188:191], v[96:99], v[136:139]\n"
"s_lshl_b32 s95, s51, 9\n"
"s_add_u32 s95, s95, 0x140000\n"
"v_mfma_f32_16x16x32_bf16 v[164:167], v[188:191], v[100:103], v[164:167]\n"
"ds_read2_b32 v[184:185], v231 offset0:64 offset1:65\n"
"s_lshr_b32 s94, s46, 9\n"
"v_mfma_f32_16x16x32_bf16 v[156:159], v[188:191], v[104:107], v[156:159]\n"
"ds_read2_b32 v[186:187], v231 offset0:66 offset1:67\n"
"s_add_u32 s94, s94, s95\n"
"v_mfma_f32_16x16x32_bf16 v[148:151], v[188:191], v[112:115], v[148:151]\n"
"s_add_u32 s18, s30, s94\n"
"s_addc_u32 s19, s31, 0\n"
"v_mfma_f32_16x16x32_bf16 v[128:131], v[188:191], v[56:59], v[128:131]\n"
"s_lshr_b32 s94, s46, 1\n"
"s_add_u32 s94, s94, 0\n"
"v_mfma_f32_16x16x32_bf16 v[72:75], v[188:191], v[116:119], v[72:75]\n"
"s_add_u32 s32, s16, s94\n"
"s_addc_u32 s33, s17, 0\n"
"v_mfma_f32_16x16x32_bf16 v[52:55], v[188:191], v[108:111], v[52:55]\n"
"s_add_u32 s94, s46, 0\n"
"v_mfma_f32_16x16x32_bf16 v[64:67], v[132:135], v[96:99], v[64:67]\n"
"s_add_u32 s86, s36, s94\n"
"s_addc_u32 s87, s37, 0\n"
"v_mfma_f32_16x16x32_bf16 v[136:139], v[132:135], v[100:103], v[136:139]\n"
"ds_read_b128 v[96:99], v230 offset:3840\n"
"s_lshr_b32 s94, s47, 9\n"
"v_mfma_f32_16x16x32_bf16 v[164:167], v[132:135], v[104:107], v[164:167]\n"
"ds_read_b128 v[100:103], v230 offset:3328\n"
"s_add_u32 s94, s94, s95\n"
"v_mfma_f32_16x16x32_bf16 v[156:159], v[132:135], v[112:115], v[156:159]\n"
"ds_read_b128 v[104:107], v230 offset:2816\n"
"v_mfma_f32_16x16x32_bf16 v[148:151], v[132:135], v[56:59], v[148:151]\n"
"ds_read_b128 v[56:59], v230 offset:2304\n"
"v_mfma_f32_16x16x32_bf16 v[128:131], v[132:135], v[116:119], v[128:131]\n"
"ds_read_b128 v[112:115], v230 offset:1792\n"
"v_mfma_f32_16x16x32_bf16 v[72:75], v[132:135], v[108:111], v[72:75]\n"
"ds_read_b128 v[108:111], v230 offset:1280\n"
"v_mfma_f32_16x16x32_bf16 v[52:55], v[132:135], v[120:123], v[52:55]\n"
"ds_read_b128 v[116:119], v230 offset:768\n"
"s_waitcnt lgkmcnt(6)\n"
"v_mfma_f32_16x16x32_bf16 v[52:55], v[140:143], v[96:99], v[52:55]\n"
"ds_read_b128 v[120:123], v230 offset:256\n"
"v_mfma_f32_16x16x32_bf16 v[72:75], v[144:147], v[96:99], v[72:75]\n"
"ds_read2_b32 v[132:133], v229 offset0:64 offset1:65\n"
"s_waitcnt lgkmcnt(7)\n"
"v_mfma_f32_16x16x32_bf16 v[52:55], v[144:147], v[100:103], v[52:55]\n"
"ds_read2_b32 v[134:135], v229 offset0:66 offset1:67\n"
"v_mfma_f32_16x16x32_bf16 v[128:131], v[160:163], v[96:99], v[128:131]\n"
"ds_read2_b32 v[140:141], v233 offset0:208 offset1:209\n"
"v_mfma_f32_16x16x32_bf16 v[72:75], v[160:163], v[100:103], v[72:75]\n"
"ds_read2_b32 v[142:143], v233 offset0:210 offset1:211\n"
"s_waitcnt lgkmcnt(9)\n"
"v_mfma_f32_16x16x32_bf16 v[52:55], v[160:163], v[104:107], v[52:55]\n"
"ds_read2_b32 v[144:145], v233 offset0:80 offset1:81\n"
"v_mfma_f32_16x16x32_bf16 v[148:151], v[204:207], v[96:99], v[148:151]\n"
"ds_read2_b32 v[146:147], v233 offset0:82 offset1:83\n"
"v_mfma_f32_16x16x32_bf16 v[128:131], v[204:207], v[100:103], v[128:131]\n"
"ds_read2_b32 v[160:161], v232 offset0:208 offset1:209\n"
"v_mfma_f32_16x16x32_bf16 v[72:75], v[204:207], v[104:107], v[72:75]\n"
"ds_read2_b32 v[162:163], v232 offset0:210 offset1:211\n"
"s_waitcnt lgkmcnt(12)\n"
"v_mfma_f32_16x16x32_bf16 v[52:55], v[204:207], v[56:59], v[52:55]\n"
"ds_read2_b32 v[188:189], v229 offset0:192 offset1:193\n"
"v_mfma_f32_16x16x32_bf16 v[156:159], v[176:179], v[96:99], v[156:159]\n"
"ds_read2_b32 v[190:191], v229 offset0:194 offset1:195\n"
"v_mfma_f32_16x16x32_bf16 v[148:151], v[176:179], v[100:103], v[148:151]\n"
"ds_read2_b32 v[204:205], v232 offset0:80 offset1:81\n"
"v_mfma_f32_16x16x32_bf16 v[128:131], v[176:179], v[104:107], v[128:131]\n"
"ds_read2_b32 v[206:207], v232 offset0:82 offset1:83\n"
"v_mfma_f32_16x16x32_bf16 v[72:75], v[176:179], v[56:59], v[72:75]\n"
"s_waitcnt lgkmcnt(15)\n"
"v_mfma_f32_16x16x32_bf16 v[52:55], v[176:179], v[112:115], v[52:55]\n"
"s_add_u32 s20, s30, s94\n"
"s_addc_u32 s21, s31, 0\n"
"v_mfma_f32_16x16x32_bf16 v[164:167], v[184:187], v[96:99], v[164:167]\n"
"s_lshr_b32 s94, s47, 1\n"
"s_add_u32 s94, s94, 0\n"
"v_mfma_f32_16x16x32_bf16 v[156:159], v[184:187], v[100:103], v[156:159]\n"
"ds_read2_b32 v[176:177], v231 offset0:208 offset1:209\n"
"v_mfma_f32_16x16x32_bf16 v[148:151], v[184:187], v[104:107], v[148:151]\n"
"ds_read2_b32 v[178:179], v231 offset0:210 offset1:211\n"
"v_mfma_f32_16x16x32_bf16 v[128:131], v[184:187], v[56:59], v[128:131]\n"
"s_add_u32 s34, s16, s94\n"
"s_addc_u32 s35, s17, 0\n"
"v_mfma_f32_16x16x32_bf16 v[72:75], v[184:187], v[112:115], v[72:75]\n"
"s_add_u32 s94, s47, 0\n"
"s_waitcnt lgkmcnt(15)\n"
"v_mfma_f32_16x16x32_bf16 v[52:55], v[184:187], v[108:111], v[52:55]\n"
"s_add_u32 s88, s36, s94\n"
"s_addc_u32 s89, s37, 0\n"
"s_waitcnt lgkmcnt(4)\n"
"v_mfma_f32_16x16x32_bf16 v[136:139], v[188:191], v[96:99], v[136:139]\n"
"s_lshr_b32 s94, s46, 9\n"
"s_add_u32 s94, s94, s95\n"
"v_mfma_f32_16x16x32_bf16 v[164:167], v[188:191], v[100:103], v[164:167]\n"
"ds_read2_b32 v[184:185], v231 offset0:80 offset1:81\n"
"s_add_u32 s94, s94, 0x2000\n"
"v_mfma_f32_16x16x32_bf16 v[156:159], v[188:191], v[104:107], v[156:159]\n"
"ds_read2_b32 v[186:187], v231 offset0:82 offset1:83\n"
"v_mfma_f32_16x16x32_bf16 v[148:151], v[188:191], v[56:59], v[148:151]\n"
"s_add_u32 s22, s30, s94\n"
"s_addc_u32 s23, s31, 0\n"
"v_mfma_f32_16x16x32_bf16 v[128:131], v[188:191], v[112:115], v[128:131]\n"
"s_lshr_b32 s94, s46, 1\n"
"s_add_u32 s94, s94, 0x200000\n"
"v_mfma_f32_16x16x32_bf16 v[72:75], v[188:191], v[108:111], v[72:75]\n"
"s_add_u32 s28, s16, s94\n"
"s_addc_u32 s29, s17, 0\n"
"v_mfma_f32_16x16x32_bf16 v[52:55], v[188:191], v[116:119], v[52:55]\n"
"s_add_u32 s94, s46, 0x400000\n"
"v_mfma_f32_16x16x32_bf16 v[64:67], v[132:135], v[96:99], v[64:67]\n"
"s_add_u32 s90, s36, s94\n"
"s_addc_u32 s91, s37, 0\n"
"v_mfma_f32_16x16x32_bf16 v[136:139], v[132:135], v[100:103], v[136:139]\n"
"ds_read_b128 v[96:99], v230 offset:3904\n"
"s_lshr_b32 s94, s47, 9\n"
"v_mfma_f32_16x16x32_bf16 v[164:167], v[132:135], v[104:107], v[164:167]\n"
"ds_read_b128 v[100:103], v230 offset:3392\n"
"s_add_u32 s94, s94, s95\n"
"v_mfma_f32_16x16x32_bf16 v[156:159], v[132:135], v[56:59], v[156:159]\n"
"ds_read_b128 v[56:59], v230 offset:2880\n"
"s_add_u32 s94, s94, 0x2000\n"
"v_mfma_f32_16x16x32_bf16 v[148:151], v[132:135], v[112:115], v[148:151]\n"
"ds_read_b128 v[104:107], v230 offset:2368\n"
"v_mfma_f32_16x16x32_bf16 v[128:131], v[132:135], v[108:111], v[128:131]\n"
"ds_read_b128 v[108:111], v230 offset:1856\n"
"v_mfma_f32_16x16x32_bf16 v[72:75], v[132:135], v[116:119], v[72:75]\n"
"ds_read_b128 v[112:115], v230 offset:1344\n"
"v_mfma_f32_16x16x32_bf16 v[52:55], v[132:135], v[120:123], v[52:55]\n"
"ds_read_b128 v[116:119], v230 offset:832\n"
"s_waitcnt lgkmcnt(6)\n"
"v_mfma_f32_16x16x32_bf16 v[52:55], v[140:143], v[96:99], v[52:55]\n"
"ds_read_b128 v[120:123], v230 offset:320\n"
"v_mfma_f32_16x16x32_bf16 v[72:75], v[144:147], v[96:99], v[72:75]\n"
"ds_read2_b32 v[132:133], v229 offset0:80 offset1:81\n"
"s_waitcnt lgkmcnt(7)\n"
"v_mfma_f32_16x16x32_bf16 v[52:55], v[144:147], v[100:103], v[52:55]\n"
"ds_read2_b32 v[134:135], v229 offset0:82 offset1:83\n"
"v_mfma_f32_16x16x32_bf16 v[128:131], v[160:163], v[96:99], v[128:131]\n"
"ds_read2_b32 v[140:141], v233 offset0:224 offset1:225\n"
"v_mfma_f32_16x16x32_bf16 v[72:75], v[160:163], v[100:103], v[72:75]\n"
"ds_read2_b32 v[142:143], v233 offset0:226 offset1:227\n"
"s_waitcnt lgkmcnt(9)\n"
"v_mfma_f32_16x16x32_bf16 v[52:55], v[160:163], v[56:59], v[52:55]\n"
"ds_read2_b32 v[144:145], v233 offset0:96 offset1:97\n"
"v_mfma_f32_16x16x32_bf16 v[148:151], v[204:207], v[96:99], v[148:151]\n"
"ds_read2_b32 v[146:147], v233 offset0:98 offset1:99\n"
"v_mfma_f32_16x16x32_bf16 v[128:131], v[204:207], v[100:103], v[128:131]\n"
"ds_read2_b32 v[160:161], v232 offset0:224 offset1:225\n"
"v_mfma_f32_16x16x32_bf16 v[72:75], v[204:207], v[56:59], v[72:75]\n"
"ds_read2_b32 v[162:163], v232 offset0:226 offset1:227\n"
"s_waitcnt lgkmcnt(12)\n"
"v_mfma_f32_16x16x32_bf16 v[52:55], v[204:207], v[104:107], v[52:55]\n"
"ds_read2_b32 v[188:189], v229 offset0:208 offset1:209\n"
"v_mfma_f32_16x16x32_bf16 v[156:159], v[176:179], v[96:99], v[156:159]\n"
"ds_read2_b32 v[190:191], v229 offset0:210 offset1:211\n"
"v_mfma_f32_16x16x32_bf16 v[148:151], v[176:179], v[100:103], v[148:151]\n"
"ds_read2_b32 v[204:205], v232 offset0:96 offset1:97\n"
"v_mfma_f32_16x16x32_bf16 v[128:131], v[176:179], v[56:59], v[128:131]\n"
"ds_read2_b32 v[206:207], v232 offset0:98 offset1:99\n"
"v_mfma_f32_16x16x32_bf16 v[72:75], v[176:179], v[104:107], v[72:75]\n"
"s_waitcnt lgkmcnt(15)\n"
"v_mfma_f32_16x16x32_bf16 v[52:55], v[176:179], v[108:111], v[52:55]\n"
"s_add_u32 s24, s30, s94\n"
"s_addc_u32 s25, s31, 0\n"
"v_mfma_f32_16x16x32_bf16 v[164:167], v[184:187], v[96:99], v[164:167]\n"
"s_lshr_b32 s94, s47, 1\n"
"s_add_u32 s94, s94, 0x200000\n"
"v_mfma_f32_16x16x32_bf16 v[156:159], v[184:187], v[100:103], v[156:159]\n"
"ds_read2_b32 v[176:177], v231 offset0:224 offset1:225\n"
"v_mfma_f32_16x16x32_bf16 v[148:151], v[184:187], v[56:59], v[148:151]\n"
"ds_read2_b32 v[178:179], v231 offset0:226 offset1:227\n"
"v_mfma_f32_16x16x32_bf16 v[128:131], v[184:187], v[104:107], v[128:131]\n"
"s_add_u32 s84, s16, s94\n"
"s_addc_u32 s85, s17, 0\n"
"v_mfma_f32_16x16x32_bf16 v[72:75], v[184:187], v[108:111], v[72:75]\n"
"s_add_u32 s94, s47, 0x400000\n"
"s_waitcnt lgkmcnt(15)\n"
"v_mfma_f32_16x16x32_bf16 v[52:55], v[184:187], v[112:115], v[52:55]\n"
"s_add_u32 s92, s36, s94\n"
"s_addc_u32 s93, s37, 0\n"
"s_waitcnt lgkmcnt(4)\n"
"v_mfma_f32_16x16x32_bf16 v[136:139], v[188:191], v[96:99], v[136:139]\n"
"v_mfma_f32_16x16x32_bf16 v[164:167], v[188:191], v[100:103], v[164:167]\n"
"ds_read2_b32 v[184:185], v231 offset0:96 offset1:97\n"
"v_mfma_f32_16x16x32_bf16 v[156:159], v[188:191], v[56:59], v[156:159]\n"
"ds_read2_b32 v[186:187], v231 offset0:98 offset1:99\n"
"v_mfma_f32_16x16x32_bf16 v[148:151], v[188:191], v[104:107], v[148:151]\n"
"v_mfma_f32_16x16x32_bf16 v[128:131], v[188:191], v[108:111], v[128:131]\n"
"v_mfma_f32_16x16x32_bf16 v[72:75], v[188:191], v[112:115], v[72:75]\n"
"v_mfma_f32_16x16x32_bf16 v[52:55], v[188:191], v[116:119], v[52:55]\n"
"v_mfma_f32_16x16x32_bf16 v[64:67], v[132:135], v[96:99], v[64:67]\n"
"v_mfma_f32_16x16x32_bf16 v[136:139], v[132:135], v[100:103], v[136:139]\n"
"ds_read_b128 v[96:99], v230 offset:3968\n"
"v_mfma_f32_16x16x32_bf16 v[164:167], v[132:135], v[56:59], v[164:167]\n"
"ds_read_b128 v[56:59], v230 offset:3456\n"
"v_mfma_f32_16x16x32_bf16 v[156:159], v[132:135], v[104:107], v[156:159]\n"
"ds_read_b128 v[100:103], v230 offset:2944\n"
"v_mfma_f32_16x16x32_bf16 v[148:151], v[132:135], v[108:111], v[148:151]\n"
"ds_read_b128 v[104:107], v230 offset:2432\n"
"v_mfma_f32_16x16x32_bf16 v[128:131], v[132:135], v[112:115], v[128:131]\n"
"ds_read_b128 v[108:111], v230 offset:1920\n"
"v_mfma_f32_16x16x32_bf16 v[72:75], v[132:135], v[116:119], v[72:75]\n"
"ds_read_b128 v[112:115], v230 offset:1408\n"
"v_mfma_f32_16x16x32_bf16 v[52:55], v[132:135], v[120:123], v[52:55]\n"
"ds_read_b128 v[116:119], v230 offset:896\n"
"s_waitcnt lgkmcnt(6)\n"
"v_mfma_f32_16x16x32_bf16 v[52:55], v[140:143], v[96:99], v[52:55]\n"
"ds_read_b128 v[120:123], v230 offset:384\n"
"v_mfma_f32_16x16x32_bf16 v[72:75], v[144:147], v[96:99], v[72:75]\n"
"ds_read2_b32 v[132:133], v229 offset0:96 offset1:97\n"
"s_waitcnt lgkmcnt(7)\n"
"v_mfma_f32_16x16x32_bf16 v[52:55], v[144:147], v[56:59], v[52:55]\n"
"ds_read2_b32 v[134:135], v229 offset0:98 offset1:99\n"
"v_mfma_f32_16x16x32_bf16 v[128:131], v[160:163], v[96:99], v[128:131]\n"
"ds_read2_b32 v[140:141], v233 offset0:240 offset1:241\n"
"v_mfma_f32_16x16x32_bf16 v[72:75], v[160:163], v[56:59], v[72:75]\n"
"ds_read2_b32 v[142:143], v233 offset0:242 offset1:243\n"
"s_waitcnt lgkmcnt(9)\n"
"v_mfma_f32_16x16x32_bf16 v[52:55], v[160:163], v[100:103], v[52:55]\n"
"ds_read2_b32 v[144:145], v233 offset0:112 offset1:113\n"
"v_mfma_f32_16x16x32_bf16 v[148:151], v[204:207], v[96:99], v[148:151]\n"
"ds_read2_b32 v[146:147], v233 offset0:114 offset1:115\n"
"v_mfma_f32_16x16x32_bf16 v[128:131], v[204:207], v[56:59], v[128:131]\n"
"ds_read2_b32 v[160:161], v232 offset0:240 offset1:241\n"
"v_mfma_f32_16x16x32_bf16 v[72:75], v[204:207], v[100:103], v[72:75]\n"
"ds_read2_b32 v[162:163], v232 offset0:242 offset1:243\n"
"s_waitcnt lgkmcnt(12)\n"
"v_mfma_f32_16x16x32_bf16 v[52:55], v[204:207], v[104:107], v[52:55]\n"
"ds_read2_b32 v[188:189], v229 offset0:224 offset1:225\n"
"v_mfma_f32_16x16x32_bf16 v[156:159], v[176:179], v[96:99], v[156:159]\n"
"ds_read2_b32 v[190:191], v229 offset0:226 offset1:227\n"
"v_mfma_f32_16x16x32_bf16 v[148:151], v[176:179], v[56:59], v[148:151]\n"
"ds_read2_b32 v[204:205], v232 offset0:112 offset1:113\n"
"v_mfma_f32_16x16x32_bf16 v[128:131], v[176:179], v[100:103], v[128:131]\n"
"ds_read2_b32 v[206:207], v232 offset0:114 offset1:115\n"
"v_mfma_f32_16x16x32_bf16 v[72:75], v[176:179], v[104:107], v[72:75]\n"
"s_waitcnt lgkmcnt(15)\n"
"v_mfma_f32_16x16x32_bf16 v[52:55], v[176:179], v[108:111], v[52:55]\n"
"v_bfe_u32 v233, v227, 1, 1\n"
"v_mul_u32_u24 v232, 0x140, v233\n"
"v_mfma_f32_16x16x32_bf16 v[164:167], v[184:187], v[96:99], v[164:167]\n"
"v_lshrrev_b32 v233, 5, v227\n"
"v_lshl_or_b32 v232, v233, 9, v232\n"
"v_mfma_f32_16x16x32_bf16 v[156:159], v[184:187], v[56:59], v[156:159]\n"
"ds_read2_b32 v[176:177], v231 offset0:240 offset1:241\n"
"v_mfma_f32_16x16x32_bf16 v[148:151], v[184:187], v[100:103], v[148:151]\n"
"ds_read2_b32 v[178:179], v231 offset0:242 offset1:243\n"
"v_mfma_f32_16x16x32_bf16 v[128:131], v[184:187], v[104:107], v[128:131]\n"
"v_bfe_u32 v233, v227, 4, 1\n"
"v_lshl_or_b32 v232, v233, 7, v232\n"
"v_mfma_f32_16x16x32_bf16 v[72:75], v[184:187], v[108:111], v[72:75]\n"
"s_waitcnt lgkmcnt(15)\n"
"v_mfma_f32_16x16x32_bf16 v[52:55], v[184:187], v[112:115], v[52:55]\n"
"v_bfe_u32 v233, v227, 2, 2\n"
"v_lshl_or_b32 v232, v233, 4, v232\n"
"s_waitcnt lgkmcnt(4)\n"
"v_mfma_f32_16x16x32_bf16 v[136:139], v[188:191], v[96:99], v[136:139]\n"
"v_and_b32 v233, 1, v227\n"
"v_lshl_or_b32 v232, v233, 3, v232\n"
"v_mfma_f32_16x16x32_bf16 v[164:167], v[188:191], v[56:59], v[164:167]\n"
"ds_read2_b32 v[184:185], v231 offset0:112 offset1:113\n"
"v_mfma_f32_16x16x32_bf16 v[156:159], v[188:191], v[100:103], v[156:159]\n"
"ds_read2_b32 v[186:187], v231 offset0:114 offset1:115\n"
"v_mfma_f32_16x16x32_bf16 v[148:151], v[188:191], v[104:107], v[148:151]\n"
"v_xor_b32 v234, 64, v232\n"
"v_add_u32 v231, 0x10000, v232\n"
"v_mfma_f32_16x16x32_bf16 v[128:131], v[188:191], v[108:111], v[128:131]\n"
"v_add_u32 v231, 0x10000, v234\n"
"v_add_u32 v233, s57, v226\n"
"v_mfma_f32_16x16x32_bf16 v[72:75], v[188:191], v[112:115], v[72:75]\n"
"v_lshrrev_b32 v235, 3, v227\n"
"v_and_b32 v236, 7, v227\n"
"v_mfma_f32_16x16x32_bf16 v[52:55], v[188:191], v[116:119], v[52:55]\n"
"v_lshlrev_b32 v231, 13, v235\n"
"v_lshl_or_b32 v231, v236, 4, v231\n"
"v_mfma_f32_16x16x32_bf16 v[64:67], v[132:135], v[96:99], v[64:67]\n"
"v_lshrrev_b32 v189, 5, v227\n"
"v_lshlrev_b32 v188, 5, v189\n"
"v_mfma_f32_16x16x32_bf16 v[136:139], v[132:135], v[56:59], v[136:139]\n"
"ds_read_b128 v[56:59], v230 offset:4032\n"
"v_mfma_f32_16x16x32_bf16 v[164:167], v[132:135], v[100:103], v[164:167]\n"
"ds_read_b128 v[96:99], v230 offset:3520\n"
"v_mfma_f32_16x16x32_bf16 v[156:159], v[132:135], v[104:107], v[156:159]\n"
"ds_read_b128 v[100:103], v230 offset:3008\n"
"v_mfma_f32_16x16x32_bf16 v[148:151], v[132:135], v[108:111], v[148:151]\n"
"ds_read_b128 v[104:107], v230 offset:2496\n"
"v_mfma_f32_16x16x32_bf16 v[128:131], v[132:135], v[112:115], v[128:131]\n"
"ds_read_b128 v[108:111], v230 offset:1984\n"
"v_mfma_f32_16x16x32_bf16 v[72:75], v[132:135], v[116:119], v[72:75]\n"
"ds_read_b128 v[112:115], v230 offset:1472\n"
"v_mfma_f32_16x16x32_bf16 v[52:55], v[132:135], v[120:123], v[52:55]\n"
"ds_read_b128 v[116:119], v230 offset:960\n"
"s_waitcnt lgkmcnt(6)\n"
"v_mfma_f32_16x16x32_bf16 v[52:55], v[140:143], v[56:59], v[52:55]\n"
"ds_read_b128 v[120:123], v230 offset:448\n"
"v_mfma_f32_16x16x32_bf16 v[72:75], v[144:147], v[56:59], v[72:75]\n"
"ds_read2_b32 v[132:133], v229 offset0:112 offset1:113\n"
"s_waitcnt lgkmcnt(7)\n"
"v_mfma_f32_16x16x32_bf16 v[52:55], v[144:147], v[96:99], v[52:55]\n"
"ds_read2_b32 v[134:135], v229 offset0:114 offset1:115\n"
"v_mfma_f32_16x16x32_bf16 v[128:131], v[160:163], v[56:59], v[128:131]\n"
"v_lshlrev_b32 v189, 1, v189\n"
"v_xor_b32 v189, v189, v236\n"
"v_mfma_f32_16x16x32_bf16 v[72:75], v[160:163], v[96:99], v[72:75]\n"
"s_waitcnt lgkmcnt(7)\n"
"v_mfma_f32_16x16x32_bf16 v[52:55], v[160:163], v[100:103], v[52:55]\n"
"v_lshlrev_b32 v230, 8, v236\n"
"v_lshl_or_b32 v230, v189, 4, v230\n"
"v_mfma_f32_16x16x32_bf16 v[148:151], v[204:207], v[56:59], v[148:151]\n"
"v_and_b32 v189, 3, v235\n"
"v_lshl_or_b32 v230, v189, 2, v230\n"
"v_mfma_f32_16x16x32_bf16 v[128:131], v[204:207], v[96:99], v[128:131]\n"
"v_bfe_u32 v189, v227, 1, 4\n"
"v_lshl_or_b32 v188, v189, 8, v188\n"
"v_mfma_f32_16x16x32_bf16 v[72:75], v[204:207], v[100:103], v[72:75]\n"
"s_waitcnt lgkmcnt(6)\n"
"v_mfma_f32_16x16x32_bf16 v[52:55], v[204:207], v[104:107], v[52:55]\n"
"ds_read2_b32 v[140:141], v229 offset0:240 offset1:241\n"
"v_mfma_f32_16x16x32_bf16 v[156:159], v[176:179], v[56:59], v[156:159]\n"
"ds_read2_b32 v[142:143], v229 offset0:242 offset1:243\n"
"v_mfma_f32_16x16x32_bf16 v[148:151], v[176:179], v[96:99], v[148:151]\n"
"v_and_b32 v189, 1, v227\n"
"v_lshl_or_b32 v188, v189, 4, v188\n"
"v_mfma_f32_16x16x32_bf16 v[128:131], v[176:179], v[100:103], v[128:131]\n"
"v_mfma_f32_16x16x32_bf16 v[72:75], v[176:179], v[104:107], v[72:75]\n"
"s_waitcnt lgkmcnt(7)\n"
"v_mfma_f32_16x16x32_bf16 v[52:55], v[176:179], v[108:111], v[52:55]\n"
"v_mfma_f32_16x16x32_bf16 v[164:167], v[184:187], v[56:59], v[164:167]\n"
"v_mfma_f32_16x16x32_bf16 v[156:159], v[184:187], v[96:99], v[156:159]\n"
"v_mfma_f32_16x16x32_bf16 v[148:151], v[184:187], v[100:103], v[148:151]\n"
"v_mfma_f32_16x16x32_bf16 v[128:131], v[184:187], v[104:107], v[128:131]\n"
"v_mfma_f32_16x16x32_bf16 v[72:75], v[184:187], v[108:111], v[72:75]\n"
"s_waitcnt lgkmcnt(6)\n"
"v_mfma_f32_16x16x32_bf16 v[52:55], v[184:187], v[112:115], v[52:55]\n"
"s_waitcnt lgkmcnt(0)\n"
"v_mfma_f32_16x16x32_bf16 v[136:139], v[140:143], v[56:59], v[136:139]\n"
"v_mfma_f32_16x16x32_bf16 v[164:167], v[140:143], v[96:99], v[164:167]\n"
"v_mfma_f32_16x16x32_bf16 v[156:159], v[140:143], v[100:103], v[156:159]\n"
"v_mfma_f32_16x16x32_bf16 v[148:151], v[140:143], v[104:107], v[148:151]\n"
"v_mfma_f32_16x16x32_bf16 v[128:131], v[140:143], v[108:111], v[128:131]\n"
"s_barrier\n"
"v_mfma_f32_16x16x32_bf16 v[72:75], v[140:143], v[112:115], v[72:75]\n"
"v_mfma_f32_16x16x32_bf16 v[52:55], v[140:143], v[116:119], v[52:55]\n"
"v_mfma_f32_16x16x32_bf16 v[64:67], v[132:135], v[56:59], v[64:67]\n"
"v_mfma_f32_16x16x32_bf16 v[136:139], v[132:135], v[96:99], v[136:139]\n"
"v_mfma_f32_16x16x32_bf16 v[164:167], v[132:135], v[100:103], v[164:167]\n"
"v_mfma_f32_16x16x32_bf16 v[156:159], v[132:135], v[104:107], v[156:159]\n"
"v_mfma_f32_16x16x32_bf16 v[148:151], v[132:135], v[108:111], v[148:151]\n"
"v_mfma_f32_16x16x32_bf16 v[128:131], v[132:135], v[112:115], v[128:131]\n"
"v_mfma_f32_16x16x32_bf16 v[72:75], v[132:135], v[116:119], v[72:75]\n"
"v_mfma_f32_16x16x32_bf16 v[52:55], v[132:135], v[120:123], v[52:55]\n"
"s_nop 0\n"
"v_cvt_pk_bf16_f32 v56, v64, v65\n"
"v_cvt_pk_bf16_f32 v58, v136, v137\n"
"v_cvt_pk_bf16_f32 v57, v66, v67\n"
"v_cvt_pk_bf16_f32 v59, v138, v139\n"
"v_permlane16_swap_b32 v56, v58\n"
"s_nop 0\n"
"v_permlane16_swap_b32 v57, v59\n"
"global_store_dwordx4 v188, v[56:59], s[26:27] offset:-4096 sc1\n"
"s_nop 1\n"
"v_cvt_pk_bf16_f32 v56, v164, v165\n"
"v_cvt_pk_bf16_f32 v58, v156, v157\n"
"v_cvt_pk_bf16_f32 v57, v166, v167\n"
"v_cvt_pk_bf16_f32 v59, v158, v159\n"
"v_permlane16_swap_b32 v56, v58\n"
"s_nop 0\n"
"v_permlane16_swap_b32 v57, v59\n"
"global_store_dwordx4 v188, v[56:59], s[26:27] sc1\n"
"s_nop 1\n"
"v_cvt_pk_bf16_f32 v56, v148, v149\n"
"v_cvt_pk_bf16_f32 v58, v128, v129\n"
"v_cvt_pk_bf16_f32 v57, v150, v151\n"
"v_cvt_pk_bf16_f32 v59, v130, v131\n"
"v_permlane16_swap_b32 v56, v58\n"
"s_nop 0\n"
"v_permlane16_swap_b32 v57, v59\n"
"global_store_dwordx4 v188, v[56:59], s[68:69] offset:-4096 sc1\n"
"s_nop 1\n"
"v_cvt_pk_bf16_f32 v56, v72, v73\n"
"v_cvt_pk_bf16_f32 v58, v52, v53\n"
"v_cvt_pk_bf16_f32 v57, v74, v75\n"
"v_cvt_pk_bf16_f32 v59, v54, v55\n"
"v_permlane16_swap_b32 v56, v58\n"
"s_nop 0\n"
"v_permlane16_swap_b32 v57, v59\n"
"global_store_dwordx4 v188, v[56:59], s[68:69] sc1\n"
"s_barrier\n"
"ds_read_b64_tr_b16 v[52:53], v232 offset:49152\n"
"ds_read_b64_tr_b16 v[54:55], v234 offset:49152\n"
"ds_read_b64_tr_b16 v[56:57], v232 offset:50176\n"
"ds_read_b64_tr_b16 v[58:59], v234 offset:50176\n"
"ds_read_b64_tr_b16 v[64:65], v232 offset:51200\n"
"ds_read_b64_tr_b16 v[66:67], v234 offset:51200\n"
"ds_read_b64_tr_b16 v[72:73], v232 offset:52224\n"
"ds_read_b64_tr_b16 v[74:75], v234 offset:52224\n"
"ds_read_b64_tr_b16 v[96:97], v232 offset:53248\n"
"ds_read_b64_tr_b16 v[98:99], v234 offset:53248\n"
"ds_read_b64_tr_b16 v[100:101], v232 offset:54272\n"
"ds_read_b64_tr_b16 v[102:103], v234 offset:54272\n"
"s_waitcnt lgkmcnt(8)\n"
"v_mfma_f32_16x16x32_bf16 v[104:107], v[52:55], v[8:11], 0\n"
"ds_read_b64_tr_b16 v[108:109], v232 offset:55296\n"
"ds_read_b64_tr_b16 v[110:111], v234 offset:55296\n"
"v_mfma_f32_16x16x32_bf16 v[112:115], v[52:55], v[24:27], 0\n"
"v_mfma_f32_16x16x32_bf16 v[104:107], v[56:59], v[12:15], v[104:107]\n"
"ds_read_b64_tr_b16 v[52:53], v232 offset:56320\n"
"ds_read_b64_tr_b16 v[54:55], v234 offset:56320\n"
"v_mfma_f32_16x16x32_bf16 v[112:115], v[56:59], v[28:31], v[112:115]\n"
"s_waitcnt lgkmcnt(8)\n"
"v_mfma_f32_16x16x32_bf16 v[104:107], v[64:67], v[16:19], v[104:107]\n"
"ds_read_b64_tr_b16 v[56:57], v232 offset:57344\n"
"ds_read_b64_tr_b16 v[58:59], v234 offset:57344\n"
"v_mfma_f32_16x16x32_bf16 v[112:115], v[64:67], v[32:35], v[112:115]\n"
"s_lshr_b32 s13, s10, 4\n"
"s_add_u32 s13, s13, s10\n"
"s_and_b32 s13, s13, 15\n"
"v_mov_b32 v161, s13\n"
"s_and_b32 s14, s10, 15\n"
"s_lshl4_add_u32 s13, s13, s14\n"
"v_mov_b32 v162, s13\n"
"v_mfma_f32_16x16x32_bf16 v[104:107], v[72:75], v[20:23], v[104:107]\n"
"ds_read_b64_tr_b16 v[64:65], v232 offset:58368\n"
"ds_read_b64_tr_b16 v[66:67], v234 offset:58368\n"
"v_mfma_f32_16x16x32_bf16 v[112:115], v[72:75], v[36:39], v[112:115]\n"
"s_waitcnt lgkmcnt(8)\n"
"v_mfma_f32_16x16x32_bf16 v[104:107], v[96:99], v[0:3], v[104:107]\n"
"ds_read_b64_tr_b16 v[72:73], v232 offset:59392\n"
"ds_read_b64_tr_b16 v[74:75], v234 offset:59392\n"
"v_mfma_f32_16x16x32_bf16 v[112:115], v[96:99], v[40:43], v[112:115]\n"
"v_mfma_f32_16x16x32_bf16 v[104:107], v[100:103], v[4:7], v[104:107]\n"
"ds_read_b64_tr_b16 v[96:97], v232 offset:60416\n"
"ds_read_b64_tr_b16 v[98:99], v234 offset:60416\n"
"v_mfma_f32_16x16x32_bf16 v[112:115], v[100:103], v[68:71], v[112:115]\n"
"s_waitcnt lgkmcnt(8)\n"
"v_mfma_f32_16x16x32_bf16 v[104:107], v[108:111], v[44:47], v[104:107]\n"
"ds_read_b64_tr_b16 v[100:101], v232 offset:61440\n"
"ds_read_b64_tr_b16 v[102:103], v234 offset:61440\n"
"v_mfma_f32_16x16x32_bf16 v[112:115], v[108:111], v[76:79], v[112:115]\n"
"v_mfma_f32_16x16x32_bf16 v[104:107], v[52:55], v[48:51], v[104:107]\n"
"ds_read_b64_tr_b16 v[108:109], v232 offset:62464\n"
"ds_read_b64_tr_b16 v[110:111], v234 offset:62464\n"
"v_mfma_f32_16x16x32_bf16 v[112:115], v[52:55], v[80:83], v[112:115]\n"
"s_waitcnt lgkmcnt(8)\n"
"v_mfma_f32_16x16x32_bf16 v[104:107], v[56:59], v[60:63], v[104:107]\n"
"ds_read_b64_tr_b16 v[52:53], v232 offset:63488\n"
"ds_read_b64_tr_b16 v[54:55], v234 offset:63488\n"
"v_mfma_f32_16x16x32_bf16 v[112:115], v[56:59], v[124:127], v[112:115]\n"
"v_mfma_f32_16x16x32_bf16 v[104:107], v[64:67], v[84:87], v[104:107]\n"
"ds_read_b64_tr_b16 v[56:57], v232 offset:64512\n"
"ds_read_b64_tr_b16 v[58:59], v234 offset:64512\n"
"v_lshrrev_b32 v160, 1, v231\n"
"v_and_b32 v116, 56, v160\n"
"global_load_dwordx2 v[136:137], v116, s[38:39]\n"
"v_mfma_f32_16x16x32_bf16 v[112:115], v[64:67], v[168:171], v[112:115]\n"
"s_waitcnt lgkmcnt(8)\n"
"v_mfma_f32_16x16x32_bf16 v[104:107], v[72:75], v[88:91], v[104:107]\n"
"v_lshrrev_b32 v227, 9, v231\n"
"v_and_b32 v227, 0x70, v227\n"
"v_add_u32 v230, s57, v230\n"
"v_mfma_f32_16x16x32_bf16 v[112:115], v[72:75], v[172:175], v[112:115]\n"
"v_mfma_f32_16x16x32_bf16 v[104:107], v[96:99], v[92:95], v[104:107]\n"
"s_and_b32 s13, s11, 0xffffdfff\n"
"s_or_b32 s13, s13, s7\n"
"v_mov_b32 v64, s10\n"
"v_mov_b32 v65, s13\n"
"s_waitcnt vmcnt(1)\n"
"global_store_dwordx2 v228, v[64:65], s[40:41] sc1\n"
"v_mfma_f32_16x16x32_bf16 v[112:115], v[96:99], v[180:183], v[112:115]\n"
"s_waitcnt lgkmcnt(0)\n"
"s_barrier\n"
"v_mfma_f32_16x16x32_bf16 v[104:107], v[100:103], v[152:155], v[104:107]\n"
"ds_read_b64_tr_b16 v[64:65], v232 offset:32768\n"
"ds_read_b64_tr_b16 v[66:67], v234 offset:32768\n"
"v_mfma_f32_16x16x32_bf16 v[112:115], v[100:103], v[208:211], v[112:115]\n"
"ds_read_b64_tr_b16 v[72:73], v232 offset:33792\n"
"ds_read_b64_tr_b16 v[74:75], v234 offset:33792\n"
"v_mfma_f32_16x16x32_bf16 v[104:107], v[108:111], v[192:195], v[104:107]\n"
"ds_read_b64_tr_b16 v[96:97], v232 offset:34816\n"
"ds_read_b64_tr_b16 v[98:99], v234 offset:34816\n"
"v_mfma_f32_16x16x32_bf16 v[112:115], v[108:111], v[212:215], v[112:115]\n"
"ds_read_b64_tr_b16 v[100:101], v232 offset:35840\n"
"ds_read_b64_tr_b16 v[102:103], v234 offset:35840\n"
"v_mfma_f32_16x16x32_bf16 v[104:107], v[52:55], v[196:199], v[104:107]\n"
"ds_read_b64_tr_b16 v[108:109], v232 offset:36864\n"
"ds_read_b64_tr_b16 v[110:111], v234 offset:36864\n"
"ds_read_b128 v[116:119], v233 offset:4160\n"
"ds_read_b128 v[120:123], v233 offset:5184\n"
"v_mfma_f32_16x16x32_bf16 v[112:115], v[52:55], v[216:219], v[112:115]\n"
"v_mfma_f32_16x16x32_bf16 v[104:107], v[56:59], v[200:203], v[104:107]\n"
"ds_read_b64_tr_b16 v[52:53], v232 offset:37888\n"
"ds_read_b64_tr_b16 v[54:55], v234 offset:37888\n"
"v_mfma_f32_16x16x32_bf16 v[112:115], v[56:59], v[220:223], v[112:115]\n"
"s_waitcnt lgkmcnt(10)\n"
"v_mfma_f32_16x16x32_bf16 v[56:59], v[64:67], v[8:11], 0\n"
"ds_read_b64_tr_b16 v[128:129], v232 offset:38912\n"
"ds_read_b64_tr_b16 v[130:131], v234 offset:38912\n"
"v_mfma_f32_16x16x32_bf16 v[132:135], v[64:67], v[24:27], 0\n"
"v_mfma_f32_16x16x32_bf16 v[56:59], v[72:75], v[12:15], v[56:59]\n"
"ds_read_b64_tr_b16 v[64:65], v232 offset:39936\n"
"ds_read_b64_tr_b16 v[66:67], v234 offset:39936\n"
"v_mfma_f32_16x16x32_bf16 v[132:135], v[72:75], v[28:31], v[132:135]\n"
"s_waitcnt lgkmcnt(7)\n"
"v_add_f32 v116, v116, v104\n"
"v_add_f32 v117, v117, v105\n"
"v_add_f32 v118, v118, v106\n"
"v_add_f32 v119, v119, v107\n"
"v_mfma_f32_16x16x32_bf16 v[56:59], v[96:99], v[16:19], v[56:59]\n"
"ds_read_b64_tr_b16 v[72:73], v232 offset:40960\n"
"ds_read_b64_tr_b16 v[74:75], v234 offset:40960\n"
"s_waitcnt lgkmcnt(8)\n"
"v_add_f32 v120, v120, v112\n"
"v_add_f32 v121, v121, v113\n"
"v_add_f32 v122, v122, v114\n"
"v_mfma_f32_16x16x32_bf16 v[132:135], v[96:99], v[32:35], v[132:135]\n"
"v_add_f32 v123, v123, v115\n"
"v_max3_f32 v112, |v116|, |v117|, |v118|\n"
"v_max3_f32 v112, v112, |v119|, |v120|\n"
"v_max3_f32 v112, v112, |v121|, |v122|\n"
"v_max_f32_e64 v112, v112, |v123|\n"
"v_mfma_f32_16x16x32_bf16 v[56:59], v[100:103], v[20:23], v[56:59]\n"
"ds_read_b64_tr_b16 v[96:97], v232 offset:41984\n"
"ds_read_b64_tr_b16 v[98:99], v234 offset:41984\n"
"v_lshrrev_b32 v112, 23, v112\n"
"v_sub_u32 v113, 0x7e, v112\n"
"v_ldexp_f32 v116, v116, v113\n"
"v_mfma_f32_16x16x32_bf16 v[132:135], v[100:103], v[36:39], v[132:135]\n"
"v_ldexp_f32 v117, v117, v113\n"
"v_ldexp_f32 v118, v118, v113\n"
"v_ldexp_f32 v119, v119, v113\n"
"v_ldexp_f32 v120, v120, v113\n"
"s_waitcnt lgkmcnt(8)\n"
"v_mfma_f32_16x16x32_bf16 v[56:59], v[108:111], v[0:3], v[56:59]\n"
"ds_read_b64_tr_b16 v[100:101], v232 offset:43008\n"
"ds_read_b64_tr_b16 v[102:103], v234 offset:43008\n"
"v_ldexp_f32 v121, v121, v113\n"
"v_ldexp_f32 v122, v122, v113\n"
"v_ldexp_f32 v123, v123, v113\n"
"v_mfma_f32_16x16x32_bf16 v[132:135], v[108:111], v[40:43], v[132:135]\n"
"v_lshrrev_b32 v113, 4, v112\n"
"v_cvt_pknorm_i16_f32 v104, v116, v117\n"
"v_cvt_pknorm_i16_f32 v105, v118, v119\n"
"v_cvt_pknorm_i16_f32 v106, v120, v121\n"
"v_cvt_pknorm_i16_f32 v107, v122, v123\n"
"v_mfma_f32_16x16x32_bf16 v[56:59], v[52:55], v[4:7], v[56:59]\n"
"ds_read_b64_tr_b16 v[108:109], v232 offset:44032\n"
"ds_read_b64_tr_b16 v[110:111], v234 offset:44032\n"
"v_bfi_b32 v104, 15, v162, v104\n"
"v_bfi_b32 v105, 15, v112, v105\n"
"v_bfi_b32 v106, 15, v161, v106\n"
"v_mfma_f32_16x16x32_bf16 v[132:135], v[52:55], v[68:71], v[132:135]\n"
"v_bfi_b32 v107, 15, v113, v107\n"
"global_store_dwordx4 v226, v[104:107], s[42:43] sc1\n"
"s_waitcnt lgkmcnt(8)\n"
"v_mfma_f32_16x16x32_bf16 v[56:59], v[128:131], v[44:47], v[56:59]\n"
"ds_read_b64_tr_b16 v[52:53], v232 offset:45056\n"
"ds_read_b64_tr_b16 v[54:55], v234 offset:45056\n"
"v_mfma_f32_16x16x32_bf16 v[132:135], v[128:131], v[76:79], v[132:135]\n"
"v_mfma_f32_16x16x32_bf16 v[56:59], v[64:67], v[48:51], v[56:59]\n"
"ds_read_b64_tr_b16 v[104:105], v232 offset:46080\n"
"ds_read_b64_tr_b16 v[106:107], v234 offset:46080\n"
"v_mfma_f32_16x16x32_bf16 v[132:135], v[64:67], v[80:83], v[132:135]\n"
"s_waitcnt lgkmcnt(8)\n"
"v_mfma_f32_16x16x32_bf16 v[56:59], v[72:75], v[60:63], v[56:59]\n"
"ds_read_b64_tr_b16 v[64:65], v232 offset:47104\n"
"ds_read_b64_tr_b16 v[66:67], v234 offset:47104\n"
"v_mfma_f32_16x16x32_bf16 v[132:135], v[72:75], v[124:127], v[132:135]\n"
"v_mfma_f32_16x16x32_bf16 v[56:59], v[96:99], v[84:87], v[56:59]\n"
"ds_read_b64_tr_b16 v[72:73], v232 offset:48128\n"
"ds_read_b64_tr_b16 v[74:75], v234 offset:48128\n"
"v_mfma_f32_16x16x32_bf16 v[132:135], v[96:99], v[168:171], v[132:135]\n"
"s_waitcnt lgkmcnt(8)\n"
"v_mfma_f32_16x16x32_bf16 v[56:59], v[100:103], v[88:91], v[56:59]\n"
"v_mfma_f32_16x16x32_bf16 v[132:135], v[100:103], v[172:175], v[132:135]\n"
"v_mfma_f32_16x16x32_bf16 v[56:59], v[108:111], v[92:95], v[56:59]\n"
"v_mfma_f32_16x16x32_bf16 v[132:135], v[108:111], v[180:183], v[132:135]\n"
"s_waitcnt lgkmcnt(0)\n"
"s_barrier\n"
"v_mfma_f32_16x16x32_bf16 v[56:59], v[52:55], v[152:155], v[56:59]\n"
"ds_read_b64_tr_b16 v[96:97], v232\n"
"ds_read_b64_tr_b16 v[98:99], v234\n"
"v_mfma_f32_16x16x32_bf16 v[132:135], v[52:55], v[208:211], v[132:135]\n"
"ds_read_b64_tr_b16 v[52:53], v232 offset:1024\n"
"ds_read_b64_tr_b16 v[54:55], v234 offset:1024\n"
"v_mfma_f32_16x16x32_bf16 v[56:59], v[104:107], v[192:195], v[56:59]\n"
"ds_read_b64_tr_b16 v[100:101], v232 offset:2048\n"
"ds_read_b64_tr_b16 v[102:103], v234 offset:2048\n"
"v_mfma_f32_16x16x32_bf16 v[132:135], v[104:107], v[212:215], v[132:135]\n"
"ds_read_b64_tr_b16 v[104:105], v232 offset:3072\n"
"ds_read_b64_tr_b16 v[106:107], v234 offset:3072\n"
"v_mfma_f32_16x16x32_bf16 v[56:59], v[64:67], v[196:199], v[56:59]\n"
"ds_read_b64_tr_b16 v[108:109], v232 offset:4096\n"
"ds_read_b64_tr_b16 v[110:111], v234 offset:4096\n"
"ds_read_b128 v[112:115], v233 offset:6208\n"
"ds_read_b128 v[116:119], v233 offset:7232\n"
"v_mfma_f32_16x16x32_bf16 v[132:135], v[64:67], v[216:219], v[132:135]\n"
"v_mfma_f32_16x16x32_bf16 v[56:59], v[72:75], v[200:203], v[56:59]\n"
"ds_read_b64_tr_b16 v[64:65], v232 offset:5120\n"
"ds_read_b64_tr_b16 v[66:67], v234 offset:5120\n"
"v_mfma_f32_16x16x32_bf16 v[132:135], v[72:75], v[220:223], v[132:135]\n"
"s_waitcnt lgkmcnt(10)\n"
"v_mfma_f32_16x16x32_bf16 v[72:75], v[96:99], v[8:11], 0\n"
"ds_read_b64_tr_b16 v[120:121], v232 offset:6144\n"
"ds_read_b64_tr_b16 v[122:123], v234 offset:6144\n"
"v_mfma_f32_16x16x32_bf16 v[128:131], v[96:99], v[24:27], 0\n"
"v_mfma_f32_16x16x32_bf16 v[72:75], v[52:55], v[12:15], v[72:75]\n"
"ds_read_b64_tr_b16 v[96:97], v232 offset:7168\n"
"ds_read_b64_tr_b16 v[98:99], v234 offset:7168\n"
"v_mfma_f32_16x16x32_bf16 v[128:131], v[52:55], v[28:31], v[128:131]\n"
"s_waitcnt lgkmcnt(7)\n"
"v_add_f32 v112, v112, v56\n"
"v_add_f32 v113, v113, v57\n"
"v_add_f32 v114, v114, v58\n"
"v_add_f32 v115, v115, v59\n"
"v_mfma_f32_16x16x32_bf16 v[72:75], v[100:103], v[16:19], v[72:75]\n"
"ds_read_b64_tr_b16 v[52:53], v232 offset:8192\n"
"ds_read_b64_tr_b16 v[54:55], v234 offset:8192\n"
"s_waitcnt lgkmcnt(8)\n"
"v_add_f32 v116, v116, v132\n"
"v_add_f32 v117, v117, v133\n"
"v_add_f32 v118, v118, v134\n"
"v_mfma_f32_16x16x32_bf16 v[128:131], v[100:103], v[32:35], v[128:131]\n"
"v_add_f32 v119, v119, v135\n"
"v_max3_f32 v132, |v112|, |v113|, |v114|\n"
"v_max3_f32 v132, v132, |v115|, |v116|\n"
"v_max3_f32 v132, v132, |v117|, |v118|\n"
"v_max_f32_e64 v132, v132, |v119|\n"
"v_mfma_f32_16x16x32_bf16 v[72:75], v[104:107], v[20:23], v[72:75]\n"
"ds_read_b64_tr_b16 v[56:57], v232 offset:9216\n"
"ds_read_b64_tr_b16 v[58:59], v234 offset:9216\n"
"v_lshrrev_b32 v132, 23, v132\n"
"v_sub_u32 v133, 0x7e, v132\n"
"v_ldexp_f32 v112, v112, v133\n"
"v_mfma_f32_16x16x32_bf16 v[128:131], v[104:107], v[36:39], v[128:131]\n"
"v_ldexp_f32 v113, v113, v133\n"
"v_ldexp_f32 v114, v114, v133\n"
"v_ldexp_f32 v115, v115, v133\n"
"v_ldexp_f32 v116, v116, v133\n"
"s_waitcnt lgkmcnt(8)\n"
"v_mfma_f32_16x16x32_bf16 v[72:75], v[108:111], v[0:3], v[72:75]\n"
"ds_read_b64_tr_b16 v[100:101], v232 offset:10240\n"
"ds_read_b64_tr_b16 v[102:103], v234 offset:10240\n"
"v_ldexp_f32 v117, v117, v133\n"
"v_ldexp_f32 v118, v118, v133\n"
"v_ldexp_f32 v119, v119, v133\n"
"v_mfma_f32_16x16x32_bf16 v[128:131], v[108:111], v[40:43], v[128:131]\n"
"v_lshrrev_b32 v133, 4, v132\n"
"v_cvt_pknorm_i16_f32 v104, v112, v113\n"
"v_cvt_pknorm_i16_f32 v105, v114, v115\n"
"v_cvt_pknorm_i16_f32 v106, v116, v117\n"
"v_cvt_pknorm_i16_f32 v107, v118, v119\n"
"v_mfma_f32_16x16x32_bf16 v[72:75], v[64:67], v[4:7], v[72:75]\n"
"ds_read_b64_tr_b16 v[108:109], v232 offset:11264\n"
"ds_read_b64_tr_b16 v[110:111], v234 offset:11264\n"
"v_bfi_b32 v104, 15, v162, v104\n"
"v_bfi_b32 v105, 15, v132, v105\n"
"v_bfi_b32 v106, 15, v161, v106\n"
"v_mfma_f32_16x16x32_bf16 v[128:131], v[64:67], v[68:71], v[128:131]\n"
"v_bfi_b32 v107, 15, v133, v107\n"
"global_store_dwordx4 v226, v[104:107], s[42:43] offset:1024 sc1\n"
"s_waitcnt lgkmcnt(8)\n"
"v_mfma_f32_16x16x32_bf16 v[72:75], v[120:123], v[44:47], v[72:75]\n"
"ds_read_b64_tr_b16 v[64:65], v232 offset:12288\n"
"ds_read_b64_tr_b16 v[66:67], v234 offset:12288\n"
"v_mfma_f32_16x16x32_bf16 v[128:131], v[120:123], v[76:79], v[128:131]\n"
"v_mfma_f32_16x16x32_bf16 v[72:75], v[96:99], v[48:51], v[72:75]\n"
"ds_read_b64_tr_b16 v[104:105], v232 offset:13312\n"
"ds_read_b64_tr_b16 v[106:107], v234 offset:13312\n"
"v_mfma_f32_16x16x32_bf16 v[128:131], v[96:99], v[80:83], v[128:131]\n"
"s_waitcnt lgkmcnt(8)\n"
"v_mfma_f32_16x16x32_bf16 v[72:75], v[52:55], v[60:63], v[72:75]\n"
"ds_read_b64_tr_b16 v[96:97], v232 offset:14336\n"
"ds_read_b64_tr_b16 v[98:99], v234 offset:14336\n"
"v_mfma_f32_16x16x32_bf16 v[128:131], v[52:55], v[124:127], v[128:131]\n"
"v_mfma_f32_16x16x32_bf16 v[72:75], v[56:59], v[84:87], v[72:75]\n"
"ds_read_b64_tr_b16 v[52:53], v232 offset:15360\n"
"ds_read_b64_tr_b16 v[54:55], v234 offset:15360\n"
"global_load_dwordx4 v[112:115], v227, s[18:19] sc1\n"
"global_load_dwordx4 v[116:119], v227, s[20:21] sc1\n"
"global_load_dwordx2 v[150:151], v160, s[32:33]\n"
"global_load_dwordx2 v[148:149], v160, s[32:33] offset:2048\n"
"global_load_dwordx2 v[178:179], v160, s[34:35]\n"
"v_mfma_f32_16x16x32_bf16 v[128:131], v[56:59], v[168:171], v[128:131]\n"
"global_load_dwordx2 v[166:167], v160, s[34:35] offset:2048\n"
"s_waitcnt lgkmcnt(8)\n"
"v_mfma_f32_16x16x32_bf16 v[72:75], v[100:103], v[88:91], v[72:75]\n"
"s_waitcnt vmcnt(9)\n"
"v_lshlrev_b32 v206, 16, v136\n"
"v_and_b32 v207, 0xffff0000, v136\n"
"v_lshlrev_b32 v204, 16, v137\n"
"v_and_b32 v205, 0xffff0000, v137\n"
"v_mfma_f32_16x16x32_bf16 v[128:131], v[100:103], v[172:175], v[128:131]\n"
"v_mfma_f32_16x16x32_bf16 v[72:75], v[108:111], v[92:95], v[72:75]\n"
"v_mfma_f32_16x16x32_bf16 v[128:131], v[108:111], v[180:183], v[128:131]\n"
"s_waitcnt lgkmcnt(0)\n"
"s_barrier\n"
"v_mfma_f32_16x16x32_bf16 v[72:75], v[64:67], v[152:155], v[72:75]\n"
"ds_read_b64_tr_b16 v[56:57], v232 offset:49152\n"
"ds_read_b64_tr_b16 v[58:59], v234 offset:49152\n"
"v_mfma_f32_16x16x32_bf16 v[128:131], v[64:67], v[208:211], v[128:131]\n"
"ds_read_b64_tr_b16 v[64:65], v232 offset:50176\n"
"ds_read_b64_tr_b16 v[66:67], v234 offset:50176\n"
"v_mfma_f32_16x16x32_bf16 v[72:75], v[104:107], v[192:195], v[72:75]\n"
"ds_read_b64_tr_b16 v[100:101], v232 offset:51200\n"
"ds_read_b64_tr_b16 v[102:103], v234 offset:51200\n"
"v_mfma_f32_16x16x32_bf16 v[128:131], v[104:107], v[212:215], v[128:131]\n"
"ds_read_b64_tr_b16 v[104:105], v232 offset:52224\n"
"ds_read_b64_tr_b16 v[106:107], v234 offset:52224\n"
"v_mfma_f32_16x16x32_bf16 v[72:75], v[96:99], v[196:199], v[72:75]\n"
"ds_read_b64_tr_b16 v[108:109], v232 offset:53248\n"
"ds_read_b64_tr_b16 v[110:111], v234 offset:53248\n"
"ds_read_b128 v[120:123], v233 offset:4160\n"
"ds_read_b128 v[132:135], v233 offset:5184\n"
"v_mfma_f32_16x16x32_bf16 v[128:131], v[96:99], v[216:219], v[128:131]\n"
"v_mfma_f32_16x16x32_bf16 v[72:75], v[52:55], v[200:203], v[72:75]\n"
"ds_read_b64_tr_b16 v[96:97], v232 offset:54272\n"
"ds_read_b64_tr_b16 v[98:99], v234 offset:54272\n"
"v_mfma_f32_16x16x32_bf16 v[128:131], v[52:55], v[220:223], v[128:131]\n"
"s_waitcnt lgkmcnt(10)\n"
"v_mfma_f32_16x16x32_bf16 v[52:55], v[56:59], v[8:11], 0\n"
"ds_read_b64_tr_b16 v[136:137], v232 offset:55296\n"
"ds_read_b64_tr_b16 v[138:139], v234 offset:55296\n"
"v_mfma_f32_16x16x32_bf16 v[140:143], v[56:59], v[24:27], 0\n"
"v_mfma_f32_16x16x32_bf16 v[52:55], v[64:67], v[12:15], v[52:55]\n"
"ds_read_b64_tr_b16 v[56:57], v232 offset:56320\n"
"ds_read_b64_tr_b16 v[58:59], v234 offset:56320\n"
"v_mfma_f32_16x16x32_bf16 v[140:143], v[64:67], v[28:31], v[140:143]\n"
"s_waitcnt lgkmcnt(7)\n"
"v_add_f32 v120, v120, v72\n"
"v_add_f32 v121, v121, v73\n"
"v_add_f32 v122, v122, v74\n"
"v_add_f32 v123, v123, v75\n"
"v_mfma_f32_16x16x32_bf16 v[52:55], v[100:103], v[16:19], v[52:55]\n"
"ds_read_b64_tr_b16 v[64:65], v232 offset:57344\n"
"ds_read_b64_tr_b16 v[66:67], v234 offset:57344\n"
"s_waitcnt lgkmcnt(8)\n"
"v_add_f32 v132, v132, v128\n"
"v_add_f32 v133, v133, v129\n"
"v_add_f32 v134, v134, v130\n"
"v_mfma_f32_16x16x32_bf16 v[140:143], v[100:103], v[32:35], v[140:143]\n"
"v_add_f32 v135, v135, v131\n"
"v_max3_f32 v128, |v120|, |v121|, |v122|\n"
"v_max3_f32 v128, v128, |v123|, |v132|\n"
"v_max3_f32 v128, v128, |v133|, |v134|\n"
"v_max_f32_e64 v128, v128, |v135|\n"
"v_mfma_f32_16x16x32_bf16 v[52:55], v[104:107], v[20:23], v[52:55]\n"
"ds_read_b64_tr_b16 v[72:73], v232 offset:58368\n"
"ds_read_b64_tr_b16 v[74:75], v234 offset:58368\n"
"v_lshrrev_b32 v128, 23, v128\n"
"v_sub_u32 v129, 0x7e, v128\n"
"v_ldexp_f32 v120, v120, v129\n"
"v_mfma_f32_16x16x32_bf16 v[140:143], v[104:107], v[36:39], v[140:143]\n"
"v_ldexp_f32 v121, v121, v129\n"
"v_ldexp_f32 v122, v122, v129\n"
"v_ldexp_f32 v123, v123, v129\n"
"v_ldexp_f32 v132, v132, v129\n"
"s_waitcnt lgkmcnt(8)\n"
"v_mfma_f32_16x16x32_bf16 v[52:55], v[108:111], v[0:3], v[52:55]\n"
"ds_read_b64_tr_b16 v[100:101], v232 offset:59392\n"
"ds_read_b64_tr_b16 v[102:103], v234 offset:59392\n"
"v_ldexp_f32 v133, v133, v129\n"
"v_ldexp_f32 v134, v134, v129\n"
"v_ldexp_f32 v135, v135, v129\n"
"v_mfma_f32_16x16x32_bf16 v[140:143], v[108:111], v[40:43], v[140:143]\n"
"v_lshrrev_b32 v129, 4, v128\n"
"v_cvt_pknorm_i16_f32 v104, v120, v121\n"
"v_cvt_pknorm_i16_f32 v105, v122, v123\n"
"v_cvt_pknorm_i16_f32 v106, v132, v133\n"
"v_cvt_pknorm_i16_f32 v107, v134, v135\n"
"v_mfma_f32_16x16x32_bf16 v[52:55], v[96:99], v[4:7], v[52:55]\n"
"ds_read_b64_tr_b16 v[108:109], v232 offset:60416\n"
"ds_read_b64_tr_b16 v[110:111], v234 offset:60416\n"
"v_bfi_b32 v104, 15, v162, v104\n"
"v_bfi_b32 v105, 15, v128, v105\n"
"v_bfi_b32 v106, 15, v161, v106\n"
"v_mfma_f32_16x16x32_bf16 v[140:143], v[96:99], v[68:71], v[140:143]\n"
"v_bfi_b32 v107, 15, v129, v107\n"
"global_store_dwordx4 v226, v[104:107], s[42:43] offset:2048 sc1\n"
"s_waitcnt lgkmcnt(8)\n"
"v_mfma_f32_16x16x32_bf16 v[52:55], v[136:139], v[44:47], v[52:55]\n"
"ds_read_b64_tr_b16 v[96:97], v232 offset:61440\n"
"ds_read_b64_tr_b16 v[98:99], v234 offset:61440\n"
"v_mfma_f32_16x16x32_bf16 v[140:143], v[136:139], v[76:79], v[140:143]\n"
"v_mfma_f32_16x16x32_bf16 v[52:55], v[56:59], v[48:51], v[52:55]\n"
"ds_read_b64_tr_b16 v[104:105], v232 offset:62464\n"
"ds_read_b64_tr_b16 v[106:107], v234 offset:62464\n"
"v_mfma_f32_16x16x32_bf16 v[140:143], v[56:59], v[80:83], v[140:143]\n"
"s_waitcnt lgkmcnt(8)\n"
"v_mfma_f32_16x16x32_bf16 v[52:55], v[64:67], v[60:63], v[52:55]\n"
"ds_read_b64_tr_b16 v[56:57], v232 offset:63488\n"
"ds_read_b64_tr_b16 v[58:59], v234 offset:63488\n"
"v_mfma_f32_16x16x32_bf16 v[140:143], v[64:67], v[124:127], v[140:143]\n"
"v_mfma_f32_16x16x32_bf16 v[52:55], v[72:75], v[84:87], v[52:55]\n"
"ds_read_b64_tr_b16 v[64:65], v232 offset:64512\n"
"ds_read_b64_tr_b16 v[66:67], v234 offset:64512\n"
"v_mfma_f32_16x16x32_bf16 v[140:143], v[72:75], v[168:171], v[140:143]\n"
"s_waitcnt lgkmcnt(8)\n"
"v_mfma_f32_16x16x32_bf16 v[52:55], v[100:103], v[88:91], v[52:55]\n"
"v_mfma_f32_16x16x32_bf16 v[140:143], v[100:103], v[172:175], v[140:143]\n"
"v_mfma_f32_16x16x32_bf16 v[52:55], v[108:111], v[92:95], v[52:55]\n"
"v_mfma_f32_16x16x32_bf16 v[140:143], v[108:111], v[180:183], v[140:143]\n"
"s_waitcnt lgkmcnt(0)\n"
"s_barrier\n"
"v_mfma_f32_16x16x32_bf16 v[52:55], v[96:99], v[152:155], v[52:55]\n"
"ds_read_b64_tr_b16 v[72:73], v232 offset:32768\n"
"ds_read_b64_tr_b16 v[74:75], v234 offset:32768\n"
"v_xor_b32 v108, 64, v230\n"
"v_xor_b32 v109, 16, v230\n"
"v_mfma_f32_16x16x32_bf16 v[140:143], v[96:99], v[208:211], v[140:143]\n"
"ds_read_b64_tr_b16 v[96:97], v232 offset:33792\n"
"ds_read_b64_tr_b16 v[98:99], v234 offset:33792\n"
"v_xor_b32 v110, 0x50, v230\n"
"ds_read2_b32 v[156:157], v230 offset0:0 offset1:32\n"
"v_mfma_f32_16x16x32_bf16 v[52:55], v[104:107], v[192:195], v[52:55]\n"
"ds_read_b64_tr_b16 v[100:101], v232 offset:34816\n"
"ds_read_b64_tr_b16 v[102:103], v234 offset:34816\n"
"ds_read2_b32 v[158:159], v108 offset0:0 offset1:32\n"
"ds_read2_b32 v[188:189], v109 offset0:0 offset1:32\n"
"v_mfma_f32_16x16x32_bf16 v[140:143], v[104:107], v[212:215], v[140:143]\n"
"ds_read_b64_tr_b16 v[104:105], v232 offset:35840\n"
"ds_read_b64_tr_b16 v[106:107], v234 offset:35840\n"
"ds_read2_b32 v[190:191], v110 offset0:0 offset1:32\n"
"v_mfma_f32_16x16x32_bf16 v[52:55], v[56:59], v[196:199], v[52:55]\n"
"ds_read_b64_tr_b16 v[108:109], v232 offset:36864\n"
"ds_read_b64_tr_b16 v[110:111], v234 offset:36864\n"
"ds_read_b128 v[120:123], v233 offset:6208\n"
"ds_read_b128 v[128:131], v233 offset:7232\n"
"v_mfma_f32_16x16x32_bf16 v[140:143], v[56:59], v[216:219], v[140:143]\n"
"s_waitcnt vmcnt(6)\n"
"v_xor_b32 v132, s10, v113\n"
"v_bitop3_b32 v132, v132, v115, s10 bitop3:0xf6\n"
"s_waitcnt vmcnt(5)\n"
"v_bitop3_b32 v132, v132, v117, s10 bitop3:0xf6\n"
"v_bitop3_b32 v132, v132, v119, s10 bitop3:0xf6\n"
"v_mfma_f32_16x16x32_bf16 v[52:55], v[64:67], v[200:203], v[52:55]\n"
"ds_read_b64_tr_b16 v[56:57], v232 offset:37888\n"
"ds_read_b64_tr_b16 v[58:59], v234 offset:37888\n"
"v_mfma_f32_16x16x32_bf16 v[140:143], v[64:67], v[220:223], v[140:143]\n"
"v_cmp_ne_u32 vcc, 0, v132\n"
"s_nop 0\n"
"s_cbranch_vccnz .Lr2\n"
".Lb2:\n"
"v_sub_f32 v177, 1.0, v112\n"
"s_waitcnt lgkmcnt(14)\n"
"v_mfma_f32_16x16x32_bf16 v[64:67], v[72:75], v[8:11], 0\n"
"ds_read_b64_tr_b16 v[132:133], v232 offset:38912\n"
"ds_read_b64_tr_b16 v[134:135], v234 offset:38912\n"
"v_add_f32 v228, v112, v112\n"
"v_cvt_pk_bf16_f32 v177, 0, v177\n"
"v_mfma_f32_16x16x32_bf16 v[136:139], v[72:75], v[24:27], 0\n"
"s_waitcnt lgkmcnt(15)\n"
"v_lshlrev_b32 v165, 16, v156\n"
"s_waitcnt lgkmcnt(12)\n"
"v_lshlrev_b32 v176, 16, v158\n"
"v_lshlrev_b32 v163, 16, v157\n"
"v_lshlrev_b32 v164, 16, v159\n"
"v_mfma_f32_16x16x32_bf16 v[64:67], v[96:99], v[12:15], v[64:67]\n"
"ds_read_b64_tr_b16 v[72:73], v232 offset:39936\n"
"ds_read_b64_tr_b16 v[74:75], v234 offset:39936\n"
"v_mul_f32 v144, v112, v206\n"
"v_mul_f32 v145, v112, v207\n"
"v_mfma_f32_16x16x32_bf16 v[136:139], v[96:99], v[28:31], v[136:139]\n"
"s_waitcnt lgkmcnt(7)\n"
"v_add_f32 v120, v120, v52\n"
"v_add_f32 v121, v121, v53\n"
"v_add_f32 v122, v122, v54\n"
"v_add_f32 v123, v123, v55\n"
"v_mfma_f32_16x16x32_bf16 v[64:67], v[100:103], v[16:19], v[64:67]\n"
"ds_read_b64_tr_b16 v[52:53], v232 offset:40960\n"
"ds_read_b64_tr_b16 v[54:55], v234 offset:40960\n"
"s_waitcnt lgkmcnt(8)\n"
"v_add_f32 v128, v128, v140\n"
"v_add_f32 v129, v129, v141\n"
"v_add_f32 v130, v130, v142\n"
"v_mfma_f32_16x16x32_bf16 v[136:139], v[100:103], v[32:35], v[136:139]\n"
"v_add_f32 v131, v131, v143\n"
"v_max3_f32 v140, |v120|, |v121|, |v122|\n"
"v_max3_f32 v140, v140, |v123|, |v128|\n"
"v_max3_f32 v140, v140, |v129|, |v130|\n"
"v_max_f32_e64 v140, v140, |v131|\n"
"v_mfma_f32_16x16x32_bf16 v[64:67], v[104:107], v[20:23], v[64:67]\n"
"ds_read_b64_tr_b16 v[96:97], v232 offset:41984\n"
"ds_read_b64_tr_b16 v[98:99], v234 offset:41984\n"
"v_lshrrev_b32 v140, 23, v140\n"
"v_sub_u32 v141, 0x7e, v140\n"
"v_ldexp_f32 v120, v120, v141\n"
"v_mfma_f32_16x16x32_bf16 v[136:139], v[104:107], v[36:39], v[136:139]\n"
"v_ldexp_f32 v121, v121, v141\n"
"v_ldexp_f32 v122, v122, v141\n"
"v_ldexp_f32 v123, v123, v141\n"
"v_ldexp_f32 v128, v128, v141\n"
"s_waitcnt lgkmcnt(8)\n"
"v_mfma_f32_16x16x32_bf16 v[64:67], v[108:111], v[0:3], v[64:67]\n"
"ds_read_b64_tr_b16 v[100:101], v232 offset:43008\n"
"ds_read_b64_tr_b16 v[102:103], v234 offset:43008\n"
"v_ldexp_f32 v129, v129, v141\n"
"v_ldexp_f32 v130, v130, v141\n"
"v_ldexp_f32 v131, v131, v141\n"
"v_mfma_f32_16x16x32_bf16 v[136:139], v[108:111], v[40:43], v[136:139]\n"
"v_lshrrev_b32 v141, 4, v140\n"
"v_cvt_pknorm_i16_f32 v104, v120, v121\n"
"v_cvt_pknorm_i16_f32 v105, v122, v123\n"
"v_cvt_pknorm_i16_f32 v106, v128, v129\n"
"v_cvt_pknorm_i16_f32 v107, v130, v131\n"
"v_mfma_f32_16x16x32_bf16 v[64:67], v[56:59], v[4:7], v[64:67]\n"
"ds_read_b64_tr_b16 v[108:109], v232 offset:44032\n"
"ds_read_b64_tr_b16 v[110:111], v234 offset:44032\n"
"v_bfi_b32 v104, 15, v162, v104\n"
"v_bfi_b32 v105, 15, v140, v105\n"
"v_bfi_b32 v106, 15, v161, v106\n"
"v_mfma_f32_16x16x32_bf16 v[136:139], v[56:59], v[68:71], v[136:139]\n"
"v_bfi_b32 v107, 15, v141, v107\n"
"global_store_dwordx4 v226, v[104:107], s[42:43] offset:3072 sc1\n"
"v_mul_f32 v146, v112, v204\n"
"s_waitcnt lgkmcnt(8)\n"
"v_mfma_f32_16x16x32_bf16 v[64:67], v[132:135], v[44:47], v[64:67]\n"
"ds_read_b64_tr_b16 v[56:57], v232 offset:45056\n"
"ds_read_b64_tr_b16 v[58:59], v234 offset:45056\n"
"v_mul_f32 v147, v112, v205\n"
"v_fmac_f32 v144, v177, v165\n"
"v_mfma_f32_16x16x32_bf16 v[136:139], v[132:135], v[76:79], v[136:139]\n"
"v_fmac_f32 v145, v177, v176\n"
"v_fmac_f32 v146, v177, v163\n"
"v_fmac_f32 v147, v177, v164\n"
"s_waitcnt vmcnt(5)\n"
"v_lshlrev_b32 v165, 16, v150\n"
"v_mfma_f32_16x16x32_bf16 v[64:67], v[72:75], v[48:51], v[64:67]\n"
"ds_read_b64_tr_b16 v[104:105], v232 offset:46080\n"
"ds_read_b64_tr_b16 v[106:107], v234 offset:46080\n"
"v_and_b32 v176, 0xffff0000, v150\n"
"v_lshlrev_b32 v163, 16, v151\n"
"v_mfma_f32_16x16x32_bf16 v[136:139], v[72:75], v[80:83], v[136:139]\n"
"v_and_b32 v164, 0xffff0000, v151\n"
"v_mul_f32 v144, v144, v165\n"
"v_mul_f32 v145, v145, v176\n"
"s_waitcnt lgkmcnt(8)\n"
"v_mfma_f32_16x16x32_bf16 v[64:67], v[52:55], v[60:63], v[64:67]\n"
"ds_read_b64_tr_b16 v[72:73], v232 offset:47104\n"
"ds_read_b64_tr_b16 v[74:75], v234 offset:47104\n"
"v_mul_f32 v146, v146, v163\n"
"v_mul_f32 v147, v147, v164\n"
"v_mfma_f32_16x16x32_bf16 v[136:139], v[52:55], v[124:127], v[136:139]\n"
"v_sub_f32 v134, 1.0, v114\n"
"v_add_f32 v229, v114, v114\n"
"v_cvt_pk_bf16_f32 v134, 0, v134\n"
"v_and_b32 v132, 0xffff0000, v156\n"
"v_mfma_f32_16x16x32_bf16 v[64:67], v[96:99], v[84:87], v[64:67]\n"
"ds_read_b64_tr_b16 v[52:53], v232 offset:48128\n"
"ds_read_b64_tr_b16 v[54:55], v234 offset:48128\n"
"global_load_dwordx4 v[120:123], v227, s[22:23] sc1\n"
"global_load_dwordx4 v[128:131], v227, s[24:25] sc1\n"
"global_load_dwordx2 v[186:187], v160, s[28:29]\n"
"global_load_dwordx2 v[184:185], v160, s[28:29] offset:2048\n"
"global_load_dwordx2 v[176:177], v160, s[84:85]\n"
"v_mfma_f32_16x16x32_bf16 v[136:139], v[96:99], v[168:171], v[136:139]\n"
"global_load_dwordx2 v[164:165], v160, s[84:85] offset:2048\n"
"v_and_b32 v140, 0xffff0000, v158\n"
"v_and_b32 v133, 0xffff0000, v157\n"
"s_waitcnt lgkmcnt(8)\n"
"v_mfma_f32_16x16x32_bf16 v[64:67], v[100:103], v[88:91], v[64:67]\n"
"v_and_b32 v135, 0xffff0000, v159\n"
"v_mul_f32 v96, v114, v206\n"
"v_mul_f32 v97, v114, v207\n"
"v_mul_f32 v98, v114, v204\n"
"v_mfma_f32_16x16x32_bf16 v[136:139], v[100:103], v[172:175], v[136:139]\n"
"v_mul_f32 v99, v114, v205\n"
"v_fmac_f32 v96, v134, v132\n"
"v_fmac_f32 v97, v134, v140\n"
"v_fmac_f32 v98, v134, v133\n"
"v_mfma_f32_16x16x32_bf16 v[64:67], v[108:111], v[92:95], v[64:67]\n"
"v_fmac_f32 v99, v134, v135\n"
"s_waitcnt vmcnt(10)\n"
"v_lshlrev_b32 v132, 16, v148\n"
"v_and_b32 v140, 0xffff0000, v148\n"
"v_lshlrev_b32 v133, 16, v149\n"
"v_mfma_f32_16x16x32_bf16 v[136:139], v[108:111], v[180:183], v[136:139]\n"
"v_and_b32 v135, 0xffff0000, v149\n"
"s_waitcnt lgkmcnt(0)\n"
"s_barrier\n"
"v_mfma_f32_16x16x32_bf16 v[64:67], v[56:59], v[152:155], v[64:67]\n"
"ds_read_b64_tr_b16 v[100:101], v232\n"
"ds_read_b64_tr_b16 v[102:103], v234\n"
"v_mul_f32 v96, v96, v132\n"
"v_mul_f32 v97, v97, v140\n"
"v_mfma_f32_16x16x32_bf16 v[136:139], v[56:59], v[208:211], v[136:139]\n"
"ds_read_b64_tr_b16 v[56:57], v232 offset:1024\n"
"ds_read_b64_tr_b16 v[58:59], v234 offset:1024\n"
"v_mul_f32 v98, v98, v133\n"
"v_mul_f32 v99, v99, v135\n"
"v_mfma_f32_16x16x32_bf16 v[64:67], v[104:107], v[192:195], v[64:67]\n"
"ds_read_b64_tr_b16 v[108:109], v232 offset:2048\n"
"ds_read_b64_tr_b16 v[110:111], v234 offset:2048\n"
"v_sub_f32 v238, 1.0, v116\n"
"v_add_f32 v226, v116, v116\n"
"v_mfma_f32_16x16x32_bf16 v[136:139], v[104:107], v[212:215], v[136:139]\n"
"ds_read_b64_tr_b16 v[104:105], v232 offset:3072\n"
"ds_read_b64_tr_b16 v[106:107], v234 offset:3072\n"
"v_cvt_pk_bf16_f32 v238, 0, v238\n"
"v_mfma_f32_16x16x32_bf16 v[64:67], v[72:75], v[196:199], v[64:67]\n"
"ds_read_b64_tr_b16 v[112:113], v232 offset:4096\n"
"ds_read_b64_tr_b16 v[114:115], v234 offset:4096\n"
"ds_read_b128 v[132:135], v233 offset:4160\n"
"ds_read_b128 v[140:143], v233 offset:5184\n"
"v_mfma_f32_16x16x32_bf16 v[136:139], v[72:75], v[216:219], v[136:139]\n"
"v_lshlrev_b32 v239, 16, v188\n"
"v_lshlrev_b32 v235, 16, v190\n"
"v_lshlrev_b32 v236, 16, v189\n"
"v_lshlrev_b32 v237, 16, v191\n"
"v_mfma_f32_16x16x32_bf16 v[64:67], v[52:55], v[200:203], v[64:67]\n"
"ds_read_b64_tr_b16 v[72:73], v232 offset:5120\n"
"ds_read_b64_tr_b16 v[74:75], v234 offset:5120\n"
"v_mul_f32 v148, v116, v206\n"
"v_mul_f32 v149, v116, v207\n"
"v_mfma_f32_16x16x32_bf16 v[136:139], v[52:55], v[220:223], v[136:139]\n"
"v_mul_f32 v150, v116, v204\n"
"v_mul_f32 v151, v116, v205\n"
"v_fmac_f32 v148, v238, v239\n"
"s_waitcnt lgkmcnt(10)\n"
"v_mfma_f32_16x16x32_bf16 v[52:55], v[100:103], v[8:11], 0\n"
"ds_read_b64_tr_b16 v[156:157], v232 offset:6144\n"
"ds_read_b64_tr_b16 v[158:159], v234 offset:6144\n"
"v_fmac_f32 v149, v238, v235\n"
"v_fmac_f32 v150, v238, v236\n"
"v_mfma_f32_16x16x32_bf16 v[160:163], v[100:103], v[24:27], 0\n"
"v_fmac_f32 v151, v238, v237\n"
"s_waitcnt vmcnt(9)\n"
"v_lshlrev_b32 v239, 16, v178\n"
"v_and_b32 v235, 0xffff0000, v178\n"
"v_lshlrev_b32 v236, 16, v179\n"
"v_mfma_f32_16x16x32_bf16 v[52:55], v[56:59], v[12:15], v[52:55]\n"
"ds_read_b64_tr_b16 v[100:101], v232 offset:7168\n"
"ds_read_b64_tr_b16 v[102:103], v234 offset:7168\n"
"v_and_b32 v237, 0xffff0000, v179\n"
"v_mul_f32 v148, v148, v239\n"
"v_mfma_f32_16x16x32_bf16 v[160:163], v[56:59], v[28:31], v[160:163]\n"
"s_waitcnt lgkmcnt(7)\n"
"v_add_f32 v132, v132, v64\n"
"v_add_f32 v133, v133, v65\n"
"v_add_f32 v134, v134, v66\n"
"v_add_f32 v135, v135, v67\n"
"v_mfma_f32_16x16x32_bf16 v[52:55], v[108:111], v[16:19], v[52:55]\n"
"ds_read_b64_tr_b16 v[56:57], v232 offset:8192\n"
"ds_read_b64_tr_b16 v[58:59], v234 offset:8192\n"
"s_waitcnt lgkmcnt(8)\n"
"v_add_f32 v140, v140, v136\n"
"v_add_f32 v141, v141, v137\n"
"v_add_f32 v142, v142, v138\n"
"v_mfma_f32_16x16x32_bf16 v[160:163], v[108:111], v[32:35], v[160:163]\n"
"v_add_f32 v143, v143, v139\n"
"v_mul_f32 v149, v149, v235\n"
"v_mul_f32 v150, v150, v236\n"
"v_mul_f32 v151, v151, v237\n"
"v_mfma_f32_16x16x32_bf16 v[52:55], v[104:107], v[20:23], v[52:55]\n"
"ds_read_b64_tr_b16 v[64:65], v232 offset:9216\n"
"ds_read_b64_tr_b16 v[66:67], v234 offset:9216\n"
"v_fmac_f32_dpp v144, v132, v228 quad_perm:[0,1,2,3] row_mask:0xf bank_mask:0x3\n"
"v_fmac_f32_dpp v145, v140, v228 quad_perm:[0,1,2,3] row_mask:0xf bank_mask:0x3\n"
"v_fmac_f32_dpp v146, v132, v228 row_ror:8 row_mask:0xf bank_mask:0x3\n"
"v_mfma_f32_16x16x32_bf16 v[160:163], v[104:107], v[36:39], v[160:163]\n"
"v_fmac_f32_dpp v147, v140, v228 row_ror:8 row_mask:0xf bank_mask:0x3\n"
"v_fmac_f32_dpp v144, v134, v228 row_ror:8 row_mask:0xf bank_mask:0xc\n"
"v_fmac_f32_dpp v145, v142, v228 row_ror:8 row_mask:0xf bank_mask:0xc\n"
"v_fmac_f32_dpp v146, v134, v228 quad_perm:[0,1,2,3] row_mask:0xf bank_mask:0xc\n"
"s_waitcnt lgkmcnt(8)\n"
"v_mfma_f32_16x16x32_bf16 v[52:55], v[112:115], v[0:3], v[52:55]\n"
"ds_read_b64_tr_b16 v[104:105], v232 offset:10240\n"
"ds_read_b64_tr_b16 v[106:107], v234 offset:10240\n"
"v_fmac_f32_dpp v147, v142, v228 quad_perm:[0,1,2,3] row_mask:0xf bank_mask:0xc\n"
"v_fmac_f32_dpp v96, v133, v229 quad_perm:[0,1,2,3] row_mask:0xf bank_mask:0x3\n"
"v_fmac_f32_dpp v97, v141, v229 quad_perm:[0,1,2,3] row_mask:0xf bank_mask:0x3\n"
"v_mfma_f32_16x16x32_bf16 v[160:163], v[112:115], v[40:43], v[160:163]\n"
"v_fmac_f32_dpp v98, v133, v229 row_ror:8 row_mask:0xf bank_mask:0x3\n"
"v_fmac_f32_dpp v99, v141, v229 row_ror:8 row_mask:0xf bank_mask:0x3\n"
"v_fmac_f32_dpp v96, v135, v229 row_ror:8 row_mask:0xf bank_mask:0xc\n"
"v_fmac_f32_dpp v97, v143, v229 row_ror:8 row_mask:0xf bank_mask:0xc\n"
"v_fmac_f32_dpp v98, v135, v229 quad_perm:[0,1,2,3] row_mask:0xf bank_mask:0xc\n"
"v_mfma_f32_16x16x32_bf16 v[52:55], v[72:75], v[4:7], v[52:55]\n"
"ds_read_b64_tr_b16 v[108:109], v232 offset:11264\n"
"ds_read_b64_tr_b16 v[110:111], v234 offset:11264\n"
"v_fmac_f32_dpp v99, v143, v229 quad_perm:[0,1,2,3] row_mask:0xf bank_mask:0xc\n"
"v_sub_f32 v140, 1.0, v118\n"
"v_mfma_f32_16x16x32_bf16 v[160:163], v[72:75], v[68:71], v[160:163]\n"
"v_add_f32 v228, v118, v118\n"
"v_cvt_pk_bf16_f32 v140, 0, v140\n"
"v_and_b32 v138, 0xffff0000, v188\n"
"s_waitcnt lgkmcnt(8)\n"
"v_mfma_f32_16x16x32_bf16 v[52:55], v[156:159], v[44:47], v[52:55]\n"
"ds_read_b64_tr_b16 v[72:73], v232 offset:12288\n"
"ds_read_b64_tr_b16 v[74:75], v234 offset:12288\n"
"v_and_b32 v139, 0xffff0000, v190\n"
"v_and_b32 v136, 0xffff0000, v189\n"
"v_mfma_f32_16x16x32_bf16 v[160:163], v[156:159], v[76:79], v[160:163]\n"
"v_and_b32 v137, 0xffff0000, v191\n"
"v_mul_f32 v112, v118, v206\n"
"v_mul_f32 v113, v118, v207\n"
"v_mul_f32 v114, v118, v204\n"
"v_mfma_f32_16x16x32_bf16 v[52:55], v[100:103], v[48:51], v[52:55]\n"
"ds_read_b64_tr_b16 v[132:133], v232 offset:13312\n"
"ds_read_b64_tr_b16 v[134:135], v234 offset:13312\n"
"v_mul_f32 v115, v118, v205\n"
"v_fmac_f32 v112, v140, v138\n"
"v_mfma_f32_16x16x32_bf16 v[160:163], v[100:103], v[80:83], v[160:163]\n"
"v_fmac_f32 v113, v140, v139\n"
"v_fmac_f32 v114, v140, v136\n"
"v_fmac_f32 v115, v140, v137\n"
"s_waitcnt lgkmcnt(8)\n"
"v_mfma_f32_16x16x32_bf16 v[52:55], v[56:59], v[60:63], v[52:55]\n"
"ds_read_b64_tr_b16 v[100:101], v232 offset:14336\n"
"ds_read_b64_tr_b16 v[102:103], v234 offset:14336\n"
"s_waitcnt vmcnt(8)\n"
"v_lshlrev_b32 v138, 16, v166\n"
"v_and_b32 v139, 0xffff0000, v166\n"
"v_mfma_f32_16x16x32_bf16 v[160:163], v[56:59], v[124:127], v[160:163]\n"
"v_lshlrev_b32 v136, 16, v167\n"
"v_and_b32 v137, 0xffff0000, v167\n"
"v_mul_f32 v112, v112, v138\n"
"v_mul_f32 v113, v113, v139\n"
"v_mfma_f32_16x16x32_bf16 v[52:55], v[64:67], v[84:87], v[52:55]\n"
"ds_read_b64_tr_b16 v[56:57], v232 offset:15360\n"
"ds_read_b64_tr_b16 v[58:59], v234 offset:15360\n"
"global_store_dwordx4 v231, v[144:147], s[86:87] offset:-4096 sc0 sc1\n"
"global_store_dwordx4 v231, v[96:99], s[86:87] sc0 sc1\n"
"v_mfma_f32_16x16x32_bf16 v[160:163], v[64:67], v[168:171], v[160:163]\n"
"v_mul_f32 v114, v114, v136\n"
"v_mul_f32 v115, v115, v137\n"
"s_waitcnt lgkmcnt(8)\n"
"v_mfma_f32_16x16x32_bf16 v[52:55], v[104:107], v[88:91], v[52:55]\n"
"v_mfma_f32_16x16x32_bf16 v[160:163], v[104:107], v[172:175], v[160:163]\n"
"v_mfma_f32_16x16x32_bf16 v[52:55], v[108:111], v[92:95], v[52:55]\n"
"v_mfma_f32_16x16x32_bf16 v[160:163], v[108:111], v[180:183], v[160:163]\n"
"s_waitcnt lgkmcnt(0)\n"
"s_barrier\n"
"v_mfma_f32_16x16x32_bf16 v[52:55], v[72:75], v[152:155], v[52:55]\n"
"ds_read_b64_tr_b16 v[64:65], v232 offset:49152\n"
"ds_read_b64_tr_b16 v[66:67], v234 offset:49152\n"
"v_add_u32 v230, 0x800, v230\n"
"v_xor_b32 v104, 64, v230\n"
"v_mfma_f32_16x16x32_bf16 v[160:163], v[72:75], v[208:211], v[160:163]\n"
"ds_read_b64_tr_b16 v[72:73], v232 offset:50176\n"
"ds_read_b64_tr_b16 v[74:75], v234 offset:50176\n"
"v_xor_b32 v108, 16, v230\n"
"v_xor_b32 v136, 0x50, v230\n"
"v_mfma_f32_16x16x32_bf16 v[52:55], v[132:135], v[192:195], v[52:55]\n"
"ds_read_b64_tr_b16 v[96:97], v232 offset:51200\n"
"ds_read_b64_tr_b16 v[98:99], v234 offset:51200\n"
"ds_read2_b32 v[178:179], v230 offset0:0 offset1:32\n"
"ds_read2_b32 v[166:167], v104 offset0:0 offset1:32\n"
"v_mfma_f32_16x16x32_bf16 v[160:163], v[132:135], v[212:215], v[160:163]\n"
"ds_read_b64_tr_b16 v[104:105], v232 offset:52224\n"
"ds_read_b64_tr_b16 v[106:107], v234 offset:52224\n"
"ds_read2_b32 v[156:157], v108 offset0:0 offset1:32\n"
"v_mfma_f32_16x16x32_bf16 v[52:55], v[100:103], v[196:199], v[52:55]\n"
"ds_read_b64_tr_b16 v[108:109], v232 offset:53248\n"
"ds_read_b64_tr_b16 v[110:111], v234 offset:53248\n"
"ds_read_b128 v[116:119], v233 offset:6208\n"
"ds_read_b128 v[132:135], v233 offset:7232\n"
"v_mfma_f32_16x16x32_bf16 v[160:163], v[100:103], v[216:219], v[160:163]\n"
"ds_read2_b32 v[158:159], v136 offset0:0 offset1:32\n"
"s_waitcnt vmcnt(7)\n"
"v_xor_b32 v136, s10, v121\n"
"v_bitop3_b32 v136, v136, v123, s10 bitop3:0xf6\n"
"s_waitcnt vmcnt(6)\n"
"v_bitop3_b32 v136, v136, v129, s10 bitop3:0xf6\n"
"v_mfma_f32_16x16x32_bf16 v[52:55], v[56:59], v[200:203], v[52:55]\n"
"ds_read_b64_tr_b16 v[100:101], v232 offset:54272\n"
"ds_read_b64_tr_b16 v[102:103], v234 offset:54272\n"
"v_bitop3_b32 v136, v136, v131, s10 bitop3:0xf6\n"
"v_mfma_f32_16x16x32_bf16 v[160:163], v[56:59], v[220:223], v[160:163]\n"
"v_cmp_ne_u32 vcc, 0, v136\n"
"s_nop 0\n"
"s_cbranch_vccnz .Lr3\n"
".Lb3:\n"
"v_sub_f32 v229, 1.0, v120\n"
"s_waitcnt lgkmcnt(14)\n"
"v_mfma_f32_16x16x32_bf16 v[56:59], v[64:67], v[8:11], 0\n"
"ds_read_b64_tr_b16 v[136:137], v232 offset:55296\n"
"ds_read_b64_tr_b16 v[138:139], v234 offset:55296\n"
"v_add_f32 v188, v120, v120\n"
"v_cvt_pk_bf16_f32 v229, 0, v229\n"
"v_mfma_f32_16x16x32_bf16 v[140:143], v[64:67], v[24:27], 0\n"
"s_waitcnt lgkmcnt(13)\n"
"v_lshlrev_b32 v190, 16, v178\n"
"s_waitcnt lgkmcnt(12)\n"
"v_lshlrev_b32 v191, 16, v166\n"
"v_lshlrev_b32 v227, 16, v179\n"
"v_lshlrev_b32 v189, 16, v167\n"
"v_mfma_f32_16x16x32_bf16 v[56:59], v[72:75], v[12:15], v[56:59]\n"
"ds_read_b64_tr_b16 v[64:65], v232 offset:56320\n"
"ds_read_b64_tr_b16 v[66:67], v234 offset:56320\n"
"v_mul_f32 v144, v120, v206\n"
"v_mul_f32 v145, v120, v207\n"
"v_mfma_f32_16x16x32_bf16 v[140:143], v[72:75], v[28:31], v[140:143]\n"
"s_waitcnt lgkmcnt(8)\n"
"v_add_f32 v116, v116, v52\n"
"v_add_f32 v117, v117, v53\n"
"v_add_f32 v118, v118, v54\n"
"v_add_f32 v119, v119, v55\n"
"v_mfma_f32_16x16x32_bf16 v[56:59], v[96:99], v[16:19], v[56:59]\n"
"ds_read_b64_tr_b16 v[52:53], v232 offset:57344\n"
"ds_read_b64_tr_b16 v[54:55], v234 offset:57344\n"
"s_waitcnt lgkmcnt(9)\n"
"v_add_f32 v132, v132, v160\n"
"v_add_f32 v133, v133, v161\n"
"v_add_f32 v134, v134, v162\n"
"v_mfma_f32_16x16x32_bf16 v[140:143], v[96:99], v[32:35], v[140:143]\n"
"v_add_f32 v135, v135, v163\n"
"v_mul_f32 v146, v120, v204\n"
"v_mul_f32 v147, v120, v205\n"
"v_fmac_f32 v144, v229, v190\n"
"v_mfma_f32_16x16x32_bf16 v[56:59], v[104:107], v[20:23], v[56:59]\n"
"ds_read_b64_tr_b16 v[72:73], v232 offset:58368\n"
"ds_read_b64_tr_b16 v[74:75], v234 offset:58368\n"
"v_fmac_f32_dpp v148, v116, v226 quad_perm:[0,1,2,3] row_mask:0xf bank_mask:0x3\n"
"v_fmac_f32_dpp v149, v132, v226 quad_perm:[0,1,2,3] row_mask:0xf bank_mask:0x3\n"
"v_fmac_f32_dpp v150, v116, v226 row_ror:8 row_mask:0xf bank_mask:0x3\n"
"v_mfma_f32_16x16x32_bf16 v[140:143], v[104:107], v[36:39], v[140:143]\n"
"v_fmac_f32_dpp v151, v132, v226 row_ror:8 row_mask:0xf bank_mask:0x3\n"
"v_fmac_f32_dpp v148, v118, v226 row_ror:8 row_mask:0xf bank_mask:0xc\n"
"v_fmac_f32_dpp v149, v134, v226 row_ror:8 row_mask:0xf bank_mask:0xc\n"
"v_fmac_f32_dpp v150, v118, v226 quad_perm:[0,1,2,3] row_mask:0xf bank_mask:0xc\n"
"s_waitcnt lgkmcnt(8)\n"
"v_mfma_f32_16x16x32_bf16 v[56:59], v[108:111], v[0:3], v[56:59]\n"
"ds_read_b64_tr_b16 v[96:97], v232 offset:59392\n"
"ds_read_b64_tr_b16 v[98:99], v234 offset:59392\n"
"v_fmac_f32_dpp v151, v134, v226 quad_perm:[0,1,2,3] row_mask:0xf bank_mask:0xc\n"
"v_fmac_f32_dpp v112, v117, v228 quad_perm:[0,1,2,3] row_mask:0xf bank_mask:0x3\n"
"v_fmac_f32_dpp v113, v133, v228 quad_perm:[0,1,2,3] row_mask:0xf bank_mask:0x3\n"
"v_mfma_f32_16x16x32_bf16 v[140:143], v[108:111], v[40:43], v[140:143]\n"
"v_fmac_f32_dpp v114, v117, v228 row_ror:8 row_mask:0xf bank_mask:0x3\n"
"v_fmac_f32_dpp v115, v133, v228 row_ror:8 row_mask:0xf bank_mask:0x3\n"
"v_fmac_f32_dpp v112, v119, v228 row_ror:8 row_mask:0xf bank_mask:0xc\n"
"v_fmac_f32_dpp v113, v135, v228 row_ror:8 row_mask:0xf bank_mask:0xc\n"
"v_fmac_f32_dpp v114, v119, v228 quad_perm:[0,1,2,3] row_mask:0xf bank_mask:0xc\n"
"v_mfma_f32_16x16x32_bf16 v[56:59], v[100:103], v[4:7], v[56:59]\n"
"ds_read_b64_tr_b16 v[104:105], v232 offset:60416\n"
"ds_read_b64_tr_b16 v[106:107], v234 offset:60416\n"
"v_fmac_f32_dpp v115, v135, v228 quad_perm:[0,1,2,3] row_mask:0xf bank_mask:0xc\n"
"v_fmac_f32 v145, v229, v191\n"
"v_mfma_f32_16x16x32_bf16 v[140:143], v[100:103], v[68:71], v[140:143]\n"
"v_fmac_f32 v146, v229, v227\n"
"v_fmac_f32 v147, v229, v189\n"
"s_waitcnt vmcnt(5)\n"
"v_lshlrev_b32 v190, 16, v186\n"
"s_waitcnt lgkmcnt(8)\n"
"v_mfma_f32_16x16x32_bf16 v[56:59], v[136:139], v[44:47], v[56:59]\n"
"ds_read_b64_tr_b16 v[100:101], v232 offset:61440\n"
"ds_read_b64_tr_b16 v[102:103], v234 offset:61440\n"
"v_and_b32 v191, 0xffff0000, v186\n"
"v_lshlrev_b32 v227, 16, v187\n"
"v_mfma_f32_16x16x32_bf16 v[140:143], v[136:139], v[76:79], v[140:143]\n"
"v_and_b32 v189, 0xffff0000, v187\n"
"v_mul_f32 v144, v144, v190\n"
"v_mul_f32 v145, v145, v191\n"
"v_mul_f32 v146, v146, v227\n"
"v_mfma_f32_16x16x32_bf16 v[56:59], v[64:67], v[48:51], v[56:59]\n"
"ds_read_b64_tr_b16 v[108:109], v232 offset:62464\n"
"ds_read_b64_tr_b16 v[110:111], v234 offset:62464\n"
"v_mul_f32 v147, v147, v189\n"
"v_sub_f32 v133, 1.0, v122\n"
"v_mfma_f32_16x16x32_bf16 v[140:143], v[64:67], v[80:83], v[140:143]\n"
"v_add_f32 v160, v122, v122\n"
"v_cvt_pk_bf16_f32 v133, 0, v133\n"
"v_and_b32 v132, 0xffff0000, v178\n"
"s_waitcnt lgkmcnt(8)\n"
"v_mfma_f32_16x16x32_bf16 v[56:59], v[52:55], v[60:63], v[56:59]\n"
"ds_read_b64_tr_b16 v[64:65], v232 offset:63488\n"
"ds_read_b64_tr_b16 v[66:67], v234 offset:63488\n"
"v_and_b32 v135, 0xffff0000, v166\n"
"v_and_b32 v134, 0xffff0000, v179\n"
"v_mfma_f32_16x16x32_bf16 v[140:143], v[52:55], v[124:127], v[140:143]\n"
"v_and_b32 v136, 0xffff0000, v167\n"
"v_mul_f32 v52, v122, v206\n"
"v_mul_f32 v53, v122, v207\n"
"v_mul_f32 v54, v122, v204\n"
"v_mfma_f32_16x16x32_bf16 v[56:59], v[72:75], v[84:87], v[56:59]\n"
"ds_read_b64_tr_b16 v[116:117], v232 offset:64512\n"
"ds_read_b64_tr_b16 v[118:119], v234 offset:64512\n"
"global_store_dwordx4 v231, v[148:151], s[88:89] offset:-4096 sc0 sc1\n"
"global_store_dwordx4 v231, v[112:115], s[88:89] sc0 sc1\n"
"v_mfma_f32_16x16x32_bf16 v[140:143], v[72:75], v[168:171], v[140:143]\n"
"v_mul_f32 v55, v122, v205\n"
"v_fmac_f32 v52, v133, v132\n"
"v_fmac_f32 v53, v133, v135\n"
"s_waitcnt lgkmcnt(8)\n"
"v_mfma_f32_16x16x32_bf16 v[56:59], v[96:99], v[88:91], v[56:59]\n"
"v_fmac_f32 v54, v133, v134\n"
"v_fmac_f32 v55, v133, v136\n"
"s_waitcnt vmcnt(6)\n"
"v_lshlrev_b32 v132, 16, v184\n"
"v_and_b32 v135, 0xffff0000, v184\n"
"v_mfma_f32_16x16x32_bf16 v[140:143], v[96:99], v[172:175], v[140:143]\n"
"v_lshlrev_b32 v134, 16, v185\n"
"v_and_b32 v136, 0xffff0000, v185\n"
"v_mul_f32 v52, v52, v132\n"
"v_mul_f32 v53, v53, v135\n"
"v_mfma_f32_16x16x32_bf16 v[56:59], v[104:107], v[92:95], v[56:59]\n"
"v_mul_f32 v54, v54, v134\n"
"v_mul_f32 v55, v55, v136\n"
"v_mfma_f32_16x16x32_bf16 v[140:143], v[104:107], v[180:183], v[140:143]\n"
"s_waitcnt lgkmcnt(0)\n"
"s_barrier\n"
"v_mfma_f32_16x16x32_bf16 v[56:59], v[100:103], v[152:155], v[56:59]\n"
"ds_read_b64_tr_b16 v[72:73], v232 offset:32768\n"
"ds_read_b64_tr_b16 v[74:75], v234 offset:32768\n"
"v_sub_f32 v136, 1.0, v128\n"
"v_add_f32 v148, v128, v128\n"
"v_mfma_f32_16x16x32_bf16 v[140:143], v[100:103], v[208:211], v[140:143]\n"
"ds_read_b64_tr_b16 v[96:97], v232 offset:33792\n"
"ds_read_b64_tr_b16 v[98:99], v234 offset:33792\n"
"v_cvt_pk_bf16_f32 v136, 0, v136\n"
"v_lshlrev_b32 v149, 16, v156\n"
"v_mfma_f32_16x16x32_bf16 v[56:59], v[108:111], v[192:195], v[56:59]\n"
"ds_read_b64_tr_b16 v[100:101], v232 offset:34816\n"
"ds_read_b64_tr_b16 v[102:103], v234 offset:34816\n"
"v_lshlrev_b32 v150, 16, v158\n"
"v_lshlrev_b32 v151, 16, v157\n"
"v_mfma_f32_16x16x32_bf16 v[140:143], v[108:111], v[212:215], v[140:143]\n"
"ds_read_b64_tr_b16 v[104:105], v232 offset:35840\n"
"ds_read_b64_tr_b16 v[106:107], v234 offset:35840\n"
"v_lshlrev_b32 v161, 16, v159\n"
"v_mfma_f32_16x16x32_bf16 v[56:59], v[64:67], v[196:199], v[56:59]\n"
"ds_read_b64_tr_b16 v[108:109], v232 offset:36864\n"
"ds_read_b64_tr_b16 v[110:111], v234 offset:36864\n"
"ds_read_b128 v[112:115], v233 offset:4160\n"
"ds_read_b128 v[120:123], v233 offset:5184\n"
"v_mfma_f32_16x16x32_bf16 v[140:143], v[64:67], v[216:219], v[140:143]\n"
"v_mul_f32 v64, v128, v206\n"
"v_mul_f32 v65, v128, v207\n"
"v_mul_f32 v66, v128, v204\n"
"v_mul_f32 v67, v128, v205\n"
"v_mfma_f32_16x16x32_bf16 v[56:59], v[116:119], v[200:203], v[56:59]\n"
"ds_read_b64_tr_b16 v[132:133], v232 offset:37888\n"
"ds_read_b64_tr_b16 v[134:135], v234 offset:37888\n"
"v_fmac_f32 v64, v136, v149\n"
"v_fmac_f32 v65, v136, v150\n"
"v_mfma_f32_16x16x32_bf16 v[140:143], v[116:119], v[220:223], v[140:143]\n"
"v_fmac_f32 v66, v136, v151\n"
"v_fmac_f32 v67, v136, v161\n"
"s_waitcnt vmcnt(5)\n"
"v_lshlrev_b32 v149, 16, v176\n"
"s_waitcnt lgkmcnt(10)\n"
"v_mfma_f32_16x16x32_bf16 v[116:119], v[72:75], v[8:11], 0\n"
"ds_read_b64_tr_b16 v[8:9], v232 offset:38912\n"
"ds_read_b64_tr_b16 v[10:11], v234 offset:38912\n"
"v_and_b32 v150, 0xffff0000, v176\n"
"v_lshlrev_b32 v151, 16, v177\n"
"v_mfma_f32_16x16x32_bf16 v[136:139], v[72:75], v[24:27], 0\n"
"v_and_b32 v161, 0xffff0000, v177\n"
"v_mul_f32 v64, v64, v149\n"
"v_mul_f32 v65, v65, v150\n"
"v_mul_f32 v66, v66, v151\n"
"v_mfma_f32_16x16x32_bf16 v[116:119], v[96:99], v[12:15], v[116:119]\n"
"ds_read_b64_tr_b16 v[12:13], v232 offset:39936\n"
"ds_read_b64_tr_b16 v[14:15], v234 offset:39936\n"
"v_mul_f32 v67, v67, v161\n"
"v_sub_f32 v72, 1.0, v130\n"
"v_mfma_f32_16x16x32_bf16 v[136:139], v[96:99], v[28:31], v[136:139]\n"
"s_waitcnt lgkmcnt(7)\n"
"v_add_f32 v112, v112, v56\n"
"v_add_f32 v113, v113, v57\n"
"v_add_f32 v114, v114, v58\n"
"v_add_f32 v115, v115, v59\n"
"v_mfma_f32_16x16x32_bf16 v[116:119], v[100:103], v[16:19], v[116:119]\n"
"ds_read_b64_tr_b16 v[16:17], v232 offset:40960\n"
"ds_read_b64_tr_b16 v[18:19], v234 offset:40960\n"
"s_waitcnt lgkmcnt(8)\n"
"v_add_f32 v120, v120, v140\n"
"v_add_f32 v121, v121, v141\n"
"v_add_f32 v122, v122, v142\n"
"v_mfma_f32_16x16x32_bf16 v[136:139], v[100:103], v[32:35], v[136:139]\n"
"v_add_f32 v123, v123, v143\n"
"v_add_f32 v32, v130, v130\n"
"v_cvt_pk_bf16_f32 v72, 0, v72\n"
"v_and_b32 v33, 0xffff0000, v156\n"
"v_mfma_f32_16x16x32_bf16 v[116:119], v[104:107], v[20:23], v[116:119]\n"
"ds_read_b64_tr_b16 v[20:21], v232 offset:41984\n"
"ds_read_b64_tr_b16 v[22:23], v234 offset:41984\n"
"v_fmac_f32_dpp v144, v112, v188 quad_perm:[0,1,2,3] row_mask:0xf bank_mask:0x3\n"
"v_fmac_f32_dpp v145, v120, v188 quad_perm:[0,1,2,3] row_mask:0xf bank_mask:0x3\n"
"v_fmac_f32_dpp v146, v112, v188 row_ror:8 row_mask:0xf bank_mask:0x3\n"
"v_mfma_f32_16x16x32_bf16 v[136:139], v[104:107], v[36:39], v[136:139]\n"
"v_fmac_f32_dpp v147, v120, v188 row_ror:8 row_mask:0xf bank_mask:0x3\n"
"v_fmac_f32_dpp v144, v114, v188 row_ror:8 row_mask:0xf bank_mask:0xc\n"
"v_fmac_f32_dpp v145, v122, v188 row_ror:8 row_mask:0xf bank_mask:0xc\n"
"v_fmac_f32_dpp v146, v114, v188 quad_perm:[0,1,2,3] row_mask:0xf bank_mask:0xc\n"
"s_waitcnt lgkmcnt(8)\n"
"v_mfma_f32_16x16x32_bf16 v[116:119], v[108:111], v[0:3], v[116:119]\n"
"ds_read_b64_tr_b16 v[0:1], v232 offset:43008\n"
"ds_read_b64_tr_b16 v[2:3], v234 offset:43008\n"
"v_fmac_f32_dpp v147, v122, v188 quad_perm:[0,1,2,3] row_mask:0xf bank_mask:0xc\n"
"v_fmac_f32_dpp v52, v113, v160 quad_perm:[0,1,2,3] row_mask:0xf bank_mask:0x3\n"
"v_fmac_f32_dpp v53, v121, v160 quad_perm:[0,1,2,3] row_mask:0xf bank_mask:0x3\n"
"v_mfma_f32_16x16x32_bf16 v[136:139], v[108:111], v[40:43], v[136:139]\n"
"v_fmac_f32_dpp v54, v113, v160 row_ror:8 row_mask:0xf bank_mask:0x3\n"
"v_fmac_f32_dpp v55, v121, v160 row_ror:8 row_mask:0xf bank_mask:0x3\n"
"v_fmac_f32_dpp v52, v115, v160 row_ror:8 row_mask:0xf bank_mask:0xc\n"
"v_fmac_f32_dpp v53, v123, v160 row_ror:8 row_mask:0xf bank_mask:0xc\n"
"v_fmac_f32_dpp v54, v115, v160 quad_perm:[0,1,2,3] row_mask:0xf bank_mask:0xc\n"
"v_mfma_f32_16x16x32_bf16 v[116:119], v[132:135], v[4:7], v[116:119]\n"
"ds_read_b64_tr_b16 v[4:5], v232 offset:44032\n"
"ds_read_b64_tr_b16 v[6:7], v234 offset:44032\n"
"v_fmac_f32_dpp v55, v123, v160 quad_perm:[0,1,2,3] row_mask:0xf bank_mask:0xc\n"
"global_store_dwordx4 v231, v[144:147], s[90:91] offset:-4096 sc0 sc1\n"
"global_store_dwordx4 v231, v[52:55], s[90:91] sc0 sc1\n"
"v_mfma_f32_16x16x32_bf16 v[136:139], v[132:135], v[68:71], v[136:139]\n"
"v_and_b32 v34, 0xffff0000, v158\n"
"v_and_b32 v35, 0xffff0000, v157\n"
"v_and_b32 v36, 0xffff0000, v159\n"
"s_waitcnt lgkmcnt(8)\n"
"v_mfma_f32_16x16x32_bf16 v[116:119], v[8:11], v[44:47], v[116:119]\n"
"ds_read_b64_tr_b16 v[24:25], v232 offset:45056\n"
"ds_read_b64_tr_b16 v[26:27], v234 offset:45056\n"
"v_mul_f32 v28, v130, v206\n"
"v_mul_f32 v29, v130, v207\n"
"v_mfma_f32_16x16x32_bf16 v[136:139], v[8:11], v[76:79], v[136:139]\n"
"v_mul_f32 v30, v130, v204\n"
"v_mul_f32 v31, v130, v205\n"
"v_fmac_f32 v28, v72, v33\n"
"v_fmac_f32 v29, v72, v34\n"
"v_mfma_f32_16x16x32_bf16 v[116:119], v[12:15], v[48:51], v[116:119]\n"
"ds_read_b64_tr_b16 v[8:9], v232 offset:46080\n"
"ds_read_b64_tr_b16 v[10:11], v234 offset:46080\n"
"v_fmac_f32 v30, v72, v35\n"
"v_fmac_f32 v31, v72, v36\n"
"v_mfma_f32_16x16x32_bf16 v[136:139], v[12:15], v[80:83], v[136:139]\n"
"s_waitcnt vmcnt(6)\n"
"v_lshlrev_b32 v33, 16, v164\n"
"v_and_b32 v34, 0xffff0000, v164\n"
"v_lshlrev_b32 v35, 16, v165\n"
"s_waitcnt lgkmcnt(8)\n"
"v_mfma_f32_16x16x32_bf16 v[116:119], v[16:19], v[60:63], v[116:119]\n"
"ds_read_b64_tr_b16 v[12:13], v232 offset:47104\n"
"ds_read_b64_tr_b16 v[14:15], v234 offset:47104\n"
"v_and_b32 v36, 0xffff0000, v165\n"
"v_mul_f32 v28, v28, v33\n"
"v_mfma_f32_16x16x32_bf16 v[136:139], v[16:19], v[124:127], v[136:139]\n"
"v_mul_f32 v29, v29, v34\n"
"v_mul_f32 v30, v30, v35\n"
"v_mul_f32 v31, v31, v36\n"
"v_mfma_f32_16x16x32_bf16 v[116:119], v[20:23], v[84:87], v[116:119]\n"
"ds_read_b64_tr_b16 v[16:17], v232 offset:48128\n"
"ds_read_b64_tr_b16 v[18:19], v234 offset:48128\n"
"v_mfma_f32_16x16x32_bf16 v[136:139], v[20:23], v[168:171], v[136:139]\n"
"s_waitcnt lgkmcnt(8)\n"
"v_mfma_f32_16x16x32_bf16 v[116:119], v[0:3], v[88:91], v[116:119]\n"
"v_mfma_f32_16x16x32_bf16 v[136:139], v[0:3], v[172:175], v[136:139]\n"
"v_mfma_f32_16x16x32_bf16 v[116:119], v[4:7], v[92:95], v[116:119]\n"
"v_mfma_f32_16x16x32_bf16 v[136:139], v[4:7], v[180:183], v[136:139]\n"
"s_waitcnt lgkmcnt(0)\n"
"s_barrier\n"
"v_mfma_f32_16x16x32_bf16 v[116:119], v[24:27], v[152:155], v[116:119]\n"
"v_mfma_f32_16x16x32_bf16 v[136:139], v[24:27], v[208:211], v[136:139]\n"
"v_mfma_f32_16x16x32_bf16 v[116:119], v[8:11], v[192:195], v[116:119]\n"
"v_mfma_f32_16x16x32_bf16 v[136:139], v[8:11], v[212:215], v[136:139]\n"
"v_mfma_f32_16x16x32_bf16 v[116:119], v[12:15], v[196:199], v[116:119]\n"
"ds_read_b128 v[0:3], v233 offset:6208\n"
"ds_read_b128 v[4:7], v233 offset:7232\n"
"v_mfma_f32_16x16x32_bf16 v[136:139], v[12:15], v[216:219], v[136:139]\n"
"v_mfma_f32_16x16x32_bf16 v[116:119], v[16:19], v[200:203], v[116:119]\n"
"v_mfma_f32_16x16x32_bf16 v[136:139], v[16:19], v[220:223], v[136:139]\n"
"s_waitcnt lgkmcnt(1)\n"
"s_nop 5\n"
"v_add_f32 v0, v0, v116\n"
"v_add_f32 v1, v1, v117\n"
"v_add_f32 v2, v2, v118\n"
"v_add_f32 v3, v3, v119\n"
"s_waitcnt lgkmcnt(0)\n"
"v_add_f32 v4, v4, v136\n"
"v_add_f32 v5, v5, v137\n"
"v_add_f32 v6, v6, v138\n"
"v_add_f32 v7, v7, v139\n"
"v_fmac_f32_dpp v64, v0, v148 quad_perm:[0,1,2,3] row_mask:0xf bank_mask:0x3\n"
"v_fmac_f32_dpp v65, v4, v148 quad_perm:[0,1,2,3] row_mask:0xf bank_mask:0x3\n"
"v_fmac_f32_dpp v66, v0, v148 row_ror:8 row_mask:0xf bank_mask:0x3\n"
"v_fmac_f32_dpp v67, v4, v148 row_ror:8 row_mask:0xf bank_mask:0x3\n"
"v_fmac_f32_dpp v64, v2, v148 row_ror:8 row_mask:0xf bank_mask:0xc\n"
"v_fmac_f32_dpp v65, v6, v148 row_ror:8 row_mask:0xf bank_mask:0xc\n"
"v_fmac_f32_dpp v66, v2, v148 quad_perm:[0,1,2,3] row_mask:0xf bank_mask:0xc\n"
"v_fmac_f32_dpp v67, v6, v148 quad_perm:[0,1,2,3] row_mask:0xf bank_mask:0xc\n"
"global_store_dwordx4 v231, v[64:67], s[92:93] offset:-4096 sc0 sc1\n"
"v_fmac_f32_dpp v28, v1, v32 quad_perm:[0,1,2,3] row_mask:0xf bank_mask:0x3\n"
"v_fmac_f32_dpp v29, v5, v32 quad_perm:[0,1,2,3] row_mask:0xf bank_mask:0x3\n"
"v_fmac_f32_dpp v30, v1, v32 row_ror:8 row_mask:0xf bank_mask:0x3\n"
"v_fmac_f32_dpp v31, v5, v32 row_ror:8 row_mask:0xf bank_mask:0x3\n"
"v_fmac_f32_dpp v28, v3, v32 row_ror:8 row_mask:0xf bank_mask:0xc\n"
"v_fmac_f32_dpp v29, v7, v32 row_ror:8 row_mask:0xf bank_mask:0xc\n"
"v_fmac_f32_dpp v30, v3, v32 quad_perm:[0,1,2,3] row_mask:0xf bank_mask:0xc\n"
"v_fmac_f32_dpp v31, v7, v32 quad_perm:[0,1,2,3] row_mask:0xf bank_mask:0xc\n"
"global_store_dwordx4 v231, v[28:31], s[92:93] sc0 sc1\n"
"s_lshl2_add_u32 s13, %1, s15\n"
"v_mov_b32 v0, 0\n"
"s_cmp_eq_u32 s13, 0x78\n"
"s_cbranch_scc0 .Lskip\n"
"global_store_dwordx2 v0, v[224:225], s[30:31] sc1\n"
".Lskip:\n"
"s_endpgm\n"
".Lr1:\n"
"s_mov_b32 s12, 0x40000\n"
".Ll1:\n"
"s_sleep 3\n"
"s_nop 7\n"
"buffer_load_dwordx2 v[28:29], v220, s[76:79], 0 offen offset:3584 sc1\n"
"buffer_load_dwordx2 v[26:27], v220, s[76:79], 0 offen offset:3072 sc1\n"
"buffer_load_dwordx2 v[24:25], v220, s[76:79], 0 offen offset:2560 sc1\n"
"buffer_load_dwordx2 v[44:45], v220, s[76:79], 0 offen offset:2048 sc1\n"
"s_waitcnt vmcnt(0)\n"
"v_xor_b32 v28, v28, v244\n"
"v_xor_b32 v29, v29, v244\n"
"v_xor_b32 v26, v26, v243\n"
"v_xor_b32 v27, v27, v243\n"
"v_xor_b32 v24, v24, v242\n"
"v_xor_b32 v25, v25, v242\n"
"v_xor_b32 v44, v44, v241\n"
"v_xor_b32 v45, v45, v241\n"
"v_or3_b32 v30, v28, v29, v26\n"
"v_or3_b32 v30, v30, v27, v24\n"
"v_or3_b32 v30, v30, v25, v44\n"
"v_bitop3_b32 v30, v30, v45, s96 bitop3:0xa8\n"
"v_cmp_ne_u32 vcc, 0, v30\n"
"s_nop 0\n"
"s_cbranch_vccz .Lp1\n"
"s_sub_u32 s12, s12, 1\n"
"s_cbranch_scc0 .Ll1\n"
".Lp1:\n"
"s_nop 7\n"
"s_nop 7\n"
"s_branch .Lb1\n"
".Lr2:\n"
"s_mov_b32 s12, 0x40000\n"
".Ll2:\n"
"s_sleep 2\n"
"s_nop 7\n"
"global_load_dwordx4 v[112:115], v227, s[18:19] sc1\n"
"global_load_dwordx4 v[116:119], v227, s[20:21] sc1\n"
"s_waitcnt vmcnt(0)\n"
"v_xor_b32 v132, s10, v113\n"
"v_bitop3_b32 v132, v132, v115, s10 bitop3:0xf6\n"
"v_bitop3_b32 v132, v132, v117, s10 bitop3:0xf6\n"
"v_bitop3_b32 v132, v132, v119, s10 bitop3:0xf6\n"
"v_cmp_ne_u32 vcc, 0, v132\n"
"s_nop 0\n"
"s_cbranch_vccz .Lp2\n"
"s_sub_u32 s12, s12, 1\n"
"s_cbranch_scc0 .Ll2\n"
".Lp2:\n"
"s_nop 7\n"
"s_nop 7\n"
"s_branch .Lb2\n"
".Lr3:\n"
"s_mov_b32 s12, 0x40000\n"
".Ll3:\n"
"s_sleep 2\n"
"s_nop 7\n"
"global_load_dwordx4 v[120:123], v227, s[22:23] sc1\n"
"global_load_dwordx4 v[128:131], v227, s[24:25] sc1\n"
"s_waitcnt vmcnt(0)\n"
"v_xor_b32 v136, s10, v121\n"
"v_bitop3_b32 v136, v136, v123, s10 bitop3:0xf6\n"
"v_bitop3_b32 v136, v136, v129, s10 bitop3:0xf6\n"
"v_bitop3_b32 v136, v136, v131, s10 bitop3:0xf6\n"
"v_cmp_ne_u32 vcc, 0, v136\n"
"s_nop 0\n"
"s_cbranch_vccz .Lp3\n"
"s_sub_u32 s12, s12, 1\n"
"s_cbranch_scc0 .Ll3\n"
".Lp3:\n"
"s_nop 7\n"
"s_nop 7\n"
"s_branch .Lb3\n"
        :: "s"(__builtin_amdgcn_kernarg_segment_ptr()), "s"(b), "s"(wid), "s"(__builtin_amdgcn_readfirstlane(dlat))
        : "memory", "vcc", "scc", "v0", "v1", "v2", "v3", "v4", "v5", "v6", "v7", "v8", "v9", "v10", "v11", "v12", "v13", "v14", "v15", "v16", "v17", "v18", "v19", "v20", "v21", "v22", "v23", "v24", "v25", "v26", "v27", "v28", "v29", "v30", "v31", "v32", "v33", "v34", "v35", "v36", "v37", "v38", "v39", "v40", "v41", "v42", "v43", "v44", "v45", "v46", "v47", "v48", "v49", "v50", "v51", "v52", "v53", "v54", "v55", "v56", "v57", "v58", "v59", "v60", "v61", "v62", "v63", "v64", "v65", "v66", "v67", "v68", "v69", "v70", "v71", "v72", "v73", "v74", "v75", "v76", "v77", "v78", "v79", "v80", "v81", "v82", "v83", "v84", "v85", "v86", "v87", "v88", "v89", "v90", "v91", "v92", "v93", "v94", "v95", "v96", "v97", "v98", "v99", "v100", "v101", "v102", "v103", "v104", "v105", "v106", "v107", "v108", "v109", "v110", "v111", "v112", "v113", "v114", "v115", "v116", "v117", "v118", "v119", "v120", "v121", "v122", "v123", "v124", "v125", "v126", "v127", "v128", "v129", "v130", "v131", "v132", "v133", "v134", "v135", "v136", "v137", "v138", "v139", "v140", "v141", "v142", "v143", "v144", "v145", "v146", "v147", "v148", "v149", "v150", "v151", "v152", "v153", "v154", "v155", "v156", "v157", "v158", "v159", "v160", "v161", "v162", "v163", "v164", "v165", "v166", "v167", "v168", "v169", "v170", "v171", "v172", "v173", "v174", "v175", "v176", "v177", "v178", "v179", "v180", "v181", "v182", "v183", "v184", "v185", "v186", "v187", "v188", "v189", "v190", "v191", "v192", "v193", "v194", "v195", "v196", "v197", "v198", "v199", "v200", "v201", "v202", "v203", "v204", "v205", "v206", "v207", "v208", "v209", "v210", "v211", "v212", "v213", "v214", "v215", "v216", "v217", "v218", "v219", "v220", "v221", "v222", "v223", "v224", "v225", "v226", "v227", "v228", "v229", "v230", "v231", "v232", "v233", "v234", "v235", "v236", "v237", "v238", "v239", "v240", "v241", "v242", "v243", "v244", "v245", "v246", "v247", "v248", "v249", "v250", "v251", "v252", "v253", "v254", "v255", "s7", "s8", "s9", "s10", "s11", "s12", "s13", "s14", "s15", "s16", "s17", "s18", "s19", "s20", "s21", "s22", "s23", "s24", "s25", "s26", "s27", "s28", "s29", "s30", "s31", "s32", "s33", "s34", "s35", "s36", "s37", "s38", "s39", "s40", "s41", "s42", "s43", "s44", "s45", "s46", "s47", "s48", "s49", "s50", "s51", "s52", "s53", "s54", "s55", "s56", "s57", "s58", "s59", "s60", "s61", "s62", "s63", "s64", "s65", "s66", "s67", "s68", "s69", "s70", "s71", "s72", "s73", "s74", "s75", "s76", "s77", "s78", "s79", "s80", "s81", "s82", "s83", "s84", "s85", "s86", "s87", "s88", "s89", "s90", "s91", "s92", "s93", "s94", "s95", "s96", "s97", "s98", "s99");
}

extern "C" void kernel_launch(void* const* d_in, const int* in_sizes, int n_in, void* d_out, int out_size, void* d_ws, size_t ws_size, hipStream_t stream) {
    hipFuncSetAttribute((const void*)mk_kernel, hipFuncAttributeMaxDynamicSharedMemorySize, 161408);
    mk_kernel<<<256, 512, 161408, stream>>>((const u16*)d_in[0], (const u16*)d_in[3], (const u16*)d_in[4], (const u16*)d_in[10], (const u16*)d_in[11], (const u16*)d_in[15], (const u16*)d_in[13], (char*)d_ws,
                                           (const u16*)d_in[5], (const u16*)d_in[6], (float*)d_out, (const u16*)d_in[8], (const u16*)d_in[12], (const u16*)d_in[14],
                                           (const u16*)d_in[1], (const u16*)d_in[2], (const u16*)d_in[7]);
}
```
